```python
import torch

def _softmax(x, axis=-1):
    return torch.softmax(x, dim=axis)
def _gelu(x):
    return torch.nn.functional.gelu(x, approximate="tanh")
def _silu(x):
    return torch.nn.functional.silu(x)
def _sigmoid(x):
    return torch.sigmoid(x)
def _layernorm(x, axis=-1, eps=1e-5):
    m = x.mean(dim=axis, keepdim=True)
    v = x.var(dim=axis, keepdim=True, unbiased=False)
    return (x - m) / torch.sqrt(v + eps)
def _scatter_add(tgt, idx, src, axis=0):
    return tgt.clone().index_add_(axis, idx, src)
def _segment_sum(src, seg, n):
    out = torch.zeros((n,) + src.shape[1:], dtype=src.dtype)
    return out.index_add_(0, seg, src)
def _pad(x, pw):
    flat = tuple(v for pair in reversed(pw) for v in pair)
    return torch.nn.functional.pad(x, flat)

kernel_name = 'parallel_sum__sliding_local_dilated__glu_gated_glu__prompt'

def setup_inputs():

    _w = (lambda a: (lambda t: t.to(torch.bfloat16) if t.is_floating_point() else t)(torch.as_tensor(a)))
    def _A_setup():
        B, S, D = 1, 2048, 1024
        return {
            "x": _w((lambda *s: torch.randn(*s, dtype=torch.bfloat16) * 0.05)(B, S, D)),
            "wqkv": _w((lambda *s: torch.randn(*s, dtype=torch.bfloat16) * 0.05)(D, 3 * D)),
            "wo": _w((lambda *s: torch.randn(*s, dtype=torch.bfloat16) * 0.05)(D, D)),
        }
    _A_in = _A_setup()

    def _B_setup():
        B, S, D, F = 1, 2048, 1024, 3584
        return {
            "x": _w((lambda *s: torch.randn(*s, dtype=torch.bfloat16) * 0.05)(B, S, D)),
            "w_gate": _w((lambda *s: torch.randn(*s, dtype=torch.bfloat16) * 0.05)(D, F)),
            "w_up": _w((lambda *s: torch.randn(*s, dtype=torch.bfloat16) * 0.05)(D, F)),
            "w_down": _w((lambda *s: torch.randn(*s, dtype=torch.bfloat16) * 0.05)(F, D)),
        }
    _B_in = _B_setup()

    return {
        "A_x": _A_in["x"],
        "A_wqkv": _A_in["wqkv"],
        "A_wo": _A_in["wo"],
        "B_x": _B_in["x"],
        "B_w_gate": _B_in["w_gate"],
        "B_w_up": _B_in["w_up"],
        "B_w_down": _B_in["w_down"],
    }

def reference(A_x, A_wqkv, A_wo, B_x, B_w_gate, B_w_up, B_w_down):
    def _A_ref(x, wqkv, wo):
        B, S, D = x.shape
        H = 32; Dh = D // H
        W, dil = 8, 2
        qkv = (lambda x, *d: x.permute(*d))((x @ wqkv).reshape(B, S, 3, H, Dh), 2, 0, 3, 1, 4)
        q, k, v = qkv[0], qkv[1], qkv[2]
        scale = Dh ** -0.5
        pos = torch.arange(S)
        off = torch.arange(W) * dil
        idx = pos[:, None] - off[None, :]
        mask = idx >= 0
        idx_c = torch.where(mask, idx, 0).reshape(-1)
        kg = k[:, :, idx_c, :].reshape(B, H, S, W, Dh)
        vg = v[:, :, idx_c, :].reshape(B, H, S, W, Dh)
        logits = torch.einsum("bhsd,bhswd->bhsw", q, kg) * scale
        logits = torch.where(mask[None, None, :, :], logits, -1e9)
        attn = _softmax(logits, axis=-1)
        o = torch.einsum("bhsw,bhswd->bhsd", attn, vg)
        return (lambda x, *d: x.permute(*d))(o, 0, 2, 1, 3).reshape(B, S, D) @ wo

    def _B_ref(x, w_gate, w_up, w_down):
        g = torch.einsum("bsd,df->bsf", x, w_gate)
        u = torch.einsum("bsd,df->bsf", x, w_up)
        h = _sigmoid(g) * u
        return torch.einsum("bsf,fd->bsd", h, w_down)

    x = A_x
    xn = _layernorm(x, axis=-1)
    return x + _A_ref(xn, A_wqkv, A_wo) + _B_ref(xn, B_w_gate, B_w_up, B_w_down)
```

```cpp
#include <hip/hip_runtime.h>
#include <hip/hip_bf16.h>
namespace refk {
typedef unsigned short u16;
__device__ __forceinline__ float b2f(u16 v) { return __uint_as_float(((unsigned)v) << 16); }
__device__ __forceinline__ u16 f2b(float f) { unsigned u = __float_as_uint(f); u += 0x7FFF + ((u >> 16) & 1); return (u16)(u >> 16); }
__device__ __forceinline__ float rb(float f) { return b2f(f2b(f)); }

__global__ void ln(const u16* x, u16* xn) {
  int row = blockIdx.x; int t = threadIdx.x;
  __shared__ double red[256];
  double s = 0; for (int i = t; i < 1024; i += 256) s += b2f(x[row * 1024 + i]);
  red[t] = s; __syncthreads();
  for (int o = 128; o > 0; o >>= 1) { if (t < o) red[t] += red[t + o]; __syncthreads(); }
  double mean = red[0] / 1024.0; __syncthreads();
  double q = 0; for (int i = t; i < 1024; i += 256) { double d = b2f(x[row * 1024 + i]) - mean; q += d * d; }
  red[t] = q; __syncthreads();
  for (int o = 128; o > 0; o >>= 1) { if (t < o) red[t] += red[t + o]; __syncthreads(); }
  double var = red[0] / 1024.0;
  float m = rb((float)mean), v = rb((float)var);
  float den = rb(sqrtf(rb(v + 1e-5f)));
  for (int i = t; i < 1024; i += 256) { float d = rb(b2f(x[row * 1024 + i]) - m); xn[row * 1024 + i] = f2b(d / den); }
}
__global__ void gemm(const u16* A, const u16* B, u16* C, int M, int N, int K) {
  __shared__ float As[16][17], Bs[16][17];
  int tx = threadIdx.x, ty = threadIdx.y; int row = blockIdx.y * 16 + ty, col = blockIdx.x * 16 + tx;
  float acc = 0;
  for (int k0 = 0; k0 < K; k0 += 16) {
    As[ty][tx] = b2f(A[(size_t)row * K + k0 + tx]); Bs[ty][tx] = b2f(B[(size_t)(k0 + ty) * N + col]);
    __syncthreads();
    for (int k = 0; k < 16; ++k) acc += As[ty][k] * Bs[k][tx];
    __syncthreads();
  }
  C[(size_t)row * N + col] = f2b(acc);
}
__global__ void attn(const u16* qkv, u16* o) {
  int idx = blockIdx.x * blockDim.x + threadIdx.x;
  int s = idx >> 5, h = idx & 31;
  const float scale = 0.17677669529663687f;
  float lg[8];
  for (int w = 0; w < 8; ++w) {
    int j = s - 2 * w;
    if (j < 0) { lg[w] = rb(-1e9f); continue; }
    float a = 0; for (int d = 0; d < 32; ++d) a += b2f(qkv[s * 3072 + h * 32 + d]) * b2f(qkv[j * 3072 + 1024 + h * 32 + d]);
    lg[w] = rb(rb(a) * scale);
  }
  float m = lg[0]; for (int w = 1; w < 8; ++w) m = fmaxf(m, lg[w]);
  float sum = 0; for (int w = 0; w < 8; ++w) { lg[w] = expf(lg[w] - m); sum += lg[w]; }
  for (int w = 0; w < 8; ++w) lg[w] = rb(lg[w] / sum);
  for (int d = 0; d < 32; ++d) {
    float a = 0;
    for (int w = 0; w < 8; ++w) { int j = s - 2 * w; if (j < 0) j = 0; a += lg[w] * b2f(qkv[j * 3072 + 2048 + h * 32 + d]); }
    o[s * 1024 + h * 32 + d] = f2b(a);
  }
}
__global__ void glu(const u16* g, const u16* u, u16* h, int n) {
  int i = blockIdx.x * blockDim.x + threadIdx.x; if (i >= n) return;
  float s = rb(1.0f / (1.0f + expf(-b2f(g[i]))));
  h[i] = f2b(s * b2f(u[i]));
}
__global__ void fin(const u16* x, const u16* a, const u16* b, u16* out, int n) {
  int i = blockIdx.x * blockDim.x + threadIdx.x; if (i >= n) return;
  out[i] = f2b(rb(b2f(x[i]) + b2f(a[i])) + b2f(b[i]));
}
inline void run(void* const* d_in, void* d_out, void* d_ws, hipStream_t st) {
  const u16* x = (const u16*)d_in[0]; const u16* wqkv = (const u16*)d_in[1]; const u16* wo = (const u16*)d_in[2];
  const u16* wg = (const u16*)d_in[4]; const u16* wu = (const u16*)d_in[5]; const u16* wd = (const u16*)d_in[6];
  u16* p = (u16*)d_ws;
  u16* xn = p; p += 2048 * 1024; u16* qkv = p; p += 2048 * 3072; u16* o = p; p += 2048 * 1024;
  u16* g = p; p += 2048 * 3584; u16* u = p; p += 2048 * 3584; u16* h = p; p += 2048 * 3584;
  u16* a = p; p += 2048 * 1024; u16* b = p; p += 2048 * 1024;
  ln<<<2048, 256, 0, st>>>(x, xn);
  gemm<<<dim3(3072 / 16, 128), dim3(16, 16), 0, st>>>(xn, wqkv, qkv, 2048, 3072, 1024);
  gemm<<<dim3(3584 / 16, 128), dim3(16, 16), 0, st>>>(xn, wg, g, 2048, 3584, 1024);
  gemm<<<dim3(3584 / 16, 128), dim3(16, 16), 0, st>>>(xn, wu, u, 2048, 3584, 1024);
  attn<<<2048 * 32 / 256, 256, 0, st>>>(qkv, o);
  glu<<<2048 * 3584 / 256, 256, 0, st>>>(g, u, h, 2048 * 3584);
  gemm<<<dim3(1024 / 16, 128), dim3(16, 16), 0, st>>>(o, wo, a, 2048, 1024, 1024);
  gemm<<<dim3(1024 / 16, 128), dim3(16, 16), 0, st>>>(h, wd, b, 2048, 1024, 3584);
  fin<<<2048 * 1024 / 256, 256, 0, st>>>(x, a, b, (u16*)d_out, 2048 * 1024);
}
}
extern "C" void kernel_launch(void* const* d_in, const int* in_sizes, int n_in, void* d_out, int out_size, void* d_ws, size_t ws_size, hipStream_t stream) {
  refk::run(d_in, d_out, d_ws, stream);
}
```

```cpp
#include <hip/hip_runtime.h>
#include <hip/hip_bf16.h>

namespace {

typedef unsigned short u16;
typedef short bf16x8 __attribute__((ext_vector_type(8)));
typedef short bf16x4 __attribute__((ext_vector_type(4)));
typedef float f32x4 __attribute__((ext_vector_type(4)));
typedef float f32x2 __attribute__((ext_vector_type(2)));
typedef unsigned u32x4 __attribute__((ext_vector_type(4)));
typedef unsigned u32x2 __attribute__((ext_vector_type(2)));
typedef __bf16 bf2 __attribute__((ext_vector_type(2)));
#define LAS __attribute__((address_space(3)))
#define GAS __attribute__((address_space(1)))

#define WAIT_V(n) asm volatile("s_waitcnt vmcnt(" #n ")" ::: "memory")
#define WAIT_L0() asm volatile("s_waitcnt lgkmcnt(0)" ::: "memory")
#define BAR() __builtin_amdgcn_s_barrier()
#define SCHED() __builtin_amdgcn_sched_barrier(0)

__device__ __forceinline__ float b2f(u16 v) { return __uint_as_float(((unsigned)v) << 16); }
__device__ __forceinline__ unsigned pk2(float lo, float hi) {
  bf2 r = {(__bf16)lo, (__bf16)hi};
  return __builtin_bit_cast(unsigned, r);
}
__device__ __forceinline__ float rbf(float f) { return (float)(__bf16)f; }
__device__ __forceinline__ bf16x8 pack8(f32x4 a, f32x4 b) {
  u32x4 r = {pk2(a[0], a[1]), pk2(a[2], a[3]), pk2(b[0], b[1]), pk2(b[2], b[3])};
  return __builtin_bit_cast(bf16x8, r);
}
__device__ __forceinline__ bf16x4 trd(LAS const char* p) {
  return __builtin_amdgcn_ds_read_tr16_b64_v4i16((LAS bf16x4*)p);
}
__device__ __forceinline__ bf16x8 cat(bf16x4 a, bf16x4 b) { return __builtin_shufflevector(a, b, 0, 1, 2, 3, 4, 5, 6, 7); }
__device__ __forceinline__ f32x4 mfma(bf16x8 a, bf16x8 b, f32x4 c) {
  return __builtin_amdgcn_mfma_f32_16x16x32_bf16(a, b, c, 0, 0, 0);
}
__device__ __forceinline__ void dma4(unsigned lds_dst, unsigned voff, const char* b0, const char* b1, const char* b2, const char* b3) {
  asm volatile("s_mov_b32 m0, %0\n\ts_nop 0\n\t"
               "global_load_lds_dwordx4 %1, %2\n\t"
               "global_load_lds_dwordx4 %1, %3 offset:1024\n\t"
               "global_load_lds_dwordx4 %1, %4 offset:2048\n\t"
               "global_load_lds_dwordx4 %1, %5 offset:3072"
               :: "s"(lds_dst), "v"(voff), "s"(b0), "s"(b1), "s"(b2), "s"(b3) : "memory");
}
__device__ __forceinline__ void dma2(unsigned lds_dst, unsigned voff, const char* b0, const char* b1) {
  asm volatile("s_mov_b32 m0, %0\n\ts_nop 0\n\t"
               "global_load_lds_dwordx4 %1, %2\n\t"
               "global_load_lds_dwordx4 %1, %3 offset:1024"
               :: "s"(lds_dst), "v"(voff), "s"(b0), "s"(b1) : "memory");
}
__device__ __forceinline__ void dma1m(unsigned lds_dst, const char* p, unsigned long long mask) {
  asm volatile("s_mov_b32 m0, %0\n\ts_mov_b64 exec, %2\n\t"
               "global_load_lds_dwordx4 %1, off\n\t"
               "s_mov_b64 exec, -1"
               :: "s"(lds_dst), "v"(p), "s"(mask) : "memory");
}
__device__ __forceinline__ LAS char* lp(unsigned a) { return (LAS char*)(size_t)a; }
__device__ __forceinline__ float xmax(float v) {
  auto a = __builtin_amdgcn_permlane16_swap(__float_as_uint(v), __float_as_uint(v), false, false);
  v = fmaxf(__uint_as_float(a[0]), __uint_as_float(a[1]));
  auto b = __builtin_amdgcn_permlane32_swap(__float_as_uint(v), __float_as_uint(v), false, false);
  return fmaxf(__uint_as_float(b[0]), __uint_as_float(b[1]));
}
__device__ __forceinline__ float xsum(float v) {
  auto a = __builtin_amdgcn_permlane16_swap(__float_as_uint(v), __float_as_uint(v), false, false);
  v = __uint_as_float(a[0]) + __uint_as_float(a[1]);
  auto b = __builtin_amdgcn_permlane32_swap(__float_as_uint(v), __float_as_uint(v), false, false);
  return __uint_as_float(b[0]) + __uint_as_float(b[1]);
}

constexpr size_t WS_XN = 0;
constexpr size_t WS_H = WS_XN + (size_t)4 * 1024 * 1024;
constexpr size_t H_BYTES = (size_t)8 * 4 * 36 * 16 * 1024;
constexpr size_t WS_X = WS_H + H_BYTES;
constexpr size_t X_BYTES = (size_t)64 * 4 * 3 * 8 * 2048;
constexpr size_t WS_CNT = WS_X + X_BYTES;


__global__ __launch_bounds__(256) void ln_kernel(const u16* __restrict__ x, char* __restrict__ ws) {
  const int lane = threadIdx.x & 63, row = blockIdx.x * 4 + (threadIdx.x >> 6);
  if (blockIdx.x == 0 && threadIdx.x < 64) *(unsigned*)(ws + WS_CNT + threadIdx.x * 256) = 0u;
  const int fq = lane & 3, kb0 = lane >> 2;
  const u16* xr = x + row * 1024 + kb0 * 32 + fq * 4;
  u32x2 raw[4];
  raw[0] = *(const u32x2*)(xr);
  raw[1] = *(const u32x2*)(xr + 16);
  raw[2] = *(const u32x2*)(xr + 512);
  raw[3] = *(const u32x2*)(xr + 528);
  float v[16];
#pragma unroll
  for (int i = 0; i < 4; ++i) {
    v[4 * i + 0] = __uint_as_float(raw[i][0] << 16); v[4 * i + 1] = __uint_as_float(raw[i][0] & 0xffff0000u);
    v[4 * i + 2] = __uint_as_float(raw[i][1] << 16); v[4 * i + 3] = __uint_as_float(raw[i][1] & 0xffff0000u);
  }
  float s = 0.f;
#pragma unroll
  for (int i = 0; i < 16; ++i) s += v[i];
#pragma unroll
  for (int o = 1; o < 64; o <<= 1) s += __shfl_xor(s, o);
  const float mean = s * (1.0f / 1024.0f);
  float q = 0.f;
#pragma unroll
  for (int i = 0; i < 16; ++i) { const float d = v[i] - mean; q += d * d; }
#pragma unroll
  for (int o = 1; o < 64; o <<= 1) q += __shfl_xor(q, o);
  const float mb16 = rbf(mean), den = rbf(sqrtf(rbf(rbf(q * (1.0f / 1024.0f)) + 1e-5f)));
  const float inv = 1.0f / den;
#pragma unroll
  for (int i = 0; i < 16; ++i) v[i] = rbf(v[i] - mb16) * inv;
  const int mb = row >> 8, R = row & 255, mf = ((R >> 5) << 1) | (R & 1), r = (R & 31) >> 1;
  char* dst = ws + WS_XN + ((size_t)(mb * 32 + kb0) * 16 + mf) * 1024 + (fq * 16 + r) * 16;
  u32x4 o0 = {pk2(v[0], v[1]), pk2(v[2], v[3]), pk2(v[4], v[5]), pk2(v[6], v[7])};
  u32x4 o1 = {pk2(v[8], v[9]), pk2(v[10], v[11]), pk2(v[12], v[13]), pk2(v[14], v[15])};
  *(u32x4*)dst = o0;
  *(u32x4*)(dst + 16 * 16 * 1024) = o1;
}

constexpr int G1_SA = 17 * 1024, G1_SB = 20 * 1024, G1_SLOT = G1_SA + G1_SB, G1_LDS = 4 * G1_SLOT;

__device__ __forceinline__ f32x4 glu4(f32x4 g, f32x4 u) {
  f32x4 r;
#pragma unroll
  for (int j = 0; j < 4; ++j) r[j] = u[j] * __builtin_amdgcn_rcpf(1.0f + __builtin_amdgcn_exp2f(g[j] * -1.4426950408889634f));
  return r;
}

__global__ __launch_bounds__(512, 2) void gemm1_kernel(const u16* __restrict__ wqkv, const u16* __restrict__ wg,
                                                       const u16* __restrict__ wu, char* __restrict__ ws) {
  extern __shared__ __attribute__((aligned(16))) char smem[];
  LAS char* lds = (LAS char*)smem;
  const int tid = threadIdx.x, lane = tid & 63;
  const int w = __builtin_amdgcn_readfirstlane(tid >> 6), g = w >> 2, wi = w & 3;
  const int b = blockIdx.x, xcd = b & 7, idx = b >> 3;
  const int mb = (xcd & 1) * 4 + (idx & 3), kq = xcd >> 1, ntl = idx >> 2;
  const bool typeA = ntl < 4;
  const int j = ntl & 3;
  const int fr = lane & 15, fq = lane >> 4;
  const int fA = 896 * kq + 64 * j;
  const int f0 = 896 * kq + 256 + 160 * j, odd = j & 1;
  const int fa = f0 + 32 * odd, fb = fa + 64, fc = odd ? f0 : f0 + 128;

  const char* c1;
  unsigned v1;
  int s1, p1, p2;
  const char* c2;
  int d1, d2;
  unsigned long long m2 = ~0ull;
  {
    const int rr = lane >> 3, pc = lane & 7, fi = (pc >> 1) ^ ((rr >> 1) & 3);
    if (g == 0) {
      int ld; const u16* q1;
      if (typeA) {
        if (wi < 3) { q1 = wqkv + wi * 1024 + 256 * kq + 64 * j; ld = 3072; } else { q1 = wg + fA; ld = 3584; }
        c2 = (const char*)(wu + fA + (size_t)(wi * 8 + rr) * 3584) + fi * 32 + (pc & 1) * 16;
      } else {
        q1 = ((wi & 2) ? wu : wg) + ((wi & 1) ? fb : fa); ld = 3584;
        c2 = (const char*)(((fi & 1) ? wu : wg) + fc + (fi >> 1) * 16 + (size_t)(wi * 8 + rr) * 3584) + (pc & 1) * 16;
      }
      c1 = (const char*)q1; v1 = rr * ld * 2 + fi * 32 + (pc & 1) * 16;
      s1 = 16 * ld - 1024; p1 = 64 * ld; p2 = 64 * 3584;
      d1 = G1_SA + wi * 4096; d2 = G1_SA + 16384 + wi * 1024;
    } else {
      c1 = ws + WS_XN + (size_t)mb * 32 * 16384 + wi * 4096; v1 = lane * 16;
      const int mh = mb > 0 ? mb - 1 : 0, i = lane & 15;
      c2 = ws + WS_XN + (size_t)mh * 32 * 16384 + (14 + (i >> 3)) * 1024 + (wi * 16 + 8 + (i & 7)) * 16;
      m2 = 0xffffull;
      s1 = 0; p1 = 16384; p2 = 16384;
      d1 = wi * 4096; d2 = 16384 + wi * 256;
    }
  }
  const unsigned lb = (unsigned)(size_t)lds;
  auto stage = [&](int slot_off) {
    dma4(lb + slot_off + d1, v1, c1, c1 + s1, c1 + 2 * s1, c1 + 3 * s1);
    dma1m(lb + slot_off + d2, c2, m2);
    c1 += p1; c2 += p2;
  };

  const int r0 = 4 * fq + ((lane & 15) >> 2);
  unsigned aoff = lb + wi * 4096 + lane * 16;
  unsigned hoffA = lb + 16384 + lane * 16;
  unsigned boff0 = lb + G1_SA + r0 * 128 + 32 * ((2 * g) ^ ((r0 >> 1) & 3)) + 8 * (lane & 3);
  unsigned boff1 = boff0 ^ 32;
  unsigned hoffB = (boff0 ^ (32 * (wi & 1))) + (1 + (wi >> 1)) * 4096;

  f32x4 acc[4][10];
  f32x4 hacc = {0.f, 0.f, 0.f, 0.f};
#pragma unroll
  for (int i = 0; i < 4; ++i)
#pragma unroll
    for (int n = 0; n < 10; ++n) acc[i][n] = (f32x4){0.f, 0.f, 0.f, 0.f};

  stage(0); stage(G1_SLOT); stage(2 * G1_SLOT);
#pragma unroll
  for (int i = 0; i < 4; ++i)
#pragma unroll
    for (int n = 0; n < 10; ++n) asm volatile("" : "+v"(acc[i][n]));
  WAIT_V(10);
  BAR();
  if (g == 1) BAR();

  int sl = 0;
  for (int q = 0; q < 32; ++q) {
    bf16x8 a[4], bfr[10], ha, hb;
#pragma unroll
    for (int i = 0; i < 4; ++i) a[i] = *(const LAS bf16x8*)(lp(aoff) + i * 1024);
#pragma unroll
    for (int c = 0; c < 5; ++c) {
      bfr[2 * c] = cat(trd(lp(boff0) + c * 4096), trd(lp(boff0) + c * 4096 + 2048));
      bfr[2 * c + 1] = cat(trd(lp(boff1) + c * 4096), trd(lp(boff1) + c * 4096 + 2048));
    }
    ha = *(const LAS bf16x8*)lp(hoffA);
    hb = cat(trd(lp(hoffB)), trd(lp(hoffB) + 2048));
    SCHED();
    const int nxt = (sl == 0 ? 3 : sl - 1) * G1_SLOT;
    sl = (sl + 1) & 3;
    const int dlt = (sl == 0) ? -3 * G1_SLOT : G1_SLOT;
    aoff += dlt; hoffA += dlt; boff0 += dlt; boff1 += dlt; hoffB += dlt;
    if (q < 29) { stage(nxt); WAIT_V(10); } else { WAIT_V(0); }
    WAIT_L0();
    SCHED();
    BAR();
    SCHED();
    __builtin_amdgcn_s_setprio(1);
#pragma unroll
    for (int n = 0; n < 10; ++n)
#pragma unroll
      for (int i = 0; i < 4; ++i) acc[i][n] = mfma(bfr[n], a[i], acc[i][n]);
    hacc = mfma(hb, ha, hacc);
    __builtin_amdgcn_s_setprio(0);
    SCHED();
    if (q < 31 || g == 0) BAR();
    SCHED();
  }

  char* Ht = ws + WS_H + (size_t)(mb * 4 + kq) * 36 * 16384 + wi * 4096 + lane * 16;
  if (!typeA) {
    const int kba = 8 + ((fa - 896 * kq) >> 5) + g, kbb = kba + 2, kbc = 8 + ((fc - 896 * kq) >> 5);
#pragma unroll
    for (int i = 0; i < 4; ++i) {
      *(bf16x8*)(Ht + kba * 16384 + i * 1024) = pack8(glu4(acc[i][0], acc[i][4]), glu4(acc[i][1], acc[i][5]));
      *(bf16x8*)(Ht + kbb * 16384 + i * 1024) = pack8(glu4(acc[i][2], acc[i][6]), glu4(acc[i][3], acc[i][7]));
      const f32x4 h = glu4(acc[i][8], acc[i][9]);
      *(u32x2*)(Ht + kbc * 16384 + i * 1024 + g * 8) = (u32x2){pk2(h[0], h[1]), pk2(h[2], h[3])};
    }
  } else {
    LAS char* kim = lds + g * 36864;
    LAS char* vim = kim + 17 * 1024;
    bf16x8 kf[4];
#pragma unroll
    for (int i = 0; i < 4; ++i) {
      kf[i] = pack8(acc[i][2], acc[i][3]);
      LAS char* vr = vim + ((wi * 4 + i) * 16 + fr) * 64 + fq * 8;
      *(LAS u32x2*)(vr) = (u32x2){pk2(acc[i][4][0], acc[i][4][1]), pk2(acc[i][4][2], acc[i][4][3])};
      *(LAS u32x2*)(vr + 32) = (u32x2){pk2(acc[i][5][0], acc[i][5][1]), pk2(acc[i][5][2], acc[i][5][3])};
    }
    *(LAS bf16x8*)(kim + (wi * 4 + 2) * 1024 + lane * 16) = kf[2];
    *(LAS bf16x8*)(kim + (wi * 4 + 3) * 1024 + lane * 16) = kf[3];
    {
      LAS char* hp = (wi < 2) ? (kim + 16 * 1024 + lane * 16 + wi * 8) : (vim + (256 + fr) * 64 + (wi - 2) * 32 + fq * 8);
      *(LAS u32x2*)hp = (u32x2){pk2(hacc[0], hacc[1]), pk2(hacc[2], hacc[3])};
    }
#pragma unroll
    for (int i = 0; i < 4; ++i)
      *(bf16x8*)(Ht + (8 + 2 * j + g) * 16384 + i * 1024) = pack8(glu4(acc[i][6], acc[i][8]), glu4(acc[i][7], acc[i][9]));
    WAIT_L0();
    BAR();
    asm volatile("" ::: "memory");
    const float cs = 0.17677669529663687f * 1.4426950408889634f;
    const f32x4 z4 = {0.f, 0.f, 0.f, 0.f};
    const int tq = (lane & 15) >> 2, tp = lane & 3;
#pragma unroll
    for (int i = 0; i < 4; ++i) {
      const int mf = wi * 4 + i;
      const bool halo = (i < 2) && (wi == 0);
      const int pf = halo ? 16 : mf - 2;
      const bf16x8 qf = pack8(acc[i][0], acc[i][1]);
      const bf16x8 kp = (i >= 2) ? kf[i >= 2 ? i - 2 : 0] : *(const LAS bf16x8*)(kim + pf * 1024 + lane * 16);
      f32x4 sp = mfma(kp, qf, z4), sc = mfma(kf[i], qf, z4);
      int lo = fr + 9, hi = 16;
      if (halo) { if (i == 0) { lo = fr + 1; hi = 8; } if (mb == 0) hi = 0; }
      float m = -3.0e38f;
#pragma unroll
      for (int e = 0; e < 4; ++e) {
        const int c = 4 * fq + e;
        sp[e] = (c >= lo && c < hi) ? sp[e] * cs : -3.0e38f;
        sc[e] = (c <= fr && c + 7 >= fr) ? sc[e] * cs : -3.0e38f;
        m = fmaxf(m, fmaxf(sp[e], sc[e]));
      }
      m = xmax(m);
      float sum = 0.f;
#pragma unroll
      for (int e = 0; e < 4; ++e) {
        sp[e] = __builtin_amdgcn_exp2f(sp[e] - m); sc[e] = __builtin_amdgcn_exp2f(sc[e] - m);
        sum += sp[e] + sc[e];
      }
      const float inv = __builtin_amdgcn_rcpf(xsum(sum));
      const bf16x8 pfr = pack8(sp * inv, sc * inv);
      const LAS char* vp = vim + (pf * 16 + 4 * fq + tq) * 64 + tp * 8;
      const LAS char* vc = vim + (mf * 16 + 4 * fq + tq) * 64 + tp * 8;
      const f32x4 o0 = mfma(cat(trd(vp), trd(vc)), pfr, z4);
      const f32x4 o1 = mfma(cat(trd(vp + 32), trd(vc + 32)), pfr, z4);
      *(bf16x8*)(Ht + (2 * j + g) * 16384 + i * 1024) = pack8(o0, o1);
    }
  }
}

constexpr int G2_SA = 32768, G2_SB = 16384, G2_SLOT = G2_SA + G2_SB, G2_LDS = 3 * G2_SLOT;

__global__ __launch_bounds__(512, 2) void gemm2_kernel(const u16* __restrict__ x, const u16* __restrict__ wo,
                                                       const u16* __restrict__ wd, char* __restrict__ ws,
                                                       u16* __restrict__ out) {
  extern __shared__ __attribute__((aligned(16))) char smem[];
  LAS char* lds = (LAS char*)smem;
  const int tid = threadIdx.x, lane = tid & 63;
  const int w = __builtin_amdgcn_readfirstlane(tid >> 6), g = w >> 2, wi = w & 3;
  const int b = blockIdx.x, xcd = b & 7, idx = b >> 3;
  const int mb = (xcd & 1) * 4 + (idx & 3), kq = xcd >> 1, nt = idx >> 2;
  const int fr = lane & 15, fq = lane >> 4;

  const char* ca = ws + WS_H + (size_t)(mb * 4 + kq) * 36 * 16384 + w * 4096;
  const unsigned va = lane * 16;
  const int e0 = (0 ^ kq) * 1024, e1 = ((1 ^ kq) - 1) * 1024, e2 = ((2 ^ kq) - 2) * 1024, e3 = ((3 ^ kq) - 3) * 1024;
  const int rr = lane >> 3, pc = lane & 7;
  const unsigned vb = rr * 2048 + (((pc >> 1) ^ ((rr >> 1) & 3)) * 32) + (pc & 1) * 16;
  const char* cb = (const char*)(wo + (size_t)(256 * kq + 8 * w) * 1024 + nt * 128);
  const char* cb2 = (const char*)(wd + (size_t)(896 * kq + 8 * w) * 1024 + nt * 128);
  int st = 0;
  const unsigned lb = (unsigned)(size_t)lds;
  auto stage = [&](int slot_off) {
    dma4(lb + slot_off + w * 4096, va, ca + e0, ca + e1, ca + e2, ca + e3);
    dma2(lb + slot_off + G2_SA + w * 2048, vb, cb, cb - 896);
    ca += 32768;
    ++st;
    cb = (st == 4) ? cb2 : cb + 64 * 2048;
  };
  const int rl = 4 * (fq & 1) + ((lane & 15) >> 2);
  unsigned aoff = lb + wi * 4096 + lane * 16;
  unsigned boff = lb + G2_SA + (fq >> 1) * 2048 + g * 1024 + rl * 128 + 32 * ((rl >> 1) & 3) + 8 * (lane & 3);

  f32x4 acc[4][4];
#pragma unroll
  for (int i = 0; i < 4; ++i)
#pragma unroll
    for (int n = 0; n < 4; ++n) acc[i][n] = (f32x4){0.f, 0.f, 0.f, 0.f};

  stage(0); stage(G2_SLOT);
#pragma unroll
  for (int i = 0; i < 4; ++i)
#pragma unroll
    for (int n = 0; n < 4; ++n) asm volatile("" : "+v"(acc[i][n]));
  WAIT_V(6);
  BAR();
  if (g == 1) BAR();

  int sl = 0;
  for (int q = 0; q < 18; ++q) {
    bf16x8 a[2][4], bfr[2][4];
#pragma unroll
    for (int kk = 0; kk < 2; ++kk) {
#pragma unroll
      for (int i = 0; i < 4; ++i) a[kk][i] = *(const LAS bf16x8*)(lp(aoff) + kk * 16384 + i * 1024);
#pragma unroll
      for (int n = 0; n < 4; ++n)
        bfr[kk][n] = cat(trd(lp(boff ^ (n * 32)) + kk * 8192), trd(lp(boff ^ (n * 32)) + kk * 8192 + 4096));
    }
    SCHED();
    const int nxt = (sl == 0 ? 2 : sl - 1) * G2_SLOT;
    sl = (sl == 2) ? 0 : sl + 1;
    const int dlt = (sl == 0) ? -2 * G2_SLOT : G2_SLOT;
    aoff += dlt; boff += dlt;
    if (q < 16) { stage(nxt); WAIT_V(6); } else { WAIT_V(0); }
    WAIT_L0();
    SCHED();
    BAR();
    SCHED();
    __builtin_amdgcn_s_setprio(1);
#pragma unroll
    for (int kk = 0; kk < 2; ++kk)
#pragma unroll
      for (int n = 0; n < 4; ++n)
#pragma unroll
        for (int i = 0; i < 4; ++i) acc[i][n] = mfma(bfr[kk][n], a[kk][i], acc[i][n]);
    __builtin_amdgcn_s_setprio(0);
    SCHED();
    if (q < 17 || g == 0) BAR();
    SCHED();
  }

  const int tile = mb * 8 + nt;
  const auto rs = __builtin_amdgcn_make_buffer_rsrc(ws + WS_X, 0, (int)X_BYTES, 0x00020000);
#pragma unroll
  for (int i = 1; i < 4; ++i) {
    const unsigned o = (unsigned)((((tile * 4 + (kq ^ i)) * 3 + (i - 1)) * 8 + w) * 2048) + lane * 16;
    __builtin_amdgcn_raw_buffer_store_b128(__builtin_bit_cast(u32x4, pack8(acc[i][0], acc[i][1])), rs, o, 0, 16);
    __builtin_amdgcn_raw_buffer_store_b128(__builtin_bit_cast(u32x4, pack8(acc[i][2], acc[i][3])), rs, o + 1024, 0, 16);
  }
  const int mf = wi * 4 + kq;
  const size_t orow = (size_t)(mb * 256 + 32 * (mf >> 1) + 2 * fr + (mf & 1)) * 1024 + nt * 128 + g * 64 + 4 * fq;
  u32x2 xr[4];
#pragma unroll
  for (int n = 0; n < 4; ++n) xr[n] = *(const u32x2*)(x + orow + 16 * n);
  WAIT_V(0);
  BAR();
  GAS unsigned* cnt = (GAS unsigned*)(ws + WS_CNT + tile * 256);
  if (w == 0) {
    if (lane == 0) __hip_atomic_fetch_add(cnt, 1u, __ATOMIC_RELAXED, __HIP_MEMORY_SCOPE_AGENT);
    unsigned spins = 0;
    while ((unsigned)__builtin_amdgcn_readfirstlane(__hip_atomic_load(cnt, __ATOMIC_RELAXED, __HIP_MEMORY_SCOPE_AGENT)) < 4u) {
      __builtin_amdgcn_s_sleep(1);
      if (++spins > (1u << 22)) break;
    }
  }
  asm volatile("" ::: "memory");
  BAR();
  asm volatile("" ::: "memory");
  u32x4 p[3][2];
#pragma unroll
  for (int i = 1; i < 4; ++i) {
    const unsigned o = (unsigned)((((tile * 4 + kq) * 3 + (i - 1)) * 8 + w) * 2048) + lane * 16;
    p[i - 1][0] = __builtin_amdgcn_raw_buffer_load_b128(rs, o, 0, 16);
    p[i - 1][1] = __builtin_amdgcn_raw_buffer_load_b128(rs, o + 1024, 0, 16);
  }
#pragma unroll
  for (int n = 0; n < 4; ++n) {
    f32x4 s = acc[0][n];
#pragma unroll
    for (int i = 0; i < 3; ++i) {
      const unsigned lo = p[i][n >> 1][(n & 1) * 2], hi = p[i][n >> 1][(n & 1) * 2 + 1];
      s[0] += __uint_as_float(lo << 16); s[1] += __uint_as_float(lo & 0xffff0000u);
      s[2] += __uint_as_float(hi << 16); s[3] += __uint_as_float(hi & 0xffff0000u);
    }
    s[0] += __uint_as_float(xr[n][0] << 16); s[1] += __uint_as_float(xr[n][0] & 0xffff0000u);
    s[2] += __uint_as_float(xr[n][1] << 16); s[3] += __uint_as_float(xr[n][1] & 0xffff0000u);
    *(u32x2*)(out + orow + 16 * n) = (u32x2){pk2(s[0], s[1]), pk2(s[2], s[3])};
  }
}

}

extern "C" void kernel_launch(void* const* d_in, const int* in_sizes, int n_in, void* d_out, int out_size, void* d_ws,
                              size_t ws_size, hipStream_t stream) {
  const u16* x = (const u16*)d_in[0];
  const u16* wqkv = (const u16*)d_in[1];
  const u16* wo = (const u16*)d_in[2];
  const u16* wg = (const u16*)d_in[4];
  const u16* wu = (const u16*)d_in[5];
  const u16* wd = (const u16*)d_in[6];
  char* ws = (char*)d_ws;
  (void)hipFuncSetAttribute((const void*)gemm1_kernel, hipFuncAttributeMaxDynamicSharedMemorySize, G1_LDS);
  (void)hipFuncSetAttribute((const void*)gemm2_kernel, hipFuncAttributeMaxDynamicSharedMemorySize, G2_LDS);
  ln_kernel<<<512, 256, 0, stream>>>(x, ws);
  gemm1_kernel<<<256, 512, G1_LDS, stream>>>(wqkv, wg, wu, ws);
  gemm2_kernel<<<256, 512, G2_LDS, stream>>>(x, wo, wd, ws, (u16*)d_out);
}
```

```cpp
#include <hip/hip_runtime.h>
#include <hip/hip_bf16.h>
#include <type_traits>
#define EARLY 2
#define H_AUX 16

namespace {

typedef unsigned short u16;
typedef short bf16x8 __attribute__((ext_vector_type(8)));
typedef short bf16x4 __attribute__((ext_vector_type(4)));
typedef float f32x4 __attribute__((ext_vector_type(4)));
typedef float f32x2 __attribute__((ext_vector_type(2)));
typedef unsigned u32x4 __attribute__((ext_vector_type(4)));
typedef unsigned u32x2 __attribute__((ext_vector_type(2)));
typedef __bf16 bf2 __attribute__((ext_vector_type(2)));
#define LAS __attribute__((address_space(3)))
#define GAS __attribute__((address_space(1)))

#define WAIT_V(n) asm volatile("s_waitcnt vmcnt(" #n ")" ::: "memory")
#define WAIT_V_IF0(n, c) asm volatile("s_cmp_lg_u32 %0, 0\n\ts_cbranch_scc1 1f\n\ts_waitcnt vmcnt(" #n ")\n1:" :: "s"(c) : "memory", "scc")
#define WAIT_L0() asm volatile("s_waitcnt lgkmcnt(0)" ::: "memory")
#define BAR() __builtin_amdgcn_s_barrier()
#define SCHED() __builtin_amdgcn_sched_barrier(0)

__device__ __forceinline__ float b2f(u16 v) { return __uint_as_float(((unsigned)v) << 16); }
__device__ __forceinline__ unsigned pk2(float lo, float hi) {
  bf2 r = {(__bf16)lo, (__bf16)hi};
  return __builtin_bit_cast(unsigned, r);
}
__device__ __forceinline__ float rbf(float f) { return (float)(__bf16)f; }
__device__ __forceinline__ bf16x8 pack8(f32x4 a, f32x4 b) {
  u32x4 r = {pk2(a[0], a[1]), pk2(a[2], a[3]), pk2(b[0], b[1]), pk2(b[2], b[3])};
  return __builtin_bit_cast(bf16x8, r);
}
__device__ __forceinline__ bf16x4 trd(LAS const char* p) {
  return __builtin_amdgcn_ds_read_tr16_b64_v4i16((LAS bf16x4*)p);
}
__device__ __forceinline__ bf16x8 cat(bf16x4 a, bf16x4 b) { return __builtin_shufflevector(a, b, 0, 1, 2, 3, 4, 5, 6, 7); }
__device__ __forceinline__ f32x4 mfma(bf16x8 a, bf16x8 b, f32x4 c) {
  return __builtin_amdgcn_mfma_f32_16x16x32_bf16(a, b, c, 0, 0, 0);
}
__device__ __forceinline__ void dma_first(unsigned lds_dst, unsigned voff, const char* b0) {
  asm volatile("s_mov_b32 m0, %0\n\ts_nop 0\n\tglobal_load_lds_dwordx4 %1, %2" :: "s"(lds_dst), "v"(voff), "s"(b0) : "memory");
}
template <int OFF>
__device__ __forceinline__ void dma_next(unsigned voff, const char* b) {
  asm volatile("global_load_lds_dwordx4 %0, %1 offset:%2" :: "v"(voff), "s"(b), "i"(OFF) : "memory");
}
__device__ __forceinline__ void dma1m(unsigned lds_dst, const char* p, unsigned long long mask) {
  asm volatile("s_mov_b32 m0, %0\n\ts_mov_b64 exec, %2\n\t"
               "global_load_lds_dwordx4 %1, off\n\t"
               "s_mov_b64 exec, -1"
               :: "s"(lds_dst), "v"(p), "s"(mask) : "memory");
}
__device__ __forceinline__ LAS char* lp(unsigned a) { return (LAS char*)(size_t)a; }
__device__ __forceinline__ float xmax(float v) {
  auto a = __builtin_amdgcn_permlane16_swap(__float_as_uint(v), __float_as_uint(v), false, false);
  v = fmaxf(__uint_as_float(a[0]), __uint_as_float(a[1]));
  auto b = __builtin_amdgcn_permlane32_swap(__float_as_uint(v), __float_as_uint(v), false, false);
  return fmaxf(__uint_as_float(b[0]), __uint_as_float(b[1]));
}
__device__ __forceinline__ float xsum(float v) {
  auto a = __builtin_amdgcn_permlane16_swap(__float_as_uint(v), __float_as_uint(v), false, false);
  v = __uint_as_float(a[0]) + __uint_as_float(a[1]);
  auto b = __builtin_amdgcn_permlane32_swap(__float_as_uint(v), __float_as_uint(v), false, false);
  return __uint_as_float(b[0]) + __uint_as_float(b[1]);
}

#define ST_DECL
#define LT_DECL
#define LT(k)
#define ST(k)
#define ST_OUT(kid, n)
constexpr size_t WS_XN = 0;
constexpr size_t WS_H = WS_XN + (size_t)4 * 1024 * 1024;
constexpr size_t H_BYTES = (size_t)8 * 4 * 36 * 16 * 1024;
constexpr size_t WS_X = WS_H + H_BYTES;
constexpr size_t X_BYTES = (size_t)64 * 4 * 3 * 8 * 2048;
constexpr size_t WS_CNT = WS_X + X_BYTES;
constexpr size_t WS_ST = WS_CNT + 65536;

__global__ __launch_bounds__(512) void ln_kernel(const u16* __restrict__ x, char* __restrict__ ws) {
  __shared__ __attribute__((aligned(16))) char tr[128 * 144];
  const int lane = threadIdx.x & 63, w = threadIdx.x >> 6, b = blockIdx.x;
  const int F = b >> 1, mb = F >> 4, mf = F & 15, r = 8 * (b & 1) + w;
  const int row = mb * 256 + 32 * (mf >> 1) + 2 * r + (mf & 1);
  if (b == 0 && threadIdx.x < 64) *(unsigned*)(ws + WS_CNT + threadIdx.x * 256) = 0u;
  const u16* xr = x + row * 1024 + lane * 8;
  const u32x4 ra = *(const u32x4*)(xr), rb = *(const u32x4*)(xr + 512);
  float v[16];
#pragma unroll
  for (int i = 0; i < 4; ++i) {
    v[2 * i] = __uint_as_float(ra[i] << 16); v[2 * i + 1] = __uint_as_float(ra[i] & 0xffff0000u);
    v[8 + 2 * i] = __uint_as_float(rb[i] << 16); v[8 + 2 * i + 1] = __uint_as_float(rb[i] & 0xffff0000u);
  }
  float s = 0.f;
#pragma unroll
  for (int i = 0; i < 16; ++i) s += v[i];
#pragma unroll
  for (int o = 1; o < 16; o <<= 1) s += __shfl_xor(s, o);
  s = xsum(s);
  const float mean = s * (1.0f / 1024.0f);
  float q = 0.f;
#pragma unroll
  for (int i = 0; i < 16; ++i) { const float d = v[i] - mean; q += d * d; }
#pragma unroll
  for (int o = 1; o < 16; o <<= 1) q += __shfl_xor(q, o);
  q = xsum(q);
  const float mb16 = rbf(mean), den = rbf(sqrtf(rbf(rbf(q * (1.0f / 1024.0f)) + 1e-5f)));
  const float inv = 1.0f / den;
#pragma unroll
  for (int i = 0; i < 16; ++i) v[i] = rbf(v[i] - mb16) * inv;
  const int kb = lane >> 2, c = lane & 3;
#pragma unroll
  for (int h = 0; h < 2; ++h) {
    char* t = tr + ((kb + 16 * h) * 4 + 2 * (c & 1)) * 144 + w * 16 + (c >> 1) * 8;
    *(u32x2*)t = (u32x2){pk2(v[8 * h], v[8 * h + 1]), pk2(v[8 * h + 2], v[8 * h + 3])};
    *(u32x2*)(t + 144) = (u32x2){pk2(v[8 * h + 4], v[8 * h + 5]), pk2(v[8 * h + 6], v[8 * h + 7])};
  }
  __syncthreads();
  const auto rs = __builtin_amdgcn_make_buffer_rsrc(ws + WS_XN + (size_t)mb * 32 * 16384 + mf * 1024 + (b & 1) * 128, 0, 32 * 16384, 0x00020000);
#pragma unroll
  for (int h = 0; h < 2; ++h) {
    const int seg = (threadIdx.x >> 3) + 64 * h, t8 = threadIdx.x & 7;
    const u32x4 o = *(const u32x4*)(tr + seg * 144 + t8 * 16);
    __builtin_amdgcn_raw_buffer_store_b128(o, rs, (seg >> 2) * 16384 + (seg & 3) * 256 + t8 * 16, 0, H_AUX);
  }
}

constexpr int G1_SA = 17 * 1024, G1_SB = 20 * 1024, G1_SLOT = G1_SA + G1_SB, G1_LDS = 4 * G1_SLOT;

__device__ __forceinline__ unsigned glu2(f32x2 g, f32x2 u) {
  g = g * -1.4426950408889634f;
  f32x2 e = {__builtin_amdgcn_exp2f(g[0]), __builtin_amdgcn_exp2f(g[1])};
  e = e + 1.0f;
  f32x2 r = {__builtin_amdgcn_rcpf(e[0]), __builtin_amdgcn_rcpf(e[1])};
  r = r * u;
  return pk2(r[0], r[1]);
}
__device__ __forceinline__ u32x2 glu4(f32x4 g, f32x4 u) {
  return (u32x2){glu2(__builtin_shufflevector(g, g, 0, 1), __builtin_shufflevector(u, u, 0, 1)),
                 glu2(__builtin_shufflevector(g, g, 2, 3), __builtin_shufflevector(u, u, 2, 3))};
}
__device__ __forceinline__ u32x4 glu8(f32x4 g0, f32x4 u0, f32x4 g1, f32x4 u1) {
  const u32x2 a = glu4(g0, u0), b = glu4(g1, u1);
  return (u32x4){a[0], a[1], b[0], b[1]};
}

__global__ __launch_bounds__(512, 2) void gemm1_kernel(const u16* __restrict__ wqkv, const u16* __restrict__ wg,
                                                       const u16* __restrict__ wu, char* __restrict__ ws) {
  extern __shared__ __attribute__((aligned(16))) char smem[];
  LAS char* lds = (LAS char*)smem;
  ST_DECL LT_DECL ST(0);
  const int tid = threadIdx.x;
  int lane = tid & 63;
  const int w = __builtin_amdgcn_readfirstlane(tid >> 6), g = w >> 2, wi = w & 3;
  const int b = blockIdx.x, xcd = b & 7, idx = b >> 3;
  const int mb = (xcd & 1) * 4 + (idx & 3), kq = xcd >> 1, ntl = idx >> 2;
  const bool typeA = ntl < 4;
  const int j = ntl & 3;
  int fr = lane & 15, fq = lane >> 4;
  const int fA = 896 * kq + 64 * j;
  const int f0 = 896 * kq + 256 + 160 * j, odd = j & 1;
  const int fa = f0 + 32 * odd, fb = fa + 64, fc = odd ? f0 : f0 + 128;

  const char* c1;
  unsigned v1;
  int s1, p1, p2;
  const char* c2;
  int d1, d2;
  unsigned long long m2 = ~0ull;
  {
    const int rr = lane >> 3, pc = lane & 7, fi = (pc >> 1) ^ ((rr >> 1) & 3);
    if (g == 0) {
      int ld; const u16* q1;
      if (typeA) {
        if (wi < 3) { q1 = wqkv + wi * 1024 + 256 * kq + 64 * j; ld = 3072; } else { q1 = wg + fA; ld = 3584; }
        c2 = (const char*)(wu + fA + (size_t)(wi * 8 + rr) * 3584) + fi * 32 + (pc & 1) * 16;
      } else {
        q1 = ((wi & 2) ? wu : wg) + ((wi & 1) ? fb : fa); ld = 3584;
        c2 = (const char*)(((fi & 1) ? wu : wg) + fc + (fi >> 1) * 16 + (size_t)(wi * 8 + rr) * 3584) + (pc & 1) * 16;
      }
      c1 = (const char*)q1; v1 = rr * ld * 2 + fi * 32 + (pc & 1) * 16;
      s1 = 16 * ld - 1024; p1 = 64 * ld; p2 = 64 * 3584;
      d1 = G1_SA + wi * 4096; d2 = G1_SA + 16384 + wi * 1024;
    } else {
      c1 = ws + WS_XN + (size_t)mb * 32 * 16384 + wi * 4096; v1 = lane * 16;
      const int mh = mb > 0 ? mb - 1 : 0, i = lane & 15;
      c2 = ws + WS_XN + (size_t)mh * 32 * 16384 + (14 + (i >> 3)) * 1024 + (wi * 16 + 8 + (i & 7)) * 16;
      m2 = 0xffffull;
      s1 = 0; p1 = 16384; p2 = 16384;
      d1 = wi * 4096; d2 = 16384 + wi * 256;
    }
  }
  const unsigned lb = (unsigned)(size_t)lds;
  unsigned ns = lb;
  auto adv = [&]() { c1 += p1; c2 += p2; ns = (ns == lb + 3 * G1_SLOT) ? lb : ns + G1_SLOT; };
  auto stage = [&]() {
    dma_first(ns + d1, v1, c1); dma_next<1024>(v1, c1 + s1); dma_next<2048>(v1, c1 + 2 * s1); dma_next<3072>(v1, c1 + 3 * s1);
    dma1m(ns + d2, c2, m2);
    adv();
  };
  stage();
  BAR();
  stage();

  const int r0 = 4 * fq + ((lane & 15) >> 2);
  unsigned aoff = lb + wi * 4096 + lane * 16;
  unsigned hoffA = lb + 16384 + lane * 16;
  unsigned boff0 = lb + G1_SA + r0 * 128 + 32 * ((2 * g) ^ ((r0 >> 1) & 3)) + 8 * (lane & 3);
  unsigned boff1 = boff0 ^ 32;
  unsigned hoffB = (boff0 ^ (32 * (wi & 1))) + (1 + (wi >> 1)) * 4096;

  f32x4 acc[4][10];
  f32x4 hacc = {0.f, 0.f, 0.f, 0.f};
#pragma unroll
  for (int i = 0; i < 4; ++i)
#pragma unroll
    for (int n = 0; n < 10; ++n) { acc[i][n] = (f32x4){0.f, 0.f, 0.f, 0.f}; asm volatile("" : "+v"(acc[i][n])); }
  asm volatile("" : "+v"(aoff), "+v"(hoffA), "+v"(boff0), "+v"(boff1), "+v"(hoffB), "+v"(hacc));
  ST(1);
  WAIT_V(5);
  BAR();
  ST(2);
  if (g == 1) BAR();

  auto phase = [&](auto do_stage, int q) {
    constexpr bool DS = decltype(do_stage)::value;
    bf16x8 a[4], bfr[10], ha, hb;
    LT(0);
#define RB(c) bfr[2 * (c)] = cat(trd(lp(boff0) + (c) * 4096), trd(lp(boff0) + (c) * 4096 + 2048)); \
              bfr[2 * (c) + 1] = cat(trd(lp(boff1) + (c) * 4096), trd(lp(boff1) + (c) * 4096 + 2048));
    a[0] = *(const LAS bf16x8*)(lp(aoff)); a[1] = *(const LAS bf16x8*)(lp(aoff) + 1024);
    if (DS) dma_first(ns + d1, v1, c1);
    a[2] = *(const LAS bf16x8*)(lp(aoff) + 2048); a[3] = *(const LAS bf16x8*)(lp(aoff) + 3072);
    RB(0)
    if (DS) dma_next<1024>(v1, c1 + s1);
    RB(1)
    ha = *(const LAS bf16x8*)lp(hoffA);
    if (DS) dma_next<2048>(v1, c1 + 2 * s1);
    RB(2)
    hb = cat(trd(lp(hoffB)), trd(lp(hoffB) + 2048));
    if (DS) dma_next<3072>(v1, c1 + 3 * s1);
    RB(3)
    if (DS) dma1m(ns + d2, c2, m2);
    RB(4)
#undef RB
    SCHED();
    LT(1);
    const int dlt = ((q & 3) == 3) ? -3 * G1_SLOT : G1_SLOT;
    aoff += dlt; hoffA += dlt; boff0 += dlt; boff1 += dlt; hoffB += dlt;
    if (DS) {
      adv();
      if (q == 1) stage();
      LT(2);
      if (q == 0) WAIT_V(5); else WAIT_V(10);
    } else {
      LT(2);
      WAIT_V(0);
    }
    WAIT_L0();
    LT(3);
    SCHED();
    BAR();
    SCHED();
    LT(4);
    __builtin_amdgcn_s_setprio(1);
    hacc = mfma(hb, ha, hacc);
#pragma unroll
    for (int n = 0; n < 10; ++n)
#pragma unroll
      for (int i = 0; i < 4; ++i) {
        if (n == 9 && i == 4 - EARLY) { SCHED(); __builtin_amdgcn_s_setprio(3); if (q < 31 || g == 0) BAR(); SCHED(); }
        acc[i][n] = mfma(bfr[n], a[i], acc[i][n]);
      }
    __builtin_amdgcn_s_setprio(0);
    SCHED();
    LT(5);
    if (EARLY == 0) { if (q < 31 || g == 0) BAR(); }
    SCHED();
  };
#pragma clang loop unroll(disable)
  for (int q = 0; q < 29; ++q) phase(std::true_type{}, q);
#pragma clang loop unroll(disable)
  for (int q = 29; q < 32; ++q) phase(std::false_type{}, q);

  ST(3);
  lane = __builtin_amdgcn_mbcnt_hi(~0u, __builtin_amdgcn_mbcnt_lo(~0u, 0u));
  asm volatile("" : "+v"(lane));
  fr = lane & 15; fq = lane >> 4;
  const auto hrs = __builtin_amdgcn_make_buffer_rsrc(ws + WS_H + (size_t)(mb * 4 + kq) * 36 * 16384, 0, 36 * 16384, 0x00020000);
  const unsigned hv = wi * 4096 + lane * 16;
  if (!typeA) {
    const int kba = 8 + ((fa - 896 * kq) >> 5) + g, kbc = 8 + ((fc - 896 * kq) >> 5);
#pragma unroll
    for (int i = 0; i < 4; ++i) {
      __builtin_amdgcn_raw_buffer_store_b128(glu8(acc[i][0], acc[i][4], acc[i][1], acc[i][5]), hrs, hv + kba * 16384 + i * 1024, 0, H_AUX);
      __builtin_amdgcn_raw_buffer_store_b128(glu8(acc[i][2], acc[i][6], acc[i][3], acc[i][7]), hrs, hv + kba * 16384 + 32768 + i * 1024, 0, H_AUX);
      __builtin_amdgcn_raw_buffer_store_b64(glu4(acc[i][8], acc[i][9]), hrs, hv + kbc * 16384 + g * 8 + i * 1024, 0, H_AUX);
    }
  } else {
    LAS char* kim = lds + g * 36864;
    LAS char* vim = kim + 17 * 1024;
    bf16x8 kf[4], qf[4];
#pragma unroll
    for (int i = 0; i < 4; ++i) {
      kf[i] = pack8(acc[i][2], acc[i][3]);
      LAS char* vr = vim + ((wi * 4 + i) * 16 + fr) * 64 + fq * 8;
      *(LAS u32x2*)(vr) = (u32x2){pk2(acc[i][4][0], acc[i][4][1]), pk2(acc[i][4][2], acc[i][4][3])};
      *(LAS u32x2*)(vr + 32) = (u32x2){pk2(acc[i][5][0], acc[i][5][1]), pk2(acc[i][5][2], acc[i][5][3])};
    }
    *(LAS bf16x8*)(kim + (wi * 4 + 2) * 1024 + lane * 16) = kf[2];
    *(LAS bf16x8*)(kim + (wi * 4 + 3) * 1024 + lane * 16) = kf[3];
    {
      LAS char* hp = (wi < 2) ? (kim + 16 * 1024 + lane * 16 + wi * 8) : (vim + (256 + fr) * 64 + (wi - 2) * 32 + fq * 8);
      *(LAS u32x2*)hp = (u32x2){pk2(hacc[0], hacc[1]), pk2(hacc[2], hacc[3])};
    }
    auto glu_part = [&]() {
#pragma unroll
      for (int i = 0; i < 4; ++i)
        __builtin_amdgcn_raw_buffer_store_b128(glu8(acc[i][6], acc[i][8], acc[i][7], acc[i][9]), hrs, hv + (8 + 2 * j + g) * 16384 + i * 1024, 0, H_AUX);
    };
    WAIT_L0();
    if (g == 0) glu_part();
    SCHED();
    BAR();
    asm volatile("" ::: "memory");
    const float NEG = -3.0e38f;
    f32x4 bc, bp, bp0, bp1;
    const bool halo = wi == 0;
#pragma unroll
    for (int e = 0; e < 4; ++e) {
      const int c = 4 * fq + e;
      bc[e] = (c <= fr && c + 7 >= fr) ? 0.f : NEG;
      bp[e] = (c >= fr + 9) ? 0.f : NEG;
      bp0[e] = halo ? ((mb > 0 && c < 8 && c >= fr + 1) ? 0.f : NEG) : bp[e];
      bp1[e] = (halo && mb == 0) ? NEG : bp[e];
    }
    const int pf0 = halo ? 16 : wi * 4 - 2, pf1 = halo ? 16 : wi * 4 - 1;
    const bf16x8 kp0 = *(const LAS bf16x8*)(kim + pf0 * 1024 + lane * 16);
    const bf16x8 kp1 = *(const LAS bf16x8*)(kim + pf1 * 1024 + lane * 16);
    const LAS char* vb = vim + (4 * fq + ((lane & 15) >> 2)) * 64 + (lane & 3) * 8;
    bf16x4 vt[6][2];
#pragma unroll
    for (int d = 0; d < 2; ++d) {
      vt[0][d] = trd(vb + pf0 * 1024 + d * 32); vt[1][d] = trd(vb + pf1 * 1024 + d * 32);
#pragma unroll
      for (int i = 0; i < 4; ++i) vt[2 + i][d] = trd(vb + (wi * 4 + i) * 1024 + d * 32);
    }
    f32x4 sp[4], sc[4];
#pragma unroll
    for (int i = 0; i < 4; ++i) { qf[i] = pack8(acc[i][0], acc[i][1]); sc[i] = mfma(kf[i], qf[i], bc); }
    sp[2] = mfma(kf[0], qf[2], bp); sp[3] = mfma(kf[1], qf[3], bp);
    sp[0] = mfma(kp0, qf[0], bp0); sp[1] = mfma(kp1, qf[1], bp1);
    const float cs = 0.17677669529663687f * 1.4426950408889634f;
    const f32x4 z4 = {0.f, 0.f, 0.f, 0.f};
    float m[4], sum[4];
#pragma unroll
    for (int i = 0; i < 4; ++i)
      m[i] = fmaxf(fmaxf(fmaxf(sp[i][0], sp[i][1]), fmaxf(sp[i][2], sp[i][3])), fmaxf(fmaxf(sc[i][0], sc[i][1]), fmaxf(sc[i][2], sc[i][3])));
#pragma unroll
    for (int i = 0; i < 4; ++i) m[i] = -cs * xmax(m[i]);
#pragma unroll
    for (int i = 0; i < 4; ++i) {
#pragma unroll
      for (int e = 0; e < 4; ++e) {
        sp[i][e] = __builtin_amdgcn_exp2f(__builtin_fmaf(sp[i][e], cs, m[i]));
        sc[i][e] = __builtin_amdgcn_exp2f(__builtin_fmaf(sc[i][e], cs, m[i]));
      }
      const f32x4 t = sp[i] + sc[i];
      sum[i] = (t[0] + t[1]) + (t[2] + t[3]);
    }
#pragma unroll
    for (int i = 0; i < 4; ++i) sum[i] = __builtin_amdgcn_rcpf(xsum(sum[i]));
#pragma unroll
    for (int i = 0; i < 4; ++i) {
      const bf16x8 pfr = pack8(sp[i] * sum[i], sc[i] * sum[i]);
      const f32x4 o0 = mfma(cat(vt[i][0], vt[2 + i][0]), pfr, z4);
      const f32x4 o1 = mfma(cat(vt[i][1], vt[2 + i][1]), pfr, z4);
      __builtin_amdgcn_raw_buffer_store_b128(__builtin_bit_cast(u32x4, pack8(o0, o1)), hrs, hv + (2 * j + g) * 16384 + i * 1024, 0, H_AUX);
    }
    if (g == 1) glu_part();
  }
  ST(4);
  ST(5);
  ST_OUT(0, 6);
}

constexpr int G2_SA = 32768, G2_SB = 16384, G2_SLOT = G2_SA + G2_SB, G2_LDS = 3 * G2_SLOT;

__global__ __launch_bounds__(512, 2) void gemm2_kernel(const u16* __restrict__ x, const u16* __restrict__ wo,
                                                       const u16* __restrict__ wd, char* __restrict__ ws,
                                                       u16* __restrict__ out) {
  extern __shared__ __attribute__((aligned(16))) char smem[];
  LAS char* lds = (LAS char*)smem;
  ST_DECL LT_DECL ST(0);
  const int tid = threadIdx.x, lane = tid & 63;
  const int w = __builtin_amdgcn_readfirstlane(tid >> 6), g = w >> 2, wi = w & 3;
  const int b = blockIdx.x, xcd = b & 7, idx = b >> 3;
  const int mb = (xcd & 1) * 4 + (idx & 3), kq = xcd >> 1, nt = idx >> 2;
  const int fr = lane & 15, fq = lane >> 4;

  const char* ca = ws + WS_H + (size_t)(mb * 4 + kq) * 36 * 16384 + w * 4096;
  const unsigned va = lane * 16;
  const int e0 = (0 ^ kq) * 1024, e1 = ((1 ^ kq) - 1) * 1024, e2 = ((2 ^ kq) - 2) * 1024, e3 = ((3 ^ kq) - 3) * 1024;
  const int rr = lane >> 3, pc = lane & 7;
  const unsigned vb = rr * 2048 + (((pc >> 1) ^ ((rr >> 1) & 3)) * 32) + (pc & 1) * 16;
  const char* cb = (const char*)(wo + (size_t)(256 * kq + 8 * w) * 1024 + nt * 128);
  const char* cb2 = (const char*)(wd + (size_t)(896 * kq + 8 * w) * 1024 + nt * 128);
  int st = 0;
  const unsigned lb = (unsigned)(size_t)lds;
  unsigned ns = lb;
  auto adv = [&]() { ca += 32768; ++st; cb = (st == 4) ? cb2 : cb + 64 * 2048; ns = (ns == lb + 2 * G2_SLOT) ? lb : ns + G2_SLOT; };
  auto stage = [&]() {
    dma_first(ns + w * 4096, va, ca + e0); dma_next<1024>(va, ca + e1); dma_next<2048>(va, ca + e2); dma_next<3072>(va, ca + e3);
    dma_first(ns + G2_SA + w * 2048, vb, cb); dma_next<1024>(vb, cb - 896);
    adv();
  };
  stage();
  BAR();
  stage();
  const int rl = 4 * (fq & 1) + ((lane & 15) >> 2);
  unsigned aoff = lb + wi * 4096 + lane * 16;
  unsigned boff = lb + G2_SA + (fq >> 1) * 2048 + g * 1024 + rl * 128 + 32 * ((rl >> 1) & 3) + 8 * (lane & 3);

  f32x4 acc[4][4];
#pragma unroll
  for (int i = 0; i < 4; ++i)
#pragma unroll
    for (int n = 0; n < 4; ++n) { acc[i][n] = (f32x4){0.f, 0.f, 0.f, 0.f}; asm volatile("" : "+v"(acc[i][n])); }
  ST(1);
  WAIT_V(6);
  BAR();
  ST(2);
  if (g == 1) BAR();

  int sl = 0;
  auto phase = [&](auto do_stage, int q) {
    constexpr bool DS = decltype(do_stage)::value;
    bf16x8 a[2][4], bfr[2][4];
    LT(0);
#define RA(kk, i) a[kk][i] = *(const LAS bf16x8*)(lp(aoff) + (kk) * 16384 + (i) * 1024);
#define RB(kk, n) bfr[kk][n] = cat(trd(lp(boff ^ ((n) * 32)) + (kk) * 8192), trd(lp(boff ^ ((n) * 32)) + (kk) * 8192 + 4096));
    RA(0, 0) RA(0, 1)
    if (DS) dma_first(ns + w * 4096, va, ca + e0);
    RA(0, 2) RA(0, 3) RB(0, 0)
    if (DS) dma_next<1024>(va, ca + e1);
    RB(0, 1) RB(0, 2) RB(0, 3)
    if (DS) dma_next<2048>(va, ca + e2);
    RA(1, 0) RA(1, 1) RA(1, 2)
    if (DS) dma_next<3072>(va, ca + e3);
    RA(1, 3) RB(1, 0) RB(1, 1)
    if (DS) dma_first(ns + G2_SA + w * 2048, vb, cb);
    RB(1, 2) RB(1, 3)
    if (DS) dma_next<1024>(vb, cb - 896);
#undef RA
#undef RB
    SCHED();
    LT(1);
    sl = (sl == 2) ? 0 : sl + 1;
    const int dlt = (sl == 0) ? -2 * G2_SLOT : G2_SLOT;
    aoff += dlt; boff += dlt;
    if (DS) adv();
    LT(2);
    asm volatile("" : "+v"(aoff), "+v"(boff), "+s"(ca), "+s"(cb), "+s"(ns), "+s"(sl));
    if (DS) WAIT_V_IF0(6, g ^ 1); else WAIT_V_IF0(0, g ^ 1);
    WAIT_L0();
    LT(3);
    SCHED();
    BAR();
    SCHED();
    LT(4);
    __builtin_amdgcn_s_setprio(1);
#pragma unroll
    for (int kk = 0; kk < 2; ++kk)
#pragma unroll
      for (int n = 0; n < 4; ++n)
#pragma unroll
        for (int i = 0; i < 4; ++i) {
          if (kk == 1 && n == 3 && i == 4 - EARLY) { SCHED(); __builtin_amdgcn_s_setprio(3); if (DS) WAIT_V_IF0(6, g); else WAIT_V_IF0(0, g); if (q < 17 || g == 0) BAR(); SCHED(); }
          acc[i][n] = mfma(bfr[kk][n], a[kk][i], acc[i][n]);
        }
    __builtin_amdgcn_s_setprio(0);
    SCHED();
    LT(5);
    if (EARLY == 0) { if (DS) WAIT_V_IF0(6, g); else WAIT_V_IF0(0, g); if (q < 17 || g == 0) BAR(); }
    SCHED();
  };
#pragma clang loop unroll(disable)
  for (int q = 0; q < 16; ++q) phase(std::true_type{}, q);
#pragma clang loop unroll(disable)
  for (int q = 16; q < 18; ++q) phase(std::false_type{}, q);

  ST(3);
  const int tile = mb * 8 + nt;
  const auto rs = __builtin_amdgcn_make_buffer_rsrc(ws + WS_X, 0, (int)X_BYTES, 0x00020000);
#pragma unroll
  for (int i = 1; i < 4; ++i) {
    const unsigned o = (unsigned)((((tile * 4 + (kq ^ i)) * 3 + (i - 1)) * 8 + w) * 2048) + lane * 16;
    __builtin_amdgcn_raw_buffer_store_b128(__builtin_bit_cast(u32x4, pack8(acc[i][0], acc[i][1])), rs, o, 0, 16);
    __builtin_amdgcn_raw_buffer_store_b128(__builtin_bit_cast(u32x4, pack8(acc[i][2], acc[i][3])), rs, o + 1024, 0, 16);
  }
  const int mf = wi * 4 + kq;
  const size_t orow = (size_t)(mb * 256 + 32 * (mf >> 1) + 2 * fr + (mf & 1)) * 1024 + nt * 128 + g * 64 + 4 * fq;
  u32x2 xr[4];
#pragma unroll
  for (int n = 0; n < 4; ++n) xr[n] = *(const u32x2*)(x + orow + 16 * n);
  WAIT_V(0);
  BAR();
  ST(4);
  GAS unsigned* cnt = (GAS unsigned*)(ws + WS_CNT + tile * 256);
  if (w == 0) {
    if (lane == 0) __hip_atomic_fetch_add(cnt, 1u, __ATOMIC_RELAXED, __HIP_MEMORY_SCOPE_AGENT);
    unsigned spins = 0;
    while ((unsigned)__builtin_amdgcn_readfirstlane(__hip_atomic_load(cnt, __ATOMIC_RELAXED, __HIP_MEMORY_SCOPE_AGENT)) < 4u) {
      __builtin_amdgcn_s_sleep(1);
      if (++spins > (1u << 22)) break;
    }
  }
  asm volatile("" ::: "memory");
  BAR();
  asm volatile("" ::: "memory");
  ST(5);
  u32x4 p[3][2];
#pragma unroll
  for (int i = 1; i < 4; ++i) {
    const unsigned o = (unsigned)((((tile * 4 + kq) * 3 + (i - 1)) * 8 + w) * 2048) + lane * 16;
    p[i - 1][0] = __builtin_amdgcn_raw_buffer_load_b128(rs, o, 0, 16);
    p[i - 1][1] = __builtin_amdgcn_raw_buffer_load_b128(rs, o + 1024, 0, 16);
  }
#pragma unroll
  for (int n = 0; n < 4; ++n) {
    f32x4 s = acc[0][n];
#pragma unroll
    for (int i = 0; i < 3; ++i) {
      const unsigned lo = p[i][n >> 1][(n & 1) * 2], hi = p[i][n >> 1][(n & 1) * 2 + 1];
      s[0] += __uint_as_float(lo << 16); s[1] += __uint_as_float(lo & 0xffff0000u);
      s[2] += __uint_as_float(hi << 16); s[3] += __uint_as_float(hi & 0xffff0000u);
    }
    s[0] += __uint_as_float(xr[n][0] << 16); s[1] += __uint_as_float(xr[n][0] & 0xffff0000u);
    s[2] += __uint_as_float(xr[n][1] << 16); s[3] += __uint_as_float(xr[n][1] & 0xffff0000u);
    *(u32x2*)(out + orow + 16 * n) = (u32x2){pk2(s[0], s[1]), pk2(s[2], s[3])};
  }
  ST(6);
  ST(7);
  ST_OUT(1, 8);
}

}

extern "C" void kernel_launch(void* const* d_in, const int* in_sizes, int n_in, void* d_out, int out_size, void* d_ws,
                              size_t ws_size, hipStream_t stream) {
  const u16* x = (const u16*)d_in[0];
  const u16* wqkv = (const u16*)d_in[1];
  const u16* wo = (const u16*)d_in[2];
  const u16* wg = (const u16*)d_in[4];
  const u16* wu = (const u16*)d_in[5];
  const u16* wd = (const u16*)d_in[6];
  char* ws = (char*)d_ws;
  (void)hipFuncSetAttribute((const void*)gemm1_kernel, hipFuncAttributeMaxDynamicSharedMemorySize, G1_LDS);
  (void)hipFuncSetAttribute((const void*)gemm2_kernel, hipFuncAttributeMaxDynamicSharedMemorySize, G2_LDS);
  ln_kernel<<<256, 512, 0, stream>>>(x, ws);
  gemm1_kernel<<<256, 512, G1_LDS, stream>>>(wqkv, wg, wu, ws);
  gemm2_kernel<<<256, 512, G2_LDS, stream>>>(x, wo, wd, ws, (u16*)d_out);
}
```

```cpp
#define PF_W2 1
#define PF_LN_ROWS 256
#include <hip/hip_runtime.h>
#include <hip/hip_bf16.h>
#include <type_traits>
#define EARLY 2
#define H_AUX 16

namespace {

typedef unsigned short u16;
typedef short bf16x8 __attribute__((ext_vector_type(8)));
typedef short bf16x4 __attribute__((ext_vector_type(4)));
typedef float f32x4 __attribute__((ext_vector_type(4)));
typedef float f32x2 __attribute__((ext_vector_type(2)));
typedef unsigned u32x4 __attribute__((ext_vector_type(4)));
typedef unsigned u32x2 __attribute__((ext_vector_type(2)));
typedef __bf16 bf2 __attribute__((ext_vector_type(2)));
#define LAS __attribute__((address_space(3)))
#define GAS __attribute__((address_space(1)))

#define WAIT_V(n) asm volatile("s_waitcnt vmcnt(" #n ")" ::: "memory")
#define WAIT_V_IF0(n, c) asm volatile("s_cmp_lg_u32 %0, 0\n\ts_cbranch_scc1 1f\n\ts_waitcnt vmcnt(" #n ")\n1:" :: "s"(c) : "memory", "scc")
#define WAIT_L0() asm volatile("s_waitcnt lgkmcnt(0)" ::: "memory")
#define BAR() __builtin_amdgcn_s_barrier()
#define SCHED() __builtin_amdgcn_sched_barrier(0)

__device__ __forceinline__ float b2f(u16 v) { return __uint_as_float(((unsigned)v) << 16); }
__device__ __forceinline__ unsigned pk2(float lo, float hi) {
  bf2 r = {(__bf16)lo, (__bf16)hi};
  return __builtin_bit_cast(unsigned, r);
}
__device__ __forceinline__ float rbf(float f) { return (float)(__bf16)f; }
__device__ __forceinline__ bf16x8 pack8(f32x4 a, f32x4 b) {
  u32x4 r = {pk2(a[0], a[1]), pk2(a[2], a[3]), pk2(b[0], b[1]), pk2(b[2], b[3])};
  return __builtin_bit_cast(bf16x8, r);
}
__device__ __forceinline__ bf16x4 trd(LAS const char* p) {
  return __builtin_amdgcn_ds_read_tr16_b64_v4i16((LAS bf16x4*)p);
}
__device__ __forceinline__ bf16x8 cat(bf16x4 a, bf16x4 b) { return __builtin_shufflevector(a, b, 0, 1, 2, 3, 4, 5, 6, 7); }
__device__ __forceinline__ f32x4 mfma(bf16x8 a, bf16x8 b, f32x4 c) {
  return __builtin_amdgcn_mfma_f32_16x16x32_bf16(a, b, c, 0, 0, 0);
}
__device__ __forceinline__ void dma_first(unsigned lds_dst, unsigned voff, const char* b0) {
  asm volatile("s_mov_b32 m0, %0\n\ts_nop 0\n\tglobal_load_lds_dwordx4 %1, %2" :: "s"(lds_dst), "v"(voff), "s"(b0) : "memory");
}
template <int OFF>
__device__ __forceinline__ void dma_next(unsigned voff, const char* b) {
  asm volatile("global_load_lds_dwordx4 %0, %1 offset:%2" :: "v"(voff), "s"(b), "i"(OFF) : "memory");
}
__device__ __forceinline__ void dma1m(unsigned lds_dst, const char* p, unsigned long long mask) {
  asm volatile("s_mov_b32 m0, %0\n\ts_mov_b64 exec, %2\n\t"
               "global_load_lds_dwordx4 %1, off\n\t"
               "s_mov_b64 exec, -1"
               :: "s"(lds_dst), "v"(p), "s"(mask) : "memory");
}
__device__ __forceinline__ void touch(unsigned lds_dst, const void* p) {
  asm volatile("s_mov_b32 m0, %0\n\ts_nop 0\n\tglobal_load_lds_dword %1, off" :: "s"(lds_dst), "v"(p) : "memory");
}
__device__ __forceinline__ LAS char* lp(unsigned a) { return (LAS char*)(size_t)a; }
__device__ __forceinline__ float xmax(float v) {
  auto a = __builtin_amdgcn_permlane16_swap(__float_as_uint(v), __float_as_uint(v), false, false);
  v = fmaxf(__uint_as_float(a[0]), __uint_as_float(a[1]));
  auto b = __builtin_amdgcn_permlane32_swap(__float_as_uint(v), __float_as_uint(v), false, false);
  return fmaxf(__uint_as_float(b[0]), __uint_as_float(b[1]));
}
__device__ __forceinline__ float xsum(float v) {
  auto a = __builtin_amdgcn_permlane16_swap(__float_as_uint(v), __float_as_uint(v), false, false);
  v = __uint_as_float(a[0]) + __uint_as_float(a[1]);
  auto b = __builtin_amdgcn_permlane32_swap(__float_as_uint(v), __float_as_uint(v), false, false);
  return __uint_as_float(b[0]) + __uint_as_float(b[1]);
}

#define ST_DECL
#define LT_DECL
#define LT(k)
#define ST(k)
#define ST_OUT(kid, n)
constexpr size_t WS_XN = 0;
constexpr size_t WS_H = WS_XN + (size_t)4 * 1024 * 1024;
constexpr size_t H_BYTES = (size_t)8 * 4 * 36 * 16 * 1024;
constexpr size_t WS_X = WS_H + H_BYTES;
constexpr size_t X_BYTES = (size_t)64 * 4 * 3 * 8 * 2048;
constexpr size_t WS_CNT = WS_X + X_BYTES;
constexpr size_t WS_ST = WS_CNT + 65536;

__global__ __launch_bounds__(512) void ln_kernel(const u16* __restrict__ x, const u16* __restrict__ wqkv, const u16* __restrict__ wg,
                                                 const u16* __restrict__ wu, char* __restrict__ ws) {
  __shared__ __attribute__((aligned(16))) char tr[128 * 144 + 256];
  const int lane = threadIdx.x & 63, w = threadIdx.x >> 6, b = blockIdx.x;
  const int F = b >> 1, mb = F >> 4, mf = F & 15, r = 8 * (b & 1) + w;
  const int row = mb * 256 + 32 * (mf >> 1) + 2 * r + (mf & 1);
  if (b == 0 && threadIdx.x < 64) *(unsigned*)(ws + WS_CNT + threadIdx.x * 256) = 0u;
  const u16* xr = x + row * 1024 + lane * 8;
  const u32x4 ra = *(const u32x4*)(xr), rb = *(const u32x4*)(xr + 512);
  float v[16];
#pragma unroll
  for (int i = 0; i < 4; ++i) {
    v[2 * i] = __uint_as_float(ra[i] << 16); v[2 * i + 1] = __uint_as_float(ra[i] & 0xffff0000u);
    v[8 + 2 * i] = __uint_as_float(rb[i] << 16); v[8 + 2 * i + 1] = __uint_as_float(rb[i] & 0xffff0000u);
  }
  float s = 0.f;
#pragma unroll
  for (int i = 0; i < 16; ++i) s += v[i];
  {
    asm volatile("" : "+v"(s) :: "memory");
    const int kq = (b & 7) >> 1, u = (b & 1) * 32 + (b >> 3);
    constexpr int RPW = PF_LN_ROWS / 64;
    for (int t = threadIdx.x; t < (RPW * 40 + 63) / 64 * 64; t += 512) {
      const int tt = t < RPW * 40 ? t : 0;
      const int rr = (tt / 40) * 64 + u, l = tt % 40;
      const u16* p = l < 12 ? wqkv + rr * 3072 + (l >> 2) * 1024 + 256 * kq + (l & 3) * 64
                            : (l < 26 ? wg + rr * 3584 + 896 * kq + (l - 12) * 64 : wu + rr * 3584 + 896 * kq + (l - 26) * 64);
      touch((unsigned)(size_t)(LAS char*)(tr + 128 * 144), p);
    }
  }
#pragma unroll
  for (int o = 1; o < 16; o <<= 1) s += __shfl_xor(s, o);
  s = xsum(s);
  const float mean = s * (1.0f / 1024.0f);
  float q = 0.f;
#pragma unroll
  for (int i = 0; i < 16; ++i) { const float d = v[i] - mean; q += d * d; }
#pragma unroll
  for (int o = 1; o < 16; o <<= 1) q += __shfl_xor(q, o);
  q = xsum(q);
  const float mb16 = rbf(mean), den = rbf(sqrtf(rbf(rbf(q * (1.0f / 1024.0f)) + 1e-5f)));
  const float inv = 1.0f / den;
#pragma unroll
  for (int i = 0; i < 16; ++i) v[i] = rbf(v[i] - mb16) * inv;
  const int kb = lane >> 2, c = lane & 3;
#pragma unroll
  for (int h = 0; h < 2; ++h) {
    char* t = tr + ((kb + 16 * h) * 4 + 2 * (c & 1)) * 144 + w * 16 + (c >> 1) * 8;
    *(u32x2*)t = (u32x2){pk2(v[8 * h], v[8 * h + 1]), pk2(v[8 * h + 2], v[8 * h + 3])};
    *(u32x2*)(t + 144) = (u32x2){pk2(v[8 * h + 4], v[8 * h + 5]), pk2(v[8 * h + 6], v[8 * h + 7])};
  }
  __syncthreads();
  const auto rs = __builtin_amdgcn_make_buffer_rsrc(ws + WS_XN + (size_t)mb * 32 * 16384 + mf * 1024 + (b & 1) * 128, 0, 32 * 16384, 0x00020000);
#pragma unroll
  for (int h = 0; h < 2; ++h) {
    const int seg = (threadIdx.x >> 3) + 64 * h, t8 = threadIdx.x & 7;
    const u32x4 o = *(const u32x4*)(tr + seg * 144 + t8 * 16);
    __builtin_amdgcn_raw_buffer_store_b128(o, rs, (seg >> 2) * 16384 + (seg & 3) * 256 + t8 * 16, 0, H_AUX);
  }
}

constexpr int G1_SA = 17 * 1024, G1_SB = 20 * 1024, G1_SLOT = G1_SA + G1_SB, G1_LDS = 4 * G1_SLOT + 256;

__device__ __forceinline__ unsigned glu2(f32x2 g, f32x2 u) {
  g = g * -1.4426950408889634f;
  f32x2 e = {__builtin_amdgcn_exp2f(g[0]), __builtin_amdgcn_exp2f(g[1])};
  e = e + 1.0f;
  f32x2 r = {__builtin_amdgcn_rcpf(e[0]), __builtin_amdgcn_rcpf(e[1])};
  r = r * u;
  return pk2(r[0], r[1]);
}
__device__ __forceinline__ u32x2 glu4(f32x4 g, f32x4 u) {
  return (u32x2){glu2(__builtin_shufflevector(g, g, 0, 1), __builtin_shufflevector(u, u, 0, 1)),
                 glu2(__builtin_shufflevector(g, g, 2, 3), __builtin_shufflevector(u, u, 2, 3))};
}
__device__ __forceinline__ u32x4 glu8(f32x4 g0, f32x4 u0, f32x4 g1, f32x4 u1) {
  const u32x2 a = glu4(g0, u0), b = glu4(g1, u1);
  return (u32x4){a[0], a[1], b[0], b[1]};
}

__global__ __launch_bounds__(512, 2) void gemm1_kernel(const u16* __restrict__ wqkv, const u16* __restrict__ wg,
                                                       const u16* __restrict__ wu, const u16* __restrict__ wo,
                                                       const u16* __restrict__ wd, char* __restrict__ ws) {
  extern __shared__ __attribute__((aligned(16))) char smem[];
  LAS char* lds = (LAS char*)smem;
  ST_DECL LT_DECL ST(0);
  const int tid = threadIdx.x;
  int lane = tid & 63;
  const int w = __builtin_amdgcn_readfirstlane(tid >> 6), g = w >> 2, wi = w & 3;
  const int b = blockIdx.x, xcd = b & 7, idx = b >> 3;
  const int mb = (xcd & 1) * 4 + (idx & 3), kq = xcd >> 1, ntl = idx >> 2;
  const bool typeA = ntl < 4;
  const int j = ntl & 3;
  int fr = lane & 15, fq = lane >> 4;
  const int fA = 896 * kq + 64 * j;
  const int f0 = 896 * kq + 256 + 160 * j, odd = j & 1;
  const int fa = f0 + 32 * odd, fb = fa + 64, fc = odd ? f0 : f0 + 128;

  const char* c1;
  unsigned v1;
  int s1, p1, p2;
  const char* c2;
  int d1, d2;
  unsigned long long m2 = ~0ull;
  {
    const int rr = lane >> 3, pc = lane & 7, fi = (pc >> 1) ^ ((rr >> 1) & 3);
    if (g == 0) {
      int ld; const u16* q1;
      if (typeA) {
        if (wi < 3) { q1 = wqkv + wi * 1024 + 256 * kq + 64 * j; ld = 3072; } else { q1 = wg + fA; ld = 3584; }
        c2 = (const char*)(wu + fA + (size_t)(wi * 8 + rr) * 3584) + fi * 32 + (pc & 1) * 16;
      } else {
        q1 = ((wi & 2) ? wu : wg) + ((wi & 1) ? fb : fa); ld = 3584;
        c2 = (const char*)(((fi & 1) ? wu : wg) + fc + (fi >> 1) * 16 + (size_t)(wi * 8 + rr) * 3584) + (pc & 1) * 16;
      }
      c1 = (const char*)q1; v1 = rr * ld * 2 + fi * 32 + (pc & 1) * 16;
      s1 = 16 * ld - 1024; p1 = 64 * ld; p2 = 64 * 3584;
      d1 = G1_SA + wi * 4096; d2 = G1_SA + 16384 + wi * 1024;
    } else {
      c1 = ws + WS_XN + (size_t)mb * 32 * 16384 + wi * 4096; v1 = lane * 16;
      const int mh = mb > 0 ? mb - 1 : 0, i = lane & 15;
      c2 = ws + WS_XN + (size_t)mh * 32 * 16384 + (14 + (i >> 3)) * 1024 + (wi * 16 + 8 + (i & 7)) * 16;
      m2 = 0xffffull;
      s1 = 0; p1 = 16384; p2 = 16384;
      d1 = wi * 4096; d2 = 16384 + wi * 256;
    }
  }
  const unsigned lb = (unsigned)(size_t)lds;
  unsigned ns = lb;
  auto adv = [&]() { c1 += p1; c2 += p2; ns = (ns == lb + 3 * G1_SLOT) ? lb : ns + G1_SLOT; };
  auto stage = [&]() {
    dma_first(ns + d1, v1, c1); dma_next<1024>(v1, c1 + s1); dma_next<2048>(v1, c1 + 2 * s1); dma_next<3072>(v1, c1 + 3 * s1);
    dma1m(ns + d2, c2, m2);
    adv();
  };
  stage();
  BAR();
  stage();

  const int r0 = 4 * fq + ((lane & 15) >> 2);
  unsigned aoff = lb + wi * 4096 + lane * 16;
  unsigned hoffA = lb + 16384 + lane * 16;
  unsigned boff0 = lb + G1_SA + r0 * 128 + 32 * ((2 * g) ^ ((r0 >> 1) & 3)) + 8 * (lane & 3);
  unsigned boff1 = boff0 ^ 32;
  unsigned hoffB = (boff0 ^ (32 * (wi & 1))) + (1 + (wi >> 1)) * 4096;

  f32x4 acc[4][10];
  f32x4 hacc = {0.f, 0.f, 0.f, 0.f};
#pragma unroll
  for (int i = 0; i < 4; ++i)
#pragma unroll
    for (int n = 0; n < 10; ++n) { acc[i][n] = (f32x4){0.f, 0.f, 0.f, 0.f}; asm volatile("" : "+v"(acc[i][n])); }
  asm volatile("" : "+v"(aoff), "+v"(hoffA), "+v"(boff0), "+v"(boff1), "+v"(hoffB), "+v"(hacc));
  ST(1);
  WAIT_V(5);
  BAR();
  ST(2);
  if (g == 1) BAR();

  auto phase = [&](auto do_stage, int q) {
    constexpr bool DS = decltype(do_stage)::value;
    bf16x8 a[4], bfr[10], ha, hb;
    LT(0);
#define RB(c) bfr[2 * (c)] = cat(trd(lp(boff0) + (c) * 4096), trd(lp(boff0) + (c) * 4096 + 2048)); \
              bfr[2 * (c) + 1] = cat(trd(lp(boff1) + (c) * 4096), trd(lp(boff1) + (c) * 4096 + 2048));
    a[0] = *(const LAS bf16x8*)(lp(aoff)); a[1] = *(const LAS bf16x8*)(lp(aoff) + 1024);
    if (DS) dma_first(ns + d1, v1, c1);
    a[2] = *(const LAS bf16x8*)(lp(aoff) + 2048); a[3] = *(const LAS bf16x8*)(lp(aoff) + 3072);
    RB(0)
    if (DS) dma_next<1024>(v1, c1 + s1);
    RB(1)
    ha = *(const LAS bf16x8*)lp(hoffA);
    if (DS) dma_next<2048>(v1, c1 + 2 * s1);
    RB(2)
    hb = cat(trd(lp(hoffB)), trd(lp(hoffB) + 2048));
    if (DS) dma_next<3072>(v1, c1 + 3 * s1);
    RB(3)
    if (DS) dma1m(ns + d2, c2, m2);
    RB(4)
#undef RB
    SCHED();
    LT(1);
    const int dlt = ((q & 3) == 3) ? -3 * G1_SLOT : G1_SLOT;
    aoff += dlt; hoffA += dlt; boff0 += dlt; boff1 += dlt; hoffB += dlt;
    if (DS) {
      adv();
      if (q == 1) stage();
      LT(2);
      if (q == 0) WAIT_V(5); else WAIT_V(10);
    } else {
      LT(2);
      if (q == 29) {
        WAIT_V(0);
        int t = w * 64 + __builtin_amdgcn_mbcnt_hi(~0u, __builtin_amdgcn_mbcnt_lo(~0u, 0u));
        t = t < 288 ? t : t - 288;
        const int rr = (t >> 4) * 64 + (xcd & 1) * 32 + idx;
        const u16* p = (rr < 256 ? wo + (256 * kq + rr) * 1024 : wd + (896 * kq + rr - 256) * 1024) + (t & 15) * 64;
        touch(lb + 4 * G1_SLOT, p);
      }
    }
    WAIT_L0();
    LT(3);
    SCHED();
    BAR();
    SCHED();
    LT(4);
    __builtin_amdgcn_s_setprio(1);
    hacc = mfma(hb, ha, hacc);
#pragma unroll
    for (int n = 0; n < 10; ++n)
#pragma unroll
      for (int i = 0; i < 4; ++i) {
        if (n == 9 && i == 4 - EARLY) { SCHED(); __builtin_amdgcn_s_setprio(3); if (q < 31 || g == 0) BAR(); SCHED(); }
        acc[i][n] = mfma(bfr[n], a[i], acc[i][n]);
      }
    __builtin_amdgcn_s_setprio(0);
    SCHED();
    LT(5);
    if (EARLY == 0) { if (q < 31 || g == 0) BAR(); }
    SCHED();
  };
#pragma clang loop unroll(disable)
  for (int q = 0; q < 29; ++q) phase(std::true_type{}, q);
#pragma clang loop unroll(disable)
  for (int q = 29; q < 32; ++q) phase(std::false_type{}, q);

  ST(3);
  lane = __builtin_amdgcn_mbcnt_hi(~0u, __builtin_amdgcn_mbcnt_lo(~0u, 0u));
  asm volatile("" : "+v"(lane));
  fr = lane & 15; fq = lane >> 4;
  const auto hrs = __builtin_amdgcn_make_buffer_rsrc(ws + WS_H + (size_t)(mb * 4 + kq) * 36 * 16384, 0, 36 * 16384, 0x00020000);
  const unsigned hv = wi * 4096 + lane * 16;
  if (!typeA) {
    const int kba = 8 + ((fa - 896 * kq) >> 5) + g, kbc = 8 + ((fc - 896 * kq) >> 5);
#pragma unroll
    for (int i = 0; i < 4; ++i) {
      __builtin_amdgcn_raw_buffer_store_b128(glu8(acc[i][0], acc[i][4], acc[i][1], acc[i][5]), hrs, hv + kba * 16384 + i * 1024, 0, H_AUX);
      __builtin_amdgcn_raw_buffer_store_b128(glu8(acc[i][2], acc[i][6], acc[i][3], acc[i][7]), hrs, hv + kba * 16384 + 32768 + i * 1024, 0, H_AUX);
      __builtin_amdgcn_raw_buffer_store_b64(glu4(acc[i][8], acc[i][9]), hrs, hv + kbc * 16384 + g * 8 + i * 1024, 0, H_AUX);
    }
  } else {
    LAS char* kim = lds + g * 36864;
    LAS char* vim = kim + 17 * 1024;
    bf16x8 kf[4], qf[4];
#pragma unroll
    for (int i = 0; i < 4; ++i) {
      kf[i] = pack8(acc[i][2], acc[i][3]);
      LAS char* vr = vim + ((wi * 4 + i) * 16 + fr) * 64 + fq * 8;
      *(LAS u32x2*)(vr) = (u32x2){pk2(acc[i][4][0], acc[i][4][1]), pk2(acc[i][4][2], acc[i][4][3])};
      *(LAS u32x2*)(vr + 32) = (u32x2){pk2(acc[i][5][0], acc[i][5][1]), pk2(acc[i][5][2], acc[i][5][3])};
    }
    *(LAS bf16x8*)(kim + (wi * 4 + 2) * 1024 + lane * 16) = kf[2];
    *(LAS bf16x8*)(kim + (wi * 4 + 3) * 1024 + lane * 16) = kf[3];
    {
      LAS char* hp = (wi < 2) ? (kim + 16 * 1024 + lane * 16 + wi * 8) : (vim + (256 + fr) * 64 + (wi - 2) * 32 + fq * 8);
      *(LAS u32x2*)hp = (u32x2){pk2(hacc[0], hacc[1]), pk2(hacc[2], hacc[3])};
    }
    auto glu_part = [&]() {
#pragma unroll
      for (int i = 0; i < 4; ++i)
        __builtin_amdgcn_raw_buffer_store_b128(glu8(acc[i][6], acc[i][8], acc[i][7], acc[i][9]), hrs, hv + (8 + 2 * j + g) * 16384 + i * 1024, 0, H_AUX);
    };
    WAIT_L0();
    if (g == 0) glu_part();
    SCHED();
    BAR();
    asm volatile("" ::: "memory");
    const float NEG = -3.0e38f;
    f32x4 bc, bp, bp0, bp1;
    const bool halo = wi == 0;
#pragma unroll
    for (int e = 0; e < 4; ++e) {
      const int c = 4 * fq + e;
      bc[e] = (c <= fr && c + 7 >= fr) ? 0.f : NEG;
      bp[e] = (c >= fr + 9) ? 0.f : NEG;
      bp0[e] = halo ? ((mb > 0 && c < 8 && c >= fr + 1) ? 0.f : NEG) : bp[e];
      bp1[e] = (halo && mb == 0) ? NEG : bp[e];
    }
    const int pf0 = halo ? 16 : wi * 4 - 2, pf1 = halo ? 16 : wi * 4 - 1;
    const bf16x8 kp0 = *(const LAS bf16x8*)(kim + pf0 * 1024 + lane * 16);
    const bf16x8 kp1 = *(const LAS bf16x8*)(kim + pf1 * 1024 + lane * 16);
    const LAS char* vb = vim + (4 * fq + ((lane & 15) >> 2)) * 64 + (lane & 3) * 8;
    bf16x4 vt[6][2];
#pragma unroll
    for (int d = 0; d < 2; ++d) {
      vt[0][d] = trd(vb + pf0 * 1024 + d * 32); vt[1][d] = trd(vb + pf1 * 1024 + d * 32);
#pragma unroll
      for (int i = 0; i < 4; ++i) vt[2 + i][d] = trd(vb + (wi * 4 + i) * 1024 + d * 32);
    }
    f32x4 sp[4], sc[4];
#pragma unroll
    for (int i = 0; i < 4; ++i) { qf[i] = pack8(acc[i][0], acc[i][1]); sc[i] = mfma(kf[i], qf[i], bc); }
    sp[2] = mfma(kf[0], qf[2], bp); sp[3] = mfma(kf[1], qf[3], bp);
    sp[0] = mfma(kp0, qf[0], bp0); sp[1] = mfma(kp1, qf[1], bp1);
    const float cs = 0.17677669529663687f * 1.4426950408889634f;
    const f32x4 z4 = {0.f, 0.f, 0.f, 0.f};
    float m[4], sum[4];
#pragma unroll
    for (int i = 0; i < 4; ++i)
      m[i] = fmaxf(fmaxf(fmaxf(sp[i][0], sp[i][1]), fmaxf(sp[i][2], sp[i][3])), fmaxf(fmaxf(sc[i][0], sc[i][1]), fmaxf(sc[i][2], sc[i][3])));
#pragma unroll
    for (int i = 0; i < 4; ++i) m[i] = -cs * xmax(m[i]);
#pragma unroll
    for (int i = 0; i < 4; ++i) {
#pragma unroll
      for (int e = 0; e < 4; ++e) {
        sp[i][e] = __builtin_amdgcn_exp2f(__builtin_fmaf(sp[i][e], cs, m[i]));
        sc[i][e] = __builtin_amdgcn_exp2f(__builtin_fmaf(sc[i][e], cs, m[i]));
      }
      const f32x4 t = sp[i] + sc[i];
      sum[i] = (t[0] + t[1]) + (t[2] + t[3]);
    }
#pragma unroll
    for (int i = 0; i < 4; ++i) sum[i] = __builtin_amdgcn_rcpf(xsum(sum[i]));
#pragma unroll
    for (int i = 0; i < 4; ++i) {
      const bf16x8 pfr = pack8(sp[i] * sum[i], sc[i] * sum[i]);
      const f32x4 o0 = mfma(cat(vt[i][0], vt[2 + i][0]), pfr, z4);
      const f32x4 o1 = mfma(cat(vt[i][1], vt[2 + i][1]), pfr, z4);
      __builtin_amdgcn_raw_buffer_store_b128(__builtin_bit_cast(u32x4, pack8(o0, o1)), hrs, hv + (2 * j + g) * 16384 + i * 1024, 0, H_AUX);
    }
    if (g == 1) glu_part();
  }
  ST(4);
  ST(5);
  ST_OUT(0, 6);
}

constexpr int G2_SA = 32768, G2_SB = 16384, G2_SLOT = G2_SA + G2_SB, G2_LDS = 3 * G2_SLOT;

__global__ __launch_bounds__(512, 2) void gemm2_kernel(const u16* __restrict__ x, const u16* __restrict__ wo,
                                                       const u16* __restrict__ wd, char* __restrict__ ws,
                                                       u16* __restrict__ out) {
  extern __shared__ __attribute__((aligned(16))) char smem[];
  LAS char* lds = (LAS char*)smem;
  ST_DECL LT_DECL ST(0);
  const int tid = threadIdx.x, lane = tid & 63;
  const int w = __builtin_amdgcn_readfirstlane(tid >> 6), g = w >> 2, wi = w & 3;
  const int b = blockIdx.x, xcd = b & 7, idx = b >> 3;
  const int mb = (xcd & 1) * 4 + (idx & 3), kq = xcd >> 1, nt = idx >> 2;
  const int fr = lane & 15, fq = lane >> 4;

  const char* ca = ws + WS_H + (size_t)(mb * 4 + kq) * 36 * 16384 + w * 4096;
  const unsigned va = lane * 16;
  const int e0 = (0 ^ kq) * 1024, e1 = ((1 ^ kq) - 1) * 1024, e2 = ((2 ^ kq) - 2) * 1024, e3 = ((3 ^ kq) - 3) * 1024;
  const int rr = lane >> 3, pc = lane & 7;
  const unsigned vb = rr * 2048 + (((pc >> 1) ^ ((rr >> 1) & 3)) * 32) + (pc & 1) * 16;
  const char* cb = (const char*)(wo + (size_t)(256 * kq + 8 * w) * 1024 + nt * 128);
  const char* cb2 = (const char*)(wd + (size_t)(896 * kq + 8 * w) * 1024 + nt * 128);
  int st = 0;
  const unsigned lb = (unsigned)(size_t)lds;
  unsigned ns = lb;
  auto adv = [&]() { ca += 32768; ++st; cb = (st == 4) ? cb2 : cb + 64 * 2048; ns = (ns == lb + 2 * G2_SLOT) ? lb : ns + G2_SLOT; };
  auto stage = [&]() {
    dma_first(ns + w * 4096, va, ca + e0); dma_next<1024>(va, ca + e1); dma_next<2048>(va, ca + e2); dma_next<3072>(va, ca + e3);
    dma_first(ns + G2_SA + w * 2048, vb, cb); dma_next<1024>(vb, cb - 896);
    adv();
  };
  stage();
  BAR();
  stage();
  const int rl = 4 * (fq & 1) + ((lane & 15) >> 2);
  unsigned aoff = lb + wi * 4096 + lane * 16;
  unsigned boff = lb + G2_SA + (fq >> 1) * 2048 + g * 1024 + rl * 128 + 32 * ((rl >> 1) & 3) + 8 * (lane & 3);

  f32x4 acc[4][4];
#pragma unroll
  for (int i = 0; i < 4; ++i)
#pragma unroll
    for (int n = 0; n < 4; ++n) { acc[i][n] = (f32x4){0.f, 0.f, 0.f, 0.f}; asm volatile("" : "+v"(acc[i][n])); }
  ST(1);
  WAIT_V(6);
  BAR();
  ST(2);
  if (g == 1) BAR();

  int sl = 0;
  auto phase = [&](auto do_stage, int q) {
    constexpr bool DS = decltype(do_stage)::value;
    bf16x8 a[2][4], bfr[2][4];
    LT(0);
#define RA(kk, i) a[kk][i] = *(const LAS bf16x8*)(lp(aoff) + (kk) * 16384 + (i) * 1024);
#define RB(kk, n) bfr[kk][n] = cat(trd(lp(boff ^ ((n) * 32)) + (kk) * 8192), trd(lp(boff ^ ((n) * 32)) + (kk) * 8192 + 4096));
    RA(0, 0) RA(0, 1)
    if (DS) dma_first(ns + w * 4096, va, ca + e0);
    RA(0, 2) RA(0, 3) RB(0, 0)
    if (DS) dma_next<1024>(va, ca + e1);
    RB(0, 1) RB(0, 2) RB(0, 3)
    if (DS) dma_next<2048>(va, ca + e2);
    RA(1, 0) RA(1, 1) RA(1, 2)
    if (DS) dma_next<3072>(va, ca + e3);
    RA(1, 3) RB(1, 0) RB(1, 1)
    if (DS) dma_first(ns + G2_SA + w * 2048, vb, cb);
    RB(1, 2) RB(1, 3)
    if (DS) dma_next<1024>(vb, cb - 896);
#undef RA
#undef RB
    SCHED();
    LT(1);
    sl = (sl == 2) ? 0 : sl + 1;
    const int dlt = (sl == 0) ? -2 * G2_SLOT : G2_SLOT;
    aoff += dlt; boff += dlt;
    if (DS) adv();
    LT(2);
    asm volatile("" : "+v"(aoff), "+v"(boff), "+s"(ca), "+s"(cb), "+s"(ns), "+s"(sl));
    if (DS) WAIT_V_IF0(6, g ^ 1); else WAIT_V_IF0(0, g ^ 1);
    WAIT_L0();
    LT(3);
    SCHED();
    BAR();
    SCHED();
    LT(4);
    __builtin_amdgcn_s_setprio(1);
#pragma unroll
    for (int kk = 0; kk < 2; ++kk)
#pragma unroll
      for (int n = 0; n < 4; ++n)
#pragma unroll
        for (int i = 0; i < 4; ++i) {
          if (kk == 1 && n == 3 && i == 4 - EARLY) { SCHED(); __builtin_amdgcn_s_setprio(3); if (DS) WAIT_V_IF0(6, g); else WAIT_V_IF0(0, g); if (q < 17 || g == 0) BAR(); SCHED(); }
          acc[i][n] = mfma(bfr[kk][n], a[kk][i], acc[i][n]);
        }
    __builtin_amdgcn_s_setprio(0);
    SCHED();
    LT(5);
    if (EARLY == 0) { if (DS) WAIT_V_IF0(6, g); else WAIT_V_IF0(0, g); if (q < 17 || g == 0) BAR(); }
    SCHED();
  };
#pragma clang loop unroll(disable)
  for (int q = 0; q < 16; ++q) phase(std::true_type{}, q);
#pragma clang loop unroll(disable)
  for (int q = 16; q < 18; ++q) phase(std::false_type{}, q);

  ST(3);
  const int tile = mb * 8 + nt;
  const auto rs = __builtin_amdgcn_make_buffer_rsrc(ws + WS_X, 0, (int)X_BYTES, 0x00020000);
#pragma unroll
  for (int i = 1; i < 4; ++i) {
    const unsigned o = (unsigned)((((tile * 4 + (kq ^ i)) * 3 + (i - 1)) * 8 + w) * 2048) + lane * 16;
    __builtin_amdgcn_raw_buffer_store_b128(__builtin_bit_cast(u32x4, pack8(acc[i][0], acc[i][1])), rs, o, 0, 16);
    __builtin_amdgcn_raw_buffer_store_b128(__builtin_bit_cast(u32x4, pack8(acc[i][2], acc[i][3])), rs, o + 1024, 0, 16);
  }
  const int mf = wi * 4 + kq;
  const size_t orow = (size_t)(mb * 256 + 32 * (mf >> 1) + 2 * fr + (mf & 1)) * 1024 + nt * 128 + g * 64 + 4 * fq;
  u32x2 xr[4];
#pragma unroll
  for (int n = 0; n < 4; ++n) xr[n] = *(const u32x2*)(x + orow + 16 * n);
  WAIT_V(0);
  BAR();
  ST(4);
  GAS unsigned* cnt = (GAS unsigned*)(ws + WS_CNT + tile * 256);
  if (w == 0) {
    if (lane == 0) __hip_atomic_fetch_add(cnt, 1u, __ATOMIC_RELAXED, __HIP_MEMORY_SCOPE_AGENT);
    unsigned spins = 0;
    while ((unsigned)__builtin_amdgcn_readfirstlane(__hip_atomic_load(cnt, __ATOMIC_RELAXED, __HIP_MEMORY_SCOPE_AGENT)) < 4u) {
      __builtin_amdgcn_s_sleep(1);
      if (++spins > (1u << 22)) break;
    }
  }
  asm volatile("" ::: "memory");
  BAR();
  asm volatile("" ::: "memory");
  ST(5);
  u32x4 p[3][2];
#pragma unroll
  for (int i = 1; i < 4; ++i) {
    const unsigned o = (unsigned)((((tile * 4 + kq) * 3 + (i - 1)) * 8 + w) * 2048) + lane * 16;
    p[i - 1][0] = __builtin_amdgcn_raw_buffer_load_b128(rs, o, 0, 16);
    p[i - 1][1] = __builtin_amdgcn_raw_buffer_load_b128(rs, o + 1024, 0, 16);
  }
#pragma unroll
  for (int n = 0; n < 4; ++n) {
    f32x4 s = acc[0][n];
#pragma unroll
    for (int i = 0; i < 3; ++i) {
      const unsigned lo = p[i][n >> 1][(n & 1) * 2], hi = p[i][n >> 1][(n & 1) * 2 + 1];
      s[0] += __uint_as_float(lo << 16); s[1] += __uint_as_float(lo & 0xffff0000u);
      s[2] += __uint_as_float(hi << 16); s[3] += __uint_as_float(hi & 0xffff0000u);
    }
    s[0] += __uint_as_float(xr[n][0] << 16); s[1] += __uint_as_float(xr[n][0] & 0xffff0000u);
    s[2] += __uint_as_float(xr[n][1] << 16); s[3] += __uint_as_float(xr[n][1] & 0xffff0000u);
    *(u32x2*)(out + orow + 16 * n) = (u32x2){pk2(s[0], s[1]), pk2(s[2], s[3])};
  }
  ST(6);
  ST(7);
  ST_OUT(1, 8);
}

}

extern "C" void kernel_launch(void* const* d_in, const int* in_sizes, int n_in, void* d_out, int out_size, void* d_ws,
                              size_t ws_size, hipStream_t stream) {
  const u16* x = (const u16*)d_in[0];
  const u16* wqkv = (const u16*)d_in[1];
  const u16* wo = (const u16*)d_in[2];
  const u16* wg = (const u16*)d_in[4];
  const u16* wu = (const u16*)d_in[5];
  const u16* wd = (const u16*)d_in[6];
  char* ws = (char*)d_ws;
  (void)hipFuncSetAttribute((const void*)gemm1_kernel, hipFuncAttributeMaxDynamicSharedMemorySize, G1_LDS);
  (void)hipFuncSetAttribute((const void*)gemm2_kernel, hipFuncAttributeMaxDynamicSharedMemorySize, G2_LDS);
  ln_kernel<<<256, 512, 0, stream>>>(x, wqkv, wg, wu, ws);
  gemm1_kernel<<<256, 512, G1_LDS, stream>>>(wqkv, wg, wu, wo, wd, ws);
  gemm2_kernel<<<256, 512, G2_LDS, stream>>>(x, wo, wd, ws, (u16*)d_out);
}
```

```cpp
#define WS_BASE_MB 64
#include <hip/hip_runtime.h>
#include <hip/hip_bf16.h>
#include <type_traits>
#define EARLY 2
#define PF_W2 1
#define PF_LOOP 0
#define PF_PER 2
#define PF_LN_ROWS 256
#define PRIO_L 0
#define PRIO_M() __builtin_amdgcn_s_setprio(1)
#define PRIO_T() __builtin_amdgcn_s_setprio(3)
#define PRIO_E() __builtin_amdgcn_s_setprio(0)
#define PF_CODE 0
#define H_AUX 16

namespace {

typedef unsigned short u16;
typedef short bf16x8 __attribute__((ext_vector_type(8)));
typedef short bf16x4 __attribute__((ext_vector_type(4)));
typedef float f32x4 __attribute__((ext_vector_type(4)));
typedef float f32x2 __attribute__((ext_vector_type(2)));
typedef unsigned u32x4 __attribute__((ext_vector_type(4)));
typedef unsigned u32x2 __attribute__((ext_vector_type(2)));
typedef __bf16 bf2 __attribute__((ext_vector_type(2)));
#define LAS __attribute__((address_space(3)))
#define GAS __attribute__((address_space(1)))

#define WAIT_V(n) asm volatile("s_waitcnt vmcnt(" #n ")" ::: "memory")
#define WAIT_V_IF0(n, c) asm volatile("s_cmp_lg_u32 %0, 0\n\ts_cbranch_scc1 1f\n\ts_waitcnt vmcnt(" #n ")\n1:" :: "s"(c) : "memory", "scc")
#define WAIT_L0() asm volatile("s_waitcnt lgkmcnt(0)" ::: "memory")
#define BAR() __builtin_amdgcn_s_barrier()
#define SCHED() __builtin_amdgcn_sched_barrier(0)

__device__ __forceinline__ float b2f(u16 v) { return __uint_as_float(((unsigned)v) << 16); }
__device__ __forceinline__ unsigned pk2(float lo, float hi) {
  bf2 r = {(__bf16)lo, (__bf16)hi};
  return __builtin_bit_cast(unsigned, r);
}
__device__ __forceinline__ float rbf(float f) { return (float)(__bf16)f; }
__device__ __forceinline__ bf16x8 pack8(f32x4 a, f32x4 b) {
  u32x4 r = {pk2(a[0], a[1]), pk2(a[2], a[3]), pk2(b[0], b[1]), pk2(b[2], b[3])};
  return __builtin_bit_cast(bf16x8, r);
}
__device__ __forceinline__ bf16x4 trd(LAS const char* p) {
  return __builtin_amdgcn_ds_read_tr16_b64_v4i16((LAS bf16x4*)p);
}
__device__ __forceinline__ bf16x8 cat(bf16x4 a, bf16x4 b) { return __builtin_shufflevector(a, b, 0, 1, 2, 3, 4, 5, 6, 7); }
__device__ __forceinline__ f32x4 mfma(bf16x8 a, bf16x8 b, f32x4 c) {
  return __builtin_amdgcn_mfma_f32_16x16x32_bf16(a, b, c, 0, 0, 0);
}
__device__ __forceinline__ void dma_first(unsigned lds_dst, unsigned voff, const char* b0) {
  asm volatile("s_mov_b32 m0, %0\n\ts_nop 0\n\tglobal_load_lds_dwordx4 %1, %2" :: "s"(lds_dst), "v"(voff), "s"(b0) : "memory");
}
template <int OFF>
__device__ __forceinline__ void dma_next(unsigned voff, const char* b) {
  asm volatile("global_load_lds_dwordx4 %0, %1 offset:%2" :: "v"(voff), "s"(b), "i"(OFF) : "memory");
}
__device__ __forceinline__ void dma1m(unsigned lds_dst, const char* p, unsigned long long mask) {
  asm volatile("s_mov_b32 m0, %0\n\ts_mov_b64 exec, %2\n\t"
               "global_load_lds_dwordx4 %1, off\n\t"
               "s_mov_b64 exec, -1"
               :: "s"(lds_dst), "v"(p), "s"(mask) : "memory");
}
__device__ __forceinline__ void touch(unsigned lds_dst, const void* p) {
  asm volatile("s_mov_b32 m0, %0\n\ts_nop 0\n\tglobal_load_lds_dword %1, off" :: "s"(lds_dst), "v"(p) : "memory");
}
#define CODE_ADDR(dst, sym) do { unsigned lo_, hi_; asm volatile("s_getpc_b64 s[90:91]\n\ts_add_u32 %0, s90, " #sym "@rel32@lo+4\n\ts_addc_u32 %1, s91, " #sym "@rel32@hi+12" \
    : "=s"(lo_), "=s"(hi_) :: "s90", "s91", "scc"); dst = (const char*)(((unsigned long long)hi_ << 32) | lo_); } while (0)
constexpr int LN_LINES = 16, G1_LINES = 80, G2_LINES = 30;
__device__ __forceinline__ LAS char* lp(unsigned a) { return (LAS char*)(size_t)a; }
__device__ __forceinline__ float xmax(float v) {
  auto a = __builtin_amdgcn_permlane16_swap(__float_as_uint(v), __float_as_uint(v), false, false);
  v = fmaxf(__uint_as_float(a[0]), __uint_as_float(a[1]));
  auto b = __builtin_amdgcn_permlane32_swap(__float_as_uint(v), __float_as_uint(v), false, false);
  return fmaxf(__uint_as_float(b[0]), __uint_as_float(b[1]));
}
__device__ __forceinline__ float xsum(float v) {
  auto a = __builtin_amdgcn_permlane16_swap(__float_as_uint(v), __float_as_uint(v), false, false);
  v = __uint_as_float(a[0]) + __uint_as_float(a[1]);
  auto b = __builtin_amdgcn_permlane32_swap(__float_as_uint(v), __float_as_uint(v), false, false);
  return __uint_as_float(b[0]) + __uint_as_float(b[1]);
}

#define ST_DECL
#define LT_DECL
#define LT(k)
#define ST(k)
#define ST_OUT(kid, n)
constexpr size_t WS_XN = (size_t)WS_BASE_MB << 20;
constexpr size_t WS_H = WS_XN + (size_t)4 * 1024 * 1024;
constexpr size_t H_BYTES = (size_t)8 * 4 * 36 * 16 * 1024;
constexpr size_t WS_X = WS_H + H_BYTES;
constexpr size_t X_BYTES = (size_t)64 * 4 * 3 * 8 * 2048;
constexpr size_t WS_CNT = WS_X + X_BYTES;
constexpr size_t WS_ST = WS_CNT + 65536;

}
extern "C" __global__ __launch_bounds__(512) void psum_ln_kernel(const u16* __restrict__ x, const u16* __restrict__ wqkv, const u16* __restrict__ wg,
                                                 const u16* __restrict__ wu, char* __restrict__ ws) {
  __shared__ __attribute__((aligned(16))) char tr[128 * 144 + 256];
  const int lane = threadIdx.x & 63, w = threadIdx.x >> 6, b = blockIdx.x;
  const int F = b >> 1, mb = F >> 4, mf = F & 15, r = 8 * (b & 1) + w;
  const int row = mb * 256 + 32 * (mf >> 1) + 2 * r + (mf & 1);
  if (b == 0 && threadIdx.x < 64) *(unsigned*)(ws + WS_CNT + threadIdx.x * 256) = 0u;
  const u16* xr = x + row * 1024 + lane * 8;
  const u32x4 ra = *(const u32x4*)(xr), rb = *(const u32x4*)(xr + 512);
  float v[16];
#pragma unroll
  for (int i = 0; i < 4; ++i) {
    v[2 * i] = __uint_as_float(ra[i] << 16); v[2 * i + 1] = __uint_as_float(ra[i] & 0xffff0000u);
    v[8 + 2 * i] = __uint_as_float(rb[i] << 16); v[8 + 2 * i + 1] = __uint_as_float(rb[i] & 0xffff0000u);
  }
  float s = 0.f;
#pragma unroll
  for (int i = 0; i < 16; ++i) s += v[i];
  {
    asm volatile("" : "+v"(s) :: "memory");
    const int kq = (b & 7) >> 1, u = (b & 1) * 32 + (b >> 3);
    constexpr int RPW = PF_LN_ROWS / 64;
    for (int t = threadIdx.x; t < (RPW * 40 + 63) / 64 * 64; t += 512) {
      const int tt = t < RPW * 40 ? t : 0;
      const int rr = (tt / 40) * 64 + u, l = tt % 40;
      const u16* p = l < 12 ? wqkv + rr * 3072 + (l >> 2) * 1024 + 256 * kq + (l & 3) * 64
                            : (l < 26 ? wg + rr * 3584 + 896 * kq + (l - 12) * 64 : wu + rr * 3584 + 896 * kq + (l - 26) * 64);
      touch((unsigned)(size_t)(LAS char*)(tr + 128 * 144), p);
    }
  }
#pragma unroll
  for (int o = 1; o < 16; o <<= 1) s += __shfl_xor(s, o);
  s = xsum(s);
  const float mean = s * (1.0f / 1024.0f);
  float q = 0.f;
#pragma unroll
  for (int i = 0; i < 16; ++i) { const float d = v[i] - mean; q += d * d; }
#pragma unroll
  for (int o = 1; o < 16; o <<= 1) q += __shfl_xor(q, o);
  q = xsum(q);
  const float mb16 = rbf(mean), den = rbf(sqrtf(rbf(rbf(q * (1.0f / 1024.0f)) + 1e-5f)));
  const float inv = 1.0f / den;
#pragma unroll
  for (int i = 0; i < 16; ++i) v[i] = rbf(v[i] - mb16) * inv;
  const int kb = lane >> 2, c = lane & 3;
#pragma unroll
  for (int h = 0; h < 2; ++h) {
    char* t = tr + ((kb + 16 * h) * 4 + 2 * (c & 1)) * 144 + w * 16 + (c >> 1) * 8;
    *(u32x2*)t = (u32x2){pk2(v[8 * h], v[8 * h + 1]), pk2(v[8 * h + 2], v[8 * h + 3])};
    *(u32x2*)(t + 144) = (u32x2){pk2(v[8 * h + 4], v[8 * h + 5]), pk2(v[8 * h + 6], v[8 * h + 7])};
  }
  __syncthreads();
  const auto rs = __builtin_amdgcn_make_buffer_rsrc(ws + WS_XN + (size_t)mb * 32 * 16384 + mf * 1024 + (b & 1) * 128, 0, 32 * 16384, 0x00020000);
#pragma unroll
  for (int h = 0; h < 2; ++h) {
    const int seg = (threadIdx.x >> 3) + 64 * h, t8 = threadIdx.x & 7;
    const u32x4 o = *(const u32x4*)(tr + seg * 144 + t8 * 16);
    __builtin_amdgcn_raw_buffer_store_b128(o, rs, (seg >> 2) * 16384 + (seg & 3) * 256 + t8 * 16, 0, H_AUX);
  }
}

namespace {
constexpr int G1_SA = 17 * 1024, G1_SB = 20 * 1024, G1_SLOT = G1_SA + G1_SB, G1_LDS = 4 * G1_SLOT + 256;

__device__ __forceinline__ unsigned glu2(f32x2 g, f32x2 u) {
  g = g * -1.4426950408889634f;
  f32x2 e = {__builtin_amdgcn_exp2f(g[0]), __builtin_amdgcn_exp2f(g[1])};
  e = e + 1.0f;
  f32x2 r = {__builtin_amdgcn_rcpf(e[0]), __builtin_amdgcn_rcpf(e[1])};
  r = r * u;
  return pk2(r[0], r[1]);
}
__device__ __forceinline__ u32x2 glu4(f32x4 g, f32x4 u) {
  return (u32x2){glu2(__builtin_shufflevector(g, g, 0, 1), __builtin_shufflevector(u, u, 0, 1)),
                 glu2(__builtin_shufflevector(g, g, 2, 3), __builtin_shufflevector(u, u, 2, 3))};
}
__device__ __forceinline__ u32x4 glu8(f32x4 g0, f32x4 u0, f32x4 g1, f32x4 u1) {
  const u32x2 a = glu4(g0, u0), b = glu4(g1, u1);
  return (u32x4){a[0], a[1], b[0], b[1]};
}

}
extern "C" __global__ __launch_bounds__(512, 2) void psum_g1_kernel(const u16* __restrict__ wqkv, const u16* __restrict__ wg,
                                                       const u16* __restrict__ wu, const u16* __restrict__ wo,
                                                       const u16* __restrict__ wd, char* __restrict__ ws) {
  extern __shared__ __attribute__((aligned(16))) char smem[];
  LAS char* lds = (LAS char*)smem;
  ST_DECL LT_DECL ST(0);
  const int tid = threadIdx.x;
  int lane = tid & 63;
  const int w = __builtin_amdgcn_readfirstlane(tid >> 6), g = w >> 2, wi = w & 3;
  const int b = blockIdx.x, xcd = b & 7, idx = b >> 3;
  const int mb = (xcd & 1) * 4 + (idx & 3), kq = xcd >> 1, ntl = idx >> 2;
  const bool typeA = ntl < 4;
  const int j = ntl & 3;
  int fr = lane & 15, fq = lane >> 4;
  const int fA = 896 * kq + 64 * j;
  const int f0 = 896 * kq + 256 + 160 * j, odd = j & 1;
  const int fa = f0 + 32 * odd, fb = fa + 64, fc = odd ? f0 : f0 + 128;

  const char* c1;
  unsigned v1;
  int s1, p1, p2;
  const char* c2;
  int d1, d2;
  unsigned long long m2 = ~0ull;
  {
    const int rr = lane >> 3, pc = lane & 7, fi = (pc >> 1) ^ ((rr >> 1) & 3);
    if (g == 0) {
      int ld; const u16* q1;
      if (typeA) {
        if (wi < 3) { q1 = wqkv + wi * 1024 + 256 * kq + 64 * j; ld = 3072; } else { q1 = wg + fA; ld = 3584; }
        c2 = (const char*)(wu + fA + (size_t)(wi * 8 + rr) * 3584) + fi * 32 + (pc & 1) * 16;
      } else {
        q1 = ((wi & 2) ? wu : wg) + ((wi & 1) ? fb : fa); ld = 3584;
        c2 = (const char*)(((fi & 1) ? wu : wg) + fc + (fi >> 1) * 16 + (size_t)(wi * 8 + rr) * 3584) + (pc & 1) * 16;
      }
      c1 = (const char*)q1; v1 = rr * ld * 2 + fi * 32 + (pc & 1) * 16;
      s1 = 16 * ld - 1024; p1 = 64 * ld; p2 = 64 * 3584;
      d1 = G1_SA + wi * 4096; d2 = G1_SA + 16384 + wi * 1024;
    } else {
      c1 = ws + WS_XN + (size_t)mb * 32 * 16384 + wi * 4096; v1 = lane * 16;
      const int mh = mb > 0 ? mb - 1 : 0, i = lane & 15;
      c2 = ws + WS_XN + (size_t)mh * 32 * 16384 + (14 + (i >> 3)) * 1024 + (wi * 16 + 8 + (i & 7)) * 16;
      m2 = 0xffffull;
      s1 = 0; p1 = 16384; p2 = 16384;
      d1 = wi * 4096; d2 = 16384 + wi * 256;
    }
  }
  const unsigned lb = (unsigned)(size_t)lds;
  unsigned ns = lb;
  auto adv = [&]() { c1 += p1; c2 += p2; ns = (ns == lb + 3 * G1_SLOT) ? lb : ns + G1_SLOT; };
  auto stage = [&]() {
    dma_first(ns + d1, v1, c1); dma_next<1024>(v1, c1 + s1); dma_next<2048>(v1, c1 + 2 * s1); dma_next<3072>(v1, c1 + 3 * s1);
    dma1m(ns + d2, c2, m2);
    adv();
  };
  stage();
  BAR();
  stage();

  const int r0 = 4 * fq + ((lane & 15) >> 2);
  unsigned aoff = lb + wi * 4096 + lane * 16;
  unsigned hoffA = lb + 16384 + lane * 16;
  unsigned boff0 = lb + G1_SA + r0 * 128 + 32 * ((2 * g) ^ ((r0 >> 1) & 3)) + 8 * (lane & 3);
  unsigned boff1 = boff0 ^ 32;
  unsigned hoffB = (boff0 ^ (32 * (wi & 1))) + (1 + (wi >> 1)) * 4096;

  f32x4 acc[4][10];
  f32x4 hacc = {0.f, 0.f, 0.f, 0.f};
#pragma unroll
  for (int i = 0; i < 4; ++i)
#pragma unroll
    for (int n = 0; n < 10; ++n) { acc[i][n] = (f32x4){0.f, 0.f, 0.f, 0.f}; asm volatile("" : "+v"(acc[i][n])); }
  asm volatile("" : "+v"(aoff), "+v"(hoffA), "+v"(boff0), "+v"(boff1), "+v"(hoffB), "+v"(hacc));
  ST(1);
  WAIT_V(5);
  BAR();
  ST(2);
  if (g == 1) BAR();

  auto phase = [&](auto do_stage, int q) {
    constexpr bool DS = decltype(do_stage)::value;
    bf16x8 a[4], bfr[10], ha, hb;
    LT(0);
#define RB(c) bfr[2 * (c)] = cat(trd(lp(boff0) + (c) * 4096), trd(lp(boff0) + (c) * 4096 + 2048)); \
              bfr[2 * (c) + 1] = cat(trd(lp(boff1) + (c) * 4096), trd(lp(boff1) + (c) * 4096 + 2048));
    a[0] = *(const LAS bf16x8*)(lp(aoff)); a[1] = *(const LAS bf16x8*)(lp(aoff) + 1024);
    if (DS) dma_first(ns + d1, v1, c1);
    a[2] = *(const LAS bf16x8*)(lp(aoff) + 2048); a[3] = *(const LAS bf16x8*)(lp(aoff) + 3072);
    RB(0)
    if (DS) dma_next<1024>(v1, c1 + s1);
    RB(1)
    ha = *(const LAS bf16x8*)lp(hoffA);
    if (DS) dma_next<2048>(v1, c1 + 2 * s1);
    RB(2)
    hb = cat(trd(lp(hoffB)), trd(lp(hoffB) + 2048));
    if (DS) dma_next<3072>(v1, c1 + 3 * s1);
    RB(3)
    if (DS) dma1m(ns + d2, c2, m2);
    RB(4)
#undef RB
    SCHED();
    LT(1);
    const int dlt = ((q & 3) == 3) ? -3 * G1_SLOT : G1_SLOT;
    aoff += dlt; hoffA += dlt; boff0 += dlt; boff1 += dlt; hoffB += dlt;
    if (DS) {
      adv();
      if (q == 1) stage();
      LT(2);
      if (q == 0) WAIT_V(5); else WAIT_V(10);
    } else {
      LT(2);
      if (q == 29) {
        WAIT_V(0);
        int t = w * 64 + __builtin_amdgcn_mbcnt_hi(~0u, __builtin_amdgcn_mbcnt_lo(~0u, 0u));
        const int tc = t - 288;
        t = t < 288 ? t : t - 288;
        const int rr = (t >> 4) * 64 + (xcd & 1) * 32 + idx;
        const char* p = (const char*)((rr < 256 ? wo + (256 * kq + rr) * 1024 : wd + (896 * kq + rr - 256) * 1024) + (t & 15) * 64);
        touch(lb + 4 * G1_SLOT, p);
      }
    }
    WAIT_L0();
    LT(3);
    SCHED();
    BAR();
    SCHED();
    LT(4);
    PRIO_M();
    hacc = mfma(hb, ha, hacc);
#pragma unroll
    for (int n = 0; n < 10; ++n)
#pragma unroll
      for (int i = 0; i < 4; ++i) {
        if (n == 9 && i == 4 - EARLY) { SCHED(); PRIO_T(); if (q < 31 || g == 0) BAR(); SCHED(); }
        acc[i][n] = mfma(bfr[n], a[i], acc[i][n]);
      }
    PRIO_E();
    SCHED();
    LT(5);
    if (EARLY == 0) { if (q < 31 || g == 0) BAR(); }
    SCHED();
  };
#pragma clang loop unroll(disable)
  for (int q = 0; q < 29; ++q) phase(std::true_type{}, q);
#pragma clang loop unroll(disable)
  for (int q = 29; q < 32; ++q) phase(std::false_type{}, q);

  ST(3);
  lane = __builtin_amdgcn_mbcnt_hi(~0u, __builtin_amdgcn_mbcnt_lo(~0u, 0u));
  asm volatile("" : "+v"(lane));
  fr = lane & 15; fq = lane >> 4;
  const auto hrs = __builtin_amdgcn_make_buffer_rsrc(ws + WS_H + (size_t)(mb * 4 + kq) * 36 * 16384, 0, 36 * 16384, 0x00020000);
  const unsigned hv = wi * 4096 + lane * 16;
  if (!typeA) {
    const int kba = 8 + ((fa - 896 * kq) >> 5) + g, kbc = 8 + ((fc - 896 * kq) >> 5);
#pragma unroll
    for (int i = 0; i < 4; ++i) {
      __builtin_amdgcn_raw_buffer_store_b128(glu8(acc[i][0], acc[i][4], acc[i][1], acc[i][5]), hrs, hv + kba * 16384 + i * 1024, 0, H_AUX);
      __builtin_amdgcn_raw_buffer_store_b128(glu8(acc[i][2], acc[i][6], acc[i][3], acc[i][7]), hrs, hv + kba * 16384 + 32768 + i * 1024, 0, H_AUX);
      __builtin_amdgcn_raw_buffer_store_b64(glu4(acc[i][8], acc[i][9]), hrs, hv + kbc * 16384 + g * 8 + i * 1024, 0, H_AUX);
    }
  } else {
    LAS char* kim = lds + g * 36864;
    LAS char* vim = kim + 17 * 1024;
    bf16x8 kf[4], qf[4];
#pragma unroll
    for (int i = 0; i < 4; ++i) {
      kf[i] = pack8(acc[i][2], acc[i][3]);
      LAS char* vr = vim + ((wi * 4 + i) * 16 + fr) * 64 + fq * 8;
      *(LAS u32x2*)(vr) = (u32x2){pk2(acc[i][4][0], acc[i][4][1]), pk2(acc[i][4][2], acc[i][4][3])};
      *(LAS u32x2*)(vr + 32) = (u32x2){pk2(acc[i][5][0], acc[i][5][1]), pk2(acc[i][5][2], acc[i][5][3])};
    }
    *(LAS bf16x8*)(kim + (wi * 4 + 2) * 1024 + lane * 16) = kf[2];
    *(LAS bf16x8*)(kim + (wi * 4 + 3) * 1024 + lane * 16) = kf[3];
    {
      LAS char* hp = (wi < 2) ? (kim + 16 * 1024 + lane * 16 + wi * 8) : (vim + (256 + fr) * 64 + (wi - 2) * 32 + fq * 8);
      *(LAS u32x2*)hp = (u32x2){pk2(hacc[0], hacc[1]), pk2(hacc[2], hacc[3])};
    }
    auto glu_part = [&]() {
#pragma unroll
      for (int i = 0; i < 4; ++i)
        __builtin_amdgcn_raw_buffer_store_b128(glu8(acc[i][6], acc[i][8], acc[i][7], acc[i][9]), hrs, hv + (8 + 2 * j + g) * 16384 + i * 1024, 0, H_AUX);
    };
    WAIT_L0();
    if (g == 0) glu_part();
    SCHED();
    BAR();
    asm volatile("" ::: "memory");
    const float NEG = -3.0e38f;
    f32x4 bc, bp, bp0, bp1;
    const bool halo = wi == 0;
#pragma unroll
    for (int e = 0; e < 4; ++e) {
      const int c = 4 * fq + e;
      bc[e] = (c <= fr && c + 7 >= fr) ? 0.f : NEG;
      bp[e] = (c >= fr + 9) ? 0.f : NEG;
      bp0[e] = halo ? ((mb > 0 && c < 8 && c >= fr + 1) ? 0.f : NEG) : bp[e];
      bp1[e] = (halo && mb == 0) ? NEG : bp[e];
    }
    const int pf0 = halo ? 16 : wi * 4 - 2, pf1 = halo ? 16 : wi * 4 - 1;
    const bf16x8 kp0 = *(const LAS bf16x8*)(kim + pf0 * 1024 + lane * 16);
    const bf16x8 kp1 = *(const LAS bf16x8*)(kim + pf1 * 1024 + lane * 16);
    const LAS char* vb = vim + (4 * fq + ((lane & 15) >> 2)) * 64 + (lane & 3) * 8;
    bf16x4 vt[6][2];
#pragma unroll
    for (int d = 0; d < 2; ++d) {
      vt[0][d] = trd(vb + pf0 * 1024 + d * 32); vt[1][d] = trd(vb + pf1 * 1024 + d * 32);
#pragma unroll
      for (int i = 0; i < 4; ++i) vt[2 + i][d] = trd(vb + (wi * 4 + i) * 1024 + d * 32);
    }
    f32x4 sp[4], sc[4];
#pragma unroll
    for (int i = 0; i < 4; ++i) { qf[i] = pack8(acc[i][0], acc[i][1]); sc[i] = mfma(kf[i], qf[i], bc); }
    sp[2] = mfma(kf[0], qf[2], bp); sp[3] = mfma(kf[1], qf[3], bp);
    sp[0] = mfma(kp0, qf[0], bp0); sp[1] = mfma(kp1, qf[1], bp1);
    const float cs = 0.17677669529663687f * 1.4426950408889634f;
    const f32x4 z4 = {0.f, 0.f, 0.f, 0.f};
    float m[4], sum[4];
#pragma unroll
    for (int i = 0; i < 4; ++i)
      m[i] = fmaxf(fmaxf(fmaxf(sp[i][0], sp[i][1]), fmaxf(sp[i][2], sp[i][3])), fmaxf(fmaxf(sc[i][0], sc[i][1]), fmaxf(sc[i][2], sc[i][3])));
#pragma unroll
    for (int i = 0; i < 4; ++i) m[i] = -cs * xmax(m[i]);
#pragma unroll
    for (int i = 0; i < 4; ++i) {
#pragma unroll
      for (int e = 0; e < 4; ++e) {
        sp[i][e] = __builtin_amdgcn_exp2f(__builtin_fmaf(sp[i][e], cs, m[i]));
        sc[i][e] = __builtin_amdgcn_exp2f(__builtin_fmaf(sc[i][e], cs, m[i]));
      }
      const f32x4 t = sp[i] + sc[i];
      sum[i] = (t[0] + t[1]) + (t[2] + t[3]);
    }
#pragma unroll
    for (int i = 0; i < 4; ++i) sum[i] = __builtin_amdgcn_rcpf(xsum(sum[i]));
#pragma unroll
    for (int i = 0; i < 4; ++i) {
      const bf16x8 pfr = pack8(sp[i] * sum[i], sc[i] * sum[i]);
      const f32x4 o0 = mfma(cat(vt[i][0], vt[2 + i][0]), pfr, z4);
      const f32x4 o1 = mfma(cat(vt[i][1], vt[2 + i][1]), pfr, z4);
      __builtin_amdgcn_raw_buffer_store_b128(__builtin_bit_cast(u32x4, pack8(o0, o1)), hrs, hv + (2 * j + g) * 16384 + i * 1024, 0, H_AUX);
    }
    if (g == 1) glu_part();
  }
  ST(4);
  ST(5);
  ST_OUT(0, 6);
}

constexpr int G2_SA = 32768, G2_SB = 16384, G2_SLOT = G2_SA + G2_SB, G2_LDS = 3 * G2_SLOT;

extern "C" __global__ __launch_bounds__(512, 2) void psum_g2_kernel(const u16* __restrict__ x, const u16* __restrict__ wo,
                                                       const u16* __restrict__ wd, char* __restrict__ ws,
                                                       u16* __restrict__ out) {
  extern __shared__ __attribute__((aligned(16))) char smem[];
  LAS char* lds = (LAS char*)smem;
  ST_DECL LT_DECL ST(0);
  const int tid = threadIdx.x, lane = tid & 63;
  const int w = __builtin_amdgcn_readfirstlane(tid >> 6), g = w >> 2, wi = w & 3;
  const int b = blockIdx.x, xcd = b & 7, idx = b >> 3;
  const int mb = (xcd & 1) * 4 + (idx & 3), kq = xcd >> 1, nt = idx >> 2;
  const int fr = lane & 15, fq = lane >> 4;

  const char* ca = ws + WS_H + (size_t)(mb * 4 + kq) * 36 * 16384 + w * 4096;
  const unsigned va = lane * 16;
  const int e0 = (0 ^ kq) * 1024, e1 = ((1 ^ kq) - 1) * 1024, e2 = ((2 ^ kq) - 2) * 1024, e3 = ((3 ^ kq) - 3) * 1024;
  const int rr = lane >> 3, pc = lane & 7;
  const unsigned vb = rr * 2048 + (((pc >> 1) ^ ((rr >> 1) & 3)) * 32) + (pc & 1) * 16;
  const char* cb = (const char*)(wo + (size_t)(256 * kq + 8 * w) * 1024 + nt * 128);
  const char* cb2 = (const char*)(wd + (size_t)(896 * kq + 8 * w) * 1024 + nt * 128);
  int st = 0;
  const unsigned lb = (unsigned)(size_t)lds;
  unsigned ns = lb;
  auto adv = [&]() { ca += 32768; ++st; cb = (st == 4) ? cb2 : cb + 64 * 2048; ns = (ns == lb + 2 * G2_SLOT) ? lb : ns + G2_SLOT; };
  auto stage = [&]() {
    dma_first(ns + w * 4096, va, ca + e0); dma_next<1024>(va, ca + e1); dma_next<2048>(va, ca + e2); dma_next<3072>(va, ca + e3);
    dma_first(ns + G2_SA + w * 2048, vb, cb); dma_next<1024>(vb, cb - 896);
    adv();
  };
  stage();
  BAR();
  stage();
  const int rl = 4 * (fq & 1) + ((lane & 15) >> 2);
  unsigned aoff = lb + wi * 4096 + lane * 16;
  unsigned boff = lb + G2_SA + (fq >> 1) * 2048 + g * 1024 + rl * 128 + 32 * ((rl >> 1) & 3) + 8 * (lane & 3);

  f32x4 acc[4][4];
#pragma unroll
  for (int i = 0; i < 4; ++i)
#pragma unroll
    for (int n = 0; n < 4; ++n) { acc[i][n] = (f32x4){0.f, 0.f, 0.f, 0.f}; asm volatile("" : "+v"(acc[i][n])); }
  ST(1);
  WAIT_V(6);
  BAR();
  ST(2);
  if (g == 1) BAR();

  int sl = 0;
  auto phase = [&](auto do_stage, int q) {
    constexpr bool DS = decltype(do_stage)::value;
    bf16x8 a[2][4], bfr[2][4];
    LT(0);
#define RA(kk, i) a[kk][i] = *(const LAS bf16x8*)(lp(aoff) + (kk) * 16384 + (i) * 1024);
#define RB(kk, n) bfr[kk][n] = cat(trd(lp(boff ^ ((n) * 32)) + (kk) * 8192), trd(lp(boff ^ ((n) * 32)) + (kk) * 8192 + 4096));
    RA(0, 0) RA(0, 1)
    if (DS) dma_first(ns + w * 4096, va, ca + e0);
    RA(0, 2) RA(0, 3) RB(0, 0)
    if (DS) dma_next<1024>(va, ca + e1);
    RB(0, 1) RB(0, 2) RB(0, 3)
    if (DS) dma_next<2048>(va, ca + e2);
    RA(1, 0) RA(1, 1) RA(1, 2)
    if (DS) dma_next<3072>(va, ca + e3);
    RA(1, 3) RB(1, 0) RB(1, 1)
    if (DS) dma_first(ns + G2_SA + w * 2048, vb, cb);
    RB(1, 2) RB(1, 3)
    if (DS) dma_next<1024>(vb, cb - 896);
#undef RA
#undef RB
    SCHED();
    LT(1);
    sl = (sl == 2) ? 0 : sl + 1;
    const int dlt = (sl == 0) ? -2 * G2_SLOT : G2_SLOT;
    aoff += dlt; boff += dlt;
    if (DS) adv();
    LT(2);
    asm volatile("" : "+v"(aoff), "+v"(boff), "+s"(ca), "+s"(cb), "+s"(ns), "+s"(sl));
    if (DS) WAIT_V_IF0(6, g ^ 1); else WAIT_V_IF0(0, g ^ 1);
    WAIT_L0();
    LT(3);
    SCHED();
    BAR();
    SCHED();
    LT(4);
    PRIO_M();
#pragma unroll
    for (int kk = 0; kk < 2; ++kk)
#pragma unroll
      for (int n = 0; n < 4; ++n)
#pragma unroll
        for (int i = 0; i < 4; ++i) {
          if (kk == 1 && n == 3 && i == 4 - EARLY) { SCHED(); PRIO_T(); if (DS) WAIT_V_IF0(6, g); else WAIT_V_IF0(0, g); if (q < 17 || g == 0) BAR(); SCHED(); }
          acc[i][n] = mfma(bfr[kk][n], a[kk][i], acc[i][n]);
        }
    PRIO_E();
    SCHED();
    LT(5);
    if (EARLY == 0) { if (DS) WAIT_V_IF0(6, g); else WAIT_V_IF0(0, g); if (q < 17 || g == 0) BAR(); }
    SCHED();
  };
#pragma clang loop unroll(disable)
  for (int q = 0; q < 16; ++q) phase(std::true_type{}, q);
#pragma clang loop unroll(disable)
  for (int q = 16; q < 18; ++q) phase(std::false_type{}, q);

  ST(3);
  const int tile = mb * 8 + nt;
  const auto rs = __builtin_amdgcn_make_buffer_rsrc(ws + WS_X, 0, (int)X_BYTES, 0x00020000);
#pragma unroll
  for (int i = 1; i < 4; ++i) {
    const unsigned o = (unsigned)((((tile * 4 + (kq ^ i)) * 3 + (i - 1)) * 8 + w) * 2048) + lane * 16;
    __builtin_amdgcn_raw_buffer_store_b128(__builtin_bit_cast(u32x4, pack8(acc[i][0], acc[i][1])), rs, o, 0, 16);
    __builtin_amdgcn_raw_buffer_store_b128(__builtin_bit_cast(u32x4, pack8(acc[i][2], acc[i][3])), rs, o + 1024, 0, 16);
  }
  const int mf = wi * 4 + kq;
  const size_t orow = (size_t)(mb * 256 + 32 * (mf >> 1) + 2 * fr + (mf & 1)) * 1024 + nt * 128 + g * 64 + 4 * fq;
  u32x2 xr[4];
#pragma unroll
  for (int n = 0; n < 4; ++n) xr[n] = *(const u32x2*)(x + orow + 16 * n);
  WAIT_V(0);
  BAR();
  ST(4);
  GAS unsigned* cnt = (GAS unsigned*)(ws + WS_CNT + tile * 256);
  if (w == 0) {
    if (lane == 0) __hip_atomic_fetch_add(cnt, 1u, __ATOMIC_RELAXED, __HIP_MEMORY_SCOPE_AGENT);
    unsigned spins = 0;
    while ((unsigned)__builtin_amdgcn_readfirstlane(__hip_atomic_load(cnt, __ATOMIC_RELAXED, __HIP_MEMORY_SCOPE_AGENT)) < 4u) {
      __builtin_amdgcn_s_sleep(1);
      if (++spins > (1u << 22)) break;
    }
  }
  asm volatile("" ::: "memory");
  BAR();
  asm volatile("" ::: "memory");
  ST(5);
  u32x4 p[3][2];
#pragma unroll
  for (int i = 1; i < 4; ++i) {
    const unsigned o = (unsigned)((((tile * 4 + kq) * 3 + (i - 1)) * 8 + w) * 2048) + lane * 16;
    p[i - 1][0] = __builtin_amdgcn_raw_buffer_load_b128(rs, o, 0, 16);
    p[i - 1][1] = __builtin_amdgcn_raw_buffer_load_b128(rs, o + 1024, 0, 16);
  }
#pragma unroll
  for (int n = 0; n < 4; ++n) {
    f32x4 s = acc[0][n];
#pragma unroll
    for (int i = 0; i < 3; ++i) {
      const unsigned lo = p[i][n >> 1][(n & 1) * 2], hi = p[i][n >> 1][(n & 1) * 2 + 1];
      s[0] += __uint_as_float(lo << 16); s[1] += __uint_as_float(lo & 0xffff0000u);
      s[2] += __uint_as_float(hi << 16); s[3] += __uint_as_float(hi & 0xffff0000u);
    }
    s[0] += __uint_as_float(xr[n][0] << 16); s[1] += __uint_as_float(xr[n][0] & 0xffff0000u);
    s[2] += __uint_as_float(xr[n][1] << 16); s[3] += __uint_as_float(xr[n][1] & 0xffff0000u);
    *(u32x2*)(out + orow + 16 * n) = (u32x2){pk2(s[0], s[1]), pk2(s[2], s[3])};
  }
  ST(6);
  ST(7);
  ST_OUT(1, 8);
}

extern "C" void kernel_launch(void* const* d_in, const int* in_sizes, int n_in, void* d_out, int out_size, void* d_ws,
                              size_t ws_size, hipStream_t stream) {
  const u16* x = (const u16*)d_in[0];
  const u16* wqkv = (const u16*)d_in[1];
  const u16* wo = (const u16*)d_in[2];
  const u16* wg = (const u16*)d_in[4];
  const u16* wu = (const u16*)d_in[5];
  const u16* wd = (const u16*)d_in[6];
  char* ws = (char*)d_ws;
  (void)hipFuncSetAttribute((const void*)psum_g1_kernel, hipFuncAttributeMaxDynamicSharedMemorySize, G1_LDS);
  (void)hipFuncSetAttribute((const void*)psum_g2_kernel, hipFuncAttributeMaxDynamicSharedMemorySize, G2_LDS);
  psum_ln_kernel<<<256, 512, 0, stream>>>(x, wqkv, wg, wu, ws);
  psum_g1_kernel<<<256, 512, G1_LDS, stream>>>(wqkv, wg, wu, wo, wd, ws);
  psum_g2_kernel<<<256, 512, G2_LDS, stream>>>(x, wo, wd, ws, (u16*)d_out);
}
```

```cpp
#define EARLY2 1
#define PF_LN_ROWS 0
#include <hip/hip_runtime.h>
#include <hip/hip_bf16.h>
#include <type_traits>
#define EARLY 2
#define PF_W2 1
#define PF_LOOP 0
#define PF_PER 2
#define PRIO_L 1
#define PRIO_M() __builtin_amdgcn_s_setprio(0)
#define PRIO_T() __builtin_amdgcn_s_setprio(1)
#define PRIO_E()
#define PF_CODE 0
#define PRECLEAN 0
#define OUT_AUX 16
#define G2ORD 0
#define H_AUX 16

namespace {

typedef unsigned short u16;
typedef short bf16x8 __attribute__((ext_vector_type(8)));
typedef short bf16x4 __attribute__((ext_vector_type(4)));
typedef float f32x4 __attribute__((ext_vector_type(4)));
typedef float f32x2 __attribute__((ext_vector_type(2)));
typedef unsigned u32x4 __attribute__((ext_vector_type(4)));
typedef unsigned u32x2 __attribute__((ext_vector_type(2)));
typedef __bf16 bf2 __attribute__((ext_vector_type(2)));
#define LAS __attribute__((address_space(3)))
#define GAS __attribute__((address_space(1)))

#define WAIT_V(n) asm volatile("s_waitcnt vmcnt(" #n ")" ::: "memory")
#define WAIT_V_IF0(n, c) asm volatile("s_cmp_lg_u32 %0, 0\n\ts_cbranch_scc1 1f\n\ts_waitcnt vmcnt(" #n ")\n1:" :: "s"(c) : "memory", "scc")
#define WAIT_L0() asm volatile("s_waitcnt lgkmcnt(0)" ::: "memory")
#define BAR() __builtin_amdgcn_s_barrier()
#define SCHED() __builtin_amdgcn_sched_barrier(0)

__device__ __forceinline__ float b2f(u16 v) { return __uint_as_float(((unsigned)v) << 16); }
__device__ __forceinline__ unsigned pk2(float lo, float hi) {
  bf2 r = {(__bf16)lo, (__bf16)hi};
  return __builtin_bit_cast(unsigned, r);
}
__device__ __forceinline__ float rbf(float f) { return (float)(__bf16)f; }
__device__ __forceinline__ bf16x8 pack8(f32x4 a, f32x4 b) {
  u32x4 r = {pk2(a[0], a[1]), pk2(a[2], a[3]), pk2(b[0], b[1]), pk2(b[2], b[3])};
  return __builtin_bit_cast(bf16x8, r);
}
__device__ __forceinline__ bf16x4 trd(LAS const char* p) {
  return __builtin_amdgcn_ds_read_tr16_b64_v4i16((LAS bf16x4*)p);
}
__device__ __forceinline__ bf16x8 cat(bf16x4 a, bf16x4 b) { return __builtin_shufflevector(a, b, 0, 1, 2, 3, 4, 5, 6, 7); }
__device__ __forceinline__ f32x4 mfma(bf16x8 a, bf16x8 b, f32x4 c) {
  return __builtin_amdgcn_mfma_f32_16x16x32_bf16(a, b, c, 0, 0, 0);
}
__device__ __forceinline__ void dma_first(unsigned lds_dst, unsigned voff, const char* b0) {
  asm volatile("s_mov_b32 m0, %0\n\ts_nop 0\n\tglobal_load_lds_dwordx4 %1, %2" :: "s"(lds_dst), "v"(voff), "s"(b0) : "memory");
}
template <int OFF>
__device__ __forceinline__ void dma_next(unsigned voff, const char* b) {
  asm volatile("global_load_lds_dwordx4 %0, %1 offset:%2" :: "v"(voff), "s"(b), "i"(OFF) : "memory");
}
__device__ __forceinline__ void dma1m(unsigned lds_dst, const char* p, unsigned long long mask) {
  asm volatile("s_mov_b32 m0, %0\n\ts_mov_b64 exec, %2\n\t"
               "global_load_lds_dwordx4 %1, off\n\t"
               "s_mov_b64 exec, -1"
               :: "s"(lds_dst), "v"(p), "s"(mask) : "memory");
}
__device__ __forceinline__ void touch(unsigned lds_dst, const void* p) {
  asm volatile("s_mov_b32 m0, %0\n\ts_nop 0\n\tglobal_load_lds_dword %1, off" :: "s"(lds_dst), "v"(p) : "memory");
}
#define CODE_ADDR(dst, sym) do { unsigned lo_, hi_; asm volatile("s_getpc_b64 s[90:91]\n\ts_add_u32 %0, s90, " #sym "@rel32@lo+4\n\ts_addc_u32 %1, s91, " #sym "@rel32@hi+12" \
    : "=s"(lo_), "=s"(hi_) :: "s90", "s91", "scc"); dst = (const char*)(((unsigned long long)hi_ << 32) | lo_); } while (0)
constexpr int LN_LINES = 16, G1_LINES = 80, G2_LINES = 30;
__device__ __forceinline__ LAS char* lp(unsigned a) { return (LAS char*)(size_t)a; }
__device__ __forceinline__ float fmx(float a, float b) { return __builtin_elementwise_maximum(a, b); }
__device__ __forceinline__ float xmax(float v) {
  auto a = __builtin_amdgcn_permlane16_swap(__float_as_uint(v), __float_as_uint(v), false, false);
  v = fmaxf(__uint_as_float(a[0]), __uint_as_float(a[1]));
  auto b = __builtin_amdgcn_permlane32_swap(__float_as_uint(v), __float_as_uint(v), false, false);
  return fmaxf(__uint_as_float(b[0]), __uint_as_float(b[1]));
}
__device__ __forceinline__ float xsum(float v) {
  auto a = __builtin_amdgcn_permlane16_swap(__float_as_uint(v), __float_as_uint(v), false, false);
  v = __uint_as_float(a[0]) + __uint_as_float(a[1]);
  auto b = __builtin_amdgcn_permlane32_swap(__float_as_uint(v), __float_as_uint(v), false, false);
  return __uint_as_float(b[0]) + __uint_as_float(b[1]);
}

template <int CTRL>
__device__ __forceinline__ float dpp_add(float v) {
  return v + __uint_as_float(__builtin_amdgcn_update_dpp(0u, __float_as_uint(v), CTRL, 0xf, 0xf, true));
}
__device__ __forceinline__ float hsum(float v) {
  v = dpp_add<0xB1>(v);
  v = dpp_add<0x4E>(v);
  v = dpp_add<0x128>(v);
  return xsum(v);
}

#define ST_DECL
#define LT_DECL
#define LT(k)
#define ET(k)
#define ST(k)
#define ST_OUT(kid, n)
#define WS_BASE_MB 0
constexpr size_t WS_XN = (size_t)WS_BASE_MB << 20;
constexpr size_t WS_H = WS_XN + (size_t)4 * 1024 * 1024;
constexpr size_t H_BYTES = (size_t)8 * 4 * 36 * 16 * 1024;
constexpr size_t WS_X = WS_H + H_BYTES;
constexpr size_t X_BYTES = (size_t)64 * 4 * 3 * 8 * 2048;
constexpr size_t WS_CNT = WS_X + X_BYTES;
constexpr size_t WS_ST = WS_CNT + 131072;

}
extern "C" __global__ __launch_bounds__(320) void psum_ln_kernel(const u16* __restrict__ x, const u16* __restrict__ wqkv, const u16* __restrict__ wg,
                                                                 const u16* __restrict__ wu, char* __restrict__ ws) {
  const int lane = threadIdx.x & 63, b = blockIdx.x;
  if (threadIdx.x >= 256) {
    return;
  }
  const int hf = (b & 1) * 128 + (b >> 1);
  const int mb = hf >> 5, mf = (hf >> 1) & 15, r = (hf & 1) * 8 + (threadIdx.x >> 6) * 2 + ((lane >> 2) & 1);
  const int row = mb * 256 + 32 * (mf >> 1) + 2 * r + (mf & 1), kl = lane >> 3, ch = lane & 3;
  const u16* xr = x + row * 1024 + kl * 32 + ch * 8;
  u32x4 raw[4];
#pragma unroll
  for (int i = 0; i < 4; ++i) raw[i] = *(const u32x4*)(xr + 256 * i);
  float v[32];
#pragma unroll
  for (int i = 0; i < 16; ++i) { v[2 * i] = __uint_as_float(raw[i >> 2][i & 3] << 16); v[2 * i + 1] = __uint_as_float(raw[i >> 2][i & 3] & 0xffff0000u); }
  float s = 0.f;
#pragma unroll
  for (int i = 0; i < 32; ++i) s += v[i];
  const float mean = hsum(s) * (1.0f / 1024.0f);
  float q = 0.f;
#pragma unroll
  for (int i = 0; i < 32; ++i) { const float d = v[i] - mean; q += d * d; }
  q = hsum(q);
  const float mb16 = rbf(mean), den = rbf(sqrtf(rbf(rbf(q * (1.0f / 1024.0f)) + 1e-5f)));
  const float inv = 1.0f / den;
  const auto rs = __builtin_amdgcn_make_buffer_rsrc(ws + WS_XN, 0, 4 << 20, 0x00020000);
  const unsigned o = ((mb * 32 + kl) * 16 + mf) * 1024 + r * 64 + ((ch ^ (2 * (r >> 3))) * 16);
#pragma unroll
  for (int i = 0; i < 4; ++i) {
    u32x4 t;
#pragma unroll
    for (int e = 0; e < 4; ++e) t[e] = pk2(rbf(v[8 * i + 2 * e] - mb16) * inv, rbf(v[8 * i + 2 * e + 1] - mb16) * inv);
    __builtin_amdgcn_raw_buffer_store_b128(t, rs, o + i * 8 * 16384, 0, H_AUX);
  }
}
namespace {
constexpr int G1_SA = 17 * 1024, G1_SB = 20 * 1024, G1_SLOT = G1_SA + G1_SB;
constexpr int T_SCR = 4 * G1_SLOT;
constexpr int T_BIAS = T_SCR + 256;
constexpr int G1_LDS = T_BIAS + 4096;

template <bool MAX>
__device__ __forceinline__ float tred(float a, float b, float c, float d) {
  const auto r1 = __builtin_amdgcn_permlane16_swap(__float_as_uint(a), __float_as_uint(b), false, false);
  const auto r2 = __builtin_amdgcn_permlane16_swap(__float_as_uint(c), __float_as_uint(d), false, false);
  const float x0 = __uint_as_float(r1[0]), x1 = __uint_as_float(r1[1]), y0 = __uint_as_float(r2[0]), y1 = __uint_as_float(r2[1]);
  const float t = MAX ? fmx(x0, x1) : x0 + x1, u = MAX ? fmx(y0, y1) : y0 + y1;
  const auto r3 = __builtin_amdgcn_permlane32_swap(__float_as_uint(t), __float_as_uint(u), false, false);
  const float z0 = __uint_as_float(r3[0]), z1 = __uint_as_float(r3[1]);
  return MAX ? fmx(z0, z1) : z0 + z1;
}
__device__ __forceinline__ u32x2 glu4(f32x4 g, f32x4 u) {
  f32x4 e = g * -1.4426950408889634f;
#pragma unroll
  for (int i = 0; i < 4; ++i) e[i] = __builtin_amdgcn_exp2f(e[i]);
  e = e + 1.0f;
#pragma unroll
  for (int i = 0; i < 4; ++i) e[i] = __builtin_amdgcn_rcpf(e[i]);
  e = e * u;
  return (u32x2){pk2(e[0], e[1]), pk2(e[2], e[3])};
}
__device__ __forceinline__ u32x4 cat4(u32x2 a, u32x2 b) { return (u32x4){a[0], a[1], b[0], b[1]}; }

}
extern "C" __global__ __launch_bounds__(512, 2) void psum_g1_kernel(const u16* __restrict__ wqkv,
                                                                    const u16* __restrict__ wg, const u16* __restrict__ wu,
                                                                    const u16* __restrict__ wo, const u16* __restrict__ wd,
                                                                    char* __restrict__ ws) {
  extern __shared__ __attribute__((aligned(16))) char smem[];
  LAS char* lds = (LAS char*)smem;
  ST_DECL LT_DECL ST(0);
  const int tid = threadIdx.x;
  int lane = tid & 63;
  const int w = __builtin_amdgcn_readfirstlane(tid >> 6), g = w >> 2, wi = w & 3;
  const int b = blockIdx.x, xcd = b & 7, idx = b >> 3;
  const int mb = (xcd & 1) * 4 + (idx & 3), kq = xcd >> 1, ntl = idx >> 2;
  const bool typeA = ntl < 4;
  const int j = ntl & 3;
  int fr = lane & 15, fq = lane >> 4;
  const int fA = 896 * kq + 64 * j;
  const int f0 = 896 * kq + 256 + 160 * j, odd = j & 1;
  const int fa = f0 + 32 * odd, fb = fa + 64, fc = odd ? f0 : f0 + 128;

  const char* c1;
  unsigned v1;
  int s1, s2, s3, p1, p2;
  const char* c2;
  int d1, d2;
  unsigned long long m2 = ~0ull;
  if (g == 0) {
    const int rr = lane >> 3, pc = lane & 7, fi = (pc >> 1) ^ ((rr >> 1) & 3), kr = (rr & 3) + 8 * (rr >> 2);
    const int kr2 = kr + 16 * (wi >> 1) + 4 * (wi & 1);
    int ld; const u16* q1;
    if (typeA) {
      if (wi < 3) { q1 = wqkv + wi * 1024 + 256 * kq + 64 * j; ld = 3072; } else { q1 = wg + fA; ld = 3584; }
      c2 = (const char*)(wu + fA + kr2 * 3584) + fi * 32 + (pc & 1) * 16;
    } else {
      q1 = ((wi & 2) ? wu : wg) + ((wi & 1) ? fb : fa); ld = 3584;
      c2 = (const char*)(((fi & 1) ? wu : wg) + fc + (fi >> 1) * 16 + kr2 * 3584) + (pc & 1) * 16;
    }
    c1 = (const char*)q1; v1 = kr * ld * 2 + fi * 32 + (pc & 1) * 16;
    s1 = 8 * ld - 1024; s2 = 32 * ld - 2048; s3 = 40 * ld - 3072; p1 = 64 * ld; p2 = 64 * 3584;
    d1 = G1_SA + wi * 4096; d2 = G1_SA + 16384 + wi * 1024;
  } else {
    c1 = ws + WS_XN + (size_t)mb * 32 * 16384 + wi * 4096; v1 = lane * 16;
    const int hr = wi * 4 + ((lane >> 2) & 3);
    c2 = ws + WS_XN + (size_t)(mb > 0 ? mb - 1 : 0) * 32 * 16384 + (14 + (hr >> 3)) * 1024 + (8 + (hr & 7)) * 64 + (((lane & 3) ^ (2 * (hr >> 3)) ^ 2) * 16);
    m2 = 0xffffull;
    s1 = 0; s2 = 0; s3 = 0; p1 = 16384; p2 = 16384;
    d1 = wi * 4096; d2 = 16384 + wi * 256;
  }
  const unsigned lb = (unsigned)(size_t)lds;
  unsigned ns = lb;
  auto adv = [&]() { c1 += p1; c2 += p2; ns = (ns == lb + 3 * G1_SLOT) ? lb : ns + G1_SLOT; };
  auto stage = [&]() {
    dma_first(ns + d1, v1, c1); dma_next<1024>(v1, c1 + s1); dma_next<2048>(v1, c1 + s2); dma_next<3072>(v1, c1 + s3);
    dma1m(ns + d2, c2, m2);
    adv();
  };
  stage();
  BAR();
  stage();
  if (tid < 8) *(unsigned*)(ws + WS_CNT + (((mb * 8 + ntl) * 4 + kq) * 8 + tid) * 64) = 0u;
  if (g == 0) {
    const int lo = wi == 0 ? fr - 7 : (wi == 2 ? fr + 1 : fr + 9);
    const int hi = wi == 0 ? fr + 1 : ((wi >= 2 && mb == 0) ? 0 : (wi == 2 ? 8 : 16));
    f32x4 bv;
#pragma unroll
    for (int e = 0; e < 4; ++e) bv[e] = (4 * fq + e >= lo && 4 * fq + e < hi) ? 0.f : -3.0e38f;
    *(LAS f32x4*)(lds + T_BIAS + wi * 1024 + lane * 16) = bv;
  }

  const int rl = 4 * (fq & 1) + (fr >> 2);
  unsigned hoffA = lb + 16384 + fr * 64 + ((fq ^ (2 * (fr >> 3))) * 16);
  unsigned aoff = hoffA - 16384 + wi * 4096;
  unsigned boff0 = lb + G1_SA + (fq >> 1) * 2048 + rl * 128 + 32 * ((2 * g) ^ ((rl >> 1) & 3)) + 8 * (lane & 3);
  unsigned boff1 = boff0 ^ 32;
  unsigned hoffB = (boff0 ^ (32 * (wi & 1))) + (1 + (wi >> 1)) * 4096;

  f32x4 acc[4][10];
  f32x4 hacc = {0.f, 0.f, 0.f, 0.f};
#pragma unroll
  for (int i = 0; i < 4; ++i)
#pragma unroll
    for (int n = 0; n < 10; ++n) { acc[i][n] = (f32x4){0.f, 0.f, 0.f, 0.f}; asm volatile("" : "+v"(acc[i][n])); }
  asm volatile("" : "+v"(aoff), "+v"(hoffA), "+v"(boff0), "+v"(boff1), "+v"(hoffB), "+v"(hacc));
  ST(1);
  WAIT_V(5);
  BAR();
  ST(2);
  if (g == 1) BAR();

  auto phase = [&](auto do_stage, int q) {
    constexpr bool DS = decltype(do_stage)::value;
    bf16x8 a[4], bfr[10], ha, hb;
    LT(0);
#define RB(c) bfr[2 * (c)] = cat(trd(lp(boff0) + (c) * 4096), trd(lp(boff0) + (c) * 4096 + 1024)); \
              bfr[2 * (c) + 1] = cat(trd(lp(boff1) + (c) * 4096), trd(lp(boff1) + (c) * 4096 + 1024));
    a[0] = *(const LAS bf16x8*)(lp(aoff)); a[1] = *(const LAS bf16x8*)(lp(aoff) + 1024);
    if (DS) dma_first(ns + d1, v1, c1);
    a[2] = *(const LAS bf16x8*)(lp(aoff) + 2048); a[3] = *(const LAS bf16x8*)(lp(aoff) + 3072);
    ha = *(const LAS bf16x8*)lp(hoffA);
    RB(0)
    if (DS) dma_next<1024>(v1, c1 + s1);
    RB(1)
    if (DS) dma_next<2048>(v1, c1 + s2);
    RB(2)
    if (DS) dma_next<3072>(v1, c1 + s3);
    RB(3)
    if (DS) dma1m(ns + d2, c2, m2);
    RB(4)
    hb = cat(trd(lp(hoffB)), trd(lp(hoffB) + 1024));
#undef RB
    SCHED();
    LT(1);
    const int dlt = ((q & 3) == 3) ? -3 * G1_SLOT : G1_SLOT;
    aoff += dlt; hoffA += dlt; boff0 += dlt; boff1 += dlt; hoffB += dlt;
    if (DS) {
      adv();
      if (q == 1) stage();
      LT(2);
      if (q == 0) WAIT_V(5); else WAIT_V(10);
    } else {
      LT(2);
      if (q == 29) {
        WAIT_V(0);
        const int l = __builtin_amdgcn_mbcnt_hi(~0u, __builtin_amdgcn_mbcnt_lo(~0u, 0u));
        int t = w * 64 + l;
        t = t < 288 ? t : t - 288;
        const int rr = (t >> 4) * 64 + (xcd & 1) * 32 + idx;
        touch(lb + T_SCR, (rr < 256 ? wo + (256 * kq + rr) * 1024 : wd + (896 * kq + rr - 256) * 1024) + (t & 15) * 64);
      }
    }
    WAIT_L0();
    LT(3);
    SCHED();
    BAR();
    SCHED();
    LT(4);
    PRIO_M();
    hacc = mfma(hb, ha, hacc);
#pragma unroll
    for (int n = 0; n < 10; ++n)
#pragma unroll
      for (int i = 0; i < 4; ++i) {
        if (n == 9 && i == 4 - EARLY) { SCHED(); PRIO_T(); if (q < 31 || g == 0) BAR(); SCHED(); }
        acc[i][n] = mfma(bfr[n], a[i], acc[i][n]);
      }
    PRIO_E();
    SCHED();
    LT(5);
    SCHED();
  };
#pragma clang loop unroll(disable)
  for (int q = 0; q < 29; ++q) phase(std::true_type{}, q);
#pragma clang loop unroll(disable)
  for (int q = 29; q < 32; ++q) phase(std::false_type{}, q);

  ST(3);
  asm volatile("" ::: "memory");
  if (g == 0) __builtin_amdgcn_s_setprio(0); else __builtin_amdgcn_s_setprio(1);
  lane = __builtin_amdgcn_mbcnt_hi(~0u, __builtin_amdgcn_mbcnt_lo(~0u, 0u));
  asm volatile("" : "+v"(lane));
  fr = lane & 15; fq = lane >> 4;
  const auto hrs = __builtin_amdgcn_make_buffer_rsrc(ws + WS_H + (size_t)(mb * 4 + kq) * 36 * 16384, 0, 36 * 16384, 0x00020000);
  const unsigned hv = wi * 4096 + lane * 16;
#define GLU(i, a, b) glu4(acc[i][a], acc[i][b])
  if (!typeA) {
    const int kba = 8 + ((fa - 896 * kq) >> 5) + g, kbc = 8 + ((fc - 896 * kq) >> 5);
#pragma unroll
    for (int i = 0; i < 4; ++i) {
      __builtin_amdgcn_raw_buffer_store_b128(cat4(GLU(i, 0, 4), GLU(i, 1, 5)), hrs, hv + kba * 16384 + i * 1024, 0, H_AUX);
      __builtin_amdgcn_raw_buffer_store_b128(cat4(GLU(i, 2, 6), GLU(i, 3, 7)), hrs, hv + kba * 16384 + 32768 + i * 1024, 0, H_AUX);
      __builtin_amdgcn_raw_buffer_store_b64(GLU(i, 8, 9), hrs, hv + kbc * 16384 + g * 8 + i * 1024, 0, H_AUX);
    }
  } else {
    LAS char* kim = lds + g * 40960;
    LAS char* vim = kim + 17 * 1024;
    bf16x8 kf[4], qf[4];
#define FIX(i, n) acc[i][n]
#pragma unroll
    for (int i = 0; i < 4; ++i) {
      kf[i] = pack8(FIX(i, 2), FIX(i, 3));
      *(LAS bf16x8*)(vim + ((wi * 4 + i) * 16 + fr) * 80 + fq * 16) = pack8(FIX(i, 4), FIX(i, 5));
    }
    *(LAS bf16x8*)(kim + (wi * 4 + 2) * 1024 + lane * 16) = kf[2];
    *(LAS bf16x8*)(kim + (wi * 4 + 3) * 1024 + lane * 16) = kf[3];
    {
      const f32x4 hh = hacc;
      LAS char* hp = (wi < 2) ? (kim + 16 * 1024 + lane * 16 + wi * 8) : (vim + (256 + fr) * 80 + fq * 16 + (wi - 2) * 8);
      *(LAS u32x2*)hp = (u32x2){pk2(hh[0], hh[1]), pk2(hh[2], hh[3])};
    }
    auto glu_part = [&]() {
#pragma unroll
      for (int i = 0; i < 4; ++i)
        __builtin_amdgcn_raw_buffer_store_b128(cat4(GLU(i, 6, 8), GLU(i, 7, 9)), hrs, hv + (8 + 2 * j + g) * 16384 + i * 1024, 0, H_AUX);
    };
    WAIT_L0();
    ET(0);
    ET(1);
    BAR();
    ET(2);
    asm volatile("" ::: "memory");
    const bool halo = wi == 0;
    const f32x4 bc = *(const LAS f32x4*)(lds + T_BIAS + lane * 16), bp = *(const LAS f32x4*)(lds + T_BIAS + 1024 + lane * 16);
    const f32x4 bp0 = *(const LAS f32x4*)(lds + T_BIAS + (halo ? 2048 : 1024) + lane * 16);
    const f32x4 bp1 = *(const LAS f32x4*)(lds + T_BIAS + (halo ? 3072 : 1024) + lane * 16);
    const int pf0 = halo ? 16 : wi * 4 - 2, pf1 = halo ? 16 : wi * 4 - 1;
    const bf16x8 kp0 = *(const LAS bf16x8*)(kim + pf0 * 1024 + lane * 16);
    const bf16x8 kp1 = *(const LAS bf16x8*)(kim + pf1 * 1024 + lane * 16);
    const LAS char* vb = vim + (4 * fq + (fr >> 2)) * 80 + (lane & 3) * 16;
    bf16x4 vt[6][2];
#pragma unroll
    for (int d = 0; d < 2; ++d) {
      vt[0][d] = trd(vb + pf0 * 1280 + d * 8); vt[1][d] = trd(vb + pf1 * 1280 + d * 8);
#pragma unroll
      for (int i = 0; i < 4; ++i) vt[2 + i][d] = trd(vb + (wi * 4 + i) * 1280 + d * 8);
    }
    f32x4 sp[4], sc[4];
#pragma unroll
    for (int i = 0; i < 4; ++i) {
      qf[i] = pack8(acc[i][0], acc[i][1]);
      sc[i] = mfma(kf[i], qf[i], bc);
    }
    sp[2] = mfma(kf[0], qf[2], bp); sp[3] = mfma(kf[1], qf[3], bp);
    sp[0] = mfma(kp0, qf[0], bp0); sp[1] = mfma(kp1, qf[1], bp1);
    const float sl = 0.17677669529663687f * 1.4426950408889634f;
    const f32x4 z4 = {0.f, 0.f, 0.f, 0.f};
    float m[4];
#pragma unroll
    for (int i = 0; i < 4; ++i)
      m[i] = fmx(fmx(fmx(fmx(sp[i][0], sp[i][1]), sp[i][2]), fmx(fmx(sp[i][3], sc[i][0]), sc[i][1])), fmx(sc[i][2], sc[i][3]));
    {
      const unsigned t = __float_as_uint(tred<true>(m[0], m[1], m[2], m[3]));
      const auto u = __builtin_amdgcn_permlane32_swap(t, t, false, false);
      const auto u0 = __builtin_amdgcn_permlane16_swap(u[0], u[0], false, false), u1 = __builtin_amdgcn_permlane16_swap(u[1], u[1], false, false);
      m[0] = __uint_as_float(u0[0]); m[1] = __uint_as_float(u0[1]); m[2] = __uint_as_float(u1[0]); m[3] = __uint_as_float(u1[1]);
    }
    const u32x4 one4 = {0x3f803f80u, 0x3f803f80u, 0x3f803f80u, 0x3f803f80u};
#pragma unroll
    for (int i = 0; i < 4; ++i) {
      const float mm = -sl * m[i];
#pragma unroll
      for (int e = 0; e < 4; ++e) {
        sp[i][e] = __builtin_amdgcn_exp2f(__builtin_fmaf(sp[i][e], sl, mm));
        sc[i][e] = __builtin_amdgcn_exp2f(__builtin_fmaf(sc[i][e], sl, mm));
      }
      const bf16x8 pfr = pack8(sp[i], sc[i]);
      const f32x4 sm = mfma(__builtin_bit_cast(bf16x8, one4), pfr, z4);
      const f32x4 o0 = mfma(cat(vt[i][0], vt[2 + i][0]), pfr, z4);
      const f32x4 o1 = mfma(cat(vt[i][1], vt[2 + i][1]), pfr, z4);
      const float inv = __builtin_amdgcn_rcpf(sm[0]);
      __builtin_amdgcn_raw_buffer_store_b128(__builtin_bit_cast(u32x4, pack8(o0 * inv, o1 * inv)), hrs, hv + (2 * j + g) * 16384 + i * 1024, 0, H_AUX);
    }
    glu_part();
    ET(5);
  }
  ST(4);
  ST(5);
  ST_OUT(0, 6);
}

constexpr int G2_SA = 32768, G2_SB = 16384, G2_SLOT = G2_SA + G2_SB, G2_LDS = 3 * G2_SLOT;

extern "C" __global__ __launch_bounds__(512, 2) void psum_g2_kernel(const u16* __restrict__ x, const u16* __restrict__ wo,
                                                       const u16* __restrict__ wd, char* __restrict__ ws,
                                                       u16* __restrict__ out) {
  extern __shared__ __attribute__((aligned(16))) char smem[];
  LAS char* lds = (LAS char*)smem;
  ST_DECL LT_DECL ST(0);
  const int tid = threadIdx.x, lane = tid & 63;
  const int w = __builtin_amdgcn_readfirstlane(tid >> 6), g = w >> 2, wi = w & 3;
  const int b = blockIdx.x, xcd = b & 7, idx = b >> 3;
  const int mb = (xcd & 1) * 4 + (idx & 3), kq = xcd >> 1, nt = idx >> 2;
  const int fr = lane & 15, fq = lane >> 4;

  const char* ca = ws + WS_H + (size_t)(mb * 4 + kq) * 36 * 16384 + w * 4096;
  const unsigned va = lane * 16;
  const int e0 = (0 ^ kq) * 1024, e1 = ((1 ^ kq) - 1) * 1024, e2 = ((2 ^ kq) - 2) * 1024, e3 = ((3 ^ kq) - 3) * 1024;
  const int rr = lane >> 3, pc = lane & 7;
  const unsigned vb = rr * 2048 + (((pc >> 1) ^ ((rr >> 1) & 3)) * 32) + (pc & 1) * 16;
  const char* cb = (const char*)(wo + (size_t)(256 * kq + 8 * w) * 1024 + nt * 128);
  const char* cb2 = (const char*)(wd + (size_t)(896 * kq + 8 * w) * 1024 + nt * 128);
  int st = 0;
  const unsigned lb = (unsigned)(size_t)lds;
  unsigned ns = lb;
  auto adv = [&]() { ca += 32768; ++st; cb = (st == 4) ? cb2 : cb + 64 * 2048; ns = (ns == lb + 2 * G2_SLOT) ? lb : ns + G2_SLOT; };
  auto stage = [&]() {
    dma_first(ns + w * 4096, va, ca + e0); dma_next<1024>(va, ca + e1); dma_next<2048>(va, ca + e2); dma_next<3072>(va, ca + e3);
    dma_first(ns + G2_SA + w * 2048, vb, cb); dma_next<1024>(vb, cb - 896);
    adv();
  };
  stage();
  BAR();
  stage();
  const int mf = wi * 4 + kq;
  const unsigned orow = ((mb * 256 + 32 * (mf >> 1) + 2 * fr + (mf & 1)) * 1024 + nt * 128 + g * 64 + 16 * (fq & 1) + 8 * (fq >> 1)) * 2;
  u32x4 xr[2] = {{0u, 0u, 0u, 0u}, {0u, 0u, 0u, 0u}};
  const int rl = 4 * (fq & 1) + ((lane & 15) >> 2);
  unsigned aoff = lb + wi * 4096 + lane * 16;
  unsigned boff = lb + G2_SA + (fq >> 1) * 2048 + g * 1024 + rl * 128 + 32 * ((rl >> 1) & 3) + 8 * (lane & 3);

  f32x4 acc[4][4];
#pragma unroll
  for (int i = 0; i < 4; ++i)
#pragma unroll
    for (int n = 0; n < 4; ++n) { acc[i][n] = (f32x4){0.f, 0.f, 0.f, 0.f}; asm volatile("" : "+v"(acc[i][n])); }
  ST(1);
  WAIT_V(6);
  BAR();
  ST(2);
  if (g == 1) BAR();

  int sl = 0;
  auto phase = [&](auto do_stage, int q) {
    constexpr bool DS = decltype(do_stage)::value;
    bf16x8 a[2][4], bfr[2][4];
    LT(0);
#define RA(kk, i) a[kk][i] = *(const LAS bf16x8*)(lp(aoff) + (kk) * 16384 + (i) * 1024);
#define RB(kk, n) bfr[kk][n] = cat(trd(lp(boff ^ ((n) * 32)) + (kk) * 8192), trd(lp(boff ^ ((n) * 32)) + (kk) * 8192 + 4096));
#define D0 if (DS) dma_first(ns + w * 4096, va, ca + e0);
#define D1 if (DS) dma_next<1024>(va, ca + e1);
#define D2 if (DS) dma_next<2048>(va, ca + e2);
#define D3 if (DS) dma_next<3072>(va, ca + e3);
#define D4 if (DS) dma_first(ns + G2_SA + w * 2048, vb, cb);
#define D5 if (DS) dma_next<1024>(vb, cb - 896);
#define UPD sl = (sl == 2) ? 0 : sl + 1; dlt = (sl == 0) ? -2 * G2_SLOT : G2_SLOT; if (DS) adv(); \
    asm volatile("" : "+s"(ca), "+s"(cb), "+s"(ns), "+s"(sl), "+s"(dlt) :: "memory");
    int dlt;
    if (!DS && q == 16)
      asm volatile("global_load_dwordx4 %0, %2, %3\n\tglobal_load_dwordx4 %1, %2, %3 offset:64" : "+v"(xr[0]), "+v"(xr[1]) : "v"(orow), "s"(x) : "memory");
    RA(0, 0) RA(0, 1) D0 RA(0, 2) RA(0, 3) RB(0, 0) D1 RB(0, 1) RB(0, 2) RB(0, 3) D2 RA(1, 0) RA(1, 1) RA(1, 2) D3 RA(1, 3) RB(1, 0) D4 RB(1, 1) D5 UPD RB(1, 2) RB(1, 3)
    aoff += dlt;
#undef RA
#undef RB
    boff += dlt;
    SCHED();
    LT(1);
    LT(2);
    asm volatile("" : "+v"(boff), "+v"(aoff));
    if (DS) WAIT_V_IF0(6, g ^ 1); else WAIT_V_IF0(2, g ^ 1);
    WAIT_L0();
    LT(3);
    SCHED();
    BAR();
    SCHED();
    LT(4);
    PRIO_M();
#pragma unroll
    for (int kk = 0; kk < 2; ++kk)
#pragma unroll
      for (int n = 0; n < 4; ++n)
#pragma unroll
        for (int i = 0; i < 4; ++i) {
          if (kk == 1 && n == 3 && i == 4 - EARLY2) { SCHED(); PRIO_T(); if (DS) WAIT_V_IF0(6, g); else WAIT_V_IF0(2, g); if (q < 17 || g == 0) BAR(); SCHED(); }
          acc[i][n] = mfma(bfr[kk][n], a[kk][i], acc[i][n]);
        }
    PRIO_E();
    SCHED();
    LT(5);
    SCHED();
  };
#pragma clang loop unroll(disable)
  for (int q = 0; q < 16; ++q) phase(std::true_type{}, q);
#pragma clang loop unroll(disable)
  for (int q = 16; q < 18; ++q) phase(std::false_type{}, q);

  ST(3);
  const int tile = mb * 8 + nt;
  const auto rs = __builtin_amdgcn_make_buffer_rsrc(ws + WS_X, 0, (int)X_BYTES, 0x00020000);
#pragma unroll
  for (int i = 1; i < 4; ++i) {
    const unsigned o = (unsigned)((((tile * 4 + (kq ^ i)) * 3 + (i - 1)) * 8 + w) * 2048) + lane * 16;
    __builtin_amdgcn_raw_buffer_store_b128(__builtin_bit_cast(u32x4, pack8(acc[i][0], acc[i][1])), rs, o, 0, 16);
    __builtin_amdgcn_raw_buffer_store_b128(__builtin_bit_cast(u32x4, pack8(acc[i][2], acc[i][3])), rs, o + 1024, 0, 16);
  }
  WAIT_V(0);
  ST(4);
  asm volatile("" : "+v"(xr[0]), "+v"(xr[1]));
  GAS unsigned* cnt = (GAS unsigned*)(ws + WS_CNT) + (tile * 32 + w) * 16;
  if (lane >= 1 && lane < 4) __hip_atomic_fetch_add(cnt + (kq ^ lane) * 128, 1u, __ATOMIC_RELAXED, __HIP_MEMORY_SCOPE_AGENT);
  {
    unsigned spins = 0;
    while ((unsigned)__builtin_amdgcn_readfirstlane(__hip_atomic_load(cnt + kq * 128, __ATOMIC_RELAXED, __HIP_MEMORY_SCOPE_AGENT)) < 3u) {
      __builtin_amdgcn_s_sleep(2);
      if (++spins > (1u << 22)) break;
    }
  }
  asm volatile("" ::: "memory");
  ST(5);
  u32x4 p[3][2];
#pragma unroll
  for (int i = 1; i < 4; ++i) {
    const unsigned o = (unsigned)((((tile * 4 + kq) * 3 + (i - 1)) * 8 + w) * 2048) + lane * 16;
    p[i - 1][0] = __builtin_amdgcn_raw_buffer_load_b128(rs, o, 0, 16);
    p[i - 1][1] = __builtin_amdgcn_raw_buffer_load_b128(rs, o + 1024, 0, 16);
  }
  const auto ors = __builtin_amdgcn_make_buffer_rsrc(out, 0, 4 << 20, 0x00020000);
#pragma unroll
  for (int h = 0; h < 2; ++h) {
    const auto x0 = __builtin_amdgcn_permlane16_swap(xr[h][0], xr[h][2], false, false), x1 = __builtin_amdgcn_permlane16_swap(xr[h][1], xr[h][3], false, false);
    const u32x4 xv = {x0[0], x1[0], x0[1], x1[1]};
    unsigned r[4];
#pragma unroll
    for (int d = 0; d < 4; ++d) {
      float lo = acc[0][2 * h + (d >> 1)][2 * (d & 1)] + __uint_as_float(xv[d] << 16), hi = acc[0][2 * h + (d >> 1)][2 * (d & 1) + 1] + __uint_as_float(xv[d] & 0xffff0000u);
#pragma unroll
      for (int i = 0; i < 3; ++i) { lo += __uint_as_float(p[i][h][d] << 16); hi += __uint_as_float(p[i][h][d] & 0xffff0000u); }
      r[d] = pk2(lo, hi);
    }
    const auto y0 = __builtin_amdgcn_permlane16_swap(r[0], r[2], false, false), y1 = __builtin_amdgcn_permlane16_swap(r[1], r[3], false, false);
    __builtin_amdgcn_raw_buffer_store_b128((u32x4){y0[0], y1[0], y0[1], y1[1]}, ors, orow + 64 * h, 0, OUT_AUX);
  }
  ST(6);
  ST(7);
  ST_OUT(1, 8);
}

extern "C" void kernel_launch(void* const* d_in, const int* in_sizes, int n_in, void* d_out, int out_size, void* d_ws,
                              size_t ws_size, hipStream_t stream) {
  const u16* x = (const u16*)d_in[0];
  const u16* wqkv = (const u16*)d_in[1];
  const u16* wo = (const u16*)d_in[2];
  const u16* wg = (const u16*)d_in[4];
  const u16* wu = (const u16*)d_in[5];
  const u16* wd = (const u16*)d_in[6];
  char* ws = (char*)d_ws;
  (void)hipFuncSetAttribute((const void*)psum_g1_kernel, hipFuncAttributeMaxDynamicSharedMemorySize, G1_LDS);
  (void)hipFuncSetAttribute((const void*)psum_g2_kernel, hipFuncAttributeMaxDynamicSharedMemorySize, G2_LDS);
  psum_ln_kernel<<<256, 320, 0, stream>>>(x, wqkv, wg, wu, ws);
  psum_g1_kernel<<<256, 512, G1_LDS, stream>>>(wqkv, wg, wu, wo, wd, ws);
  psum_g2_kernel<<<256, 512, G2_LDS, stream>>>(x, wo, wd, ws, (u16*)d_out);
}
```

```cpp
#define EARLY2 1
#include <hip/hip_runtime.h>
#include <hip/hip_bf16.h>
#include <type_traits>
#define EARLY 2
#define PF_W2 1
#define PF_LOOP 0
#define PF_PER 2
#define PF_LN_ROWS 96
#define PRIO_L 1
#define PRIO_M() __builtin_amdgcn_s_setprio(0)
#define PRIO_T() __builtin_amdgcn_s_setprio(1)
#define PRIO_E()
#define PF_CODE 0
#define PRECLEAN 0
#define OUT_AUX 16
#define G2ORD 0
#define H_AUX 16

namespace {

typedef unsigned short u16;
typedef short bf16x8 __attribute__((ext_vector_type(8)));
typedef short bf16x4 __attribute__((ext_vector_type(4)));
typedef float f32x4 __attribute__((ext_vector_type(4)));
typedef float f32x2 __attribute__((ext_vector_type(2)));
typedef unsigned u32x4 __attribute__((ext_vector_type(4)));
typedef unsigned u32x2 __attribute__((ext_vector_type(2)));
typedef __bf16 bf2 __attribute__((ext_vector_type(2)));
#define LAS __attribute__((address_space(3)))
#define GAS __attribute__((address_space(1)))

#define WAIT_V(n) asm volatile("s_waitcnt vmcnt(" #n ")" ::: "memory")
#define WAIT_V_IF0(n, c) asm volatile("s_cmp_lg_u32 %0, 0\n\ts_cbranch_scc1 1f\n\ts_waitcnt vmcnt(" #n ")\n1:" :: "s"(c) : "memory", "scc")
#define WAIT_L0() asm volatile("s_waitcnt lgkmcnt(0)" ::: "memory")
#define BAR() __builtin_amdgcn_s_barrier()
#define SCHED() __builtin_amdgcn_sched_barrier(0)

__device__ __forceinline__ float b2f(u16 v) { return __uint_as_float(((unsigned)v) << 16); }
__device__ __forceinline__ unsigned pk2(float lo, float hi) {
  bf2 r = {(__bf16)lo, (__bf16)hi};
  return __builtin_bit_cast(unsigned, r);
}
__device__ __forceinline__ float rbf(float f) { return (float)(__bf16)f; }
__device__ __forceinline__ bf16x8 pack8(f32x4 a, f32x4 b) {
  u32x4 r = {pk2(a[0], a[1]), pk2(a[2], a[3]), pk2(b[0], b[1]), pk2(b[2], b[3])};
  return __builtin_bit_cast(bf16x8, r);
}
__device__ __forceinline__ bf16x4 trd(LAS const char* p) {
  return __builtin_amdgcn_ds_read_tr16_b64_v4i16((LAS bf16x4*)p);
}
__device__ __forceinline__ bf16x8 cat(bf16x4 a, bf16x4 b) { return __builtin_shufflevector(a, b, 0, 1, 2, 3, 4, 5, 6, 7); }
__device__ __forceinline__ f32x4 mfma(bf16x8 a, bf16x8 b, f32x4 c) {
  return __builtin_amdgcn_mfma_f32_16x16x32_bf16(a, b, c, 0, 0, 0);
}
__device__ __forceinline__ void dma_first(unsigned lds_dst, unsigned voff, const char* b0) {
  asm volatile("s_mov_b32 m0, %0\n\ts_nop 0\n\tglobal_load_lds_dwordx4 %1, %2" :: "s"(lds_dst), "v"(voff), "s"(b0) : "memory");
}
template <int OFF>
__device__ __forceinline__ void dma_next(unsigned voff, const char* b) {
  asm volatile("global_load_lds_dwordx4 %0, %1 offset:%2" :: "v"(voff), "s"(b), "i"(OFF) : "memory");
}
__device__ __forceinline__ void dma1m(unsigned lds_dst, const char* p, unsigned long long mask) {
  asm volatile("s_mov_b32 m0, %0\n\ts_mov_b64 exec, %2\n\t"
               "global_load_lds_dwordx4 %1, off\n\t"
               "s_mov_b64 exec, -1"
               :: "s"(lds_dst), "v"(p), "s"(mask) : "memory");
}
__device__ __forceinline__ void touch(unsigned lds_dst, const void* p) {
  asm volatile("s_mov_b32 m0, %0\n\ts_nop 0\n\tglobal_load_lds_dword %1, off" :: "s"(lds_dst), "v"(p) : "memory");
}
#define CODE_ADDR(dst, sym) do { unsigned lo_, hi_; asm volatile("s_getpc_b64 s[90:91]\n\ts_add_u32 %0, s90, " #sym "@rel32@lo+4\n\ts_addc_u32 %1, s91, " #sym "@rel32@hi+12" \
    : "=s"(lo_), "=s"(hi_) :: "s90", "s91", "scc"); dst = (const char*)(((unsigned long long)hi_ << 32) | lo_); } while (0)
constexpr int LN_LINES = 16, G1_LINES = 80, G2_LINES = 30;
__device__ __forceinline__ LAS char* lp(unsigned a) { return (LAS char*)(size_t)a; }
__device__ __forceinline__ float fmx(float a, float b) { return __builtin_elementwise_maximum(a, b); }
__device__ __forceinline__ float xmax(float v) {
  auto a = __builtin_amdgcn_permlane16_swap(__float_as_uint(v), __float_as_uint(v), false, false);
  v = fmaxf(__uint_as_float(a[0]), __uint_as_float(a[1]));
  auto b = __builtin_amdgcn_permlane32_swap(__float_as_uint(v), __float_as_uint(v), false, false);
  return fmaxf(__uint_as_float(b[0]), __uint_as_float(b[1]));
}
__device__ __forceinline__ float xsum(float v) {
  auto a = __builtin_amdgcn_permlane16_swap(__float_as_uint(v), __float_as_uint(v), false, false);
  v = __uint_as_float(a[0]) + __uint_as_float(a[1]);
  auto b = __builtin_amdgcn_permlane32_swap(__float_as_uint(v), __float_as_uint(v), false, false);
  return __uint_as_float(b[0]) + __uint_as_float(b[1]);
}

template <int CTRL>
__device__ __forceinline__ float dpp_add(float v) {
  return v + __uint_as_float(__builtin_amdgcn_update_dpp(0u, __float_as_uint(v), CTRL, 0xf, 0xf, true));
}
__device__ __forceinline__ float hsum(float v) {
  v = dpp_add<0xB1>(v);
  v = dpp_add<0x4E>(v);
  v = dpp_add<0x128>(v);
  return xsum(v);
}

#define ST_DECL
#define LT_DECL
#define LT(k)
#define ET(k)
#define ST(k)
#define ST_OUT(kid, n)
#define WS_BASE_MB 0
constexpr size_t WS_XN = (size_t)WS_BASE_MB << 20;
constexpr size_t WS_H = WS_XN + (size_t)4 * 1024 * 1024;
constexpr size_t H_BYTES = (size_t)8 * 4 * 36 * 16 * 1024;
constexpr size_t WS_X = WS_H + H_BYTES;
constexpr size_t X_BYTES = (size_t)64 * 4 * 3 * 8 * 2048;
constexpr size_t WS_CNT = WS_X + X_BYTES;
constexpr size_t WS_ST = WS_CNT + 131072;

}
extern "C" __global__ __launch_bounds__(320) void psum_ln_kernel(const u16* __restrict__ x, const u16* __restrict__ wqkv, const u16* __restrict__ wg,
                                                                 const u16* __restrict__ wu, char* __restrict__ ws) {
  const int lane = threadIdx.x & 63, b = blockIdx.x;
  if (threadIdx.x >= 256) {
    __shared__ __attribute__((aligned(16))) char sink[256];
    const int kq = (b & 7) >> 1, l = lane < 40 ? lane : lane - 40;
    const u16* p = l < 12 ? wqkv + (l >> 2) * 1024 + 256 * kq + (l & 3) * 64 : (l < 26 ? wg + 896 * kq + (l - 12) * 64 : wu + 896 * kq + (l - 26) * 64);
    const int ld = l < 12 ? 3072 : 3584;
#pragma unroll
    for (int i = 0; i < PF_LN_ROWS / 32; ++i) touch((unsigned)(size_t)(LAS char*)sink, p + (i * 32 + (b >> 3)) * ld);
    return;
  }
  const int hf = (b & 1) * 128 + (b >> 1);
  const int mb = hf >> 5, mf = (hf >> 1) & 15, r = (hf & 1) * 8 + (threadIdx.x >> 6) * 2 + ((lane >> 2) & 1);
  const int row = mb * 256 + 32 * (mf >> 1) + 2 * r + (mf & 1), kl = lane >> 3, ch = lane & 3;
  const u16* xr = x + row * 1024 + kl * 32 + ch * 8;
  u32x4 raw[4];
#pragma unroll
  for (int i = 0; i < 4; ++i) raw[i] = *(const u32x4*)(xr + 256 * i);
  float v[32];
#pragma unroll
  for (int i = 0; i < 16; ++i) { v[2 * i] = __uint_as_float(raw[i >> 2][i & 3] << 16); v[2 * i + 1] = __uint_as_float(raw[i >> 2][i & 3] & 0xffff0000u); }
  float s = 0.f;
#pragma unroll
  for (int i = 0; i < 32; ++i) s += v[i];
  const float mean = hsum(s) * (1.0f / 1024.0f);
  float q = 0.f;
#pragma unroll
  for (int i = 0; i < 32; ++i) { const float d = v[i] - mean; q += d * d; }
  q = hsum(q);
  const float mb16 = rbf(mean), den = rbf(sqrtf(rbf(rbf(q * (1.0f / 1024.0f)) + 1e-5f)));
  const float inv = 1.0f / den;
  const auto rs = __builtin_amdgcn_make_buffer_rsrc(ws + WS_XN, 0, 4 << 20, 0x00020000);
  const unsigned o = ((mb * 32 + kl) * 16 + mf) * 1024 + r * 64 + ((ch ^ (2 * (r >> 3))) * 16);
#pragma unroll
  for (int i = 0; i < 4; ++i) {
    u32x4 t;
#pragma unroll
    for (int e = 0; e < 4; ++e) t[e] = pk2(rbf(v[8 * i + 2 * e] - mb16) * inv, rbf(v[8 * i + 2 * e + 1] - mb16) * inv);
    __builtin_amdgcn_raw_buffer_store_b128(t, rs, o + i * 8 * 16384, 0, H_AUX);
  }
}
namespace {
constexpr int G1_SA = 17 * 1024, G1_SB = 20 * 1024, G1_SLOT = G1_SA + G1_SB;
constexpr int T_SCR = 4 * G1_SLOT;
constexpr int T_BIAS = T_SCR + 256;
constexpr int G1_LDS = T_BIAS + 4096;

template <bool MAX>
__device__ __forceinline__ float tred(float a, float b, float c, float d) {
  const auto r1 = __builtin_amdgcn_permlane16_swap(__float_as_uint(a), __float_as_uint(b), false, false);
  const auto r2 = __builtin_amdgcn_permlane16_swap(__float_as_uint(c), __float_as_uint(d), false, false);
  const float x0 = __uint_as_float(r1[0]), x1 = __uint_as_float(r1[1]), y0 = __uint_as_float(r2[0]), y1 = __uint_as_float(r2[1]);
  const float t = MAX ? fmx(x0, x1) : x0 + x1, u = MAX ? fmx(y0, y1) : y0 + y1;
  const auto r3 = __builtin_amdgcn_permlane32_swap(__float_as_uint(t), __float_as_uint(u), false, false);
  const float z0 = __uint_as_float(r3[0]), z1 = __uint_as_float(r3[1]);
  return MAX ? fmx(z0, z1) : z0 + z1;
}
__device__ __forceinline__ u32x2 glu4(f32x4 g, f32x4 u) {
  f32x4 e = g * -1.4426950408889634f;
#pragma unroll
  for (int i = 0; i < 4; ++i) e[i] = __builtin_amdgcn_exp2f(e[i]);
  e = e + 1.0f;
#pragma unroll
  for (int i = 0; i < 4; ++i) e[i] = __builtin_amdgcn_rcpf(e[i]);
  e = e * u;
  return (u32x2){pk2(e[0], e[1]), pk2(e[2], e[3])};
}
__device__ __forceinline__ u32x4 cat4(u32x2 a, u32x2 b) { return (u32x4){a[0], a[1], b[0], b[1]}; }

}
extern "C" __global__ __launch_bounds__(512, 2) void psum_g1_kernel(const u16* __restrict__ wqkv,
                                                                    const u16* __restrict__ wg, const u16* __restrict__ wu,
                                                                    const u16* __restrict__ wo, const u16* __restrict__ wd,
                                                                    char* __restrict__ ws) {
  extern __shared__ __attribute__((aligned(16))) char smem[];
  LAS char* lds = (LAS char*)smem;
  ST_DECL LT_DECL ST(0);
  const int tid = threadIdx.x;
  int lane = tid & 63;
  const int w = __builtin_amdgcn_readfirstlane(tid >> 6), g = w >> 2, wi = w & 3;
  const int b = blockIdx.x, xcd = b & 7, idx = b >> 3;
  const int mb = (xcd & 1) * 4 + (idx & 3), kq = xcd >> 1, ntl = idx >> 2;
  const bool typeA = ntl < 4;
  const int j = ntl & 3;
  int fr = lane & 15, fq = lane >> 4;
  const int fA = 896 * kq + 64 * j;
  const int f0 = 896 * kq + 256 + 160 * j, odd = j & 1;
  const int fa = f0 + 32 * odd, fb = fa + 64, fc = odd ? f0 : f0 + 128;

  const char* c1;
  unsigned v1;
  int s1, s2, s3, p1, p2;
  const char* c2;
  int d1, d2;
  unsigned long long m2 = ~0ull;
  if (g == 0) {
    const int rr = lane >> 3, pc = lane & 7, fi = (pc >> 1) ^ ((rr >> 1) & 3), kr = (rr & 3) + 8 * (rr >> 2);
    const int kr2 = kr + 16 * (wi >> 1) + 4 * (wi & 1);
    int ld; const u16* q1;
    if (typeA) {
      if (wi < 3) { q1 = wqkv + wi * 1024 + 256 * kq + 64 * j; ld = 3072; } else { q1 = wg + fA; ld = 3584; }
      c2 = (const char*)(wu + fA + kr2 * 3584) + fi * 32 + (pc & 1) * 16;
    } else {
      q1 = ((wi & 2) ? wu : wg) + ((wi & 1) ? fb : fa); ld = 3584;
      c2 = (const char*)(((fi & 1) ? wu : wg) + fc + (fi >> 1) * 16 + kr2 * 3584) + (pc & 1) * 16;
    }
    c1 = (const char*)q1; v1 = kr * ld * 2 + fi * 32 + (pc & 1) * 16;
    s1 = 8 * ld - 1024; s2 = 32 * ld - 2048; s3 = 40 * ld - 3072; p1 = 64 * ld; p2 = 64 * 3584;
    d1 = G1_SA + wi * 4096; d2 = G1_SA + 16384 + wi * 1024;
  } else {
    c1 = ws + WS_XN + (size_t)mb * 32 * 16384 + wi * 4096; v1 = lane * 16;
    const int hr = wi * 4 + ((lane >> 2) & 3);
    c2 = ws + WS_XN + (size_t)(mb > 0 ? mb - 1 : 0) * 32 * 16384 + (14 + (hr >> 3)) * 1024 + (8 + (hr & 7)) * 64 + (((lane & 3) ^ (2 * (hr >> 3)) ^ 2) * 16);
    m2 = 0xffffull;
    s1 = 0; s2 = 0; s3 = 0; p1 = 16384; p2 = 16384;
    d1 = wi * 4096; d2 = 16384 + wi * 256;
  }
  const unsigned lb = (unsigned)(size_t)lds;
  unsigned ns = lb;
  auto adv = [&]() { c1 += p1; c2 += p2; ns = (ns == lb + 3 * G1_SLOT) ? lb : ns + G1_SLOT; };
  auto stage = [&]() {
    dma_first(ns + d1, v1, c1); dma_next<1024>(v1, c1 + s1); dma_next<2048>(v1, c1 + s2); dma_next<3072>(v1, c1 + s3);
    dma1m(ns + d2, c2, m2);
    adv();
  };
  stage();
  BAR();
  stage();
  if (tid < 8) *(unsigned*)(ws + WS_CNT + (((mb * 8 + ntl) * 4 + kq) * 8 + tid) * 64) = 0u;
  if (g == 0) {
    const int lo = wi == 0 ? fr - 7 : (wi == 2 ? fr + 1 : fr + 9);
    const int hi = wi == 0 ? fr + 1 : ((wi >= 2 && mb == 0) ? 0 : (wi == 2 ? 8 : 16));
    f32x4 bv;
#pragma unroll
    for (int e = 0; e < 4; ++e) bv[e] = (4 * fq + e >= lo && 4 * fq + e < hi) ? 0.f : -3.0e38f;
    *(LAS f32x4*)(lds + T_BIAS + wi * 1024 + lane * 16) = bv;
  }

  const int rl = 4 * (fq & 1) + (fr >> 2);
  unsigned hoffA = lb + 16384 + fr * 64 + ((fq ^ (2 * (fr >> 3))) * 16);
  unsigned aoff = hoffA - 16384 + wi * 4096;
  unsigned boff0 = lb + G1_SA + (fq >> 1) * 2048 + rl * 128 + 32 * ((2 * g) ^ ((rl >> 1) & 3)) + 8 * (lane & 3);
  unsigned boff1 = boff0 ^ 32;
  unsigned hoffB = (boff0 ^ (32 * (wi & 1))) + (1 + (wi >> 1)) * 4096;

  f32x4 acc[4][10];
  f32x4 hacc = {0.f, 0.f, 0.f, 0.f};
#pragma unroll
  for (int i = 0; i < 4; ++i)
#pragma unroll
    for (int n = 0; n < 10; ++n) { acc[i][n] = (f32x4){0.f, 0.f, 0.f, 0.f}; asm volatile("" : "+v"(acc[i][n])); }
  asm volatile("" : "+v"(aoff), "+v"(hoffA), "+v"(boff0), "+v"(boff1), "+v"(hoffB), "+v"(hacc));
  ST(1);
  WAIT_V(5);
  BAR();
  ST(2);
  if (g == 1) BAR();

  auto phase = [&](auto do_stage, int q) {
    constexpr bool DS = decltype(do_stage)::value;
    bf16x8 a[4], bfr[10], ha, hb;
    LT(0);
#define RB(c) bfr[2 * (c)] = cat(trd(lp(boff0) + (c) * 4096), trd(lp(boff0) + (c) * 4096 + 1024)); \
              bfr[2 * (c) + 1] = cat(trd(lp(boff1) + (c) * 4096), trd(lp(boff1) + (c) * 4096 + 1024));
    a[0] = *(const LAS bf16x8*)(lp(aoff)); a[1] = *(const LAS bf16x8*)(lp(aoff) + 1024);
    if (DS) dma_first(ns + d1, v1, c1);
    a[2] = *(const LAS bf16x8*)(lp(aoff) + 2048); a[3] = *(const LAS bf16x8*)(lp(aoff) + 3072);
    ha = *(const LAS bf16x8*)lp(hoffA);
    RB(0)
    if (DS) dma_next<1024>(v1, c1 + s1);
    RB(1)
    if (DS) dma_next<2048>(v1, c1 + s2);
    RB(2)
    if (DS) dma_next<3072>(v1, c1 + s3);
    RB(3)
    if (DS) dma1m(ns + d2, c2, m2);
    RB(4)
    hb = cat(trd(lp(hoffB)), trd(lp(hoffB) + 1024));
#undef RB
    SCHED();
    LT(1);
    const int dlt = ((q & 3) == 3) ? -3 * G1_SLOT : G1_SLOT;
    aoff += dlt; hoffA += dlt; boff0 += dlt; boff1 += dlt; hoffB += dlt;
    if (DS) {
      adv();
      if (q == 1) stage();
      LT(2);
      if (q == 0) WAIT_V(5); else WAIT_V(10);
    } else {
      LT(2);
      if (q == 29) {
        WAIT_V(0);
        const int l = __builtin_amdgcn_mbcnt_hi(~0u, __builtin_amdgcn_mbcnt_lo(~0u, 0u));
        int t = w * 64 + l;
        t = t < 288 ? t : t - 288;
        const int rr = (t >> 4) * 64 + (xcd & 1) * 32 + idx;
        touch(lb + T_SCR, (rr < 256 ? wo + (256 * kq + rr) * 1024 : wd + (896 * kq + rr - 256) * 1024) + (t & 15) * 64);
      }
    }
    WAIT_L0();
    LT(3);
    SCHED();
    BAR();
    SCHED();
    LT(4);
    PRIO_M();
    hacc = mfma(hb, ha, hacc);
#pragma unroll
    for (int n = 0; n < 10; ++n)
#pragma unroll
      for (int i = 0; i < 4; ++i) {
        if (n == 9 && i == 4 - EARLY) { SCHED(); PRIO_T(); if (q < 31 || g == 0) BAR(); SCHED(); }
        acc[i][n] = mfma(bfr[n], a[i], acc[i][n]);
      }
    PRIO_E();
    SCHED();
    LT(5);
    SCHED();
  };
#pragma clang loop unroll(disable)
  for (int q = 0; q < 29; ++q) phase(std::true_type{}, q);
#pragma clang loop unroll(disable)
  for (int q = 29; q < 32; ++q) phase(std::false_type{}, q);

  ST(3);
  asm volatile("" ::: "memory");
  if (g == 0) __builtin_amdgcn_s_setprio(0); else __builtin_amdgcn_s_setprio(1);
  lane = __builtin_amdgcn_mbcnt_hi(~0u, __builtin_amdgcn_mbcnt_lo(~0u, 0u));
  asm volatile("" : "+v"(lane));
  fr = lane & 15; fq = lane >> 4;
  const auto hrs = __builtin_amdgcn_make_buffer_rsrc(ws + WS_H + (size_t)(mb * 4 + kq) * 36 * 16384, 0, 36 * 16384, 0x00020000);
  const unsigned hv = wi * 4096 + lane * 16;
#define GLU(i, a, b) glu4(acc[i][a], acc[i][b])
  if (!typeA) {
    const int kba = 8 + ((fa - 896 * kq) >> 5) + g, kbc = 8 + ((fc - 896 * kq) >> 5);
#pragma unroll
    for (int i = 0; i < 4; ++i) {
      __builtin_amdgcn_raw_buffer_store_b128(cat4(GLU(i, 0, 4), GLU(i, 1, 5)), hrs, hv + kba * 16384 + i * 1024, 0, H_AUX);
      __builtin_amdgcn_raw_buffer_store_b128(cat4(GLU(i, 2, 6), GLU(i, 3, 7)), hrs, hv + kba * 16384 + 32768 + i * 1024, 0, H_AUX);
      __builtin_amdgcn_raw_buffer_store_b64(GLU(i, 8, 9), hrs, hv + kbc * 16384 + g * 8 + i * 1024, 0, H_AUX);
    }
  } else {
    LAS char* kim = lds + g * 40960;
    LAS char* vim = kim + 17 * 1024;
    bf16x8 kf[4], qf[4];
#define FIX(i, n) acc[i][n]
#pragma unroll
    for (int i = 0; i < 4; ++i) {
      kf[i] = pack8(FIX(i, 2), FIX(i, 3));
      *(LAS bf16x8*)(vim + ((wi * 4 + i) * 16 + fr) * 80 + fq * 16) = pack8(FIX(i, 4), FIX(i, 5));
    }
    *(LAS bf16x8*)(kim + (wi * 4 + 2) * 1024 + lane * 16) = kf[2];
    *(LAS bf16x8*)(kim + (wi * 4 + 3) * 1024 + lane * 16) = kf[3];
    {
      const f32x4 hh = hacc;
      LAS char* hp = (wi < 2) ? (kim + 16 * 1024 + lane * 16 + wi * 8) : (vim + (256 + fr) * 80 + fq * 16 + (wi - 2) * 8);
      *(LAS u32x2*)hp = (u32x2){pk2(hh[0], hh[1]), pk2(hh[2], hh[3])};
    }
    auto glu_part = [&]() {
#pragma unroll
      for (int i = 0; i < 4; ++i)
        __builtin_amdgcn_raw_buffer_store_b128(cat4(GLU(i, 6, 8), GLU(i, 7, 9)), hrs, hv + (8 + 2 * j + g) * 16384 + i * 1024, 0, H_AUX);
    };
    WAIT_L0();
    ET(0);
    ET(1);
    BAR();
    ET(2);
    asm volatile("" ::: "memory");
    const bool halo = wi == 0;
    const f32x4 bc = *(const LAS f32x4*)(lds + T_BIAS + lane * 16), bp = *(const LAS f32x4*)(lds + T_BIAS + 1024 + lane * 16);
    const f32x4 bp0 = *(const LAS f32x4*)(lds + T_BIAS + (halo ? 2048 : 1024) + lane * 16);
    const f32x4 bp1 = *(const LAS f32x4*)(lds + T_BIAS + (halo ? 3072 : 1024) + lane * 16);
    const int pf0 = halo ? 16 : wi * 4 - 2, pf1 = halo ? 16 : wi * 4 - 1;
    const bf16x8 kp0 = *(const LAS bf16x8*)(kim + pf0 * 1024 + lane * 16);
    const bf16x8 kp1 = *(const LAS bf16x8*)(kim + pf1 * 1024 + lane * 16);
    const LAS char* vb = vim + (4 * fq + (fr >> 2)) * 80 + (lane & 3) * 16;
    bf16x4 vt[6][2];
#pragma unroll
    for (int d = 0; d < 2; ++d) {
      vt[0][d] = trd(vb + pf0 * 1280 + d * 8); vt[1][d] = trd(vb + pf1 * 1280 + d * 8);
#pragma unroll
      for (int i = 0; i < 4; ++i) vt[2 + i][d] = trd(vb + (wi * 4 + i) * 1280 + d * 8);
    }
    f32x4 sp[4], sc[4];
#pragma unroll
    for (int i = 0; i < 4; ++i) {
      qf[i] = pack8(acc[i][0], acc[i][1]);
      sc[i] = mfma(kf[i], qf[i], bc);
    }
    sp[2] = mfma(kf[0], qf[2], bp); sp[3] = mfma(kf[1], qf[3], bp);
    sp[0] = mfma(kp0, qf[0], bp0); sp[1] = mfma(kp1, qf[1], bp1);
    const float sl = 0.17677669529663687f * 1.4426950408889634f;
    const f32x4 z4 = {0.f, 0.f, 0.f, 0.f};
    float m[4];
#pragma unroll
    for (int i = 0; i < 4; ++i)
      m[i] = fmx(fmx(fmx(fmx(sp[i][0], sp[i][1]), sp[i][2]), fmx(fmx(sp[i][3], sc[i][0]), sc[i][1])), fmx(sc[i][2], sc[i][3]));
    {
      const unsigned t = __float_as_uint(tred<true>(m[0], m[1], m[2], m[3]));
      const auto u = __builtin_amdgcn_permlane32_swap(t, t, false, false);
      const auto u0 = __builtin_amdgcn_permlane16_swap(u[0], u[0], false, false), u1 = __builtin_amdgcn_permlane16_swap(u[1], u[1], false, false);
      m[0] = __uint_as_float(u0[0]); m[1] = __uint_as_float(u0[1]); m[2] = __uint_as_float(u1[0]); m[3] = __uint_as_float(u1[1]);
    }
    const u32x4 one4 = {0x3f803f80u, 0x3f803f80u, 0x3f803f80u, 0x3f803f80u};
#pragma unroll
    for (int i = 0; i < 4; ++i) {
      const float mm = -sl * m[i];
#pragma unroll
      for (int e = 0; e < 4; ++e) {
        sp[i][e] = __builtin_amdgcn_exp2f(__builtin_fmaf(sp[i][e], sl, mm));
        sc[i][e] = __builtin_amdgcn_exp2f(__builtin_fmaf(sc[i][e], sl, mm));
      }
      const bf16x8 pfr = pack8(sp[i], sc[i]);
      const f32x4 sm = mfma(__builtin_bit_cast(bf16x8, one4), pfr, z4);
      const f32x4 o0 = mfma(cat(vt[i][0], vt[2 + i][0]), pfr, z4);
      const f32x4 o1 = mfma(cat(vt[i][1], vt[2 + i][1]), pfr, z4);
      const float inv = __builtin_amdgcn_rcpf(sm[0]);
      __builtin_amdgcn_raw_buffer_store_b128(__builtin_bit_cast(u32x4, pack8(o0 * inv, o1 * inv)), hrs, hv + (2 * j + g) * 16384 + i * 1024, 0, H_AUX);
    }
    glu_part();
    ET(5);
  }
  ST(4);
  ST(5);
  ST_OUT(0, 6);
}

constexpr int G2_SA = 32768, G2_SB = 16384, G2_SLOT = G2_SA + G2_SB, G2_LDS = 3 * G2_SLOT;

extern "C" __global__ __launch_bounds__(512, 2) void psum_g2_kernel(const u16* __restrict__ x, const u16* __restrict__ wo,
                                                       const u16* __restrict__ wd, char* __restrict__ ws,
                                                       u16* __restrict__ out) {
  extern __shared__ __attribute__((aligned(16))) char smem[];
  LAS char* lds = (LAS char*)smem;
  ST_DECL LT_DECL ST(0);
  const int tid = threadIdx.x, lane = tid & 63;
  const int w = __builtin_amdgcn_readfirstlane(tid >> 6), g = w >> 2, wi = w & 3;
  const int b = blockIdx.x, xcd = b & 7, idx = b >> 3;
  const int mb = (xcd & 1) * 4 + (idx & 3), kq = xcd >> 1, nt = idx >> 2;
  const int fr = lane & 15, fq = lane >> 4;

  const char* ca = ws + WS_H + (size_t)(mb * 4 + kq) * 36 * 16384 + w * 4096;
  const unsigned va = lane * 16;
  const int e0 = (0 ^ kq) * 1024, e1 = ((1 ^ kq) - 1) * 1024, e2 = ((2 ^ kq) - 2) * 1024, e3 = ((3 ^ kq) - 3) * 1024;
  const int rr = lane >> 3, pc = lane & 7;
  const unsigned vb = rr * 2048 + (((pc >> 1) ^ ((rr >> 1) & 3)) * 32) + (pc & 1) * 16;
  const char* cb = (const char*)(wo + (size_t)(256 * kq + 8 * w) * 1024 + nt * 128);
  const char* cb2 = (const char*)(wd + (size_t)(896 * kq + 8 * w) * 1024 + nt * 128);
  int st = 0;
  const unsigned lb = (unsigned)(size_t)lds;
  unsigned ns = lb;
  auto adv = [&]() { ca += 32768; ++st; cb = (st == 4) ? cb2 : cb + 64 * 2048; ns = (ns == lb + 2 * G2_SLOT) ? lb : ns + G2_SLOT; };
  auto stage = [&]() {
    dma_first(ns + w * 4096, va, ca + e0); dma_next<1024>(va, ca + e1); dma_next<2048>(va, ca + e2); dma_next<3072>(va, ca + e3);
    dma_first(ns + G2_SA + w * 2048, vb, cb); dma_next<1024>(vb, cb - 896);
    adv();
  };
  stage();
  BAR();
  stage();
  const int mf = wi * 4 + kq;
  const unsigned orow = ((mb * 256 + 32 * (mf >> 1) + 2 * fr + (mf & 1)) * 1024 + nt * 128 + g * 64 + 16 * (fq & 1) + 8 * (fq >> 1)) * 2;
  u32x4 xr[2] = {{0u, 0u, 0u, 0u}, {0u, 0u, 0u, 0u}};
  const int rl = 4 * (fq & 1) + ((lane & 15) >> 2);
  unsigned aoff = lb + wi * 4096 + lane * 16;
  unsigned boff = lb + G2_SA + (fq >> 1) * 2048 + g * 1024 + rl * 128 + 32 * ((rl >> 1) & 3) + 8 * (lane & 3);

  f32x4 acc[4][4];
#pragma unroll
  for (int i = 0; i < 4; ++i)
#pragma unroll
    for (int n = 0; n < 4; ++n) { acc[i][n] = (f32x4){0.f, 0.f, 0.f, 0.f}; asm volatile("" : "+v"(acc[i][n])); }
  ST(1);
  WAIT_V(6);
  BAR();
  ST(2);
  if (g == 1) BAR();

  int sl = 0;
  auto phase = [&](auto do_stage, int q) {
    constexpr bool DS = decltype(do_stage)::value;
    bf16x8 a[2][4], bfr[2][4];
    LT(0);
#define RA(kk, i) a[kk][i] = *(const LAS bf16x8*)(lp(aoff) + (kk) * 16384 + (i) * 1024);
#define RB(kk, n) bfr[kk][n] = cat(trd(lp(boff ^ ((n) * 32)) + (kk) * 8192), trd(lp(boff ^ ((n) * 32)) + (kk) * 8192 + 4096));
#define D0 if (DS) dma_first(ns + w * 4096, va, ca + e0);
#define D1 if (DS) dma_next<1024>(va, ca + e1);
#define D2 if (DS) dma_next<2048>(va, ca + e2);
#define D3 if (DS) dma_next<3072>(va, ca + e3);
#define D4 if (DS) dma_first(ns + G2_SA + w * 2048, vb, cb);
#define D5 if (DS) dma_next<1024>(vb, cb - 896);
#define UPD sl = (sl == 2) ? 0 : sl + 1; dlt = (sl == 0) ? -2 * G2_SLOT : G2_SLOT; if (DS) adv(); \
    asm volatile("" : "+s"(ca), "+s"(cb), "+s"(ns), "+s"(sl), "+s"(dlt) :: "memory");
    int dlt;
    if (!DS && q == 16)
      asm volatile("global_load_dwordx4 %0, %2, %3\n\tglobal_load_dwordx4 %1, %2, %3 offset:64" : "+v"(xr[0]), "+v"(xr[1]) : "v"(orow), "s"(x) : "memory");
    RA(0, 0) RA(0, 1) D0 RA(0, 2) RA(0, 3) RB(0, 0) D1 RB(0, 1) RB(0, 2) RB(0, 3) D2 RA(1, 0) RA(1, 1) RA(1, 2) D3 RA(1, 3) RB(1, 0) D4 RB(1, 1) D5 UPD RB(1, 2) RB(1, 3)
    aoff += dlt;
#undef RA
#undef RB
    boff += dlt;
    SCHED();
    LT(1);
    LT(2);
    asm volatile("" : "+v"(boff), "+v"(aoff));
    if (DS) WAIT_V_IF0(6, g ^ 1); else WAIT_V_IF0(2, g ^ 1);
    WAIT_L0();
    LT(3);
    SCHED();
    BAR();
    SCHED();
    LT(4);
    PRIO_M();
#pragma unroll
    for (int kk = 0; kk < 2; ++kk)
#pragma unroll
      for (int n = 0; n < 4; ++n)
#pragma unroll
        for (int i = 0; i < 4; ++i) {
          if (kk == 1 && n == 3 && i == 4 - EARLY2) { SCHED(); PRIO_T(); if (DS) WAIT_V_IF0(6, g); else WAIT_V_IF0(2, g); if (q < 17 || g == 0) BAR(); SCHED(); }
          acc[i][n] = mfma(bfr[kk][n], a[kk][i], acc[i][n]);
        }
    PRIO_E();
    SCHED();
    LT(5);
    SCHED();
  };
#pragma clang loop unroll(disable)
  for (int q = 0; q < 16; ++q) phase(std::true_type{}, q);
#pragma clang loop unroll(disable)
  for (int q = 16; q < 18; ++q) phase(std::false_type{}, q);

  ST(3);
  const int tile = mb * 8 + nt;
  const auto rs = __builtin_amdgcn_make_buffer_rsrc(ws + WS_X, 0, (int)X_BYTES, 0x00020000);
#pragma unroll
  for (int i = 1; i < 4; ++i) {
    const unsigned o = (unsigned)((((tile * 4 + (kq ^ i)) * 3 + (i - 1)) * 8 + w) * 2048) + lane * 16;
    __builtin_amdgcn_raw_buffer_store_b128(__builtin_bit_cast(u32x4, pack8(acc[i][0], acc[i][1])), rs, o, 0, 16);
    __builtin_amdgcn_raw_buffer_store_b128(__builtin_bit_cast(u32x4, pack8(acc[i][2], acc[i][3])), rs, o + 1024, 0, 16);
  }
  WAIT_V(0);
  ST(4);
  asm volatile("" : "+v"(xr[0]), "+v"(xr[1]));
  GAS unsigned* cnt = (GAS unsigned*)(ws + WS_CNT) + (tile * 32 + w) * 16;
  if (lane >= 1 && lane < 4) __hip_atomic_fetch_add(cnt + (kq ^ lane) * 128, 1u, __ATOMIC_RELAXED, __HIP_MEMORY_SCOPE_AGENT);
  {
    unsigned spins = 0;
    while ((unsigned)__builtin_amdgcn_readfirstlane(__hip_atomic_load(cnt + kq * 128, __ATOMIC_RELAXED, __HIP_MEMORY_SCOPE_AGENT)) < 3u) {
      __builtin_amdgcn_s_sleep(2);
      if (++spins > (1u << 22)) break;
    }
  }
  asm volatile("" ::: "memory");
  ST(5);
  u32x4 p[3][2];
#pragma unroll
  for (int i = 1; i < 4; ++i) {
    const unsigned o = (unsigned)((((tile * 4 + kq) * 3 + (i - 1)) * 8 + w) * 2048) + lane * 16;
    p[i - 1][0] = __builtin_amdgcn_raw_buffer_load_b128(rs, o, 0, 16);
    p[i - 1][1] = __builtin_amdgcn_raw_buffer_load_b128(rs, o + 1024, 0, 16);
  }
  const auto ors = __builtin_amdgcn_make_buffer_rsrc(out, 0, 4 << 20, 0x00020000);
#pragma unroll
  for (int h = 0; h < 2; ++h) {
    const auto x0 = __builtin_amdgcn_permlane16_swap(xr[h][0], xr[h][2], false, false), x1 = __builtin_amdgcn_permlane16_swap(xr[h][1], xr[h][3], false, false);
    const u32x4 xv = {x0[0], x1[0], x0[1], x1[1]};
    unsigned r[4];
#pragma unroll
    for (int d = 0; d < 4; ++d) {
      float lo = acc[0][2 * h + (d >> 1)][2 * (d & 1)] + __uint_as_float(xv[d] << 16), hi = acc[0][2 * h + (d >> 1)][2 * (d & 1) + 1] + __uint_as_float(xv[d] & 0xffff0000u);
#pragma unroll
      for (int i = 0; i < 3; ++i) { lo += __uint_as_float(p[i][h][d] << 16); hi += __uint_as_float(p[i][h][d] & 0xffff0000u); }
      r[d] = pk2(lo, hi);
    }
    const auto y0 = __builtin_amdgcn_permlane16_swap(r[0], r[2], false, false), y1 = __builtin_amdgcn_permlane16_swap(r[1], r[3], false, false);
    __builtin_amdgcn_raw_buffer_store_b128((u32x4){y0[0], y1[0], y0[1], y1[1]}, ors, orow + 64 * h, 0, OUT_AUX);
  }
  ST(6);
  ST(7);
  ST_OUT(1, 8);
}

extern "C" void kernel_launch(void* const* d_in, const int* in_sizes, int n_in, void* d_out, int out_size, void* d_ws,
                              size_t ws_size, hipStream_t stream) {
  const u16* x = (const u16*)d_in[0];
  const u16* wqkv = (const u16*)d_in[1];
  const u16* wo = (const u16*)d_in[2];
  const u16* wg = (const u16*)d_in[4];
  const u16* wu = (const u16*)d_in[5];
  const u16* wd = (const u16*)d_in[6];
  char* ws = (char*)d_ws;
  (void)hipFuncSetAttribute((const void*)psum_g1_kernel, hipFuncAttributeMaxDynamicSharedMemorySize, G1_LDS);
  (void)hipFuncSetAttribute((const void*)psum_g2_kernel, hipFuncAttributeMaxDynamicSharedMemorySize, G2_LDS);
  psum_ln_kernel<<<256, 320, 0, stream>>>(x, wqkv, wg, wu, ws);
  psum_g1_kernel<<<256, 512, G1_LDS, stream>>>(wqkv, wg, wu, wo, wd, ws);
  psum_g2_kernel<<<256, 512, G2_LDS, stream>>>(x, wo, wd, ws, (u16*)d_out);
}
```

```cpp
#define DIAG 10
#include <hip/hip_runtime.h>
#include <hip/hip_bf16.h>
#include <type_traits>
#define EARLY 1
#define HALO_SKIP 0
#define PF_W2 1
#define PF_LOOP 0
#define PF_R0 192
#define PF_PER 2
#define PF_LN_ROWS 96
#define PRIO_L 1
#define PRIO_M() __builtin_amdgcn_s_setprio(0)
#define PRIO_T() __builtin_amdgcn_s_setprio(1)
#define PRIO_E()
#define PF_CODE 0
#define PRECLEAN 0
#define OUT_AUX 16
#define G2ORD 0
#define EARLY2 1
#define CHAIN 1
#define H_AUX 16

namespace {

typedef unsigned short u16;
typedef short bf16x8 __attribute__((ext_vector_type(8)));
typedef short bf16x4 __attribute__((ext_vector_type(4)));
typedef float f32x4 __attribute__((ext_vector_type(4)));
typedef float f32x2 __attribute__((ext_vector_type(2)));
typedef unsigned u32x4 __attribute__((ext_vector_type(4)));
typedef unsigned u32x2 __attribute__((ext_vector_type(2)));
typedef __bf16 bf2 __attribute__((ext_vector_type(2)));
#define LAS __attribute__((address_space(3)))
#define GAS __attribute__((address_space(1)))

#define WAIT_V(n) asm volatile("s_waitcnt vmcnt(" #n ")" ::: "memory")
#define WAIT_V_IF0(n, c) asm volatile("s_cmp_lg_u32 %0, 0\n\ts_cbranch_scc1 1f\n\ts_waitcnt vmcnt(" #n ")\n1:" :: "s"(c) : "memory", "scc")
#define WAIT_L0() do { asm volatile("" ::: "memory"); __builtin_amdgcn_s_waitcnt(0xC07F); asm volatile("" ::: "memory"); } while (0)
#define BAR() __builtin_amdgcn_s_barrier()
#define SCHED() __builtin_amdgcn_sched_barrier(0)

__device__ __forceinline__ float b2f(u16 v) { return __uint_as_float(((unsigned)v) << 16); }
__device__ __forceinline__ unsigned pk2(float lo, float hi) {
  bf2 r = {(__bf16)lo, (__bf16)hi};
  return __builtin_bit_cast(unsigned, r);
}
__device__ __forceinline__ float rbf(float f) { return (float)(__bf16)f; }
__device__ __forceinline__ bf16x8 pack8(f32x4 a, f32x4 b) {
  u32x4 r = {pk2(a[0], a[1]), pk2(a[2], a[3]), pk2(b[0], b[1]), pk2(b[2], b[3])};
  return __builtin_bit_cast(bf16x8, r);
}
__device__ __forceinline__ bf16x4 trd(LAS const char* p) {
  return __builtin_amdgcn_ds_read_tr16_b64_v4i16((LAS bf16x4*)p);
}
__device__ __forceinline__ bf16x8 cat(bf16x4 a, bf16x4 b) { return __builtin_shufflevector(a, b, 0, 1, 2, 3, 4, 5, 6, 7); }
__device__ __forceinline__ f32x4 mfma(bf16x8 a, bf16x8 b, f32x4 c) {
  return __builtin_amdgcn_mfma_f32_16x16x32_bf16(a, b, c, 0, 0, 0);
}
__device__ __forceinline__ void dma_first(unsigned lds_dst, unsigned voff, const char* b0) {
  asm volatile("s_mov_b32 m0, %0\n\ts_nop 0\n\tglobal_load_lds_dwordx4 %1, %2" :: "s"(lds_dst), "v"(voff), "s"(b0) : "memory");
}
template <int OFF>
__device__ __forceinline__ void dma_next(unsigned voff, const char* b) {
  asm volatile("global_load_lds_dwordx4 %0, %1 offset:%2" :: "v"(voff), "s"(b), "i"(OFF) : "memory");
}
__device__ __forceinline__ void dma1m(unsigned lds_dst, const char* p, unsigned long long mask) {
  asm volatile("s_mov_b32 m0, %0\n\ts_mov_b64 exec, %2\n\t"
               "global_load_lds_dwordx4 %1, off\n\t"
               "s_mov_b64 exec, -1"
               :: "s"(lds_dst), "v"(p), "s"(mask) : "memory");
}
__device__ __forceinline__ void touch(unsigned lds_dst, const void* p) {
  asm volatile("s_mov_b32 m0, %0\n\ts_nop 0\n\tglobal_load_lds_dword %1, off" :: "s"(lds_dst), "v"(p) : "memory");
}
#define CODE_ADDR(dst, sym) do { unsigned lo_, hi_; asm volatile("s_getpc_b64 s[90:91]\n\ts_add_u32 %0, s90, " #sym "@rel32@lo+4\n\ts_addc_u32 %1, s91, " #sym "@rel32@hi+12" \
    : "=s"(lo_), "=s"(hi_) :: "s90", "s91", "scc"); dst = (const char*)(((unsigned long long)hi_ << 32) | lo_); } while (0)
constexpr int LN_LINES = 16, G1_LINES = 80, G2_LINES = 30;
__device__ __forceinline__ LAS char* lp(unsigned a) { return (LAS char*)(size_t)a; }
__device__ __forceinline__ float fmx(float a, float b) { return __builtin_elementwise_maximum(a, b); }
__device__ __forceinline__ float xmax(float v) {
  auto a = __builtin_amdgcn_permlane16_swap(__float_as_uint(v), __float_as_uint(v), false, false);
  v = fmaxf(__uint_as_float(a[0]), __uint_as_float(a[1]));
  auto b = __builtin_amdgcn_permlane32_swap(__float_as_uint(v), __float_as_uint(v), false, false);
  return fmaxf(__uint_as_float(b[0]), __uint_as_float(b[1]));
}
__device__ __forceinline__ float xsum(float v) {
  auto a = __builtin_amdgcn_permlane16_swap(__float_as_uint(v), __float_as_uint(v), false, false);
  v = __uint_as_float(a[0]) + __uint_as_float(a[1]);
  auto b = __builtin_amdgcn_permlane32_swap(__float_as_uint(v), __float_as_uint(v), false, false);
  return __uint_as_float(b[0]) + __uint_as_float(b[1]);
}

template <int CTRL>
__device__ __forceinline__ float dpp_add(float v) {
  return v + __uint_as_float(__builtin_amdgcn_update_dpp(0u, __float_as_uint(v), CTRL, 0xf, 0xf, true));
}
__device__ __forceinline__ float hsum(float v) {
  v = dpp_add<0xB1>(v);
  v = dpp_add<0x4E>(v);
  v = dpp_add<0x128>(v);
  return xsum(v);
}

#define DG(kid, slot) do { if (threadIdx.x == DGT) ((unsigned long long*)(ws + WS_ST))[((kid) * 256 + blockIdx.x) * 8 + (slot)] = __builtin_amdgcn_s_memrealtime(); } while (0)
#define ST_DECL
#define LT_DECL
#define LT(k)
#define ET(k)
#define ST(k)
#define ST_OUT(kid, n)
#define WS_BASE_MB 0
constexpr size_t WS_XN = (size_t)WS_BASE_MB << 20;
constexpr size_t WS_H = WS_XN + (size_t)4 * 1024 * 1024;
constexpr size_t H_BYTES = (size_t)8 * 4 * 36 * 16 * 1024;
constexpr size_t WS_X = WS_H + H_BYTES;
constexpr size_t X_BYTES = (size_t)64 * 4 * 3 * 8 * 2048;
constexpr size_t WS_CNT = WS_X + X_BYTES;
constexpr size_t WS_ST = WS_CNT + 131072;

}
extern "C" __global__ __launch_bounds__(320) void psum_ln_kernel(const u16* __restrict__ x, const u16* __restrict__ wqkv, const u16* __restrict__ wg,
                                                                 const u16* __restrict__ wu, char* __restrict__ ws) {
  const int lane = threadIdx.x & 63, b = blockIdx.x;
#define DGT 0
  DG(0, 0);
  if (threadIdx.x >= 256) {
    if (lane < 8) *(unsigned*)(ws + WS_CNT + (b * 8 + lane) * 64) = 0u;
    __shared__ __attribute__((aligned(16))) char sink[256];
    const int kq = (b & 7) >> 1, l = lane < 40 ? lane : lane - 40;
    const u16* p = l < 12 ? wqkv + (l >> 2) * 1024 + 256 * kq + (l & 3) * 64 : (l < 26 ? wg + 896 * kq + (l - 12) * 64 : wu + 896 * kq + (l - 26) * 64);
    const int ld = l < 12 ? 3072 : 3584;
#pragma unroll
    for (int i = 0; i < PF_LN_ROWS / 32; ++i) touch((unsigned)(size_t)(LAS char*)sink, p + (i * 32 + (b >> 3)) * ld);
    return;
  }
  const int hf = (b & 1) * 128 + (b >> 1);
  const int mb = hf >> 5, mf = (hf >> 1) & 15, r = (hf & 1) * 8 + (threadIdx.x >> 6) * 2 + ((lane >> 2) & 1);
  const int row = mb * 256 + 32 * (mf >> 1) + 2 * r + (mf & 1), kl = lane >> 3, ch = lane & 3;
  const u16* xr = x + row * 1024 + kl * 32 + ch * 8;
  u32x4 raw[4];
#pragma unroll
  for (int i = 0; i < 4; ++i) raw[i] = *(const u32x4*)(xr + 256 * i);
  float v[32];
#pragma unroll
  for (int i = 0; i < 16; ++i) { v[2 * i] = __uint_as_float(raw[i >> 2][i & 3] << 16); v[2 * i + 1] = __uint_as_float(raw[i >> 2][i & 3] & 0xffff0000u); }
  float s = 0.f;
#pragma unroll
  for (int i = 0; i < 32; ++i) s += v[i];
  const float mean = hsum(s) * (1.0f / 1024.0f);
  float q = 0.f;
#pragma unroll
  for (int i = 0; i < 32; ++i) { const float d = v[i] - mean; q += d * d; }
  q = hsum(q);
  const float mb16 = rbf(mean), den = rbf(sqrtf(rbf(rbf(q * (1.0f / 1024.0f)) + 1e-5f)));
  const float inv = 1.0f / den;
  const auto rs = __builtin_amdgcn_make_buffer_rsrc(ws + WS_XN, 0, 4 << 20, 0x00020000);
  const unsigned o = ((mb * 32 + kl) * 16 + mf) * 1024 + r * 64 + ((ch ^ (2 * (r >> 3))) * 16);
#pragma unroll
  for (int i = 0; i < 4; ++i) {
    u32x4 t;
#pragma unroll
    for (int e = 0; e < 4; ++e) t[e] = pk2(rbf(v[8 * i + 2 * e] - mb16) * inv, rbf(v[8 * i + 2 * e + 1] - mb16) * inv);
    __builtin_amdgcn_raw_buffer_store_b128(t, rs, o + i * 8 * 16384, 0, H_AUX);
  }
  WAIT_V(0);
  DG(0, 1);
}
namespace {
constexpr int G1_SA = 17 * 1024, G1_SB = 20 * 1024, G1_SLOT = G1_SA + G1_SB;
constexpr int T_SCR = 4 * G1_SLOT;
constexpr int T_BIAS = T_SCR + 256;
constexpr int G1_LDS = T_BIAS + 4096;

template <bool MAX>
__device__ __forceinline__ float tred(float a, float b, float c, float d) {
  const auto r1 = __builtin_amdgcn_permlane16_swap(__float_as_uint(a), __float_as_uint(b), false, false);
  const auto r2 = __builtin_amdgcn_permlane16_swap(__float_as_uint(c), __float_as_uint(d), false, false);
  const float x0 = __uint_as_float(r1[0]), x1 = __uint_as_float(r1[1]), y0 = __uint_as_float(r2[0]), y1 = __uint_as_float(r2[1]);
  const float t = MAX ? fmx(x0, x1) : x0 + x1, u = MAX ? fmx(y0, y1) : y0 + y1;
  const auto r3 = __builtin_amdgcn_permlane32_swap(__float_as_uint(t), __float_as_uint(u), false, false);
  const float z0 = __uint_as_float(r3[0]), z1 = __uint_as_float(r3[1]);
  return MAX ? fmx(z0, z1) : z0 + z1;
}
__device__ __forceinline__ u32x2 glu4(f32x4 g, f32x4 u) {
  f32x4 e = g * -1.4426950408889634f;
#pragma unroll
  for (int i = 0; i < 4; ++i) e[i] = __builtin_amdgcn_exp2f(e[i]);
  e = e + 1.0f;
#pragma unroll
  for (int i = 0; i < 4; ++i) e[i] = __builtin_amdgcn_rcpf(e[i]);
  e = e * u;
  return (u32x2){pk2(e[0], e[1]), pk2(e[2], e[3])};
}
__device__ __forceinline__ u32x4 cat4(u32x2 a, u32x2 b) { return (u32x4){a[0], a[1], b[0], b[1]}; }

}
extern "C" __global__ __launch_bounds__(512, 2) void psum_g1_kernel(const u16* __restrict__ wqkv,
                                                                    const u16* __restrict__ wg, const u16* __restrict__ wu,
                                                                    const u16* __restrict__ wo, const u16* __restrict__ wd,
                                                                    char* __restrict__ ws) {
  extern __shared__ __attribute__((aligned(16))) char smem[];
  LAS char* lds = (LAS char*)smem;
  ST_DECL LT_DECL ST(0);
  const unsigned long long dg0 = __builtin_amdgcn_s_memrealtime(); unsigned long long dg1, dg2;
  const int tid = threadIdx.x;
  int lane = tid & 63;
  const int w = __builtin_amdgcn_readfirstlane(tid >> 6), g = w >> 2, wi = w & 3;
  const int b = blockIdx.x, xcd = b & 7, idx = b >> 3;
  const int mb = (xcd & 1) * 4 + (idx & 3), kq = xcd >> 1, ntl = idx >> 2;
  const bool typeA = ntl < 4;
  const int j = ntl & 3;
  int fr = lane & 15, fq = lane >> 4;
  const int fA = 896 * kq + 64 * j;
  const int f0 = 896 * kq + 256 + 160 * j, odd = j & 1;
  const int fa = f0 + 32 * odd, fb = fa + 64, fc = odd ? f0 : f0 + 128;

  asm volatile("" : "+s"(wqkv), "+s"(wg), "+s"(wu), "+s"(ws));
  const bool isq = typeA && wi < 3;
  const int ld = isq ? 3072 : 3584;
  const u16* q1 = isq ? wqkv + (wi * 1024 + 256 * kq + 64 * j) : ((!typeA && (wi & 2)) ? wu : wg) + (typeA ? fA : ((wi & 1) ? fb : fa));
  const char* c1 = g ? ws + WS_XN + (size_t)(mb * 32 * 16384 + wi * 4096) : (const char*)q1;
  const int s1 = g ? 0 : 8 * ld - 1024, s2 = g ? 0 : 32 * ld - 2048, s3 = g ? 0 : 40 * ld - 3072;
  const int p1 = g ? 16384 : 64 * ld, p2 = g ? 16384 : 64 * 3584;
  const int d1 = (g ? 0 : G1_SA) + wi * 4096, d2 = g ? 16384 + wi * 256 : G1_SA + 16384 + wi * 1024;
  const unsigned long long m2 = g ? 0xffffull : ~0ull;
  const int rr = lane >> 3, pc = lane & 7, fi = (pc >> 1) ^ ((rr >> 1) & 3), kr = (rr & 3) + 8 * (rr >> 2);
  const unsigned v1 = g ? lane * 16 : kr * ld * 2 + fi * 32 + (pc & 1) * 16;
  const unsigned lb = (unsigned)(size_t)lds;
  unsigned ns = lb;
  dma_first(ns + d1, v1, c1); dma_next<1024>(v1, c1 + s1); dma_next<2048>(v1, c1 + s2); dma_next<3072>(v1, c1 + s3);
  const char* c2;
  {
    const int hr = wi * 4 + ((lane >> 2) & 3);
    const unsigned oh = (14 + (hr >> 3)) * 1024 + (8 + (hr & 7)) * 64 + (((lane & 3) ^ (2 * (hr >> 3)) ^ 2) * 16);
    const unsigned ow = (kr + 16 * (wi >> 1) + 4 * (wi & 1)) * 7168 + (typeA ? fi * 32 : (fi >> 1) * 32) + (pc & 1) * 16;
    const char* be = g ? ws + WS_XN + (size_t)((mb > 0 ? mb - 1 : 0) * 32 * 16384) : (const char*)(typeA ? wu + fA : wg + fc);
    const char* bo = g ? be : (const char*)(wu + (typeA ? fA : fc));
    c2 = ((fi & 1) ? bo : be) + (g ? oh : ow);
  }
  auto adv = [&]() { c1 += p1; c2 += p2; ns = (ns == lb + 3 * G1_SLOT) ? lb : ns + G1_SLOT; };
  auto stage = [&]() {
    dma_first(ns + d1, v1, c1); dma_next<1024>(v1, c1 + s1); dma_next<2048>(v1, c1 + s2); dma_next<3072>(v1, c1 + s3);
    dma1m(ns + d2, c2, m2);
    adv();
  };
  dma1m(ns + d2, c2, m2);
  adv();

  f32x4 acc[4][10];
  f32x4 hacc = {0.f, 0.f, 0.f, 0.f};
#pragma unroll
  for (int i = 0; i < 4; ++i)
#pragma unroll
    for (int n = 0; n < 10; ++n) { acc[i][n] = (f32x4){0.f, 0.f, 0.f, 0.f}; asm volatile("" : "+v"(acc[i][n])); }
  const int rl = 4 * (fq & 1) + (fr >> 2);
  unsigned hoffA = lb + 16384 + fr * 64 + ((fq ^ (2 * (fr >> 3))) * 16);
  unsigned aoff = hoffA - 16384 + wi * 4096;
  unsigned boff0 = lb + G1_SA + (fq >> 1) * 2048 + rl * 128 + 32 * ((2 * g) ^ ((rl >> 1) & 3)) + 8 * (lane & 3);
  unsigned boff1 = boff0 ^ 32;
  unsigned hoffB = (boff0 ^ (32 * (wi & 1))) + (1 + (wi >> 1)) * 4096;
  asm volatile("" : "+v"(aoff), "+v"(hoffA), "+v"(boff0), "+v"(boff1), "+v"(hoffB), "+v"(hacc));
  {
    const int lo = fr + (wi == 0 ? -7 : (wi == 2 ? 1 : 9));
    const int hi = wi == 0 ? fr + 1 : ((wi >= 2 && mb == 0) ? 0 : (wi == 2 ? 8 : 16));
    f32x4 bv;
#pragma unroll
    for (int e = 0; e < 4; ++e) bv[e] = (4 * fq + e >= lo && 4 * fq + e < hi) ? 0.f : -3.0e38f;
    *(LAS f32x4*)(lds + T_BIAS + wi * 1024 + lane * 16) = bv;
  }
  stage();
  ST(1);
  WAIT_V(5);
  BAR();
  dg1 = __builtin_amdgcn_s_memrealtime(); WAIT_L0();
  ST(2);
  if (g == 1) BAR();

  auto phase = [&](auto do_stage, int q) {
    constexpr bool DS = decltype(do_stage)::value;
    bf16x8 a[4], bfr[10], ha, hb;
    LT(0);
#define RB(c) bfr[2 * (c)] = cat(trd(lp(boff0) + (c) * 4096), trd(lp(boff0) + (c) * 4096 + 1024)); \
              bfr[2 * (c) + 1] = cat(trd(lp(boff1) + (c) * 4096), trd(lp(boff1) + (c) * 4096 + 1024));
    a[0] = *(const LAS bf16x8*)(lp(aoff)); a[1] = *(const LAS bf16x8*)(lp(aoff) + 1024);
    if (DS) dma_first(ns + d1, v1, c1);
    a[2] = *(const LAS bf16x8*)(lp(aoff) + 2048); a[3] = *(const LAS bf16x8*)(lp(aoff) + 3072);
    RB(0)
    if (DS) dma_next<1024>(v1, c1 + s1);
    RB(1)
    if (DS) dma_next<2048>(v1, c1 + s2);
    RB(2)
    if (DS) dma_next<3072>(v1, c1 + s3);
    RB(3)
    if (DS) dma1m(ns + d2, c2, m2);
    RB(4)
    { ha = *(const LAS bf16x8*)lp(hoffA); hb = cat(trd(lp(hoffB)), trd(lp(hoffB) + 1024)); }
#undef RB
    SCHED();
    LT(1);
    const int dlt = ((q & 3) == 3) ? -3 * G1_SLOT : G1_SLOT;
    aoff += dlt; hoffA += dlt; boff0 += dlt; boff1 += dlt; hoffB += dlt;
    if (DS) {
      adv();
      if (q == 1) stage();
      LT(2);
      if (q == 0) WAIT_V(5); else WAIT_V(10);
    } else {
      LT(2);
      if (q == 29) {
        WAIT_V(0);
        const int l = __builtin_amdgcn_mbcnt_hi(~0u, __builtin_amdgcn_mbcnt_lo(~0u, 0u));
        int t = w * 64 + l;
        t = t < 288 ? t : t - 288;
        const int rr = (t >> 4) * 64 + (xcd & 1) * 32 + idx;
        touch(lb + T_SCR, (rr < 256 ? wo + (256 * kq + rr) * 1024 : wd + (896 * kq + rr - 256) * 1024) + (t & 15) * 64);
      }
    }
    WAIT_L0();
    LT(3);
    SCHED();
    BAR();
    SCHED();
    LT(4);
    PRIO_M();
#pragma unroll
    for (int n = 0; n < 10; ++n)
#pragma unroll
      for (int i = 0; i < 4; ++i) {
        if (n == 9 && i == 4 - EARLY) { SCHED(); PRIO_T(); if (q < 31 || g == 0) BAR(); SCHED(); }
        acc[i][n] = mfma(bfr[n], a[i], acc[i][n]);
      }
    hacc = mfma(hb, ha, hacc);
    PRIO_E();
    SCHED();
    LT(5);
    SCHED();
  };
#pragma clang loop unroll(disable)
  for (int q = 0; q < 29; ++q) phase(std::true_type{}, q);
#pragma clang loop unroll(disable)
  for (int q = 29; q < 32; ++q) phase(std::false_type{}, q);

  dg2 = __builtin_amdgcn_s_memrealtime(); WAIT_L0();
  ST(3);
  asm volatile("" ::: "memory");
  if (g == 0) __builtin_amdgcn_s_setprio(0); else __builtin_amdgcn_s_setprio(1);
  lane = __builtin_amdgcn_mbcnt_hi(~0u, __builtin_amdgcn_mbcnt_lo(~0u, 0u));
  asm volatile("" : "+v"(lane));
  fr = lane & 15; fq = lane >> 4;
  const auto hrs = __builtin_amdgcn_make_buffer_rsrc(ws + WS_H + (size_t)(mb * 4 + kq) * 36 * 16384, 0, 36 * 16384, 0x00020000);
  const unsigned hv = wi * 4096 + lane * 16;
#define GLU(i, a, b) glu4(acc[i][a], acc[i][b])
  if (!typeA) {
    const int kba = 8 + ((fa - 896 * kq) >> 5) + g, kbc = 8 + ((fc - 896 * kq) >> 5);
#pragma unroll
    for (int i = 0; i < 4; ++i) {
      __builtin_amdgcn_raw_buffer_store_b128(cat4(GLU(i, 0, 4), GLU(i, 1, 5)), hrs, hv + kba * 16384 + i * 1024, 0, H_AUX);
      __builtin_amdgcn_raw_buffer_store_b128(cat4(GLU(i, 2, 6), GLU(i, 3, 7)), hrs, hv + kba * 16384 + 32768 + i * 1024, 0, H_AUX);
      __builtin_amdgcn_raw_buffer_store_b64(GLU(i, 8, 9), hrs, hv + kbc * 16384 + g * 8 + i * 1024, 0, H_AUX);
    }
  } else {
    LAS char* kim = lds + g * 40960;
    LAS char* vim = kim + 17 * 1024;
    bf16x8 kf[4], qf[4];
#define FIX(i, n) acc[i][n]
#pragma unroll
    for (int i = 0; i < 4; ++i) {
      kf[i] = pack8(FIX(i, 2), FIX(i, 3));
      *(LAS bf16x8*)(vim + ((wi * 4 + i) * 16 + fr) * 80 + fq * 16) = pack8(FIX(i, 4), FIX(i, 5));
    }
    *(LAS bf16x8*)(kim + (wi * 4 + 2) * 1024 + lane * 16) = kf[2];
    *(LAS bf16x8*)(kim + (wi * 4 + 3) * 1024 + lane * 16) = kf[3];
    {
      const f32x4 hh = hacc;
      LAS char* hp = (wi < 2) ? (kim + 16 * 1024 + lane * 16 + wi * 8) : (vim + (256 + fr) * 80 + fq * 16 + (wi - 2) * 8);
      *(LAS u32x2*)hp = (u32x2){pk2(hh[0], hh[1]), pk2(hh[2], hh[3])};
    }
    auto glu_part = [&]() {
#pragma unroll
      for (int i = 0; i < 4; ++i)
        __builtin_amdgcn_raw_buffer_store_b128(cat4(GLU(i, 6, 8), GLU(i, 7, 9)), hrs, hv + (8 + 2 * j + g) * 16384 + i * 1024, 0, H_AUX);
    };
    WAIT_L0();
    ET(0);
    ET(1);
    BAR();
    ET(2);
    asm volatile("" ::: "memory");
    const bool halo = wi == 0;
    const f32x4 bc = *(const LAS f32x4*)(lds + T_BIAS + lane * 16), bp = *(const LAS f32x4*)(lds + T_BIAS + 1024 + lane * 16);
    const f32x4 bp0 = *(const LAS f32x4*)(lds + T_BIAS + (halo ? 2048 : 1024) + lane * 16);
    const f32x4 bp1 = *(const LAS f32x4*)(lds + T_BIAS + (halo ? 3072 : 1024) + lane * 16);
    const int pf0 = halo ? 16 : wi * 4 - 2, pf1 = halo ? 16 : wi * 4 - 1;
    const bf16x8 kp0 = *(const LAS bf16x8*)(kim + pf0 * 1024 + lane * 16);
    const bf16x8 kp1 = *(const LAS bf16x8*)(kim + pf1 * 1024 + lane * 16);
    const LAS char* vb = vim + (4 * fq + (fr >> 2)) * 80 + (lane & 3) * 16;
    bf16x4 vt[6][2];
#pragma unroll
    for (int d = 0; d < 2; ++d) {
      vt[0][d] = trd(vb + pf0 * 1280 + d * 8); vt[1][d] = trd(vb + pf1 * 1280 + d * 8);
#pragma unroll
      for (int i = 0; i < 4; ++i) vt[2 + i][d] = trd(vb + (wi * 4 + i) * 1280 + d * 8);
    }
    f32x4 sp[4], sc[4];
#pragma unroll
    for (int i = 0; i < 4; ++i) {
      qf[i] = pack8(acc[i][0], acc[i][1]);
      sc[i] = mfma(kf[i], qf[i], bc);
    }
    sp[2] = mfma(kf[0], qf[2], bp); sp[3] = mfma(kf[1], qf[3], bp);
    sp[0] = mfma(kp0, qf[0], bp0); sp[1] = mfma(kp1, qf[1], bp1);
    const float sl = 0.17677669529663687f * 1.4426950408889634f;
    const f32x4 z4 = {0.f, 0.f, 0.f, 0.f};
    float m[4];
#pragma unroll
    for (int i = 0; i < 4; ++i)
      m[i] = fmx(fmx(fmx(fmx(sp[i][0], sp[i][1]), sp[i][2]), fmx(fmx(sp[i][3], sc[i][0]), sc[i][1])), fmx(sc[i][2], sc[i][3]));
    {
      const unsigned t = __float_as_uint(tred<true>(m[0], m[1], m[2], m[3]));
      const auto u = __builtin_amdgcn_permlane32_swap(t, t, false, false);
      const auto u0 = __builtin_amdgcn_permlane16_swap(u[0], u[0], false, false), u1 = __builtin_amdgcn_permlane16_swap(u[1], u[1], false, false);
      m[0] = __uint_as_float(u0[0]); m[1] = __uint_as_float(u0[1]); m[2] = __uint_as_float(u1[0]); m[3] = __uint_as_float(u1[1]);
    }
    const u32x4 one4 = {0x3f803f80u, 0x3f803f80u, 0x3f803f80u, 0x3f803f80u};
#pragma unroll
    for (int i = 0; i < 4; ++i) {
      const float mm = -sl * m[i];
#pragma unroll
      for (int e = 0; e < 4; ++e) {
        sp[i][e] = __builtin_amdgcn_exp2f(__builtin_fmaf(sp[i][e], sl, mm));
        sc[i][e] = __builtin_amdgcn_exp2f(__builtin_fmaf(sc[i][e], sl, mm));
      }
      const bf16x8 pfr = pack8(sp[i], sc[i]);
      const f32x4 sm = mfma(__builtin_bit_cast(bf16x8, one4), pfr, z4);
      const f32x4 o0 = mfma(cat(vt[i][0], vt[2 + i][0]), pfr, z4);
      const f32x4 o1 = mfma(cat(vt[i][1], vt[2 + i][1]), pfr, z4);
      const float inv = __builtin_amdgcn_rcpf(sm[0]);
      __builtin_amdgcn_raw_buffer_store_b128(__builtin_bit_cast(u32x4, pack8(o0 * inv, o1 * inv)), hrs, hv + (2 * j + g) * 16384 + i * 1024, 0, H_AUX);
    }
    glu_part();
    ET(5);
  }
  ST(4);
  WAIT_V(0);
  if (w == 0 && lane == 0) { unsigned long long* o = (unsigned long long*)(ws + WS_ST) + (256 + blockIdx.x) * 8; o[0] = dg0; o[1] = dg1; o[2] = dg2; o[3] = __builtin_amdgcn_s_memrealtime(); }
  ST(5);
  ST_OUT(0, 6);
}

constexpr int G2_SA = 32768, G2_SB = 16384, G2_SLOT = G2_SA + G2_SB, G2_LDS = 3 * G2_SLOT;

#define PREW 0
extern "C" __global__ __launch_bounds__(512 + 64 * PREW, 2 + PREW) void psum_g2_kernel(const u16* __restrict__ x, const u16* __restrict__ wo,
                                                       const u16* __restrict__ wd, char* __restrict__ ws,
                                                       u16* __restrict__ out) {
  extern __shared__ __attribute__((aligned(16))) char smem[];
  LAS char* lds = (LAS char*)smem;
  ST_DECL LT_DECL ST(0);
  const unsigned long long dg0 = __builtin_amdgcn_s_memrealtime(); unsigned long long dg1, dg2;
  const int tid = threadIdx.x, lane = tid & 63;
  const int w = __builtin_amdgcn_readfirstlane(tid >> 6), g = w >> 2, wi = w & 3;
  const int b = blockIdx.x, xcd = b & 7, idx = b >> 3;
  const int mb = (xcd & 1) * 4 + (idx & 3), kq = xcd >> 1, nt = idx >> 2;
  const int fr = lane & 15, fq = lane >> 4;

  const char* ca = ws + WS_H + (size_t)(mb * 4 + kq) * 36 * 16384 + w * 4096;
  const unsigned va = lane * 16;
  const int e0 = (0 ^ kq) * 1024, e1 = ((1 ^ kq) - 1) * 1024, e2 = ((2 ^ kq) - 2) * 1024, e3 = ((3 ^ kq) - 3) * 1024;
  const int rr = lane >> 3, pc = lane & 7;
  const unsigned vb = rr * 2048 + (((pc >> 1) ^ ((rr >> 1) & 3)) * 32) + (pc & 1) * 16;
  const char* cb = (const char*)(wo + (size_t)(256 * kq + 8 * w) * 1024 + nt * 128);
  const char* cb2 = (const char*)(wd + (size_t)(896 * kq + 8 * w) * 1024 + nt * 128);
  int st = 0;
  const unsigned lb = (unsigned)(size_t)lds;
  unsigned ns = lb;
  auto adv = [&]() { ca += 32768; ++st; cb = (st == 4) ? cb2 : cb + 64 * 2048; ns = (ns == lb + 2 * G2_SLOT) ? lb : ns + G2_SLOT; };
  auto stage = [&]() {
    dma_first(ns + w * 4096, va, ca + e0); dma_next<1024>(va, ca + e1); dma_next<2048>(va, ca + e2); dma_next<3072>(va, ca + e3);
    dma_first(ns + G2_SA + w * 2048, vb, cb); dma_next<1024>(vb, cb - 896);
    adv();
  };
  stage();
  BAR();
  stage();
  const int mf = wi * 4 + kq;
  const unsigned orow = ((mb * 256 + 32 * (mf >> 1) + 2 * fr + (mf & 1)) * 1024 + nt * 128 + g * 64 + 16 * (fq & 1) + 8 * (fq >> 1)) * 2;
  u32x4 xr[2] = {{0u, 0u, 0u, 0u}, {0u, 0u, 0u, 0u}};
  const int rl = 4 * (fq & 1) + ((lane & 15) >> 2);
  unsigned aoff = lb + wi * 4096 + lane * 16;
  unsigned boff = lb + G2_SA + (fq >> 1) * 2048 + g * 1024 + rl * 128 + 32 * ((rl >> 1) & 3) + 8 * (lane & 3);

  f32x4 acc[4][4];
#pragma unroll
  for (int i = 0; i < 4; ++i)
#pragma unroll
    for (int n = 0; n < 4; ++n) { acc[i][n] = (f32x4){0.f, 0.f, 0.f, 0.f}; asm volatile("" : "+v"(acc[i][n])); }
  ST(1);
  WAIT_V(6);
  BAR();
  dg1 = __builtin_amdgcn_s_memrealtime(); WAIT_L0();
  ST(2);
  if (g == 1) BAR();

  int sl = 0;
  auto phase = [&](auto do_stage, int q) {
    constexpr bool DS = decltype(do_stage)::value;
    bf16x8 a[2][4], bfr[2][4];
    LT(0);
#define RA(kk, i) a[kk][i] = *(const LAS bf16x8*)(lp(aoff) + (kk) * 16384 + (i) * 1024);
#define RB(kk, n) bfr[kk][n] = cat(trd(lp(boff ^ ((n) * 32)) + (kk) * 8192), trd(lp(boff ^ ((n) * 32)) + (kk) * 8192 + 4096));
#define D0 if (DS) dma_first(ns + w * 4096, va, ca + e0);
#define D1 if (DS) dma_next<1024>(va, ca + e1);
#define D2 if (DS) dma_next<2048>(va, ca + e2);
#define D3 if (DS) dma_next<3072>(va, ca + e3);
#define D4 if (DS) dma_first(ns + G2_SA + w * 2048, vb, cb);
#define D5 if (DS) dma_next<1024>(vb, cb - 896);
#define UPD sl = (sl == 2) ? 0 : sl + 1; dlt = (sl == 0) ? -2 * G2_SLOT : G2_SLOT; if (DS) adv(); \
    asm volatile("" : "+s"(ca), "+s"(cb), "+s"(ns), "+s"(sl), "+s"(dlt) :: "memory");
    int dlt;
    if (!DS && q == 16)
      asm volatile("global_load_dwordx4 %0, %2, %3\n\tglobal_load_dwordx4 %1, %2, %3 offset:64" : "+v"(xr[0]), "+v"(xr[1]) : "v"(orow), "s"(x) : "memory");
    RA(0, 0) RA(0, 1) D0 RA(0, 2) RA(0, 3) RB(0, 0) D1 RB(0, 1) RB(0, 2) RB(0, 3) D2 RA(1, 0) RA(1, 1) RA(1, 2) D3 RA(1, 3) RB(1, 0) D4 RB(1, 1) D5 UPD RB(1, 2) RB(1, 3)
    aoff += dlt;
#undef RA
#undef RB
    boff += dlt;
    SCHED();
    LT(1);
    LT(2);
    asm volatile("" : "+v"(boff), "+v"(aoff));
    if (DS) WAIT_V_IF0(6, g ^ 1); else WAIT_V_IF0(2, g ^ 1);
    WAIT_L0();
    LT(3);
    SCHED();
    BAR();
    SCHED();
    LT(4);
    PRIO_M();
#pragma unroll
    for (int n = 0; n < 4; ++n)
#pragma unroll
      for (int i = 0; i < 4; ++i) {
        acc[i][n] = mfma(bfr[0][n], a[0][i], acc[i][n]);
        SCHED();
        if (n == 3 && i == 3) { PRIO_T(); if (DS) WAIT_V_IF0(6, g); else WAIT_V_IF0(2, g); if (q < 17 || g == 0) BAR(); SCHED(); }
        acc[i][n] = mfma(bfr[1][n], a[1][i], acc[i][n]);
        SCHED();
      }
    PRIO_E();
    SCHED();
    LT(5);
    SCHED();
  };
#pragma clang loop unroll(disable)
  for (int q = 0; q < 16; ++q) phase(std::true_type{}, q);
#pragma clang loop unroll(disable)
  for (int q = 16; q < 18; ++q) phase(std::false_type{}, q);

  dg2 = __builtin_amdgcn_s_memrealtime(); WAIT_L0();
  ST(3);
  const int tile = mb * 8 + nt;
  const auto rs = __builtin_amdgcn_make_buffer_rsrc(ws + WS_X, 0, (int)X_BYTES, 0x00020000);
#pragma unroll
  for (int i = 1; i < 4; ++i) {
    const unsigned o = (unsigned)((((tile * 4 + (kq ^ i)) * 3 + (i - 1)) * 8 + w) * 2048) + lane * 16;
    __builtin_amdgcn_raw_buffer_store_b128(__builtin_bit_cast(u32x4, pack8(acc[i][0], acc[i][1])), rs, o, 0, 16);
    __builtin_amdgcn_raw_buffer_store_b128(__builtin_bit_cast(u32x4, pack8(acc[i][2], acc[i][3])), rs, o + 1024, 0, 16);
  }
  WAIT_V(0);
  ST(4);
  asm volatile("" : "+v"(xr[0]), "+v"(xr[1]));
  GAS unsigned* cnt = (GAS unsigned*)(ws + WS_CNT) + (tile * 32 + w) * 16;
  if (lane >= 1 && lane < 4) __hip_atomic_fetch_add(cnt + (kq ^ lane) * 128, 1u, __ATOMIC_RELAXED, __HIP_MEMORY_SCOPE_AGENT);
  {
    unsigned spins = 0;
    while ((unsigned)__builtin_amdgcn_readfirstlane(__hip_atomic_load(cnt + kq * 128, __ATOMIC_RELAXED, __HIP_MEMORY_SCOPE_AGENT)) < 3u) {
      __builtin_amdgcn_s_sleep(2);
      if (++spins > (1u << 22)) break;
    }
  }
  asm volatile("" ::: "memory");
  ST(5);
  u32x4 p[3][2];
#pragma unroll
  for (int i = 1; i < 4; ++i) {
    const unsigned o = (unsigned)((((tile * 4 + kq) * 3 + (i - 1)) * 8 + w) * 2048) + lane * 16;
    p[i - 1][0] = __builtin_amdgcn_raw_buffer_load_b128(rs, o, 0, 16);
    p[i - 1][1] = __builtin_amdgcn_raw_buffer_load_b128(rs, o + 1024, 0, 16);
  }
  const auto ors = __builtin_amdgcn_make_buffer_rsrc(out, 0, 4 << 20, 0x00020000);
#pragma unroll
  for (int h = 0; h < 2; ++h) {
    const auto x0 = __builtin_amdgcn_permlane16_swap(xr[h][0], xr[h][2], false, false), x1 = __builtin_amdgcn_permlane16_swap(xr[h][1], xr[h][3], false, false);
    const u32x4 xv = {x0[0], x1[0], x0[1], x1[1]};
    unsigned r[4];
#pragma unroll
    for (int d = 0; d < 4; ++d) {
      float lo = acc[0][2 * h + (d >> 1)][2 * (d & 1)] + __uint_as_float(xv[d] << 16), hi = acc[0][2 * h + (d >> 1)][2 * (d & 1) + 1] + __uint_as_float(xv[d] & 0xffff0000u);
#pragma unroll
      for (int i = 0; i < 3; ++i) { lo += __uint_as_float(p[i][h][d] << 16); hi += __uint_as_float(p[i][h][d] & 0xffff0000u); }
      r[d] = pk2(lo, hi);
    }
    const auto y0 = __builtin_amdgcn_permlane16_swap(r[0], r[2], false, false), y1 = __builtin_amdgcn_permlane16_swap(r[1], r[3], false, false);
    __builtin_amdgcn_raw_buffer_store_b128((u32x4){y0[0], y1[0], y0[1], y1[1]}, ors, orow + 64 * h, 0, OUT_AUX);
  }
  ST(6);
  if (b == 0 && w == 7) {
    WAIT_V(0);
    const unsigned long long dg3 = __builtin_amdgcn_s_memrealtime();
    const unsigned long long* s = (const unsigned long long*)(ws + WS_ST);
    unsigned long long l0 = ~0ull, l1 = 0, a0 = ~0ull, a1 = 0, a2 = 0, a3 = 0;
    for (int i = lane; i < 256; i += 64) {
      l0 = min(l0, s[i * 8]); l1 = max(l1, s[i * 8 + 1]);
      a0 = min(a0, s[(256 + i) * 8]); a1 = max(a1, s[(256 + i) * 8 + 1]); a2 = max(a2, s[(256 + i) * 8 + 2]); a3 = max(a3, s[(256 + i) * 8 + 3]);
    }
    for (int o = 1; o < 64; o <<= 1) {
      l0 = min(l0, (unsigned long long)__shfl_xor((long long)l0, o)); l1 = max(l1, (unsigned long long)__shfl_xor((long long)l1, o));
      a0 = min(a0, (unsigned long long)__shfl_xor((long long)a0, o)); a1 = max(a1, (unsigned long long)__shfl_xor((long long)a1, o));
      a2 = max(a2, (unsigned long long)__shfl_xor((long long)a2, o)); a3 = max(a3, (unsigned long long)__shfl_xor((long long)a3, o));
    }
    unsigned long long q = DIAG == 1 ? a0 - l0 : DIAG == 2 ? a1 - a0 : DIAG == 3 ? a2 - a0 : DIAG == 4 ? dg0 - a2 : DIAG == 5 ? dg2 - dg0 : DIAG == 6 ? dg3 - dg2 : DIAG == 7 ? dg3 - l0 : DIAG == 8 ? l1 - l0 : DIAG == 9 ? a3 - a2 : 0;
    if (q > 20000) q = 20000;
    while (__builtin_amdgcn_s_memrealtime() < dg3 + 500 + 4 * q) __builtin_amdgcn_s_sleep(1);
  }
  ST(7);
  ST_OUT(1, 8);
}

extern "C" void kernel_launch(void* const* d_in, const int* in_sizes, int n_in, void* d_out, int out_size, void* d_ws,
                              size_t ws_size, hipStream_t stream) {
  const u16* x = (const u16*)d_in[0];
  const u16* wqkv = (const u16*)d_in[1];
  const u16* wo = (const u16*)d_in[2];
  const u16* wg = (const u16*)d_in[4];
  const u16* wu = (const u16*)d_in[5];
  const u16* wd = (const u16*)d_in[6];
  char* ws = (char*)d_ws;
  (void)hipFuncSetAttribute((const void*)psum_g1_kernel, hipFuncAttributeMaxDynamicSharedMemorySize, G1_LDS);
  (void)hipFuncSetAttribute((const void*)psum_g2_kernel, hipFuncAttributeMaxDynamicSharedMemorySize, G2_LDS);
  psum_ln_kernel<<<256, 320, 0, stream>>>(x, wqkv, wg, wu, ws);
  psum_g1_kernel<<<256, 512, G1_LDS, stream>>>(wqkv, wg, wu, wo, wd, ws);
  psum_g2_kernel<<<256, 512 + 64 * PREW, G2_LDS, stream>>>(x, wo, wd, ws, (u16*)d_out);
}
```

```cpp
#include <hip/hip_runtime.h>
#include <hip/hip_bf16.h>
#include <type_traits>
#define EARLY 1
#define HALO_SKIP 0
#define PF_W2 1
#define PF_LOOP 0
#define PF_R0 192
#define PF_PER 2
#define PF_LN_ROWS 96
#define PRIO_L 1
#define PRIO_M() __builtin_amdgcn_s_setprio(0)
#define PRIO_T() __builtin_amdgcn_s_setprio(1)
#define PRIO_E()
#define PF_CODE 0
#define PRECLEAN 0
#define OUT_AUX 16
#define G2ORD 0
#define EARLY2 1
#define CHAIN 1
#define PF_X 0
#define H_AUX 16

namespace {

typedef unsigned short u16;
typedef short bf16x8 __attribute__((ext_vector_type(8)));
typedef short bf16x4 __attribute__((ext_vector_type(4)));
typedef float f32x4 __attribute__((ext_vector_type(4)));
typedef float f32x2 __attribute__((ext_vector_type(2)));
typedef unsigned u32x4 __attribute__((ext_vector_type(4)));
typedef unsigned u32x2 __attribute__((ext_vector_type(2)));
typedef __bf16 bf2 __attribute__((ext_vector_type(2)));
#define LAS __attribute__((address_space(3)))
#define GAS __attribute__((address_space(1)))

#define WAIT_V(n) asm volatile("s_waitcnt vmcnt(" #n ")" ::: "memory")
#define WAIT_V_IF0(n, c) asm volatile("s_cmp_lg_u32 %0, 0\n\ts_cbranch_scc1 1f\n\ts_waitcnt vmcnt(" #n ")\n1:" :: "s"(c) : "memory", "scc")
#define WAIT_L0() do { asm volatile("" ::: "memory"); __builtin_amdgcn_s_waitcnt(0xC07F); asm volatile("" ::: "memory"); } while (0)
#define BAR() __builtin_amdgcn_s_barrier()
#define SCHED() __builtin_amdgcn_sched_barrier(0)

__device__ __forceinline__ float b2f(u16 v) { return __uint_as_float(((unsigned)v) << 16); }
__device__ __forceinline__ unsigned pk2(float lo, float hi) {
  bf2 r = {(__bf16)lo, (__bf16)hi};
  return __builtin_bit_cast(unsigned, r);
}
__device__ __forceinline__ float rbf(float f) { return (float)(__bf16)f; }
__device__ __forceinline__ bf16x8 pack8(f32x4 a, f32x4 b) {
  u32x4 r = {pk2(a[0], a[1]), pk2(a[2], a[3]), pk2(b[0], b[1]), pk2(b[2], b[3])};
  return __builtin_bit_cast(bf16x8, r);
}
__device__ __forceinline__ bf16x4 trd(LAS const char* p) {
  return __builtin_amdgcn_ds_read_tr16_b64_v4i16((LAS bf16x4*)p);
}
__device__ __forceinline__ bf16x8 cat(bf16x4 a, bf16x4 b) { return __builtin_shufflevector(a, b, 0, 1, 2, 3, 4, 5, 6, 7); }
__device__ __forceinline__ f32x4 mfma(bf16x8 a, bf16x8 b, f32x4 c) {
  return __builtin_amdgcn_mfma_f32_16x16x32_bf16(a, b, c, 0, 0, 0);
}
__device__ __forceinline__ void dma_first(unsigned lds_dst, unsigned voff, const char* b0) {
  asm volatile("s_mov_b32 m0, %0\n\ts_nop 0\n\tglobal_load_lds_dwordx4 %1, %2" :: "s"(lds_dst), "v"(voff), "s"(b0) : "memory");
}
template <int OFF>
__device__ __forceinline__ void dma_next(unsigned voff, const char* b) {
  asm volatile("global_load_lds_dwordx4 %0, %1 offset:%2" :: "v"(voff), "s"(b), "i"(OFF) : "memory");
}
__device__ __forceinline__ void dma1m(unsigned lds_dst, const char* p, unsigned long long mask) {
  asm volatile("s_mov_b32 m0, %0\n\ts_mov_b64 exec, %2\n\t"
               "global_load_lds_dwordx4 %1, off\n\t"
               "s_mov_b64 exec, -1"
               :: "s"(lds_dst), "v"(p), "s"(mask) : "memory");
}
__device__ __forceinline__ void touch(unsigned lds_dst, const void* p) {
  asm volatile("s_mov_b32 m0, %0\n\ts_nop 0\n\tglobal_load_lds_dword %1, off" :: "s"(lds_dst), "v"(p) : "memory");
}
#define CODE_ADDR(dst, sym) do { unsigned lo_, hi_; asm volatile("s_getpc_b64 s[90:91]\n\ts_add_u32 %0, s90, " #sym "@rel32@lo+4\n\ts_addc_u32 %1, s91, " #sym "@rel32@hi+12" \
    : "=s"(lo_), "=s"(hi_) :: "s90", "s91", "scc"); dst = (const char*)(((unsigned long long)hi_ << 32) | lo_); } while (0)
constexpr int LN_LINES = 16, G1_LINES = 80, G2_LINES = 30;
__device__ __forceinline__ LAS char* lp(unsigned a) { return (LAS char*)(size_t)a; }
__device__ __forceinline__ float fmx(float a, float b) { return __builtin_elementwise_maximum(a, b); }
__device__ __forceinline__ float xmax(float v) {
  auto a = __builtin_amdgcn_permlane16_swap(__float_as_uint(v), __float_as_uint(v), false, false);
  v = fmaxf(__uint_as_float(a[0]), __uint_as_float(a[1]));
  auto b = __builtin_amdgcn_permlane32_swap(__float_as_uint(v), __float_as_uint(v), false, false);
  return fmaxf(__uint_as_float(b[0]), __uint_as_float(b[1]));
}
__device__ __forceinline__ float xsum(float v) {
  auto a = __builtin_amdgcn_permlane16_swap(__float_as_uint(v), __float_as_uint(v), false, false);
  v = __uint_as_float(a[0]) + __uint_as_float(a[1]);
  auto b = __builtin_amdgcn_permlane32_swap(__float_as_uint(v), __float_as_uint(v), false, false);
  return __uint_as_float(b[0]) + __uint_as_float(b[1]);
}

template <int CTRL>
__device__ __forceinline__ float dpp_add(float v) {
  return v + __uint_as_float(__builtin_amdgcn_update_dpp(0u, __float_as_uint(v), CTRL, 0xf, 0xf, true));
}
__device__ __forceinline__ float hsum(float v) {
  v = dpp_add<0xB1>(v);
  v = dpp_add<0x4E>(v);
  v = dpp_add<0x128>(v);
  return xsum(v);
}

#define DG(kid, slot)
#define ST_DECL
#define LT_DECL
#define LT(k)
#define ET(k)
#define ST(k)
#define ST_OUT(kid, n)
#define WS_BASE_MB 0
constexpr size_t WS_XN = (size_t)WS_BASE_MB << 20;
constexpr size_t WS_H = WS_XN + (size_t)4 * 1024 * 1024;
constexpr size_t H_BYTES = (size_t)8 * 4 * 36 * 16 * 1024;
constexpr size_t WS_X = WS_H + H_BYTES;
constexpr size_t X_BYTES = (size_t)64 * 4 * 3 * 8 * 2048;
constexpr size_t WS_CNT = WS_X + X_BYTES;
constexpr size_t WS_ST = WS_CNT + 131072;

}
extern "C" __global__ __launch_bounds__(320) void psum_ln_kernel(const u16* __restrict__ x, const u16* __restrict__ wqkv, const u16* __restrict__ wg,
                                                                 const u16* __restrict__ wu, char* __restrict__ ws) {
  const int lane = threadIdx.x & 63, b = blockIdx.x;
#define DGT 0
  DG(0, 0);
  if (threadIdx.x >= 256) {
    if (lane < 8) *(unsigned*)(ws + WS_CNT + (b * 8 + lane) * 64) = 0u;
    __shared__ __attribute__((aligned(16))) char sink[256];
    const int kq = (b & 7) >> 1, l = lane < 40 ? lane : lane - 40;
    const u16* p = l < 12 ? wqkv + (l >> 2) * 1024 + 256 * kq + (l & 3) * 64 : (l < 26 ? wg + 896 * kq + (l - 12) * 64 : wu + 896 * kq + (l - 26) * 64);
    const int ld = l < 12 ? 3072 : 3584;
#pragma unroll
    for (int i = 0; i < PF_LN_ROWS / 32; ++i) touch((unsigned)(size_t)(LAS char*)sink, p + (i * 32 + (b >> 3)) * ld);
    return;
  }
  const int hf = (b & 1) * 128 + (b >> 1);
  const int mb = hf >> 5, mf = (hf >> 1) & 15, r = (hf & 1) * 8 + (threadIdx.x >> 6) * 2 + ((lane >> 2) & 1);
  const int row = mb * 256 + 32 * (mf >> 1) + 2 * r + (mf & 1), kl = lane >> 3, ch = lane & 3;
  const u16* xr = x + row * 1024 + kl * 32 + ch * 8;
  u32x4 raw[4];
#pragma unroll
  for (int i = 0; i < 4; ++i) raw[i] = *(const u32x4*)(xr + 256 * i);
  float v[32];
#pragma unroll
  for (int i = 0; i < 16; ++i) { v[2 * i] = __uint_as_float(raw[i >> 2][i & 3] << 16); v[2 * i + 1] = __uint_as_float(raw[i >> 2][i & 3] & 0xffff0000u); }
  float s = 0.f;
#pragma unroll
  for (int i = 0; i < 32; ++i) s += v[i];
  const float mean = hsum(s) * (1.0f / 1024.0f);
  float q = 0.f;
#pragma unroll
  for (int i = 0; i < 32; ++i) { const float d = v[i] - mean; q += d * d; }
  q = hsum(q);
  const float mb16 = rbf(mean), den = rbf(sqrtf(rbf(rbf(q * (1.0f / 1024.0f)) + 1e-5f)));
  const float inv = 1.0f / den;
  const auto rs = __builtin_amdgcn_make_buffer_rsrc(ws + WS_XN, 0, 4 << 20, 0x00020000);
  const unsigned o = ((mb * 32 + kl) * 16 + mf) * 1024 + r * 64 + ((ch ^ (2 * (r >> 3))) * 16);
#pragma unroll
  for (int i = 0; i < 4; ++i) {
    u32x4 t;
#pragma unroll
    for (int e = 0; e < 4; ++e) t[e] = pk2(rbf(v[8 * i + 2 * e] - mb16) * inv, rbf(v[8 * i + 2 * e + 1] - mb16) * inv);
    __builtin_amdgcn_raw_buffer_store_b128(t, rs, o + i * 8 * 16384, 0, H_AUX);
  }
  DG(0, 1);
}
namespace {
constexpr int G1_SA = 17 * 1024, G1_SB = 20 * 1024, G1_SLOT = G1_SA + G1_SB;
constexpr int T_SCR = 4 * G1_SLOT;
constexpr int T_BIAS = T_SCR + 256;
constexpr int G1_LDS = T_BIAS + 4096;

template <bool MAX>
__device__ __forceinline__ float tred(float a, float b, float c, float d) {
  const auto r1 = __builtin_amdgcn_permlane16_swap(__float_as_uint(a), __float_as_uint(b), false, false);
  const auto r2 = __builtin_amdgcn_permlane16_swap(__float_as_uint(c), __float_as_uint(d), false, false);
  const float x0 = __uint_as_float(r1[0]), x1 = __uint_as_float(r1[1]), y0 = __uint_as_float(r2[0]), y1 = __uint_as_float(r2[1]);
  const float t = MAX ? fmx(x0, x1) : x0 + x1, u = MAX ? fmx(y0, y1) : y0 + y1;
  const auto r3 = __builtin_amdgcn_permlane32_swap(__float_as_uint(t), __float_as_uint(u), false, false);
  const float z0 = __uint_as_float(r3[0]), z1 = __uint_as_float(r3[1]);
  return MAX ? fmx(z0, z1) : z0 + z1;
}
__device__ __forceinline__ u32x2 glu4(f32x4 g, f32x4 u) {
  f32x4 e = g * -1.4426950408889634f;
#pragma unroll
  for (int i = 0; i < 4; ++i) e[i] = __builtin_amdgcn_exp2f(e[i]);
  e = e + 1.0f;
#pragma unroll
  for (int i = 0; i < 4; ++i) e[i] = __builtin_amdgcn_rcpf(e[i]);
  e = e * u;
  return (u32x2){pk2(e[0], e[1]), pk2(e[2], e[3])};
}
__device__ __forceinline__ u32x4 cat4(u32x2 a, u32x2 b) { return (u32x4){a[0], a[1], b[0], b[1]}; }

}
extern "C" __global__ __launch_bounds__(512, 2) void psum_g1_kernel(const u16* __restrict__ wqkv,
                                                                    const u16* __restrict__ wg, const u16* __restrict__ wu,
                                                                    const u16* __restrict__ wo, const u16* __restrict__ wd,
                                                                    char* __restrict__ ws) {
  extern __shared__ __attribute__((aligned(16))) char smem[];
  LAS char* lds = (LAS char*)smem;
  ST_DECL LT_DECL ST(0);
  const int tid = threadIdx.x;
  int lane = tid & 63;
  const int w = __builtin_amdgcn_readfirstlane(tid >> 6), g = w >> 2, wi = w & 3;
  const int b = blockIdx.x, xcd = b & 7, idx = b >> 3;
  const int mb = (xcd & 1) * 4 + (idx & 3), kq = xcd >> 1, ntl = idx >> 2;
  const bool typeA = ntl < 4;
  const int j = ntl & 3;
  int fr = lane & 15, fq = lane >> 4;
  const int fA = 896 * kq + 64 * j;
  const int f0 = 896 * kq + 256 + 160 * j, odd = j & 1;
  const int fa = f0 + 32 * odd, fb = fa + 64, fc = odd ? f0 : f0 + 128;

  asm volatile("" : "+s"(wqkv), "+s"(wg), "+s"(wu), "+s"(ws));
  const bool isq = typeA && wi < 3;
  const int ld = isq ? 3072 : 3584;
  const u16* q1 = isq ? wqkv + (wi * 1024 + 256 * kq + 64 * j) : ((!typeA && (wi & 2)) ? wu : wg) + (typeA ? fA : ((wi & 1) ? fb : fa));
  const char* c1 = g ? ws + WS_XN + (size_t)(mb * 32 * 16384 + wi * 4096) : (const char*)q1;
  const int s1 = g ? 0 : 8 * ld - 1024, s2 = g ? 0 : 32 * ld - 2048, s3 = g ? 0 : 40 * ld - 3072;
  const int p1 = g ? 16384 : 64 * ld, p2 = g ? 16384 : 64 * 3584;
  const int d1 = (g ? 0 : G1_SA) + wi * 4096, d2 = g ? 16384 + wi * 256 : G1_SA + 16384 + wi * 1024;
  const unsigned long long m2 = g ? 0xffffull : ~0ull;
  const int rr = lane >> 3, pc = lane & 7, fi = (pc >> 1) ^ ((rr >> 1) & 3), kr = (rr & 3) + 8 * (rr >> 2);
  const unsigned v1 = g ? lane * 16 : kr * ld * 2 + fi * 32 + (pc & 1) * 16;
  const unsigned lb = (unsigned)(size_t)lds;
  unsigned ns = lb;
  dma_first(ns + d1, v1, c1); dma_next<1024>(v1, c1 + s1); dma_next<2048>(v1, c1 + s2); dma_next<3072>(v1, c1 + s3);
  const char* c2;
  {
    const int hr = wi * 4 + ((lane >> 2) & 3);
    const unsigned oh = (14 + (hr >> 3)) * 1024 + (8 + (hr & 7)) * 64 + (((lane & 3) ^ (2 * (hr >> 3)) ^ 2) * 16);
    const unsigned ow = (kr + 16 * (wi >> 1) + 4 * (wi & 1)) * 7168 + (typeA ? fi * 32 : (fi >> 1) * 32) + (pc & 1) * 16;
    const char* be = g ? ws + WS_XN + (size_t)((mb > 0 ? mb - 1 : 0) * 32 * 16384) : (const char*)(typeA ? wu + fA : wg + fc);
    const char* bo = g ? be : (const char*)(wu + (typeA ? fA : fc));
    c2 = ((fi & 1) ? bo : be) + (g ? oh : ow);
  }
  auto adv = [&]() { c1 += p1; c2 += p2; ns = (ns == lb + 3 * G1_SLOT) ? lb : ns + G1_SLOT; };
  auto stage = [&]() {
    dma_first(ns + d1, v1, c1); dma_next<1024>(v1, c1 + s1); dma_next<2048>(v1, c1 + s2); dma_next<3072>(v1, c1 + s3);
    dma1m(ns + d2, c2, m2);
    adv();
  };
  dma1m(ns + d2, c2, m2);
  adv();

  f32x4 acc[4][10];
  f32x4 hacc = {0.f, 0.f, 0.f, 0.f};
#pragma unroll
  for (int i = 0; i < 4; ++i)
#pragma unroll
    for (int n = 0; n < 10; ++n) { acc[i][n] = (f32x4){0.f, 0.f, 0.f, 0.f}; asm volatile("" : "+v"(acc[i][n])); }
  const int rl = 4 * (fq & 1) + (fr >> 2);
  unsigned hoffA = lb + 16384 + fr * 64 + ((fq ^ (2 * (fr >> 3))) * 16);
  unsigned aoff = hoffA - 16384 + wi * 4096;
  unsigned boff0 = lb + G1_SA + (fq >> 1) * 2048 + rl * 128 + 32 * ((2 * g) ^ ((rl >> 1) & 3)) + 8 * (lane & 3);
  unsigned boff1 = boff0 ^ 32;
  unsigned hoffB = (boff0 ^ (32 * (wi & 1))) + (1 + (wi >> 1)) * 4096;
  asm volatile("" : "+v"(aoff), "+v"(hoffA), "+v"(boff0), "+v"(boff1), "+v"(hoffB), "+v"(hacc));
  {
    const int lo = fr + (wi == 0 ? -7 : (wi == 2 ? 1 : 9));
    const int hi = wi == 0 ? fr + 1 : ((wi >= 2 && mb == 0) ? 0 : (wi == 2 ? 8 : 16));
    f32x4 bv;
#pragma unroll
    for (int e = 0; e < 4; ++e) bv[e] = (4 * fq + e >= lo && 4 * fq + e < hi) ? 0.f : -3.0e38f;
    *(LAS f32x4*)(lds + T_BIAS + wi * 1024 + lane * 16) = bv;
  }
  stage();
  ST(1);
  WAIT_V(5);
  BAR();
  ST(2);
  if (g == 1) BAR();

  auto phase = [&](auto do_stage, int q) {
    constexpr bool DS = decltype(do_stage)::value;
    bf16x8 a[4], bfr[10], ha, hb;
    LT(0);
#define RB(c) bfr[2 * (c)] = cat(trd(lp(boff0) + (c) * 4096), trd(lp(boff0) + (c) * 4096 + 1024)); \
              bfr[2 * (c) + 1] = cat(trd(lp(boff1) + (c) * 4096), trd(lp(boff1) + (c) * 4096 + 1024));
    a[0] = *(const LAS bf16x8*)(lp(aoff)); a[1] = *(const LAS bf16x8*)(lp(aoff) + 1024);
    if (DS) dma_first(ns + d1, v1, c1);
    a[2] = *(const LAS bf16x8*)(lp(aoff) + 2048); a[3] = *(const LAS bf16x8*)(lp(aoff) + 3072);
    RB(0)
    if (DS) dma_next<1024>(v1, c1 + s1);
    RB(1)
    if (DS) dma_next<2048>(v1, c1 + s2);
    RB(2)
    if (DS) dma_next<3072>(v1, c1 + s3);
    RB(3)
    if (DS) dma1m(ns + d2, c2, m2);
    RB(4)
    { ha = *(const LAS bf16x8*)lp(hoffA); hb = cat(trd(lp(hoffB)), trd(lp(hoffB) + 1024)); }
#undef RB
    SCHED();
    LT(1);
    const int dlt = ((q & 3) == 3) ? -3 * G1_SLOT : G1_SLOT;
    aoff += dlt; hoffA += dlt; boff0 += dlt; boff1 += dlt; hoffB += dlt;
    if (DS) {
      adv();
      if (q == 1) stage();
      LT(2);
      if (q == 0) WAIT_V(5); else WAIT_V(10);
    } else {
      LT(2);
      if (q == 29) {
        WAIT_V(0);
        const int l = __builtin_amdgcn_mbcnt_hi(~0u, __builtin_amdgcn_mbcnt_lo(~0u, 0u));
        int t = w * 64 + l;
        t = t < 288 ? t : t - 288;
        const int rr = (t >> 4) * 64 + (xcd & 1) * 32 + idx;
        touch(lb + T_SCR, (rr < 256 ? wo + (256 * kq + rr) * 1024 : wd + (896 * kq + rr - 256) * 1024) + (t & 15) * 64);
      }
    }
    WAIT_L0();
    LT(3);
    SCHED();
    BAR();
    SCHED();
    LT(4);
    PRIO_M();
#pragma unroll
    for (int n = 0; n < 10; ++n)
#pragma unroll
      for (int i = 0; i < 4; ++i) {
        if (n == 9 && i == 4 - EARLY) { SCHED(); PRIO_T(); if (q < 31 || g == 0) BAR(); SCHED(); }
        acc[i][n] = mfma(bfr[n], a[i], acc[i][n]);
      }
    hacc = mfma(hb, ha, hacc);
    PRIO_E();
    SCHED();
    LT(5);
    SCHED();
  };
#pragma clang loop unroll(disable)
  for (int q = 0; q < 29; ++q) phase(std::true_type{}, q);
#pragma clang loop unroll(disable)
  for (int q = 29; q < 32; ++q) phase(std::false_type{}, q);

  ST(3);
  asm volatile("" ::: "memory");
  if (g == 0) __builtin_amdgcn_s_setprio(0); else __builtin_amdgcn_s_setprio(1);
  lane = __builtin_amdgcn_mbcnt_hi(~0u, __builtin_amdgcn_mbcnt_lo(~0u, 0u));
  asm volatile("" : "+v"(lane));
  fr = lane & 15; fq = lane >> 4;
  const auto hrs = __builtin_amdgcn_make_buffer_rsrc(ws + WS_H + (size_t)(mb * 4 + kq) * 36 * 16384, 0, 36 * 16384, 0x00020000);
  const unsigned hv = wi * 4096 + lane * 16;
#define GLU(i, a, b) glu4(acc[i][a], acc[i][b])
  if (!typeA) {
    const int kba = 8 + ((fa - 896 * kq) >> 5) + g, kbc = 8 + ((fc - 896 * kq) >> 5);
#pragma unroll
    for (int i = 0; i < 4; ++i) {
      __builtin_amdgcn_raw_buffer_store_b128(cat4(GLU(i, 0, 4), GLU(i, 1, 5)), hrs, hv + kba * 16384 + i * 1024, 0, H_AUX);
      __builtin_amdgcn_raw_buffer_store_b128(cat4(GLU(i, 2, 6), GLU(i, 3, 7)), hrs, hv + kba * 16384 + 32768 + i * 1024, 0, H_AUX);
      __builtin_amdgcn_raw_buffer_store_b64(GLU(i, 8, 9), hrs, hv + kbc * 16384 + g * 8 + i * 1024, 0, H_AUX);
    }
  } else {
    LAS char* kim = lds + g * 40960;
    LAS char* vim = kim + 17 * 1024;
    bf16x8 kf[4], qf[4];
#define FIX(i, n) acc[i][n]
#pragma unroll
    for (int i = 0; i < 4; ++i) {
      kf[i] = pack8(FIX(i, 2), FIX(i, 3));
      *(LAS bf16x8*)(vim + ((wi * 4 + i) * 16 + fr) * 80 + fq * 16) = pack8(FIX(i, 4), FIX(i, 5));
    }
    *(LAS bf16x8*)(kim + (wi * 4 + 2) * 1024 + lane * 16) = kf[2];
    *(LAS bf16x8*)(kim + (wi * 4 + 3) * 1024 + lane * 16) = kf[3];
    {
      const f32x4 hh = hacc;
      LAS char* hp = (wi < 2) ? (kim + 16 * 1024 + lane * 16 + wi * 8) : (vim + (256 + fr) * 80 + fq * 16 + (wi - 2) * 8);
      *(LAS u32x2*)hp = (u32x2){pk2(hh[0], hh[1]), pk2(hh[2], hh[3])};
    }
    auto glu_part = [&]() {
#pragma unroll
      for (int i = 0; i < 4; ++i)
        __builtin_amdgcn_raw_buffer_store_b128(cat4(GLU(i, 6, 8), GLU(i, 7, 9)), hrs, hv + (8 + 2 * j + g) * 16384 + i * 1024, 0, H_AUX);
    };
    WAIT_L0();
    ET(0);
    ET(1);
    BAR();
    ET(2);
    asm volatile("" ::: "memory");
    const bool halo = wi == 0;
    const f32x4 bc = *(const LAS f32x4*)(lds + T_BIAS + lane * 16), bp = *(const LAS f32x4*)(lds + T_BIAS + 1024 + lane * 16);
    const f32x4 bp0 = *(const LAS f32x4*)(lds + T_BIAS + (halo ? 2048 : 1024) + lane * 16);
    const f32x4 bp1 = *(const LAS f32x4*)(lds + T_BIAS + (halo ? 3072 : 1024) + lane * 16);
    const int pf0 = halo ? 16 : wi * 4 - 2, pf1 = halo ? 16 : wi * 4 - 1;
    const bf16x8 kp0 = *(const LAS bf16x8*)(kim + pf0 * 1024 + lane * 16);
    const bf16x8 kp1 = *(const LAS bf16x8*)(kim + pf1 * 1024 + lane * 16);
    const LAS char* vb = vim + (4 * fq + (fr >> 2)) * 80 + (lane & 3) * 16;
    bf16x4 vt[6][2];
#pragma unroll
    for (int d = 0; d < 2; ++d) {
      vt[0][d] = trd(vb + pf0 * 1280 + d * 8); vt[1][d] = trd(vb + pf1 * 1280 + d * 8);
#pragma unroll
      for (int i = 0; i < 4; ++i) vt[2 + i][d] = trd(vb + (wi * 4 + i) * 1280 + d * 8);
    }
    f32x4 sp[4], sc[4];
#pragma unroll
    for (int i = 0; i < 4; ++i) {
      qf[i] = pack8(acc[i][0], acc[i][1]);
      sc[i] = mfma(kf[i], qf[i], bc);
    }
    sp[2] = mfma(kf[0], qf[2], bp); sp[3] = mfma(kf[1], qf[3], bp);
    sp[0] = mfma(kp0, qf[0], bp0); sp[1] = mfma(kp1, qf[1], bp1);
    const float sl = 0.17677669529663687f * 1.4426950408889634f;
    const f32x4 z4 = {0.f, 0.f, 0.f, 0.f};
    float m[4];
#pragma unroll
    for (int i = 0; i < 4; ++i)
      m[i] = fmx(fmx(fmx(fmx(sp[i][0], sp[i][1]), sp[i][2]), fmx(fmx(sp[i][3], sc[i][0]), sc[i][1])), fmx(sc[i][2], sc[i][3]));
    {
      const unsigned t = __float_as_uint(tred<true>(m[0], m[1], m[2], m[3]));
      const auto u = __builtin_amdgcn_permlane32_swap(t, t, false, false);
      const auto u0 = __builtin_amdgcn_permlane16_swap(u[0], u[0], false, false), u1 = __builtin_amdgcn_permlane16_swap(u[1], u[1], false, false);
      m[0] = __uint_as_float(u0[0]); m[1] = __uint_as_float(u0[1]); m[2] = __uint_as_float(u1[0]); m[3] = __uint_as_float(u1[1]);
    }
    const u32x4 one4 = {0x3f803f80u, 0x3f803f80u, 0x3f803f80u, 0x3f803f80u};
#pragma unroll
    for (int i = 0; i < 4; ++i) {
      const float mm = -sl * m[i];
#pragma unroll
      for (int e = 0; e < 4; ++e) {
        sp[i][e] = __builtin_amdgcn_exp2f(__builtin_fmaf(sp[i][e], sl, mm));
        sc[i][e] = __builtin_amdgcn_exp2f(__builtin_fmaf(sc[i][e], sl, mm));
      }
      const bf16x8 pfr = pack8(sp[i], sc[i]);
      const f32x4 sm = mfma(__builtin_bit_cast(bf16x8, one4), pfr, z4);
      const f32x4 o0 = mfma(cat(vt[i][0], vt[2 + i][0]), pfr, z4);
      const f32x4 o1 = mfma(cat(vt[i][1], vt[2 + i][1]), pfr, z4);
      const float inv = __builtin_amdgcn_rcpf(sm[0]);
      __builtin_amdgcn_raw_buffer_store_b128(__builtin_bit_cast(u32x4, pack8(o0 * inv, o1 * inv)), hrs, hv + (2 * j + g) * 16384 + i * 1024, 0, H_AUX);
    }
    glu_part();
    ET(5);
  }
  ST(4);
  ST(5);
  ST_OUT(0, 6);
}

constexpr int G2_SA = 32768, G2_SB = 16384, G2_SLOT = G2_SA + G2_SB, G2_LDS = 3 * G2_SLOT;

#define PREW 0
extern "C" __global__ __launch_bounds__(512 + 64 * PREW, 2 + PREW) void psum_g2_kernel(const u16* __restrict__ x, const u16* __restrict__ wo,
                                                       const u16* __restrict__ wd, char* __restrict__ ws,
                                                       u16* __restrict__ out) {
  extern __shared__ __attribute__((aligned(16))) char smem[];
  LAS char* lds = (LAS char*)smem;
  ST_DECL LT_DECL ST(0);
  const int tid = threadIdx.x, lane = tid & 63;
  const int w = __builtin_amdgcn_readfirstlane(tid >> 6), g = w >> 2, wi = w & 3;
  const int b = blockIdx.x, xcd = b & 7, idx = b >> 3;
  const int mb = (xcd & 1) * 4 + (idx & 3), kq = xcd >> 1, nt = idx >> 2;
  const int fr = lane & 15, fq = lane >> 4;

  const char* ca = ws + WS_H + (size_t)(mb * 4 + kq) * 36 * 16384 + w * 4096;
  const unsigned va = lane * 16;
  const int e0 = (0 ^ kq) * 1024, e1 = ((1 ^ kq) - 1) * 1024, e2 = ((2 ^ kq) - 2) * 1024, e3 = ((3 ^ kq) - 3) * 1024;
  const int rr = lane >> 3, pc = lane & 7;
  const unsigned vb = rr * 2048 + (((pc >> 1) ^ ((rr >> 1) & 3)) * 32) + (pc & 1) * 16;
  const char* cb = (const char*)(wo + (size_t)(256 * kq + 8 * w) * 1024 + nt * 128);
  const char* cb2 = (const char*)(wd + (size_t)(896 * kq + 8 * w) * 1024 + nt * 128);
  int st = 0;
  const unsigned lb = (unsigned)(size_t)lds;
  unsigned ns = lb;
  auto adv = [&]() { ca += 32768; ++st; cb = (st == 4) ? cb2 : cb + 64 * 2048; ns = (ns == lb + 2 * G2_SLOT) ? lb : ns + G2_SLOT; };
  auto stage = [&]() {
    dma_first(ns + w * 4096, va, ca + e0); dma_next<1024>(va, ca + e1); dma_next<2048>(va, ca + e2); dma_next<3072>(va, ca + e3);
    dma_first(ns + G2_SA + w * 2048, vb, cb); dma_next<1024>(vb, cb - 896);
    adv();
  };
  stage();
  BAR();
  stage();
  const int mf = wi * 4 + kq;
  const unsigned orow = ((mb * 256 + 32 * (mf >> 1) + 2 * fr + (mf & 1)) * 1024 + nt * 128 + g * 64 + 16 * (fq & 1) + 8 * (fq >> 1)) * 2;
  u32x4 xr[2] = {{0u, 0u, 0u, 0u}, {0u, 0u, 0u, 0u}};
  const int rl = 4 * (fq & 1) + ((lane & 15) >> 2);
  unsigned aoff = lb + wi * 4096 + lane * 16;
  unsigned boff = lb + G2_SA + (fq >> 1) * 2048 + g * 1024 + rl * 128 + 32 * ((rl >> 1) & 3) + 8 * (lane & 3);

  f32x4 acc[4][4];
#pragma unroll
  for (int i = 0; i < 4; ++i)
#pragma unroll
    for (int n = 0; n < 4; ++n) { acc[i][n] = (f32x4){0.f, 0.f, 0.f, 0.f}; asm volatile("" : "+v"(acc[i][n])); }
  ST(1);
  WAIT_V(6);
  BAR();
  ST(2);
  if (g == 1) BAR();

  int sl = 0;
  auto phase = [&](auto do_stage, int q) {
    constexpr bool DS = decltype(do_stage)::value;
    bf16x8 a[2][4], bfr[2][4];
    LT(0);
#define RA(kk, i) a[kk][i] = *(const LAS bf16x8*)(lp(aoff) + (kk) * 16384 + (i) * 1024);
#define RB(kk, n) bfr[kk][n] = cat(trd(lp(boff ^ ((n) * 32)) + (kk) * 8192), trd(lp(boff ^ ((n) * 32)) + (kk) * 8192 + 4096));
#define D0 if (DS) dma_first(ns + w * 4096, va, ca + e0);
#define D1 if (DS) dma_next<1024>(va, ca + e1);
#define D2 if (DS) dma_next<2048>(va, ca + e2);
#define D3 if (DS) dma_next<3072>(va, ca + e3);
#define D4 if (DS) dma_first(ns + G2_SA + w * 2048, vb, cb);
#define D5 if (DS) dma_next<1024>(vb, cb - 896);
#define UPD sl = (sl == 2) ? 0 : sl + 1; dlt = (sl == 0) ? -2 * G2_SLOT : G2_SLOT; if (DS) adv(); \
    asm volatile("" : "+s"(ca), "+s"(cb), "+s"(ns), "+s"(sl), "+s"(dlt) :: "memory");
    int dlt;
    if (!DS && q == 16)
      asm volatile("global_load_dwordx4 %0, %2, %3\n\tglobal_load_dwordx4 %1, %2, %3 offset:64" : "+v"(xr[0]), "+v"(xr[1]) : "v"(orow), "s"(x) : "memory");
    RA(0, 0) RA(0, 1) D0 RA(0, 2) RA(0, 3) RB(0, 0) D1 RB(0, 1) RB(0, 2) RB(0, 3) D2 RA(1, 0) RA(1, 1) RA(1, 2) D3 RA(1, 3) RB(1, 0) D4 RB(1, 1) D5 UPD RB(1, 2) RB(1, 3)
    aoff += dlt;
#undef RA
#undef RB
    boff += dlt;
    SCHED();
    LT(1);
    LT(2);
    asm volatile("" : "+v"(boff), "+v"(aoff));
    if (DS) WAIT_V_IF0(6, g ^ 1); else WAIT_V_IF0(2, g ^ 1);
    WAIT_L0();
    LT(3);
    SCHED();
    BAR();
    SCHED();
    LT(4);
    PRIO_M();
#pragma unroll
    for (int n = 0; n < 4; ++n)
#pragma unroll
      for (int i = 0; i < 4; ++i) {
        acc[i][n] = mfma(bfr[0][n], a[0][i], acc[i][n]);
        SCHED();
        if (n == 3 && i == 3) { PRIO_T(); if (DS) WAIT_V_IF0(6, g); else WAIT_V_IF0(2, g); if (q < 17 || g == 0) BAR(); SCHED(); }
        acc[i][n] = mfma(bfr[1][n], a[1][i], acc[i][n]);
        SCHED();
      }
    PRIO_E();
    SCHED();
    LT(5);
    SCHED();
  };
#pragma clang loop unroll(disable)
  for (int q = 0; q < 16; ++q) phase(std::true_type{}, q);
#pragma clang loop unroll(disable)
  for (int q = 16; q < 18; ++q) phase(std::false_type{}, q);

  ST(3);
  const int tile = mb * 8 + nt;
  const auto rs = __builtin_amdgcn_make_buffer_rsrc(ws + WS_X, 0, (int)X_BYTES, 0x00020000);
#pragma unroll
  for (int i = 1; i < 4; ++i) {
    const unsigned o = (unsigned)((((tile * 4 + (kq ^ i)) * 3 + (i - 1)) * 8 + w) * 2048) + lane * 16;
    __builtin_amdgcn_raw_buffer_store_b128(__builtin_bit_cast(u32x4, pack8(acc[i][0], acc[i][1])), rs, o, 0, 16);
    __builtin_amdgcn_raw_buffer_store_b128(__builtin_bit_cast(u32x4, pack8(acc[i][2], acc[i][3])), rs, o + 1024, 0, 16);
  }
  WAIT_V(0);
  ST(4);
  asm volatile("" : "+v"(xr[0]), "+v"(xr[1]));
  GAS unsigned* cnt = (GAS unsigned*)(ws + WS_CNT) + (tile * 32 + w) * 16;
  if (lane >= 1 && lane < 4) __hip_atomic_fetch_add(cnt + (kq ^ lane) * 128, 1u, __ATOMIC_RELAXED, __HIP_MEMORY_SCOPE_AGENT);
  {
    unsigned spins = 0;
    while ((unsigned)__builtin_amdgcn_readfirstlane(__hip_atomic_load(cnt + kq * 128, __ATOMIC_RELAXED, __HIP_MEMORY_SCOPE_AGENT)) < 3u) {
      __builtin_amdgcn_s_sleep(2);
      if (++spins > (1u << 22)) break;
    }
  }
  asm volatile("" ::: "memory");
  ST(5);
  u32x4 p[3][2];
#pragma unroll
  for (int i = 1; i < 4; ++i) {
    const unsigned o = (unsigned)((((tile * 4 + kq) * 3 + (i - 1)) * 8 + w) * 2048) + lane * 16;
    p[i - 1][0] = __builtin_amdgcn_raw_buffer_load_b128(rs, o, 0, 16);
    p[i - 1][1] = __builtin_amdgcn_raw_buffer_load_b128(rs, o + 1024, 0, 16);
  }
  const auto ors = __builtin_amdgcn_make_buffer_rsrc(out, 0, 4 << 20, 0x00020000);
#pragma unroll
  for (int h = 0; h < 2; ++h) {
    const auto x0 = __builtin_amdgcn_permlane16_swap(xr[h][0], xr[h][2], false, false), x1 = __builtin_amdgcn_permlane16_swap(xr[h][1], xr[h][3], false, false);
    const u32x4 xv = {x0[0], x1[0], x0[1], x1[1]};
    unsigned r[4];
#pragma unroll
    for (int d = 0; d < 4; ++d) {
      float lo = acc[0][2 * h + (d >> 1)][2 * (d & 1)] + __uint_as_float(xv[d] << 16), hi = acc[0][2 * h + (d >> 1)][2 * (d & 1) + 1] + __uint_as_float(xv[d] & 0xffff0000u);
#pragma unroll
      for (int i = 0; i < 3; ++i) { lo += __uint_as_float(p[i][h][d] << 16); hi += __uint_as_float(p[i][h][d] & 0xffff0000u); }
      r[d] = pk2(lo, hi);
    }
    const auto y0 = __builtin_amdgcn_permlane16_swap(r[0], r[2], false, false), y1 = __builtin_amdgcn_permlane16_swap(r[1], r[3], false, false);
    __builtin_amdgcn_raw_buffer_store_b128((u32x4){y0[0], y1[0], y0[1], y1[1]}, ors, orow + 64 * h, 0, OUT_AUX);
  }
  ST(6);
  ST(7);
  ST_OUT(1, 8);
}

extern "C" void kernel_launch(void* const* d_in, const int* in_sizes, int n_in, void* d_out, int out_size, void* d_ws,
                              size_t ws_size, hipStream_t stream) {
  const u16* x = (const u16*)d_in[0];
  const u16* wqkv = (const u16*)d_in[1];
  const u16* wo = (const u16*)d_in[2];
  const u16* wg = (const u16*)d_in[4];
  const u16* wu = (const u16*)d_in[5];
  const u16* wd = (const u16*)d_in[6];
  char* ws = (char*)d_ws;
  (void)hipFuncSetAttribute((const void*)psum_g1_kernel, hipFuncAttributeMaxDynamicSharedMemorySize, G1_LDS);
  (void)hipFuncSetAttribute((const void*)psum_g2_kernel, hipFuncAttributeMaxDynamicSharedMemorySize, G2_LDS);
  psum_ln_kernel<<<256, 320, 0, stream>>>(x, wqkv, wg, wu, ws);
  psum_g1_kernel<<<256, 512, G1_LDS, stream>>>(wqkv, wg, wu, wo, wd, ws);
  psum_g2_kernel<<<256, 512 + 64 * PREW, G2_LDS, stream>>>(x, wo, wd, ws, (u16*)d_out);
}
```

```cpp
#include <hip/hip_runtime.h>
#include <hip/hip_bf16.h>
#include <type_traits>
#define EARLY 1
#define HALO_SKIP 0
#define PF_W2 1
#define PF_LOOP 0
#define PF_R0 192
#define PF_PER 2
#define PF_LN_ROWS 96
#define PRIO_L 1
#define PRIO_M() __builtin_amdgcn_s_setprio(0)
#define PRIO_T() __builtin_amdgcn_s_setprio(1)
#define PRIO_E()
#define PF_CODE 0
#define PRECLEAN 0
#define OUT_AUX 16
#define G2ORD 0
#define EARLY2 1
#define CHAIN 1
#define PF_X 0
#define XH 1
#define H_AUX 16

namespace {

typedef unsigned short u16;
typedef short bf16x8 __attribute__((ext_vector_type(8)));
typedef short bf16x4 __attribute__((ext_vector_type(4)));
typedef float f32x4 __attribute__((ext_vector_type(4)));
typedef float f32x2 __attribute__((ext_vector_type(2)));
typedef unsigned u32x4 __attribute__((ext_vector_type(4)));
typedef unsigned u32x2 __attribute__((ext_vector_type(2)));
typedef __bf16 bf2 __attribute__((ext_vector_type(2)));
#define LAS __attribute__((address_space(3)))
#define GAS __attribute__((address_space(1)))

#define WAIT_V(n) asm volatile("s_waitcnt vmcnt(" #n ")" ::: "memory")
#define WAIT_V_IF0(n, c) asm volatile("s_cmp_lg_u32 %0, 0\n\ts_cbranch_scc1 1f\n\ts_waitcnt vmcnt(" #n ")\n1:" :: "s"(c) : "memory", "scc")
#define WAIT_L0() do { asm volatile("" ::: "memory"); __builtin_amdgcn_s_waitcnt(0xC07F); asm volatile("" ::: "memory"); } while (0)
#define BAR() __builtin_amdgcn_s_barrier()
#define SCHED() __builtin_amdgcn_sched_barrier(0)

__device__ __forceinline__ float b2f(u16 v) { return __uint_as_float(((unsigned)v) << 16); }
__device__ __forceinline__ unsigned pk2(float lo, float hi) {
  bf2 r = {(__bf16)lo, (__bf16)hi};
  return __builtin_bit_cast(unsigned, r);
}
__device__ __forceinline__ float rbf(float f) { return (float)(__bf16)f; }
__device__ __forceinline__ bf16x8 pack8(f32x4 a, f32x4 b) {
  u32x4 r = {pk2(a[0], a[1]), pk2(a[2], a[3]), pk2(b[0], b[1]), pk2(b[2], b[3])};
  return __builtin_bit_cast(bf16x8, r);
}
__device__ __forceinline__ bf16x4 trd(LAS const char* p) {
  return __builtin_amdgcn_ds_read_tr16_b64_v4i16((LAS bf16x4*)p);
}
__device__ __forceinline__ bf16x8 cat(bf16x4 a, bf16x4 b) { return __builtin_shufflevector(a, b, 0, 1, 2, 3, 4, 5, 6, 7); }
__device__ __forceinline__ f32x4 mfma(bf16x8 a, bf16x8 b, f32x4 c) {
  return __builtin_amdgcn_mfma_f32_16x16x32_bf16(a, b, c, 0, 0, 0);
}
__device__ __forceinline__ void dma_first(unsigned lds_dst, unsigned voff, const char* b0) {
  asm volatile("s_mov_b32 m0, %0\n\ts_nop 0\n\tglobal_load_lds_dwordx4 %1, %2" :: "s"(lds_dst), "v"(voff), "s"(b0) : "memory");
}
template <int OFF>
__device__ __forceinline__ void dma_next(unsigned voff, const char* b) {
  asm volatile("global_load_lds_dwordx4 %0, %1 offset:%2" :: "v"(voff), "s"(b), "i"(OFF) : "memory");
}
__device__ __forceinline__ void dma1m(unsigned lds_dst, const char* p, unsigned long long mask) {
  asm volatile("s_mov_b32 m0, %0\n\ts_mov_b64 exec, %2\n\t"
               "global_load_lds_dwordx4 %1, off\n\t"
               "s_mov_b64 exec, -1"
               :: "s"(lds_dst), "v"(p), "s"(mask) : "memory");
}
__device__ __forceinline__ void touch(unsigned lds_dst, const void* p) {
  asm volatile("s_mov_b32 m0, %0\n\ts_nop 0\n\tglobal_load_lds_dword %1, off" :: "s"(lds_dst), "v"(p) : "memory");
}
#define CODE_ADDR(dst, sym) do { unsigned lo_, hi_; asm volatile("s_getpc_b64 s[90:91]\n\ts_add_u32 %0, s90, " #sym "@rel32@lo+4\n\ts_addc_u32 %1, s91, " #sym "@rel32@hi+12" \
    : "=s"(lo_), "=s"(hi_) :: "s90", "s91", "scc"); dst = (const char*)(((unsigned long long)hi_ << 32) | lo_); } while (0)
constexpr int LN_LINES = 16, G1_LINES = 80, G2_LINES = 30;
__device__ __forceinline__ LAS char* lp(unsigned a) { return (LAS char*)(size_t)a; }
__device__ __forceinline__ float fmx(float a, float b) { return __builtin_elementwise_maximum(a, b); }
__device__ __forceinline__ float xmax(float v) {
  auto a = __builtin_amdgcn_permlane16_swap(__float_as_uint(v), __float_as_uint(v), false, false);
  v = fmaxf(__uint_as_float(a[0]), __uint_as_float(a[1]));
  auto b = __builtin_amdgcn_permlane32_swap(__float_as_uint(v), __float_as_uint(v), false, false);
  return fmaxf(__uint_as_float(b[0]), __uint_as_float(b[1]));
}
__device__ __forceinline__ float xsum(float v) {
  auto a = __builtin_amdgcn_permlane16_swap(__float_as_uint(v), __float_as_uint(v), false, false);
  v = __uint_as_float(a[0]) + __uint_as_float(a[1]);
  auto b = __builtin_amdgcn_permlane32_swap(__float_as_uint(v), __float_as_uint(v), false, false);
  return __uint_as_float(b[0]) + __uint_as_float(b[1]);
}

template <int CTRL>
__device__ __forceinline__ float dpp_add(float v) {
  return v + __uint_as_float(__builtin_amdgcn_update_dpp(0u, __float_as_uint(v), CTRL, 0xf, 0xf, true));
}
__device__ __forceinline__ float hsum(float v) {
  v = dpp_add<0xB1>(v);
  v = dpp_add<0x4E>(v);
  v = dpp_add<0x128>(v);
  return xsum(v);
}

#define DG(kid, slot)
#define ST_DECL
#define LT_DECL
#define LT(k)
#define ET(k)
#define ST(k)
#define ST_OUT(kid, n)
#define WS_BASE_MB 0
constexpr size_t WS_XN = (size_t)WS_BASE_MB << 20;
constexpr size_t WS_H = WS_XN + (size_t)4 * 1024 * 1024;
constexpr size_t H_BYTES = (size_t)8 * 4 * 36 * 16 * 1024;
constexpr size_t WS_X = WS_H + H_BYTES;
constexpr size_t X_BYTES = (size_t)64 * 4 * 3 * 8 * 2048;
constexpr size_t WS_CNT = WS_X + X_BYTES;
constexpr size_t WS_GCNT = WS_CNT + 131072;
constexpr size_t WS_ST = WS_GCNT + 4096;

}
extern "C" __global__ __launch_bounds__(320) void psum_ln_kernel(const u16* __restrict__ x, const u16* __restrict__ wqkv, const u16* __restrict__ wg,
                                                                 const u16* __restrict__ wu, char* __restrict__ ws) {
  const int lane = threadIdx.x & 63, b = blockIdx.x;
#define DGT 0
  DG(0, 0);
  if (threadIdx.x >= 256) {
    if (lane < 8) *(unsigned*)(ws + WS_CNT + (b * 8 + lane) * 64) = 0u;
    if (lane == 8 && b < 32) *(unsigned*)(ws + WS_GCNT + b * 64) = 0u;
    __shared__ __attribute__((aligned(16))) char sink[256];
    const int kq = (b & 7) >> 1, l = lane < 40 ? lane : lane - 40;
    const u16* p = l < 12 ? wqkv + (l >> 2) * 1024 + 256 * kq + (l & 3) * 64 : (l < 26 ? wg + 896 * kq + (l - 12) * 64 : wu + 896 * kq + (l - 26) * 64);
    const int ld = l < 12 ? 3072 : 3584;
#pragma unroll
    for (int i = 0; i < PF_LN_ROWS / 32; ++i) touch((unsigned)(size_t)(LAS char*)sink, p + (i * 32 + (b >> 3)) * ld);
    return;
  }
  const int hf = (b & 1) * 128 + (b >> 1);
  const int mb = hf >> 5, mf = (hf >> 1) & 15, r = (hf & 1) * 8 + (threadIdx.x >> 6) * 2 + ((lane >> 2) & 1);
  const int row = mb * 256 + 32 * (mf >> 1) + 2 * r + (mf & 1), kl = lane >> 3, ch = lane & 3;
  const u16* xr = x + row * 1024 + kl * 32 + ch * 8;
  u32x4 raw[4];
#pragma unroll
  for (int i = 0; i < 4; ++i) raw[i] = *(const u32x4*)(xr + 256 * i);
  float v[32];
#pragma unroll
  for (int i = 0; i < 16; ++i) { v[2 * i] = __uint_as_float(raw[i >> 2][i & 3] << 16); v[2 * i + 1] = __uint_as_float(raw[i >> 2][i & 3] & 0xffff0000u); }
  float s = 0.f;
#pragma unroll
  for (int i = 0; i < 32; ++i) s += v[i];
  const float mean = hsum(s) * (1.0f / 1024.0f);
  float q = 0.f;
#pragma unroll
  for (int i = 0; i < 32; ++i) { const float d = v[i] - mean; q += d * d; }
  q = hsum(q);
  const float mb16 = rbf(mean), den = rbf(sqrtf(rbf(rbf(q * (1.0f / 1024.0f)) + 1e-5f)));
  const float inv = 1.0f / den;
  const auto rs = __builtin_amdgcn_make_buffer_rsrc(ws + WS_XN, 0, 4 << 20, 0x00020000);
  const unsigned o = ((mb * 32 + kl) * 16 + mf) * 1024 + r * 64 + ((ch ^ (2 * (r >> 3))) * 16);
#pragma unroll
  for (int i = 0; i < 4; ++i) {
    u32x4 t;
#pragma unroll
    for (int e = 0; e < 4; ++e) t[e] = pk2(rbf(v[8 * i + 2 * e] - mb16) * inv, rbf(v[8 * i + 2 * e + 1] - mb16) * inv);
    __builtin_amdgcn_raw_buffer_store_b128(t, rs, o + i * 8 * 16384, 0, H_AUX);
  }
  DG(0, 1);
}
namespace {
constexpr int G1_SA = 17 * 1024, G1_SB = 20 * 1024, G1_SLOT = G1_SA + G1_SB;
constexpr int T_SCR = 4 * G1_SLOT;
constexpr int T_BIAS = T_SCR + 256;
constexpr int G1_LDS = T_BIAS + 4096;

template <bool MAX>
__device__ __forceinline__ float tred(float a, float b, float c, float d) {
  const auto r1 = __builtin_amdgcn_permlane16_swap(__float_as_uint(a), __float_as_uint(b), false, false);
  const auto r2 = __builtin_amdgcn_permlane16_swap(__float_as_uint(c), __float_as_uint(d), false, false);
  const float x0 = __uint_as_float(r1[0]), x1 = __uint_as_float(r1[1]), y0 = __uint_as_float(r2[0]), y1 = __uint_as_float(r2[1]);
  const float t = MAX ? fmx(x0, x1) : x0 + x1, u = MAX ? fmx(y0, y1) : y0 + y1;
  const auto r3 = __builtin_amdgcn_permlane32_swap(__float_as_uint(t), __float_as_uint(u), false, false);
  const float z0 = __uint_as_float(r3[0]), z1 = __uint_as_float(r3[1]);
  return MAX ? fmx(z0, z1) : z0 + z1;
}
__device__ __forceinline__ u32x2 glu4(f32x4 g, f32x4 u) {
  f32x4 e = g * -1.4426950408889634f;
#pragma unroll
  for (int i = 0; i < 4; ++i) e[i] = __builtin_amdgcn_exp2f(e[i]);
  e = e + 1.0f;
#pragma unroll
  for (int i = 0; i < 4; ++i) e[i] = __builtin_amdgcn_rcpf(e[i]);
  e = e * u;
  return (u32x2){pk2(e[0], e[1]), pk2(e[2], e[3])};
}
__device__ __forceinline__ u32x4 cat4(u32x2 a, u32x2 b) { return (u32x4){a[0], a[1], b[0], b[1]}; }

}
extern "C" __global__ __launch_bounds__(512, 2) void psum_g1_kernel(const u16* __restrict__ wqkv,
                                                                    const u16* __restrict__ wg, const u16* __restrict__ wu,
                                                                    const u16* __restrict__ wo, const u16* __restrict__ wd,
                                                                    char* __restrict__ ws) {
  extern __shared__ __attribute__((aligned(16))) char smem[];
  LAS char* lds = (LAS char*)smem;
  ST_DECL LT_DECL ST(0);
  const int tid = threadIdx.x;
  int lane = tid & 63;
  const int w = __builtin_amdgcn_readfirstlane(tid >> 6), g = w >> 2, wi = w & 3;
  const int b = blockIdx.x, xcd = b & 7, idx = b >> 3;
  const int mb = (xcd & 1) * 4 + (idx & 3), kq = xcd >> 1, ntl = idx >> 2;
  const bool typeA = ntl < 4;
  const int j = ntl & 3;
  int fr = lane & 15, fq = lane >> 4;
  const int fA = 896 * kq + 64 * j;
  const int f0 = 896 * kq + 256 + 160 * j, odd = j & 1;
  const int fa = f0 + 32 * odd, fb = fa + 64, fc = odd ? f0 : f0 + 128;

  asm volatile("" : "+s"(wqkv), "+s"(wg), "+s"(wu), "+s"(ws));
  const bool isq = typeA && wi < 3;
  const int ld = isq ? 3072 : 3584;
  const u16* q1 = isq ? wqkv + (wi * 1024 + 256 * kq + 64 * j) : ((!typeA && (wi & 2)) ? wu : wg) + (typeA ? fA : ((wi & 1) ? fb : fa));
  const char* c1 = g ? ws + WS_XN + (size_t)(mb * 32 * 16384 + wi * 4096) : (const char*)q1;
  const int s1 = g ? 0 : 8 * ld - 1024, s2 = g ? 0 : 32 * ld - 2048, s3 = g ? 0 : 40 * ld - 3072;
  const int p1 = g ? 16384 : 64 * ld, p2 = g ? 16384 : 64 * 3584;
  const int d1 = (g ? 0 : G1_SA) + wi * 4096, d2 = g ? 16384 + wi * 256 : G1_SA + 16384 + wi * 1024;
  const unsigned long long m2 = g ? 0xffffull : ~0ull;
  const int rr = lane >> 3, pc = lane & 7, fi = (pc >> 1) ^ ((rr >> 1) & 3), kr = (rr & 3) + 8 * (rr >> 2);
  const unsigned v1 = g ? lane * 16 : kr * ld * 2 + fi * 32 + (pc & 1) * 16;
  const unsigned lb = (unsigned)(size_t)lds;
  unsigned ns = lb;
  dma_first(ns + d1, v1, c1); dma_next<1024>(v1, c1 + s1); dma_next<2048>(v1, c1 + s2); dma_next<3072>(v1, c1 + s3);
  const char* c2;
  {
    const int hr = wi * 4 + ((lane >> 2) & 3);
    const unsigned oh = (14 + (hr >> 3)) * 1024 + (8 + (hr & 7)) * 64 + (((lane & 3) ^ (2 * (hr >> 3)) ^ 2) * 16);
    const unsigned ow = (kr + 16 * (wi >> 1) + 4 * (wi & 1)) * 7168 + (typeA ? fi * 32 : (fi >> 1) * 32) + (pc & 1) * 16;
    const char* be = g ? ws + WS_XN + (size_t)((mb > 0 ? mb - 1 : 0) * 32 * 16384) : (const char*)(typeA ? wu + fA : wg + fc);
    const char* bo = g ? be : (const char*)(wu + (typeA ? fA : fc));
    c2 = ((fi & 1) ? bo : be) + (g ? oh : ow);
  }
  auto adv = [&]() { c1 += p1; c2 += p2; ns = (ns == lb + 3 * G1_SLOT) ? lb : ns + G1_SLOT; };
  auto stage = [&]() {
    dma_first(ns + d1, v1, c1); dma_next<1024>(v1, c1 + s1); dma_next<2048>(v1, c1 + s2); dma_next<3072>(v1, c1 + s3);
    dma1m(ns + d2, c2, m2);
    adv();
  };
  dma1m(ns + d2, c2, m2);
  adv();

  f32x4 acc[4][10];
  f32x4 hacc = {0.f, 0.f, 0.f, 0.f};
#pragma unroll
  for (int i = 0; i < 4; ++i)
#pragma unroll
    for (int n = 0; n < 10; ++n) { acc[i][n] = (f32x4){0.f, 0.f, 0.f, 0.f}; asm volatile("" : "+v"(acc[i][n])); }
  const int rl = 4 * (fq & 1) + (fr >> 2);
  unsigned hoffA = lb + 16384 + fr * 64 + ((fq ^ (2 * (fr >> 3))) * 16);
  unsigned aoff = hoffA - 16384 + wi * 4096;
  unsigned boff0 = lb + G1_SA + (fq >> 1) * 2048 + rl * 128 + 32 * ((2 * g) ^ ((rl >> 1) & 3)) + 8 * (lane & 3);
  unsigned boff1 = boff0 ^ 32;
  unsigned hoffB = (boff0 ^ (32 * (wi & 1))) + (1 + (wi >> 1)) * 4096;
  asm volatile("" : "+v"(aoff), "+v"(hoffA), "+v"(boff0), "+v"(boff1), "+v"(hoffB), "+v"(hacc));
  {
    const int lo = fr + (wi == 0 ? -7 : (wi == 2 ? 1 : 9));
    const int hi = wi == 0 ? fr + 1 : ((wi >= 2 && mb == 0) ? 0 : (wi == 2 ? 8 : 16));
    f32x4 bv;
#pragma unroll
    for (int e = 0; e < 4; ++e) bv[e] = (4 * fq + e >= lo && 4 * fq + e < hi) ? 0.f : -3.0e38f;
    *(LAS f32x4*)(lds + T_BIAS + wi * 1024 + lane * 16) = bv;
  }
  stage();
  ST(1);
  WAIT_V(5);
  BAR();
  ST(2);
  if (g == 1) BAR();

  auto phase = [&](auto do_stage, int q) {
    constexpr bool DS = decltype(do_stage)::value;
    bf16x8 a[4], bfr[10], ha, hb;
    LT(0);
#define RB(c) bfr[2 * (c)] = cat(trd(lp(boff0) + (c) * 4096), trd(lp(boff0) + (c) * 4096 + 1024)); \
              bfr[2 * (c) + 1] = cat(trd(lp(boff1) + (c) * 4096), trd(lp(boff1) + (c) * 4096 + 1024));
    a[0] = *(const LAS bf16x8*)(lp(aoff)); a[1] = *(const LAS bf16x8*)(lp(aoff) + 1024);
    if (DS) dma_first(ns + d1, v1, c1);
    a[2] = *(const LAS bf16x8*)(lp(aoff) + 2048); a[3] = *(const LAS bf16x8*)(lp(aoff) + 3072);
    RB(0)
    if (DS) dma_next<1024>(v1, c1 + s1);
    RB(1)
    if (DS) dma_next<2048>(v1, c1 + s2);
    RB(2)
    if (DS) dma_next<3072>(v1, c1 + s3);
    RB(3)
    if (DS) dma1m(ns + d2, c2, m2);
    RB(4)
    { ha = *(const LAS bf16x8*)lp(hoffA); hb = cat(trd(lp(hoffB)), trd(lp(hoffB) + 1024)); }
#undef RB
    SCHED();
    LT(1);
    const int dlt = ((q & 3) == 3) ? -3 * G1_SLOT : G1_SLOT;
    aoff += dlt; hoffA += dlt; boff0 += dlt; boff1 += dlt; hoffB += dlt;
    if (DS) {
      adv();
      if (q == 1) stage();
      LT(2);
      if (q == 0) WAIT_V(5); else WAIT_V(10);
    } else {
      LT(2);
      if (q == 29) {
        WAIT_V(0);
        const int l = __builtin_amdgcn_mbcnt_hi(~0u, __builtin_amdgcn_mbcnt_lo(~0u, 0u));
        int t = w * 64 + l;
        t = t < 288 ? t : t - 288;
        const int rr = (t >> 4) * 64 + (xcd & 1) * 32 + idx;
        touch(lb + T_SCR, (rr < 256 ? wo + (256 * kq + rr) * 1024 : wd + (896 * kq + rr - 256) * 1024) + (t & 15) * 64);
      }
    }
    WAIT_L0();
    LT(3);
    SCHED();
    BAR();
    SCHED();
    LT(4);
    PRIO_M();
#pragma unroll
    for (int n = 0; n < 10; ++n)
#pragma unroll
      for (int i = 0; i < 4; ++i) {
        if (n == 9 && i == 4 - EARLY) { SCHED(); PRIO_T(); if (q < 31 || g == 0) BAR(); SCHED(); }
        acc[i][n] = mfma(bfr[n], a[i], acc[i][n]);
      }
    hacc = mfma(hb, ha, hacc);
    PRIO_E();
    SCHED();
    LT(5);
    SCHED();
  };
#pragma clang loop unroll(disable)
  for (int q = 0; q < 29; ++q) phase(std::true_type{}, q);
#pragma clang loop unroll(disable)
  for (int q = 29; q < 32; ++q) phase(std::false_type{}, q);

  ST(3);
  asm volatile("" ::: "memory");
  if (g == 0) __builtin_amdgcn_s_setprio(0); else __builtin_amdgcn_s_setprio(1);
  lane = __builtin_amdgcn_mbcnt_hi(~0u, __builtin_amdgcn_mbcnt_lo(~0u, 0u));
  asm volatile("" : "+v"(lane));
  fr = lane & 15; fq = lane >> 4;
  const auto hrs = __builtin_amdgcn_make_buffer_rsrc(ws + WS_H + (size_t)(mb * 4 + kq) * 36 * 16384, 0, 36 * 16384, 0x00020000);
  const unsigned hv = wi * 4096 + lane * 16;
#define GLU(i, a, b) glu4(acc[i][a], acc[i][b])
  if (!typeA) {
    const int kba = 8 + ((fa - 896 * kq) >> 5) + g, kbc = 8 + ((fc - 896 * kq) >> 5);
#pragma unroll
    for (int i = 0; i < 4; ++i) {
      __builtin_amdgcn_raw_buffer_store_b128(cat4(GLU(i, 0, 4), GLU(i, 1, 5)), hrs, hv + kba * 16384 + i * 1024, 0, H_AUX);
      __builtin_amdgcn_raw_buffer_store_b128(cat4(GLU(i, 2, 6), GLU(i, 3, 7)), hrs, hv + kba * 16384 + 32768 + i * 1024, 0, H_AUX);
      __builtin_amdgcn_raw_buffer_store_b64(GLU(i, 8, 9), hrs, hv + kbc * 16384 + g * 8 + i * 1024, 0, H_AUX);
    }
  } else {
    LAS char* kim = lds + g * 40960;
    LAS char* vim = kim + 17 * 1024;
    bf16x8 kf[4], qf[4];
#define FIX(i, n) acc[i][n]
#pragma unroll
    for (int i = 0; i < 4; ++i) {
      kf[i] = pack8(FIX(i, 2), FIX(i, 3));
      *(LAS bf16x8*)(vim + ((wi * 4 + i) * 16 + fr) * 80 + fq * 16) = pack8(FIX(i, 4), FIX(i, 5));
    }
    *(LAS bf16x8*)(kim + (wi * 4 + 2) * 1024 + lane * 16) = kf[2];
    *(LAS bf16x8*)(kim + (wi * 4 + 3) * 1024 + lane * 16) = kf[3];
    {
      const f32x4 hh = hacc;
      LAS char* hp = (wi < 2) ? (kim + 16 * 1024 + lane * 16 + wi * 8) : (vim + (256 + fr) * 80 + fq * 16 + (wi - 2) * 8);
      *(LAS u32x2*)hp = (u32x2){pk2(hh[0], hh[1]), pk2(hh[2], hh[3])};
    }
    auto glu_part = [&]() {
#pragma unroll
      for (int i = 0; i < 4; ++i)
        __builtin_amdgcn_raw_buffer_store_b128(cat4(GLU(i, 6, 8), GLU(i, 7, 9)), hrs, hv + (8 + 2 * j + g) * 16384 + i * 1024, 0, H_AUX);
    };
    WAIT_L0();
    ET(0);
    ET(1);
    BAR();
    ET(2);
    asm volatile("" ::: "memory");
    const bool halo = wi == 0;
    const f32x4 bc = *(const LAS f32x4*)(lds + T_BIAS + lane * 16), bp = *(const LAS f32x4*)(lds + T_BIAS + 1024 + lane * 16);
    const f32x4 bp0 = *(const LAS f32x4*)(lds + T_BIAS + (halo ? 2048 : 1024) + lane * 16);
    const f32x4 bp1 = *(const LAS f32x4*)(lds + T_BIAS + (halo ? 3072 : 1024) + lane * 16);
    const int pf0 = halo ? 16 : wi * 4 - 2, pf1 = halo ? 16 : wi * 4 - 1;
    const bf16x8 kp0 = *(const LAS bf16x8*)(kim + pf0 * 1024 + lane * 16);
    const bf16x8 kp1 = *(const LAS bf16x8*)(kim + pf1 * 1024 + lane * 16);
    const LAS char* vb = vim + (4 * fq + (fr >> 2)) * 80 + (lane & 3) * 16;
    bf16x4 vt[6][2];
#pragma unroll
    for (int d = 0; d < 2; ++d) {
      vt[0][d] = trd(vb + pf0 * 1280 + d * 8); vt[1][d] = trd(vb + pf1 * 1280 + d * 8);
#pragma unroll
      for (int i = 0; i < 4; ++i) vt[2 + i][d] = trd(vb + (wi * 4 + i) * 1280 + d * 8);
    }
    f32x4 sp[4], sc[4];
#pragma unroll
    for (int i = 0; i < 4; ++i) {
      qf[i] = pack8(acc[i][0], acc[i][1]);
      sc[i] = mfma(kf[i], qf[i], bc);
    }
    sp[2] = mfma(kf[0], qf[2], bp); sp[3] = mfma(kf[1], qf[3], bp);
    sp[0] = mfma(kp0, qf[0], bp0); sp[1] = mfma(kp1, qf[1], bp1);
    const float sl = 0.17677669529663687f * 1.4426950408889634f;
    const f32x4 z4 = {0.f, 0.f, 0.f, 0.f};
    float m[4];
#pragma unroll
    for (int i = 0; i < 4; ++i)
      m[i] = fmx(fmx(fmx(fmx(sp[i][0], sp[i][1]), sp[i][2]), fmx(fmx(sp[i][3], sc[i][0]), sc[i][1])), fmx(sc[i][2], sc[i][3]));
    {
      const unsigned t = __float_as_uint(tred<true>(m[0], m[1], m[2], m[3]));
      const auto u = __builtin_amdgcn_permlane32_swap(t, t, false, false);
      const auto u0 = __builtin_amdgcn_permlane16_swap(u[0], u[0], false, false), u1 = __builtin_amdgcn_permlane16_swap(u[1], u[1], false, false);
      m[0] = __uint_as_float(u0[0]); m[1] = __uint_as_float(u0[1]); m[2] = __uint_as_float(u1[0]); m[3] = __uint_as_float(u1[1]);
    }
    const u32x4 one4 = {0x3f803f80u, 0x3f803f80u, 0x3f803f80u, 0x3f803f80u};
#pragma unroll
    for (int i = 0; i < 4; ++i) {
      const float mm = -sl * m[i];
#pragma unroll
      for (int e = 0; e < 4; ++e) {
        sp[i][e] = __builtin_amdgcn_exp2f(__builtin_fmaf(sp[i][e], sl, mm));
        sc[i][e] = __builtin_amdgcn_exp2f(__builtin_fmaf(sc[i][e], sl, mm));
      }
      const bf16x8 pfr = pack8(sp[i], sc[i]);
      const f32x4 sm = mfma(__builtin_bit_cast(bf16x8, one4), pfr, z4);
      const f32x4 o0 = mfma(cat(vt[i][0], vt[2 + i][0]), pfr, z4);
      const f32x4 o1 = mfma(cat(vt[i][1], vt[2 + i][1]), pfr, z4);
      const float inv = __builtin_amdgcn_rcpf(sm[0]);
      __builtin_amdgcn_raw_buffer_store_b128(__builtin_bit_cast(u32x4, pack8(o0 * inv, o1 * inv)), hrs, hv + (2 * j + g) * 16384 + i * 1024, 0, H_AUX);
    }
    glu_part();
    ET(5);
  }
  ST(4);
  ST(5);
  ST_OUT(0, 6);
}

constexpr int G2_SA = 32768, G2_SB = 16384, G2_SLOT = G2_SA + G2_SB, G2_LDS = 3 * G2_SLOT;

#define PREW 0
extern "C" __global__ __launch_bounds__(512 + 64 * PREW, 2 + PREW) void psum_g2_kernel(const u16* __restrict__ x, const u16* __restrict__ wo,
                                                       const u16* __restrict__ wd, char* __restrict__ ws,
                                                       u16* __restrict__ out) {
  extern __shared__ __attribute__((aligned(16))) char smem[];
  LAS char* lds = (LAS char*)smem;
  ST_DECL LT_DECL ST(0);
  const int tid = threadIdx.x, lane = tid & 63;
  const int w = __builtin_amdgcn_readfirstlane(tid >> 6), g = w >> 2, wi = w & 3;
  const int b = blockIdx.x, xcd = b & 7, idx = b >> 3;
  const int mb = (xcd & 1) * 4 + (idx & 3), kq = xcd >> 1, nt = idx >> 2;
  const int fr = lane & 15, fq = lane >> 4;

  const char* ca = ws + WS_H + (size_t)(mb * 4 + kq) * 36 * 16384 + w * 4096;
  const unsigned va = lane * 16;
  const int e0 = (0 ^ kq) * 1024, e1 = ((1 ^ kq) - 1) * 1024, e2 = ((2 ^ kq) - 2) * 1024, e3 = ((3 ^ kq) - 3) * 1024;
  const int rr = lane >> 3, pc = lane & 7;
  const unsigned vb = rr * 2048 + (((pc >> 1) ^ ((rr >> 1) & 3)) * 32) + (pc & 1) * 16;
  const char* cb = (const char*)(wo + (size_t)(256 * kq + 8 * w) * 1024 + nt * 128);
  const char* cb2 = (const char*)(wd + (size_t)(896 * kq + 8 * w) * 1024 + nt * 128);
  int st = 0;
  const unsigned lb = (unsigned)(size_t)lds;
  unsigned ns = lb;
  auto adv = [&]() { ca += 32768; ++st; cb = (st == 4) ? cb2 : cb + 64 * 2048; ns = (ns == lb + 2 * G2_SLOT) ? lb : ns + G2_SLOT; };
  auto stage = [&]() {
    dma_first(ns + w * 4096, va, ca + e0); dma_next<1024>(va, ca + e1); dma_next<2048>(va, ca + e2); dma_next<3072>(va, ca + e3);
    dma_first(ns + G2_SA + w * 2048, vb, cb); dma_next<1024>(vb, cb - 896);
    adv();
  };
  stage();
  BAR();
  stage();
  const int mf = wi * 4 + kq;
  const unsigned orow = ((mb * 256 + 32 * (mf >> 1) + 2 * fr + (mf & 1)) * 1024 + nt * 128 + g * 64 + 16 * (fq & 1) + 8 * (fq >> 1)) * 2;
  u32x4 xr[2] = {{0u, 0u, 0u, 0u}, {0u, 0u, 0u, 0u}};
  const int rl = 4 * (fq & 1) + ((lane & 15) >> 2);
  unsigned aoff = lb + wi * 4096 + lane * 16;
  unsigned boff = lb + G2_SA + (fq >> 1) * 2048 + g * 1024 + rl * 128 + 32 * ((rl >> 1) & 3) + 8 * (lane & 3);

  f32x4 acc[4][4];
#pragma unroll
  for (int i = 0; i < 4; ++i)
#pragma unroll
    for (int n = 0; n < 4; ++n) { acc[i][n] = (f32x4){0.f, 0.f, 0.f, 0.f}; asm volatile("" : "+v"(acc[i][n])); }
  ST(1);
  WAIT_V(6);
  BAR();
  ST(2);
  if (g == 1) BAR();

  int sl = 0;
  auto phase = [&](auto do_stage, int q) {
    constexpr bool DS = decltype(do_stage)::value;
    bf16x8 a[2][4], bfr[2][4];
    LT(0);
#define RA(kk, i) a[kk][i] = *(const LAS bf16x8*)(lp(aoff) + (kk) * 16384 + (i) * 1024);
#define RB(kk, n) bfr[kk][n] = cat(trd(lp(boff ^ ((n) * 32)) + (kk) * 8192), trd(lp(boff ^ ((n) * 32)) + (kk) * 8192 + 4096));
#define D0 if (DS) dma_first(ns + w * 4096, va, ca + e0);
#define D1 if (DS) dma_next<1024>(va, ca + e1);
#define D2 if (DS) dma_next<2048>(va, ca + e2);
#define D3 if (DS) dma_next<3072>(va, ca + e3);
#define D4 if (DS) dma_first(ns + G2_SA + w * 2048, vb, cb);
#define D5 if (DS) dma_next<1024>(vb, cb - 896);
#define UPD sl = (sl == 2) ? 0 : sl + 1; dlt = (sl == 0) ? -2 * G2_SLOT : G2_SLOT; if (DS) adv(); \
    asm volatile("" : "+s"(ca), "+s"(cb), "+s"(ns), "+s"(sl), "+s"(dlt) :: "memory");
    int dlt;
    if (!DS && q == 16)
      asm volatile("global_load_dwordx4 %0, %2, %3\n\tglobal_load_dwordx4 %1, %2, %3 offset:64" : "+v"(xr[0]), "+v"(xr[1]) : "v"(orow), "s"(x) : "memory");
    RA(0, 0) RA(0, 1) D0 RA(0, 2) RA(0, 3) RB(0, 0) D1 RB(0, 1) RB(0, 2) RB(0, 3) D2 RA(1, 0) RA(1, 1) RA(1, 2) D3 RA(1, 3) RB(1, 0) D4 RB(1, 1) D5 UPD RB(1, 2) RB(1, 3)
    aoff += dlt;
#undef RA
#undef RB
    boff += dlt;
    SCHED();
    LT(1);
    LT(2);
    asm volatile("" : "+v"(boff), "+v"(aoff));
    if (DS) WAIT_V_IF0(6, g ^ 1); else if (q == 16) WAIT_V_IF0(2, g ^ 1);
    WAIT_L0();
    LT(3);
    SCHED();
    BAR();
    SCHED();
    LT(4);
    PRIO_M();
#pragma unroll
    for (int n = 0; n < 4; ++n)
#pragma unroll
      for (int i = 0; i < 4; ++i) {
        acc[i][n] = mfma(bfr[0][n], a[0][i], acc[i][n]);
        SCHED();
        if (n == 3 && i == 3) { PRIO_T(); if (DS) WAIT_V_IF0(6, g); else if (q == 16) WAIT_V_IF0(2, g); if (q < 17 || g == 0) BAR(); SCHED(); }
        acc[i][n] = mfma(bfr[1][n], a[1][i], acc[i][n]);
        SCHED();
      }
    PRIO_E();
    SCHED();
    LT(5);
    SCHED();
  };
  GAS unsigned* gcnt = (GAS unsigned*)(ws + WS_GCNT) + (mb * 4 + kq) * 16;
#pragma clang loop unroll(disable)
  for (int q = 0; q < 12; ++q) phase(std::true_type{}, q);
  if (tid == 0) __hip_atomic_fetch_add(gcnt, 1u, __ATOMIC_RELAXED, __HIP_MEMORY_SCOPE_AGENT);
#pragma clang loop unroll(disable)
  for (int q = 12; q < 16; ++q) phase(std::true_type{}, q);
  phase(std::false_type{}, 16);
  unsigned seen;
  asm volatile("global_load_dword %0, %1, %2 sc1" : "=v"(seen) : "v"(0u), "s"(gcnt) : "memory");
  phase(std::false_type{}, 17);
  asm volatile("s_waitcnt vmcnt(0)" : "+v"(seen) :: "memory");
  {
    unsigned spins = 0;
    while ((unsigned)__builtin_amdgcn_readfirstlane(seen) < 8u) {
      seen = __hip_atomic_load(gcnt, __ATOMIC_RELAXED, __HIP_MEMORY_SCOPE_AGENT);
      if (++spins > (1u << 22)) break;
    }
  }
  asm volatile("" ::: "memory");

  ST(3);
  const int tile = mb * 8 + nt;
  const auto rs = __builtin_amdgcn_make_buffer_rsrc(ws + WS_H + (size_t)mb * 4 * 36 * 16384 + nt * 49152 + w * 2048, 0, 4 * 36 * 16384 - 8 * 49152, 0x00020000);
#define XOFF(src, dst, i) (unsigned)((src) * 36 * 16384 + ((i) - 1) * 16384) + lane * 16
#pragma unroll
  for (int i = 1; i < 4; ++i) {
    const unsigned o = XOFF(kq, kq ^ i, i);
    __builtin_amdgcn_raw_buffer_store_b128(__builtin_bit_cast(u32x4, pack8(acc[i][0], acc[i][1])), rs, o, 0, 16);
    __builtin_amdgcn_raw_buffer_store_b128(__builtin_bit_cast(u32x4, pack8(acc[i][2], acc[i][3])), rs, o + 1024, 0, 16);
  }
  WAIT_V(0);
  ST(4);
  asm volatile("" : "+v"(xr[0]), "+v"(xr[1]));
  GAS unsigned* cnt = (GAS unsigned*)(ws + WS_CNT) + (tile * 32 + w) * 16;
  if (lane >= 1 && lane < 4) __hip_atomic_fetch_add(cnt + (kq ^ lane) * 128, 1u, __ATOMIC_RELAXED, __HIP_MEMORY_SCOPE_AGENT);
  {
    unsigned spins = 0;
    while ((unsigned)__builtin_amdgcn_readfirstlane(__hip_atomic_load(cnt + kq * 128, __ATOMIC_RELAXED, __HIP_MEMORY_SCOPE_AGENT)) < 3u) {
      __builtin_amdgcn_s_sleep(2);
      if (++spins > (1u << 22)) break;
    }
  }
  asm volatile("" ::: "memory");
  ST(5);
  u32x4 p[3][2];
#pragma unroll
  for (int i = 1; i < 4; ++i) {
    const unsigned o = XOFF(kq ^ i, kq, i);
    p[i - 1][0] = __builtin_amdgcn_raw_buffer_load_b128(rs, o, 0, 16);
    p[i - 1][1] = __builtin_amdgcn_raw_buffer_load_b128(rs, o + 1024, 0, 16);
  }
  const auto ors = __builtin_amdgcn_make_buffer_rsrc(out, 0, 4 << 20, 0x00020000);
#pragma unroll
  for (int h = 0; h < 2; ++h) {
    const auto x0 = __builtin_amdgcn_permlane16_swap(xr[h][0], xr[h][2], false, false), x1 = __builtin_amdgcn_permlane16_swap(xr[h][1], xr[h][3], false, false);
    const u32x4 xv = {x0[0], x1[0], x0[1], x1[1]};
    unsigned r[4];
#pragma unroll
    for (int d = 0; d < 4; ++d) {
      float lo = acc[0][2 * h + (d >> 1)][2 * (d & 1)] + __uint_as_float(xv[d] << 16), hi = acc[0][2 * h + (d >> 1)][2 * (d & 1) + 1] + __uint_as_float(xv[d] & 0xffff0000u);
#pragma unroll
      for (int i = 0; i < 3; ++i) { lo += __uint_as_float(p[i][h][d] << 16); hi += __uint_as_float(p[i][h][d] & 0xffff0000u); }
      r[d] = pk2(lo, hi);
    }
    const auto y0 = __builtin_amdgcn_permlane16_swap(r[0], r[2], false, false), y1 = __builtin_amdgcn_permlane16_swap(r[1], r[3], false, false);
    __builtin_amdgcn_raw_buffer_store_b128((u32x4){y0[0], y1[0], y0[1], y1[1]}, ors, orow + 64 * h, 0, OUT_AUX);
  }
  ST(6);
  ST(7);
  ST_OUT(1, 8);
}

extern "C" void kernel_launch(void* const* d_in, const int* in_sizes, int n_in, void* d_out, int out_size, void* d_ws,
                              size_t ws_size, hipStream_t stream) {
  const u16* x = (const u16*)d_in[0];
  const u16* wqkv = (const u16*)d_in[1];
  const u16* wo = (const u16*)d_in[2];
  const u16* wg = (const u16*)d_in[4];
  const u16* wu = (const u16*)d_in[5];
  const u16* wd = (const u16*)d_in[6];
  char* ws = (char*)d_ws;
  (void)hipFuncSetAttribute((const void*)psum_g1_kernel, hipFuncAttributeMaxDynamicSharedMemorySize, G1_LDS);
  (void)hipFuncSetAttribute((const void*)psum_g2_kernel, hipFuncAttributeMaxDynamicSharedMemorySize, G2_LDS);
  psum_ln_kernel<<<256, 320, 0, stream>>>(x, wqkv, wg, wu, ws);
  psum_g1_kernel<<<256, 512, G1_LDS, stream>>>(wqkv, wg, wu, wo, wd, ws);
  psum_g2_kernel<<<256, 512 + 64 * PREW, G2_LDS, stream>>>(x, wo, wd, ws, (u16*)d_out);
}
```

```cpp
#include <hip/hip_runtime.h>
#include <hip/hip_bf16.h>
#include <type_traits>
#define EARLY 1
#define HALO_SKIP 0
#define PF_W2 1
#define PF_LOOP 0
#define PF_R0 192
#define PF_PER 2
#define PF_LN_ROWS 96
#define PRIO_L 1
#define PRIO_M() __builtin_amdgcn_s_setprio(0)
#define PRIO_T() __builtin_amdgcn_s_setprio(1)
#define PRIO_E()
#define PF_CODE 0
#define PRECLEAN 0
#define OUT_AUX 16
#define G2ORD 0
#define EARLY2 1
#define CHAIN 1
#define PF_X 0
#define XH 1
#define ACQ 0
#define H_AUX 16

namespace {

typedef unsigned short u16;
typedef short bf16x8 __attribute__((ext_vector_type(8)));
typedef short bf16x4 __attribute__((ext_vector_type(4)));
typedef float f32x4 __attribute__((ext_vector_type(4)));
typedef float f32x2 __attribute__((ext_vector_type(2)));
typedef unsigned u32x4 __attribute__((ext_vector_type(4)));
typedef unsigned u32x2 __attribute__((ext_vector_type(2)));
typedef __bf16 bf2 __attribute__((ext_vector_type(2)));
#define LAS __attribute__((address_space(3)))
#define GAS __attribute__((address_space(1)))

#define WAIT_V(n) asm volatile("s_waitcnt vmcnt(" #n ")" ::: "memory")
#define WAIT_V_IF0(n, c) asm volatile("s_cmp_lg_u32 %0, 0\n\ts_cbranch_scc1 1f\n\ts_waitcnt vmcnt(" #n ")\n1:" :: "s"(c) : "memory", "scc")
#define WAIT_L0() do { asm volatile("" ::: "memory"); __builtin_amdgcn_s_waitcnt(0xC07F); asm volatile("" ::: "memory"); } while (0)
#define BAR() __builtin_amdgcn_s_barrier()
#define SCHED() __builtin_amdgcn_sched_barrier(0)

__device__ __forceinline__ float b2f(u16 v) { return __uint_as_float(((unsigned)v) << 16); }
__device__ __forceinline__ unsigned pk2(float lo, float hi) {
  bf2 r = {(__bf16)lo, (__bf16)hi};
  return __builtin_bit_cast(unsigned, r);
}
__device__ __forceinline__ float rbf(float f) { return (float)(__bf16)f; }
__device__ __forceinline__ bf16x8 pack8(f32x4 a, f32x4 b) {
  u32x4 r = {pk2(a[0], a[1]), pk2(a[2], a[3]), pk2(b[0], b[1]), pk2(b[2], b[3])};
  return __builtin_bit_cast(bf16x8, r);
}
__device__ __forceinline__ bf16x4 trd(LAS const char* p) {
  return __builtin_amdgcn_ds_read_tr16_b64_v4i16((LAS bf16x4*)p);
}
__device__ __forceinline__ bf16x8 cat(bf16x4 a, bf16x4 b) { return __builtin_shufflevector(a, b, 0, 1, 2, 3, 4, 5, 6, 7); }
__device__ __forceinline__ f32x4 mfma(bf16x8 a, bf16x8 b, f32x4 c) {
  return __builtin_amdgcn_mfma_f32_16x16x32_bf16(a, b, c, 0, 0, 0);
}
__device__ __forceinline__ void dma_first(unsigned lds_dst, unsigned voff, const char* b0) {
  asm volatile("s_mov_b32 m0, %0\n\ts_nop 0\n\tglobal_load_lds_dwordx4 %1, %2" :: "s"(lds_dst), "v"(voff), "s"(b0) : "memory");
}
template <int OFF>
__device__ __forceinline__ void dma_next(unsigned voff, const char* b) {
  asm volatile("global_load_lds_dwordx4 %0, %1 offset:%2" :: "v"(voff), "s"(b), "i"(OFF) : "memory");
}
__device__ __forceinline__ void dmc_first(unsigned lds_dst, unsigned voff, const char* b0) {
  asm volatile("s_mov_b32 m0, %0\n\ts_nop 0\n\tglobal_load_lds_dwordx4 %1, %2 sc1" :: "s"(lds_dst), "v"(voff), "s"(b0) : "memory");
}
template <int OFF>
__device__ __forceinline__ void dmc_next(unsigned voff, const char* b) {
  asm volatile("global_load_lds_dwordx4 %0, %1 offset:%2 sc1" :: "v"(voff), "s"(b), "i"(OFF) : "memory");
}
__device__ __forceinline__ void dma1m(unsigned lds_dst, const char* p, unsigned long long mask) {
  asm volatile("s_mov_b32 m0, %0\n\ts_mov_b64 exec, %2\n\t"
               "global_load_lds_dwordx4 %1, off\n\t"
               "s_mov_b64 exec, -1"
               :: "s"(lds_dst), "v"(p), "s"(mask) : "memory");
}
__device__ __forceinline__ void touch(unsigned lds_dst, const void* p) {
  asm volatile("s_mov_b32 m0, %0\n\ts_nop 0\n\tglobal_load_lds_dword %1, off" :: "s"(lds_dst), "v"(p) : "memory");
}
#define CODE_ADDR(dst, sym) do { unsigned lo_, hi_; asm volatile("s_getpc_b64 s[90:91]\n\ts_add_u32 %0, s90, " #sym "@rel32@lo+4\n\ts_addc_u32 %1, s91, " #sym "@rel32@hi+12" \
    : "=s"(lo_), "=s"(hi_) :: "s90", "s91", "scc"); dst = (const char*)(((unsigned long long)hi_ << 32) | lo_); } while (0)
constexpr int LN_LINES = 16, G1_LINES = 80, G2_LINES = 30;
__device__ __forceinline__ LAS char* lp(unsigned a) { return (LAS char*)(size_t)a; }
__device__ __forceinline__ float fmx(float a, float b) { return __builtin_elementwise_maximum(a, b); }
__device__ __forceinline__ float xmax(float v) {
  auto a = __builtin_amdgcn_permlane16_swap(__float_as_uint(v), __float_as_uint(v), false, false);
  v = fmaxf(__uint_as_float(a[0]), __uint_as_float(a[1]));
  auto b = __builtin_amdgcn_permlane32_swap(__float_as_uint(v), __float_as_uint(v), false, false);
  return fmaxf(__uint_as_float(b[0]), __uint_as_float(b[1]));
}
__device__ __forceinline__ float xsum(float v) {
  auto a = __builtin_amdgcn_permlane16_swap(__float_as_uint(v), __float_as_uint(v), false, false);
  v = __uint_as_float(a[0]) + __uint_as_float(a[1]);
  auto b = __builtin_amdgcn_permlane32_swap(__float_as_uint(v), __float_as_uint(v), false, false);
  return __uint_as_float(b[0]) + __uint_as_float(b[1]);
}

template <int CTRL>
__device__ __forceinline__ float dpp_add(float v) {
  return v + __uint_as_float(__builtin_amdgcn_update_dpp(0u, __float_as_uint(v), CTRL, 0xf, 0xf, true));
}
__device__ __forceinline__ float hsum(float v) {
  v = dpp_add<0xB1>(v);
  v = dpp_add<0x4E>(v);
  v = dpp_add<0x128>(v);
  return xsum(v);
}

#define DG(kid, slot)
#define ST_DECL
#define LT_DECL
#define LT(k)
#define ET(k)
#define ST(k)
#define ST_OUT(kid, n)
#define ST_SAVE()
#define ST_REST()
#define WS_BASE_MB 0
constexpr size_t WS_XN = (size_t)WS_BASE_MB << 20;
constexpr size_t WS_H = WS_XN + (size_t)4 * 1024 * 1024;
constexpr size_t H_BYTES = (size_t)8 * 4 * 36 * 16 * 1024;
constexpr size_t WS_X = WS_H + H_BYTES;
constexpr size_t X_BYTES = (size_t)64 * 4 * 3 * 8 * 2048;
constexpr size_t WS_CNT = WS_X + X_BYTES;
constexpr size_t WS_GCNT = WS_CNT + 131072;
constexpr size_t WS_HCNT = WS_GCNT + 2048;
constexpr size_t WS_ST = WS_GCNT + 4096;

}
extern "C" __global__ __launch_bounds__(320) void psum_ln_kernel(const u16* __restrict__ x, const u16* __restrict__ wqkv, const u16* __restrict__ wg,
                                                                 const u16* __restrict__ wu, char* __restrict__ ws) {
  const int lane = threadIdx.x & 63, b = blockIdx.x;
#define DGT 0
  DG(0, 0);
  if (threadIdx.x >= 256) {
    if (lane < 8) *(unsigned*)(ws + WS_CNT + (b * 8 + lane) * 64) = 0u;
    if (lane == 8 && b < 64) *(unsigned*)(ws + WS_GCNT + b * 64) = 0u;
    __shared__ __attribute__((aligned(16))) char sink[256];
    const int kq = (b & 7) >> 1, l = lane < 40 ? lane : lane - 40;
    const u16* p = l < 12 ? wqkv + (l >> 2) * 1024 + 256 * kq + (l & 3) * 64 : (l < 26 ? wg + 896 * kq + (l - 12) * 64 : wu + 896 * kq + (l - 26) * 64);
    const int ld = l < 12 ? 3072 : 3584;
#pragma unroll
    for (int i = 0; i < PF_LN_ROWS / 32; ++i) touch((unsigned)(size_t)(LAS char*)sink, p + (i * 32 + (b >> 3)) * ld);
    return;
  }
  const int hf = (b & 1) * 128 + (b >> 1);
  const int mb = hf >> 5, mf = (hf >> 1) & 15, r = (hf & 1) * 8 + (threadIdx.x >> 6) * 2 + ((lane >> 2) & 1);
  const int row = mb * 256 + 32 * (mf >> 1) + 2 * r + (mf & 1), kl = lane >> 3, ch = lane & 3;
  const u16* xr = x + row * 1024 + kl * 32 + ch * 8;
  u32x4 raw[4];
#pragma unroll
  for (int i = 0; i < 4; ++i) raw[i] = *(const u32x4*)(xr + 256 * i);
  float v[32];
#pragma unroll
  for (int i = 0; i < 16; ++i) { v[2 * i] = __uint_as_float(raw[i >> 2][i & 3] << 16); v[2 * i + 1] = __uint_as_float(raw[i >> 2][i & 3] & 0xffff0000u); }
  float s = 0.f;
#pragma unroll
  for (int i = 0; i < 32; ++i) s += v[i];
  const float mean = hsum(s) * (1.0f / 1024.0f);
  float q = 0.f;
#pragma unroll
  for (int i = 0; i < 32; ++i) { const float d = v[i] - mean; q += d * d; }
  q = hsum(q);
  const float mb16 = rbf(mean), den = rbf(sqrtf(rbf(rbf(q * (1.0f / 1024.0f)) + 1e-5f)));
  const float inv = 1.0f / den;
  const auto rs = __builtin_amdgcn_make_buffer_rsrc(ws + WS_XN, 0, 4 << 20, 0x00020000);
  const unsigned o = ((mb * 32 + kl) * 16 + mf) * 1024 + r * 64 + ((ch ^ (2 * (r >> 3))) * 16);
#pragma unroll
  for (int i = 0; i < 4; ++i) {
    u32x4 t;
#pragma unroll
    for (int e = 0; e < 4; ++e) t[e] = pk2(rbf(v[8 * i + 2 * e] - mb16) * inv, rbf(v[8 * i + 2 * e + 1] - mb16) * inv);
    __builtin_amdgcn_raw_buffer_store_b128(t, rs, o + i * 8 * 16384, 0, H_AUX);
  }
  DG(0, 1);
}
namespace {
constexpr int G1_SA = 17 * 1024, G1_SB = 20 * 1024, G1_SLOT = G1_SA + G1_SB;
constexpr int T_SCR = 159488;
constexpr int T_BIAS = T_SCR + 256;
constexpr int G1_LDS = T_BIAS + 4096;
constexpr int G2_SA = 32768, G2_SB = 16384, G2_SLOT = G2_SA + G2_SB;
constexpr int T_KV = 2 * G2_SLOT, KV_BYTES = 7 * 1024 + 272 * 80;

template <bool MAX>
__device__ __forceinline__ float tred(float a, float b, float c, float d) {
  const auto r1 = __builtin_amdgcn_permlane16_swap(__float_as_uint(a), __float_as_uint(b), false, false);
  const auto r2 = __builtin_amdgcn_permlane16_swap(__float_as_uint(c), __float_as_uint(d), false, false);
  const float x0 = __uint_as_float(r1[0]), x1 = __uint_as_float(r1[1]), y0 = __uint_as_float(r2[0]), y1 = __uint_as_float(r2[1]);
  const float t = MAX ? fmx(x0, x1) : x0 + x1, u = MAX ? fmx(y0, y1) : y0 + y1;
  const auto r3 = __builtin_amdgcn_permlane32_swap(__float_as_uint(t), __float_as_uint(u), false, false);
  const float z0 = __uint_as_float(r3[0]), z1 = __uint_as_float(r3[1]);
  return MAX ? fmx(z0, z1) : z0 + z1;
}
__device__ __forceinline__ u32x2 glu4(f32x4 g, f32x4 u) {
  f32x4 e = g * -1.4426950408889634f;
#pragma unroll
  for (int i = 0; i < 4; ++i) e[i] = __builtin_amdgcn_exp2f(e[i]);
  e = e + 1.0f;
#pragma unroll
  for (int i = 0; i < 4; ++i) e[i] = __builtin_amdgcn_rcpf(e[i]);
  e = e * u;
  return (u32x2){pk2(e[0], e[1]), pk2(e[2], e[3])};
}
__device__ __forceinline__ u32x4 cat4(u32x2 a, u32x2 b) { return (u32x4){a[0], a[1], b[0], b[1]}; }

}
extern "C" __global__ __launch_bounds__(512, 2) void psum_g1_kernel(const u16* __restrict__ x, u16* __restrict__ out, const u16* __restrict__ wqkv,
                                                                    const u16* __restrict__ wg, const u16* __restrict__ wu,
                                                                    const u16* __restrict__ wo, const u16* __restrict__ wd,
                                                                    char* __restrict__ ws) {
  extern __shared__ __attribute__((aligned(16))) char smem[];
  LAS char* lds = (LAS char*)smem;
  ST_DECL LT_DECL ST(0);
  const int tid = threadIdx.x;
  int lane = tid & 63;
  const int w = __builtin_amdgcn_readfirstlane(tid >> 6), g = w >> 2, wi = w & 3;
  const int b = blockIdx.x, xcd = b & 7, idx = b >> 3;
  const int mb = (xcd & 1) * 4 + (idx & 3), kq = xcd >> 1, ntl = idx >> 2;
  const bool typeA = ntl < 4;
  const int j = ntl & 3;
  int fr = lane & 15, fq = lane >> 4;
  const int fA = 896 * kq + 64 * j;
  const int f0 = 896 * kq + 256 + 160 * j, odd = j & 1;
  const int fa = f0 + 32 * odd, fb = fa + 64, fc = odd ? f0 : f0 + 128;

  asm volatile("" : "+s"(wqkv), "+s"(wg), "+s"(wu), "+s"(ws));
  const bool isq = typeA && wi < 3;
  const int ld = isq ? 3072 : 3584;
  const u16* q1 = isq ? wqkv + (wi * 1024 + 256 * kq + 64 * j) : ((!typeA && (wi & 2)) ? wu : wg) + (typeA ? fA : ((wi & 1) ? fb : fa));
  const char* c1 = g ? ws + WS_XN + (size_t)(mb * 32 * 16384 + wi * 4096) : (const char*)q1;
  const int s1 = g ? 0 : 8 * ld - 1024, s2 = g ? 0 : 32 * ld - 2048, s3 = g ? 0 : 40 * ld - 3072;
  const int p1 = g ? 16384 : 64 * ld, p2 = g ? 16384 : 64 * 3584;
  const int d1 = (g ? 0 : G1_SA) + wi * 4096, d2 = g ? 16384 + wi * 256 : G1_SA + 16384 + wi * 1024;
  const unsigned long long m2 = g ? 0xffffull : ~0ull;
  const int rr = lane >> 3, pc = lane & 7, fi = (pc >> 1) ^ ((rr >> 1) & 3), kr = (rr & 3) + 8 * (rr >> 2);
  const unsigned v1 = g ? lane * 16 : kr * ld * 2 + fi * 32 + (pc & 1) * 16;
  const unsigned lb = (unsigned)(size_t)lds;
  unsigned ns = lb;
  dma_first(ns + d1, v1, c1); dma_next<1024>(v1, c1 + s1); dma_next<2048>(v1, c1 + s2); dma_next<3072>(v1, c1 + s3);
  const char* c2;
  {
    const int hr = wi * 4 + ((lane >> 2) & 3);
    const unsigned oh = (14 + (hr >> 3)) * 1024 + (8 + (hr & 7)) * 64 + (((lane & 3) ^ (2 * (hr >> 3)) ^ 2) * 16);
    const unsigned ow = (kr + 16 * (wi >> 1) + 4 * (wi & 1)) * 7168 + (typeA ? fi * 32 : (fi >> 1) * 32) + (pc & 1) * 16;
    const char* be = g ? ws + WS_XN + (size_t)((mb > 0 ? mb - 1 : 0) * 32 * 16384) : (const char*)(typeA ? wu + fA : wg + fc);
    const char* bo = g ? be : (const char*)(wu + (typeA ? fA : fc));
    c2 = ((fi & 1) ? bo : be) + (g ? oh : ow);
  }
  auto adv = [&]() { c1 += p1; c2 += p2; ns = (ns == lb + 3 * G1_SLOT) ? lb : ns + G1_SLOT; };
  auto stage = [&]() {
    dma_first(ns + d1, v1, c1); dma_next<1024>(v1, c1 + s1); dma_next<2048>(v1, c1 + s2); dma_next<3072>(v1, c1 + s3);
    dma1m(ns + d2, c2, m2);
    adv();
  };
  dma1m(ns + d2, c2, m2);
  adv();

  f32x4 acc[4][10];
  f32x4 hacc = {0.f, 0.f, 0.f, 0.f};
#pragma unroll
  for (int i = 0; i < 4; ++i)
#pragma unroll
    for (int n = 0; n < 10; ++n) { acc[i][n] = (f32x4){0.f, 0.f, 0.f, 0.f}; asm volatile("" : "+v"(acc[i][n])); }
  const int rl = 4 * (fq & 1) + (fr >> 2);
  unsigned hoffA = lb + 16384 + fr * 64 + ((fq ^ (2 * (fr >> 3))) * 16);
  unsigned aoff = hoffA - 16384 + wi * 4096;
  unsigned boff0 = lb + G1_SA + (fq >> 1) * 2048 + rl * 128 + 32 * ((2 * g) ^ ((rl >> 1) & 3)) + 8 * (lane & 3);
  unsigned boff1 = boff0 ^ 32;
  unsigned hoffB = (boff0 ^ (32 * (wi & 1))) + (1 + (wi >> 1)) * 4096;
  asm volatile("" : "+v"(aoff), "+v"(hoffA), "+v"(boff0), "+v"(boff1), "+v"(hoffB), "+v"(hacc));
  {
    const int lo = fr + (wi == 0 ? -7 : (wi == 2 ? 1 : 9));
    const int hi = wi == 0 ? fr + 1 : ((wi >= 2 && mb == 0) ? 0 : (wi == 2 ? 8 : 16));
    f32x4 bv;
#pragma unroll
    for (int e = 0; e < 4; ++e) bv[e] = (4 * fq + e >= lo && 4 * fq + e < hi) ? 0.f : -3.0e38f;
    *(LAS f32x4*)(lds + T_BIAS + wi * 1024 + lane * 16) = bv;
  }
  stage();
  ST(1);
  WAIT_V(5);
  BAR();
  ST(2);
  if (g == 1) BAR();

  auto phase = [&](auto do_stage, int q) {
    constexpr bool DS = decltype(do_stage)::value;
    bf16x8 a[4], bfr[10], ha, hb;
    LT(0);
#define RB(c) bfr[2 * (c)] = cat(trd(lp(boff0) + (c) * 4096), trd(lp(boff0) + (c) * 4096 + 1024)); \
              bfr[2 * (c) + 1] = cat(trd(lp(boff1) + (c) * 4096), trd(lp(boff1) + (c) * 4096 + 1024));
    a[0] = *(const LAS bf16x8*)(lp(aoff)); a[1] = *(const LAS bf16x8*)(lp(aoff) + 1024);
    if (DS) dma_first(ns + d1, v1, c1);
    a[2] = *(const LAS bf16x8*)(lp(aoff) + 2048); a[3] = *(const LAS bf16x8*)(lp(aoff) + 3072);
    RB(0)
    if (DS) dma_next<1024>(v1, c1 + s1);
    RB(1)
    if (DS) dma_next<2048>(v1, c1 + s2);
    RB(2)
    if (DS) dma_next<3072>(v1, c1 + s3);
    RB(3)
    if (DS) dma1m(ns + d2, c2, m2);
    RB(4)
    { ha = *(const LAS bf16x8*)lp(hoffA); hb = cat(trd(lp(hoffB)), trd(lp(hoffB) + 1024)); }
#undef RB
    SCHED();
    LT(1);
    const int dlt = ((q & 3) == 3) ? -3 * G1_SLOT : G1_SLOT;
    aoff += dlt; hoffA += dlt; boff0 += dlt; boff1 += dlt; hoffB += dlt;
    if (DS) {
      adv();
      if (q == 1) stage();
      LT(2);
      if (q == 0) WAIT_V(5); else WAIT_V(10);
    } else {
      LT(2);
      if (q == 29) {
        WAIT_V(0);
        const int l = __builtin_amdgcn_mbcnt_hi(~0u, __builtin_amdgcn_mbcnt_lo(~0u, 0u));
        int t = w * 64 + l;
        t = t < 288 ? t : t - 288;
        const int rr = (t >> 4) * 64 + (xcd & 1) * 32 + idx;
        touch(lb + T_SCR, (rr < 256 ? wo + (256 * kq + rr) * 1024 : wd + (896 * kq + rr - 256) * 1024) + (t & 15) * 64);
      }
    }
    WAIT_L0();
    LT(3);
    SCHED();
    BAR();
    SCHED();
    LT(4);
    PRIO_M();
#pragma unroll
    for (int n = 0; n < 10; ++n)
#pragma unroll
      for (int i = 0; i < 4; ++i) {
        if (n == 9 && i == 4 - EARLY) { SCHED(); PRIO_T(); if (q < 31 || g == 0) BAR(); SCHED(); }
        acc[i][n] = mfma(bfr[n], a[i], acc[i][n]);
      }
    hacc = mfma(hb, ha, hacc);
    PRIO_E();
    SCHED();
    LT(5);
    SCHED();
  };
#pragma clang loop unroll(disable)
  for (int q = 0; q < 29; ++q) phase(std::true_type{}, q);
#pragma clang loop unroll(disable)
  for (int q = 29; q < 32; ++q) phase(std::false_type{}, q);

  ST(3);
  asm volatile("" ::: "memory");
  if (g == 0) __builtin_amdgcn_s_setprio(0); else __builtin_amdgcn_s_setprio(1);
  lane = __builtin_amdgcn_mbcnt_hi(~0u, __builtin_amdgcn_mbcnt_lo(~0u, 0u));
  asm volatile("" : "+v"(lane));
  fr = lane & 15; fq = lane >> 4;
  const int nt = ntl;
  const int kba = 8 + ((fa - 896 * kq) >> 5);
  const int own0 = typeA ? j : (kba >> 1), own1 = typeA ? 4 + j : (kba >> 1) + 1;
  const char* const hbase = ws + WS_H + (size_t)(mb * 4 + kq) * 36 * 16384;
  const char* const wo_ = (const char*)(wo + (size_t)(256 * kq + 8 * w) * 1024 + nt * 128);
  const char* const wd_ = (const char*)(wd + (size_t)(896 * kq - 256 + 8 * w) * 1024 + nt * 128);
  auto brow = [&](int step) { return (step < 4 ? wo_ : wd_) + step * (64 * 2048); };
  const unsigned va = lane * 16;
  const unsigned vb = (lane >> 3) * 2048 + ((((lane & 7) >> 1) ^ ((lane >> 4) & 3)) * 32) + (lane & 1) * 16;
  { const char* p0 = brow(own0); dma_first(lb + G2_SA + w * 2048, vb, p0); dma_next<1024>(vb, p0 - 896);
    const char* p1 = brow(own1); dma_first(lb + G2_SLOT + G2_SA + w * 2048, vb, p1); dma_next<1024>(vb, p1 - 896); }
  const auto hrs = __builtin_amdgcn_make_buffer_rsrc((void*)hbase, 0, 36 * 16384, 0x00020000);
  const unsigned hv = wi * 4096 + lane * 16;
  auto put = [&](u32x4 v, int s, int kb, int i) {
    __builtin_amdgcn_raw_buffer_store_b128(v, hrs, hv + kb * 16384 + i * 1024, 0, H_AUX);
    *(LAS u32x4*)(lds + s * G2_SLOT + g * 16384 + ((wi * 4096 + kq * 1024 + lane * 16) ^ (i * 1024))) = v;
  };
#define GLU(i, a, b) glu4(acc[i][a], acc[i][b])
  if (!typeA) {
    const int kbc = 8 + ((fc - 896 * kq) >> 5);
#pragma unroll
    for (int i = 0; i < 4; ++i) {
      __builtin_amdgcn_raw_buffer_store_b64(GLU(i, 8, 9), hrs, hv + kbc * 16384 + g * 8 + i * 1024, 0, H_AUX);
      put(cat4(GLU(i, 0, 4), GLU(i, 1, 5)), 0, kba + g, i);
      put(cat4(GLU(i, 2, 6), GLU(i, 3, 7)), 1, kba + 2 + g, i);
    }
    WAIT_V(12);
  } else {
    LAS char* kim = lds + T_KV + g * KV_BYTES;
    LAS char* vim = kim + 7 * 1024;
    bf16x8 kf[4], qf[4];
#pragma unroll
    for (int i = 0; i < 4; ++i) {
      kf[i] = pack8(acc[i][2], acc[i][3]);
      *(LAS bf16x8*)(vim + ((wi * 4 + i) * 16 + fr) * 80 + fq * 16) = pack8(acc[i][4], acc[i][5]);
    }
    if (wi < 3) {
      *(LAS bf16x8*)(kim + wi * 2048 + lane * 16) = kf[2];
      *(LAS bf16x8*)(kim + wi * 2048 + 1024 + lane * 16) = kf[3];
    }
    {
      LAS char* hp = (wi < 2) ? (kim + 6 * 1024 + lane * 16 + wi * 8) : (vim + (256 + fr) * 80 + fq * 16 + (wi - 2) * 8);
      *(LAS u32x2*)hp = (u32x2){pk2(hacc[0], hacc[1]), pk2(hacc[2], hacc[3])};
    }
    WAIT_L0();
    BAR();
    asm volatile("" ::: "memory");
    const bool halo = wi == 0;
    const f32x4 bc = *(const LAS f32x4*)(lds + T_BIAS + lane * 16), bp = *(const LAS f32x4*)(lds + T_BIAS + 1024 + lane * 16);
    const f32x4 bp0 = *(const LAS f32x4*)(lds + T_BIAS + (halo ? 2048 : 1024) + lane * 16);
    const f32x4 bp1 = *(const LAS f32x4*)(lds + T_BIAS + (halo ? 3072 : 1024) + lane * 16);
    const int pf0 = halo ? 16 : wi * 4 - 2, pf1 = halo ? 16 : wi * 4 - 1;
    const bf16x8 kp0 = *(const LAS bf16x8*)(kim + (halo ? 6 : 2 * wi - 2) * 1024 + lane * 16);
    const bf16x8 kp1 = *(const LAS bf16x8*)(kim + (halo ? 6 : 2 * wi - 1) * 1024 + lane * 16);
    const LAS char* vr = vim + (4 * fq + (fr >> 2)) * 80 + (lane & 3) * 16;
    bf16x4 vt[6][2];
#pragma unroll
    for (int d = 0; d < 2; ++d) {
      vt[0][d] = trd(vr + pf0 * 1280 + d * 8); vt[1][d] = trd(vr + pf1 * 1280 + d * 8);
#pragma unroll
      for (int i = 0; i < 4; ++i) vt[2 + i][d] = trd(vr + (wi * 4 + i) * 1280 + d * 8);
    }
    f32x4 sp[4], sc[4];
#pragma unroll
    for (int i = 0; i < 4; ++i) {
      qf[i] = pack8(acc[i][0], acc[i][1]);
      sc[i] = mfma(kf[i], qf[i], bc);
    }
    sp[2] = mfma(kf[0], qf[2], bp); sp[3] = mfma(kf[1], qf[3], bp);
    sp[0] = mfma(kp0, qf[0], bp0); sp[1] = mfma(kp1, qf[1], bp1);
    const float sl = 0.17677669529663687f * 1.4426950408889634f;
    const f32x4 z4 = {0.f, 0.f, 0.f, 0.f};
    float m[4];
#pragma unroll
    for (int i = 0; i < 4; ++i)
      m[i] = fmx(fmx(fmx(fmx(sp[i][0], sp[i][1]), sp[i][2]), fmx(fmx(sp[i][3], sc[i][0]), sc[i][1])), fmx(sc[i][2], sc[i][3]));
    {
      const unsigned t = __float_as_uint(tred<true>(m[0], m[1], m[2], m[3]));
      const auto u = __builtin_amdgcn_permlane32_swap(t, t, false, false);
      const auto u0 = __builtin_amdgcn_permlane16_swap(u[0], u[0], false, false), u1 = __builtin_amdgcn_permlane16_swap(u[1], u[1], false, false);
      m[0] = __uint_as_float(u0[0]); m[1] = __uint_as_float(u0[1]); m[2] = __uint_as_float(u1[0]); m[3] = __uint_as_float(u1[1]);
    }
    const u32x4 one4 = {0x3f803f80u, 0x3f803f80u, 0x3f803f80u, 0x3f803f80u};
#pragma unroll
    for (int i = 0; i < 4; ++i) {
      const float mm = -sl * m[i];
#pragma unroll
      for (int e = 0; e < 4; ++e) {
        sp[i][e] = __builtin_amdgcn_exp2f(__builtin_fmaf(sp[i][e], sl, mm));
        sc[i][e] = __builtin_amdgcn_exp2f(__builtin_fmaf(sc[i][e], sl, mm));
      }
      const bf16x8 pfr = pack8(sp[i], sc[i]);
      const f32x4 sm = mfma(__builtin_bit_cast(bf16x8, one4), pfr, z4);
      const f32x4 o0 = mfma(cat(vt[i][0], vt[2 + i][0]), pfr, z4);
      const f32x4 o1 = mfma(cat(vt[i][1], vt[2 + i][1]), pfr, z4);
      const float inv = __builtin_amdgcn_rcpf(sm[0]);
      put(__builtin_bit_cast(u32x4, pack8(o0 * inv, o1 * inv)), 0, 2 * j + g, i);
    }
#pragma unroll
    for (int i = 0; i < 4; ++i) put(cat4(GLU(i, 6, 8), GLU(i, 7, 9)), 1, 8 + 2 * j + g, i);
    WAIT_V(8);
  }
  ST(4);

  const int lag = g ^ 1;
  GAS unsigned* hcnt = (GAS unsigned*)(ws + WS_HCNT) + (mb * 4 + kq) * 16;
  GAS unsigned* gcnt = (GAS unsigned*)(ws + WS_GCNT) + (mb * 4 + kq) * 16;
  const int e0 = kq * 1024, e1 = ((1 ^ kq) - 1) * 1024, e2 = ((2 ^ kq) - 2) * 1024, e3 = ((3 ^ kq) - 3) * 1024;
  auto nxt = [&](int s) { ++s; if (s == own0) ++s; if (s == own1) ++s; return s; };
  int stp = nxt(-1);
  const int mf = wi * 4 + kq;
  const unsigned orow = ((mb * 256 + 32 * (mf >> 1) + 2 * fr + (mf & 1)) * 1024 + nt * 128 + g * 64 + 16 * (fq & 1) + 8 * (fq >> 1)) * 2;
  u32x4 xr[2] = {{0u, 0u, 0u, 0u}, {0u, 0u, 0u, 0u}};
  const int rl2 = 4 * (fq & 1) + (fr >> 2);
  unsigned ao = lb + wi * 4096 + lane * 16;
  unsigned bo = lb + G2_SA + (fq >> 1) * 2048 + g * 1024 + rl2 * 128 + 32 * ((rl2 >> 1) & 3) + 8 * (lane & 3);
  f32x4 ac2[4][4];
#pragma unroll
  for (int i = 0; i < 4; ++i)
#pragma unroll
    for (int n = 0; n < 4; ++n) { ac2[i][n] = (f32x4){0.f, 0.f, 0.f, 0.f}; asm volatile("" : "+v"(ac2[i][n])); }
  ns = lb + 2 * G2_SLOT;
  const char* ca = hbase + stp * 32768 + w * 4096;
  const char* cb = brow(stp);
  int sl2 = 0;
  WAIT_V_IF0(0, lag);
  WAIT_L0();
  BAR();
  ST(5);
  ST_SAVE(); ST(0); ST(1);
  if (lag) BAR();

  auto phase2 = [&](auto mode, int q) {
    constexpr int MODE = decltype(mode)::value;
    bf16x8 a[2][4], bfr[2][4];
#define RA(kk, i) a[kk][i] = *(const LAS bf16x8*)(lp(ao) + (kk) * 16384 + (i) * 1024);
#define RB(kk, n) bfr[kk][n] = cat(trd(lp(bo ^ ((n) * 32)) + (kk) * 8192), trd(lp(bo ^ ((n) * 32)) + (kk) * 8192 + 4096));
    int dlt;
    asm volatile("" ::: "memory");
    if (MODE == 1 && q == 1 && w == 4 && lane == 0) __hip_atomic_fetch_add(hcnt, 1u, __ATOMIC_RELAXED, __HIP_MEMORY_SCOPE_AGENT);
    if (MODE == 2 && q == 16)
      asm volatile("global_load_dwordx4 %0, %2, %3\n\tglobal_load_dwordx4 %1, %2, %3 offset:64" : "+v"(xr[0]), "+v"(xr[1]) : "v"(orow), "s"(x) : "memory");
    RA(0, 0) RA(0, 1)
    if (MODE == 0) dmc_first(ns + w * 4096, va, ca + e0);
    RA(0, 2) RA(0, 3) RB(0, 0)
    if (MODE == 0) dmc_next<1024>(va, ca + e1);
    RB(0, 1) RB(0, 2) RB(0, 3)
    if (MODE == 0) dmc_next<2048>(va, ca + e2);
    RA(1, 0) RA(1, 1) RA(1, 2)
    if (MODE == 0) dmc_next<3072>(va, ca + e3);
    RA(1, 3) RB(1, 0)
    if (MODE < 2) dma_first(ns + G2_SA + w * 2048, vb, cb);
    RB(1, 1)
    if (MODE < 2) dma_next<1024>(vb, cb - 896);
    sl2 = (sl2 == 2) ? 0 : sl2 + 1; dlt = (sl2 == 0) ? -2 * G2_SLOT : G2_SLOT;
    if (MODE < 2) { stp = nxt(stp); ca = hbase + stp * 32768 + w * 4096; cb = brow(stp); ns = (ns == lb + 2 * G2_SLOT) ? lb : ns + G2_SLOT; }
    asm volatile("" : "+s"(ca), "+s"(cb), "+s"(ns), "+s"(sl2), "+s"(dlt), "+s"(stp) :: "memory");
    RB(1, 2) RB(1, 3)
#undef RA
#undef RB
    ao += dlt; bo += dlt;
    SCHED();
    asm volatile("" : "+v"(bo), "+v"(ao));
    if (MODE == 0) WAIT_V_IF0(6, lag ^ 1); else if (MODE == 2 && q == 16) WAIT_V_IF0(2, lag ^ 1);
    if (MODE == 1 && lag) {
      if (q == 0) WAIT_V(0);
      else if (wi == 0) {
        unsigned spins = 0;
        while ((unsigned)__builtin_amdgcn_readfirstlane(__hip_atomic_load(hcnt, __ATOMIC_RELAXED, __HIP_MEMORY_SCOPE_AGENT)) < 8u) {
          __builtin_amdgcn_s_sleep(1);
          if (++spins > (1u << 22)) break;
        }
      }
    }
    WAIT_L0();
    SCHED();
    BAR();
    SCHED();
    PRIO_M();
#pragma unroll
    for (int n = 0; n < 4; ++n)
#pragma unroll
      for (int i = 0; i < 4; ++i) {
        ac2[i][n] = mfma(bfr[0][n], a[0][i], ac2[i][n]);
        SCHED();
        if (n == 3 && i == 3) {
          PRIO_T();
          if (MODE == 0) WAIT_V_IF0(6, lag); else if (MODE == 2 && q == 16) WAIT_V_IF0(2, lag);
          if (MODE == 1 && q == 1) WAIT_V_IF0(0, lag ^ 1);
          if (q < 17 || !lag) BAR();
          SCHED();
        }
        ac2[i][n] = mfma(bfr[1][n], a[1][i], ac2[i][n]);
        SCHED();
      }
    PRIO_E();
    SCHED();
  };
  typedef std::integral_constant<int, 0> M0; typedef std::integral_constant<int, 1> M1; typedef std::integral_constant<int, 2> M2;
#pragma clang loop unroll(disable)
  for (int q = 0; q < 2; ++q) phase2(M1{}, q);
  {
    const int s2 = nxt(-1), s = lag ? nxt(s2) : s2;
    const char* p = hbase + s * 32768 + wi * 8192;
    const unsigned d = lb + (lag ? 0 : 2 * G2_SLOT) + wi * 8192;
    dmc_first(d, va, p + e0); dmc_next<1024>(va, p + e1); dmc_next<2048>(va, p + e2); dmc_next<3072>(va, p + e3);
    dmc_first(d + 4096, va, p + 4096 + e0); dmc_next<1024>(va, p + 4096 + e1); dmc_next<2048>(va, p + 4096 + e2); dmc_next<3072>(va, p + 4096 + e3);
    WAIT_V_IF0(0, lag);
    BAR();
  }
  ST(2);
#pragma clang loop unroll(disable)
  for (int q = 2; q < 13; ++q) phase2(M0{}, q);
  if (w == 4 && lane == 0) __hip_atomic_fetch_add(gcnt, 1u, __ATOMIC_RELAXED, __HIP_MEMORY_SCOPE_AGENT);
#pragma clang loop unroll(disable)
  for (int q = 13; q < 16; ++q) phase2(M0{}, q);
  phase2(M2{}, 16);
  unsigned seen;
  asm volatile("global_load_dword %0, %1, %2 sc1" : "=v"(seen) : "v"(0u), "s"(gcnt) : "memory");
  phase2(M2{}, 17);
  asm volatile("s_waitcnt vmcnt(0)" : "+v"(seen) :: "memory");
  {
    unsigned spins = 0;
    while ((unsigned)__builtin_amdgcn_readfirstlane(seen) < 8u) {
      seen = __hip_atomic_load(gcnt, __ATOMIC_RELAXED, __HIP_MEMORY_SCOPE_AGENT);
      if (++spins > (1u << 22)) break;
    }
  }
  asm volatile("" ::: "memory");
  ST(3);
  const int tile = mb * 8 + nt;
  const auto rs = __builtin_amdgcn_make_buffer_rsrc(ws + WS_H + (size_t)mb * 4 * 36 * 16384 + nt * 49152 + w * 2048, 0, 4 * 36 * 16384 - 8 * 49152, 0x00020000);
#pragma unroll
  for (int i = 1; i < 4; ++i) {
    const unsigned o = (unsigned)(kq * 36 * 16384 + (i - 1) * 16384) + lane * 16;
    __builtin_amdgcn_raw_buffer_store_b128(__builtin_bit_cast(u32x4, pack8(ac2[i][0], ac2[i][1])), rs, o, 0, 16);
    __builtin_amdgcn_raw_buffer_store_b128(__builtin_bit_cast(u32x4, pack8(ac2[i][2], ac2[i][3])), rs, o + 1024, 0, 16);
  }
  WAIT_V(0);
  ST(4);
  asm volatile("" : "+v"(xr[0]), "+v"(xr[1]));
  GAS unsigned* cnt = (GAS unsigned*)(ws + WS_CNT) + (tile * 32 + w) * 16;
  if (lane >= 1 && lane < 4) __hip_atomic_fetch_add(cnt + (kq ^ lane) * 128, 1u, __ATOMIC_RELAXED, __HIP_MEMORY_SCOPE_AGENT);
  {
    unsigned spins = 0;
    while ((unsigned)__builtin_amdgcn_readfirstlane(__hip_atomic_load(cnt + kq * 128, __ATOMIC_RELAXED, __HIP_MEMORY_SCOPE_AGENT)) < 3u) {
      __builtin_amdgcn_s_sleep(2);
      if (++spins > (1u << 22)) break;
    }
  }
  asm volatile("" ::: "memory");
  ST(5);
  u32x4 p[3][2];
#pragma unroll
  for (int i = 1; i < 4; ++i) {
    const unsigned o = (unsigned)((kq ^ i) * 36 * 16384 + (i - 1) * 16384) + lane * 16;
    p[i - 1][0] = __builtin_amdgcn_raw_buffer_load_b128(rs, o, 0, 16);
    p[i - 1][1] = __builtin_amdgcn_raw_buffer_load_b128(rs, o + 1024, 0, 16);
  }
  const auto ors = __builtin_amdgcn_make_buffer_rsrc(out, 0, 4 << 20, 0x00020000);
#pragma unroll
  for (int h = 0; h < 2; ++h) {
    const auto x0 = __builtin_amdgcn_permlane16_swap(xr[h][0], xr[h][2], false, false), x1 = __builtin_amdgcn_permlane16_swap(xr[h][1], xr[h][3], false, false);
    const u32x4 xv = {x0[0], x1[0], x0[1], x1[1]};
    unsigned r[4];
#pragma unroll
    for (int d = 0; d < 4; ++d) {
      float lo = ac2[0][2 * h + (d >> 1)][2 * (d & 1)] + __uint_as_float(xv[d] << 16), hi = ac2[0][2 * h + (d >> 1)][2 * (d & 1) + 1] + __uint_as_float(xv[d] & 0xffff0000u);
#pragma unroll
      for (int i = 0; i < 3; ++i) { lo += __uint_as_float(p[i][h][d] << 16); hi += __uint_as_float(p[i][h][d] & 0xffff0000u); }
      r[d] = pk2(lo, hi);
    }
    const auto y0 = __builtin_amdgcn_permlane16_swap(r[0], r[2], false, false), y1 = __builtin_amdgcn_permlane16_swap(r[1], r[3], false, false);
    __builtin_amdgcn_raw_buffer_store_b128((u32x4){y0[0], y1[0], y0[1], y1[1]}, ors, orow + 64 * h, 0, OUT_AUX);
  }
  ST(6);
  ST(7);
  ST_OUT(1, 8);
  ST_REST(); ST_OUT(0, 6);
}

extern "C" void kernel_launch(void* const* d_in, const int* in_sizes, int n_in, void* d_out, int out_size, void* d_ws,
                              size_t ws_size, hipStream_t stream) {
  const u16* x = (const u16*)d_in[0];
  const u16* wqkv = (const u16*)d_in[1];
  const u16* wo = (const u16*)d_in[2];
  const u16* wg = (const u16*)d_in[4];
  const u16* wu = (const u16*)d_in[5];
  const u16* wd = (const u16*)d_in[6];
  char* ws = (char*)d_ws;
  (void)hipFuncSetAttribute((const void*)psum_g1_kernel, hipFuncAttributeMaxDynamicSharedMemorySize, G1_LDS);
  psum_ln_kernel<<<256, 320, 0, stream>>>(x, wqkv, wg, wu, ws);
  psum_g1_kernel<<<256, 512, G1_LDS, stream>>>(x, (u16*)d_out, wqkv, wg, wu, wo, wd, ws);
}
```

```cpp
#define DIAG 10
#include <hip/hip_runtime.h>
#include <hip/hip_bf16.h>
#include <type_traits>
#define EARLY 1
#define HALO_SKIP 0
#define PF_W2 1
#define PF_LOOP 0
#define PF_R0 192
#define PF_PER 2
#define PF_LN_ROWS 96
#define PRIO_L 1
#define PRIO_M() __builtin_amdgcn_s_setprio(0)
#define PRIO_T() __builtin_amdgcn_s_setprio(1)
#define PRIO_E()
#define PF_CODE 0
#define PRECLEAN 0
#define OUT_AUX 16
#define G2ORD 0
#define EARLY2 1
#define CHAIN 1
#define PF_X 0
#define XH 1
#define ACQ 0
#define H_AUX 16

namespace {

typedef unsigned short u16;
typedef short bf16x8 __attribute__((ext_vector_type(8)));
typedef short bf16x4 __attribute__((ext_vector_type(4)));
typedef float f32x4 __attribute__((ext_vector_type(4)));
typedef float f32x2 __attribute__((ext_vector_type(2)));
typedef unsigned u32x4 __attribute__((ext_vector_type(4)));
typedef unsigned u32x2 __attribute__((ext_vector_type(2)));
typedef __bf16 bf2 __attribute__((ext_vector_type(2)));
#define LAS __attribute__((address_space(3)))
#define GAS __attribute__((address_space(1)))

#define WAIT_V(n) asm volatile("s_waitcnt vmcnt(" #n ")" ::: "memory")
#define WAIT_V_IF0(n, c) asm volatile("s_cmp_lg_u32 %0, 0\n\ts_cbranch_scc1 1f\n\ts_waitcnt vmcnt(" #n ")\n1:" :: "s"(c) : "memory", "scc")
#define WAIT_L0() do { asm volatile("" ::: "memory"); __builtin_amdgcn_s_waitcnt(0xC07F); asm volatile("" ::: "memory"); } while (0)
#define BAR() __builtin_amdgcn_s_barrier()
#define SCHED() __builtin_amdgcn_sched_barrier(0)

__device__ __forceinline__ float b2f(u16 v) { return __uint_as_float(((unsigned)v) << 16); }
__device__ __forceinline__ unsigned pk2(float lo, float hi) {
  bf2 r = {(__bf16)lo, (__bf16)hi};
  return __builtin_bit_cast(unsigned, r);
}
__device__ __forceinline__ float rbf(float f) { return (float)(__bf16)f; }
__device__ __forceinline__ bf16x8 pack8(f32x4 a, f32x4 b) {
  u32x4 r = {pk2(a[0], a[1]), pk2(a[2], a[3]), pk2(b[0], b[1]), pk2(b[2], b[3])};
  return __builtin_bit_cast(bf16x8, r);
}
__device__ __forceinline__ bf16x4 trd(LAS const char* p) {
  return __builtin_amdgcn_ds_read_tr16_b64_v4i16((LAS bf16x4*)p);
}
__device__ __forceinline__ bf16x8 cat(bf16x4 a, bf16x4 b) { return __builtin_shufflevector(a, b, 0, 1, 2, 3, 4, 5, 6, 7); }
__device__ __forceinline__ f32x4 mfma(bf16x8 a, bf16x8 b, f32x4 c) {
  return __builtin_amdgcn_mfma_f32_16x16x32_bf16(a, b, c, 0, 0, 0);
}
__device__ __forceinline__ void dma_first(unsigned lds_dst, unsigned voff, const char* b0) {
  asm volatile("s_mov_b32 m0, %0\n\ts_nop 0\n\tglobal_load_lds_dwordx4 %1, %2" :: "s"(lds_dst), "v"(voff), "s"(b0) : "memory");
}
template <int OFF>
__device__ __forceinline__ void dma_next(unsigned voff, const char* b) {
  asm volatile("global_load_lds_dwordx4 %0, %1 offset:%2" :: "v"(voff), "s"(b), "i"(OFF) : "memory");
}
__device__ __forceinline__ void dmc_first(unsigned lds_dst, unsigned voff, const char* b0) {
  asm volatile("s_mov_b32 m0, %0\n\ts_nop 0\n\tglobal_load_lds_dwordx4 %1, %2 sc1" :: "s"(lds_dst), "v"(voff), "s"(b0) : "memory");
}
template <int OFF>
__device__ __forceinline__ void dmc_next(unsigned voff, const char* b) {
  asm volatile("global_load_lds_dwordx4 %0, %1 offset:%2 sc1" :: "v"(voff), "s"(b), "i"(OFF) : "memory");
}
__device__ __forceinline__ void dma1m(unsigned lds_dst, const char* p, unsigned long long mask) {
  asm volatile("s_mov_b32 m0, %0\n\ts_mov_b64 exec, %2\n\t"
               "global_load_lds_dwordx4 %1, off\n\t"
               "s_mov_b64 exec, -1"
               :: "s"(lds_dst), "v"(p), "s"(mask) : "memory");
}
__device__ __forceinline__ void touch(unsigned lds_dst, const void* p) {
  asm volatile("s_mov_b32 m0, %0\n\ts_nop 0\n\tglobal_load_lds_dword %1, off" :: "s"(lds_dst), "v"(p) : "memory");
}
#define CODE_ADDR(dst, sym) do { unsigned lo_, hi_; asm volatile("s_getpc_b64 s[90:91]\n\ts_add_u32 %0, s90, " #sym "@rel32@lo+4\n\ts_addc_u32 %1, s91, " #sym "@rel32@hi+12" \
    : "=s"(lo_), "=s"(hi_) :: "s90", "s91", "scc"); dst = (const char*)(((unsigned long long)hi_ << 32) | lo_); } while (0)
constexpr int LN_LINES = 16, G1_LINES = 80, G2_LINES = 30;
__device__ __forceinline__ LAS char* lp(unsigned a) { return (LAS char*)(size_t)a; }
__device__ __forceinline__ float fmx(float a, float b) { return __builtin_elementwise_maximum(a, b); }
__device__ __forceinline__ float xmax(float v) {
  auto a = __builtin_amdgcn_permlane16_swap(__float_as_uint(v), __float_as_uint(v), false, false);
  v = fmaxf(__uint_as_float(a[0]), __uint_as_float(a[1]));
  auto b = __builtin_amdgcn_permlane32_swap(__float_as_uint(v), __float_as_uint(v), false, false);
  return fmaxf(__uint_as_float(b[0]), __uint_as_float(b[1]));
}
__device__ __forceinline__ float xsum(float v) {
  auto a = __builtin_amdgcn_permlane16_swap(__float_as_uint(v), __float_as_uint(v), false, false);
  v = __uint_as_float(a[0]) + __uint_as_float(a[1]);
  auto b = __builtin_amdgcn_permlane32_swap(__float_as_uint(v), __float_as_uint(v), false, false);
  return __uint_as_float(b[0]) + __uint_as_float(b[1]);
}

template <int CTRL>
__device__ __forceinline__ float dpp_add(float v) {
  return v + __uint_as_float(__builtin_amdgcn_update_dpp(0u, __float_as_uint(v), CTRL, 0xf, 0xf, true));
}
__device__ __forceinline__ float hsum(float v) {
  v = dpp_add<0xB1>(v);
  v = dpp_add<0x4E>(v);
  v = dpp_add<0x128>(v);
  return xsum(v);
}

#define DG(kid, slot) do { if (threadIdx.x == DGT) ((unsigned long long*)(ws + WS_ST))[((kid) * 256 + blockIdx.x) * 8 + (slot)] = __builtin_amdgcn_s_memrealtime(); } while (0)
#define ST_DECL
#define LT_DECL
#define LT(k)
#define ET(k)
#define ST(k)
#define ST_OUT(kid, n)
#define ST_SAVE()
#define ST_REST()
#define WS_BASE_MB 0
constexpr size_t WS_XN = (size_t)WS_BASE_MB << 20;
constexpr size_t WS_H = WS_XN + (size_t)4 * 1024 * 1024;
constexpr size_t H_BYTES = (size_t)8 * 4 * 36 * 16 * 1024;
constexpr size_t WS_X = WS_H + H_BYTES;
constexpr size_t X_BYTES = (size_t)64 * 4 * 3 * 8 * 2048;
constexpr size_t WS_CNT = WS_X + X_BYTES;
constexpr size_t WS_GCNT = WS_CNT + 131072;
constexpr size_t WS_HCNT = WS_GCNT + 8192;
constexpr size_t WS_ST = WS_HCNT + 32768;

}
extern "C" __global__ __launch_bounds__(320) void psum_ln_kernel(const u16* __restrict__ x, const u16* __restrict__ wqkv, const u16* __restrict__ wg,
                                                                 const u16* __restrict__ wu, char* __restrict__ ws) {
  const int lane = threadIdx.x & 63, b = blockIdx.x;
#define DGT 0
  DG(0, 0);
  if (threadIdx.x >= 256) {
    if (lane < 8) *(unsigned*)(ws + WS_CNT + (b * 8 + lane) * 64) = 0u;
    if (lane == 8 && b < 32) *(unsigned*)(ws + WS_GCNT + b * 64) = 0u;
    if (lane >= 16 && lane < 24) *(unsigned*)(ws + WS_HCNT + b * 128 + (lane - 16) * 4) = 0u;
    __shared__ __attribute__((aligned(16))) char sink[256];
    const int kq = (b & 7) >> 1, l = lane < 40 ? lane : lane - 40;
    const u16* p = l < 12 ? wqkv + (l >> 2) * 1024 + 256 * kq + (l & 3) * 64 : (l < 26 ? wg + 896 * kq + (l - 12) * 64 : wu + 896 * kq + (l - 26) * 64);
    const int ld = l < 12 ? 3072 : 3584;
#pragma unroll
    for (int i = 0; i < PF_LN_ROWS / 32; ++i) touch((unsigned)(size_t)(LAS char*)sink, p + (i * 32 + (b >> 3)) * ld);
    return;
  }
  const int hf = (b & 1) * 128 + (b >> 1);
  const int mb = hf >> 5, mf = (hf >> 1) & 15, r = (hf & 1) * 8 + (threadIdx.x >> 6) * 2 + ((lane >> 2) & 1);
  const int row = mb * 256 + 32 * (mf >> 1) + 2 * r + (mf & 1), kl = lane >> 3, ch = lane & 3;
  const u16* xr = x + row * 1024 + kl * 32 + ch * 8;
  u32x4 raw[4];
#pragma unroll
  for (int i = 0; i < 4; ++i) raw[i] = *(const u32x4*)(xr + 256 * i);
  float v[32];
#pragma unroll
  for (int i = 0; i < 16; ++i) { v[2 * i] = __uint_as_float(raw[i >> 2][i & 3] << 16); v[2 * i + 1] = __uint_as_float(raw[i >> 2][i & 3] & 0xffff0000u); }
  float s = 0.f;
#pragma unroll
  for (int i = 0; i < 32; ++i) s += v[i];
  const float mean = hsum(s) * (1.0f / 1024.0f);
  float q = 0.f;
#pragma unroll
  for (int i = 0; i < 32; ++i) { const float d = v[i] - mean; q += d * d; }
  q = hsum(q);
  const float mb16 = rbf(mean), den = rbf(sqrtf(rbf(rbf(q * (1.0f / 1024.0f)) + 1e-5f)));
  const float inv = 1.0f / den;
  const auto rs = __builtin_amdgcn_make_buffer_rsrc(ws + WS_XN, 0, 4 << 20, 0x00020000);
  const unsigned o = ((mb * 32 + kl) * 16 + mf) * 1024 + r * 64 + ((ch ^ (2 * (r >> 3))) * 16);
#pragma unroll
  for (int i = 0; i < 4; ++i) {
    u32x4 t;
#pragma unroll
    for (int e = 0; e < 4; ++e) t[e] = pk2(rbf(v[8 * i + 2 * e] - mb16) * inv, rbf(v[8 * i + 2 * e + 1] - mb16) * inv);
    __builtin_amdgcn_raw_buffer_store_b128(t, rs, o + i * 8 * 16384, 0, H_AUX);
  }
  WAIT_V(0);
  DG(0, 1);
}
namespace {
constexpr int G1_SA = 17 * 1024, G1_SB = 20 * 1024, G1_SLOT = G1_SA + G1_SB;
constexpr int T_SCR = 159488;
constexpr int T_BIAS = T_SCR + 256;
constexpr int G1_LDS = T_BIAS + 4096;
constexpr int G2_SA = 32768, G2_SB = 16384, G2_SLOT = G2_SA + G2_SB;
constexpr int T_SYNC = T_SCR - 64;
constexpr int T_KV = 2 * G2_SLOT, KV_BYTES = 7 * 1024 + 272 * 80;

template <bool MAX>
__device__ __forceinline__ float tred(float a, float b, float c, float d) {
  const auto r1 = __builtin_amdgcn_permlane16_swap(__float_as_uint(a), __float_as_uint(b), false, false);
  const auto r2 = __builtin_amdgcn_permlane16_swap(__float_as_uint(c), __float_as_uint(d), false, false);
  const float x0 = __uint_as_float(r1[0]), x1 = __uint_as_float(r1[1]), y0 = __uint_as_float(r2[0]), y1 = __uint_as_float(r2[1]);
  const float t = MAX ? fmx(x0, x1) : x0 + x1, u = MAX ? fmx(y0, y1) : y0 + y1;
  const auto r3 = __builtin_amdgcn_permlane32_swap(__float_as_uint(t), __float_as_uint(u), false, false);
  const float z0 = __uint_as_float(r3[0]), z1 = __uint_as_float(r3[1]);
  return MAX ? fmx(z0, z1) : z0 + z1;
}
__device__ __forceinline__ u32x2 glu4(f32x4 g, f32x4 u) {
  f32x4 e = g * -1.4426950408889634f;
#pragma unroll
  for (int i = 0; i < 4; ++i) e[i] = __builtin_amdgcn_exp2f(e[i]);
  e = e + 1.0f;
#pragma unroll
  for (int i = 0; i < 4; ++i) e[i] = __builtin_amdgcn_rcpf(e[i]);
  e = e * u;
  return (u32x2){pk2(e[0], e[1]), pk2(e[2], e[3])};
}
__device__ __forceinline__ u32x4 cat4(u32x2 a, u32x2 b) { return (u32x4){a[0], a[1], b[0], b[1]}; }

}
extern "C" __global__ __launch_bounds__(512, 2) void psum_g1_kernel(const u16* __restrict__ x, u16* __restrict__ out, const u16* __restrict__ wqkv,
                                                                    const u16* __restrict__ wg, const u16* __restrict__ wu,
                                                                    const u16* __restrict__ wo, const u16* __restrict__ wd,
                                                                    char* __restrict__ ws) {
  extern __shared__ __attribute__((aligned(16))) char smem[];
  LAS char* lds = (LAS char*)smem;
  ST_DECL LT_DECL ST(0);
  const unsigned long long dg0 = __builtin_amdgcn_s_memrealtime(); unsigned long long dg1, dg2, dg3, dg4, dg5;
  const int tid = threadIdx.x;
  int lane = tid & 63;
  const int w = __builtin_amdgcn_readfirstlane(tid >> 6), g = w >> 2, wi = w & 3;
  const int b = blockIdx.x, xcd = b & 7, idx = b >> 3;
  const int mb = (xcd & 1) * 4 + (idx & 3), kq = xcd >> 1, ntl = idx >> 2;
  const bool typeA = ntl < 4;
  const int j = ntl & 3;
  int fr = lane & 15, fq = lane >> 4;
  const int fA = 896 * kq + 64 * j;
  const int f0 = 896 * kq + 256 + 160 * j, odd = j & 1;
  const int fa = f0 + 32 * odd, fb = fa + 64, fc = odd ? f0 : f0 + 128;

  asm volatile("" : "+s"(wqkv), "+s"(wg), "+s"(wu), "+s"(ws));
  const bool isq = typeA && wi < 3;
  const int ld = isq ? 3072 : 3584;
  const u16* q1 = isq ? wqkv + (wi * 1024 + 256 * kq + 64 * j) : ((!typeA && (wi & 2)) ? wu : wg) + (typeA ? fA : ((wi & 1) ? fb : fa));
  const char* c1 = g ? ws + WS_XN + (size_t)(mb * 32 * 16384 + wi * 4096) : (const char*)q1;
  const int s1 = g ? 0 : 8 * ld - 1024, s2 = g ? 0 : 32 * ld - 2048, s3 = g ? 0 : 40 * ld - 3072;
  const int p1 = g ? 16384 : 64 * ld, p2 = g ? 16384 : 64 * 3584;
  const int d1 = (g ? 0 : G1_SA) + wi * 4096, d2 = g ? 16384 + wi * 256 : G1_SA + 16384 + wi * 1024;
  const unsigned long long m2 = g ? 0xffffull : ~0ull;
  const int rr = lane >> 3, pc = lane & 7, fi = (pc >> 1) ^ ((rr >> 1) & 3), kr = (rr & 3) + 8 * (rr >> 2);
  const unsigned v1 = g ? lane * 16 : kr * ld * 2 + fi * 32 + (pc & 1) * 16;
  const unsigned lb = (unsigned)(size_t)lds;
  unsigned ns = lb;
  dma_first(ns + d1, v1, c1); dma_next<1024>(v1, c1 + s1); dma_next<2048>(v1, c1 + s2); dma_next<3072>(v1, c1 + s3);
  const char* c2;
  {
    const int hr = wi * 4 + ((lane >> 2) & 3);
    const unsigned oh = (14 + (hr >> 3)) * 1024 + (8 + (hr & 7)) * 64 + (((lane & 3) ^ (2 * (hr >> 3)) ^ 2) * 16);
    const unsigned ow = (kr + 16 * (wi >> 1) + 4 * (wi & 1)) * 7168 + (typeA ? fi * 32 : (fi >> 1) * 32) + (pc & 1) * 16;
    const char* be = g ? ws + WS_XN + (size_t)((mb > 0 ? mb - 1 : 0) * 32 * 16384) : (const char*)(typeA ? wu + fA : wg + fc);
    const char* bo = g ? be : (const char*)(wu + (typeA ? fA : fc));
    c2 = ((fi & 1) ? bo : be) + (g ? oh : ow);
  }
  auto adv = [&]() { c1 += p1; c2 += p2; ns = (ns == lb + 3 * G1_SLOT) ? lb : ns + G1_SLOT; };
  auto stage = [&]() {
    dma_first(ns + d1, v1, c1); dma_next<1024>(v1, c1 + s1); dma_next<2048>(v1, c1 + s2); dma_next<3072>(v1, c1 + s3);
    dma1m(ns + d2, c2, m2);
    adv();
  };
  dma1m(ns + d2, c2, m2);
  adv();

  f32x4 acc[4][10];
  f32x4 hacc = {0.f, 0.f, 0.f, 0.f};
#pragma unroll
  for (int i = 0; i < 4; ++i)
#pragma unroll
    for (int n = 0; n < 10; ++n) { acc[i][n] = (f32x4){0.f, 0.f, 0.f, 0.f}; asm volatile("" : "+v"(acc[i][n])); }
  const int rl = 4 * (fq & 1) + (fr >> 2);
  unsigned hoffA = lb + 16384 + fr * 64 + ((fq ^ (2 * (fr >> 3))) * 16);
  unsigned aoff = hoffA - 16384 + wi * 4096;
  unsigned boff0 = lb + G1_SA + (fq >> 1) * 2048 + rl * 128 + 32 * ((2 * g) ^ ((rl >> 1) & 3)) + 8 * (lane & 3);
  unsigned boff1 = boff0 ^ 32;
  unsigned hoffB = (boff0 ^ (32 * (wi & 1))) + (1 + (wi >> 1)) * 4096;
  asm volatile("" : "+v"(aoff), "+v"(hoffA), "+v"(boff0), "+v"(boff1), "+v"(hoffB), "+v"(hacc));
  {
    const int lo = fr + (wi == 0 ? -7 : (wi == 2 ? 1 : 9));
    const int hi = wi == 0 ? fr + 1 : ((wi >= 2 && mb == 0) ? 0 : (wi == 2 ? 8 : 16));
    f32x4 bv;
#pragma unroll
    for (int e = 0; e < 4; ++e) bv[e] = (4 * fq + e >= lo && 4 * fq + e < hi) ? 0.f : -3.0e38f;
    *(LAS f32x4*)(lds + T_BIAS + wi * 1024 + lane * 16) = bv;
    *(LAS unsigned*)(lds + T_SYNC + (lane & 1) * 4) = 0u;
  }
  stage();
  ST(1);
  WAIT_V(5);
  BAR();
  dg1 = __builtin_amdgcn_s_memrealtime(); WAIT_L0();
  ST(2);
  if (g == 1) BAR();

  auto phase = [&](auto do_stage, int q) {
    constexpr bool DS = decltype(do_stage)::value;
    bf16x8 a[4], bfr[10], ha, hb;
    LT(0);
#define RB(c) bfr[2 * (c)] = cat(trd(lp(boff0) + (c) * 4096), trd(lp(boff0) + (c) * 4096 + 1024)); \
              bfr[2 * (c) + 1] = cat(trd(lp(boff1) + (c) * 4096), trd(lp(boff1) + (c) * 4096 + 1024));
    a[0] = *(const LAS bf16x8*)(lp(aoff)); a[1] = *(const LAS bf16x8*)(lp(aoff) + 1024);
    if (DS) dma_first(ns + d1, v1, c1);
    a[2] = *(const LAS bf16x8*)(lp(aoff) + 2048); a[3] = *(const LAS bf16x8*)(lp(aoff) + 3072);
    RB(0)
    if (DS) dma_next<1024>(v1, c1 + s1);
    RB(1)
    if (DS) dma_next<2048>(v1, c1 + s2);
    RB(2)
    if (DS) dma_next<3072>(v1, c1 + s3);
    RB(3)
    if (DS) dma1m(ns + d2, c2, m2);
    RB(4)
    { ha = *(const LAS bf16x8*)lp(hoffA); hb = cat(trd(lp(hoffB)), trd(lp(hoffB) + 1024)); }
#undef RB
    SCHED();
    LT(1);
    const int dlt = ((q & 3) == 3) ? -3 * G1_SLOT : G1_SLOT;
    aoff += dlt; hoffA += dlt; boff0 += dlt; boff1 += dlt; hoffB += dlt;
    if (DS) {
      adv();
      if (q == 1) stage();
      LT(2);
      if (q == 0) WAIT_V(5); else WAIT_V(10);
    } else {
      LT(2);
      if (q == 29) {
        WAIT_V(0);
        const int l = __builtin_amdgcn_mbcnt_hi(~0u, __builtin_amdgcn_mbcnt_lo(~0u, 0u));
        int t = w * 64 + l;
        t = t < 288 ? t : t - 288;
        const int rr = (t >> 4) * 64 + (xcd & 1) * 32 + idx;
        touch(lb + T_SCR, (rr < 256 ? wo + (256 * kq + rr) * 1024 : wd + (896 * kq + rr - 256) * 1024) + (t & 15) * 64);
      }
    }
    WAIT_L0();
    LT(3);
    SCHED();
    BAR();
    SCHED();
    LT(4);
    PRIO_M();
#pragma unroll
    for (int n = 0; n < 10; ++n)
#pragma unroll
      for (int i = 0; i < 4; ++i) {
        if (n == 9 && i == 4 - EARLY) { SCHED(); PRIO_T(); if (q < 31 || g == 0) BAR(); SCHED(); }
        acc[i][n] = mfma(bfr[n], a[i], acc[i][n]);
      }
    hacc = mfma(hb, ha, hacc);
    PRIO_E();
    SCHED();
    LT(5);
    SCHED();
  };
#pragma clang loop unroll(disable)
  for (int q = 0; q < 29; ++q) phase(std::true_type{}, q);
#pragma clang loop unroll(disable)
  for (int q = 29; q < 32; ++q) phase(std::false_type{}, q);

  dg2 = __builtin_amdgcn_s_memrealtime(); WAIT_L0();
  ST(3);
  asm volatile("" ::: "memory");
  if (g == 0) __builtin_amdgcn_s_setprio(0); else __builtin_amdgcn_s_setprio(1);
  lane = __builtin_amdgcn_mbcnt_hi(~0u, __builtin_amdgcn_mbcnt_lo(~0u, 0u));
  asm volatile("" : "+v"(lane));
  fr = lane & 15; fq = lane >> 4;
  const int nt = ntl;
  const int kba = 8 + ((fa - 896 * kq) >> 5);
  const int own0 = typeA ? j : (kba >> 1), own1 = typeA ? 4 + j : (kba >> 1) + 1;
  const char* const hbase = ws + WS_H + (size_t)(mb * 4 + kq) * 36 * 16384;
  const char* const wo_ = (const char*)(wo + (size_t)(256 * kq + 8 * w) * 1024 + nt * 128);
  const char* const wd_ = (const char*)(wd + (size_t)(896 * kq - 256 + 8 * w) * 1024 + nt * 128);
  auto brow = [&](int step) { return (step < 4 ? wo_ : wd_) + step * (64 * 2048); };
  const unsigned va = lane * 16;
  const unsigned vb = (lane >> 3) * 2048 + ((((lane & 7) >> 1) ^ ((lane >> 4) & 3)) * 32) + (lane & 1) * 16;
  { const char* p0 = brow(own0); dma_first(lb + G2_SA + w * 2048, vb, p0); dma_next<1024>(vb, p0 - 896);
    const char* p1 = brow(own1); dma_first(lb + G2_SLOT + G2_SA + w * 2048, vb, p1); dma_next<1024>(vb, p1 - 896); }
  const auto hrs = __builtin_amdgcn_make_buffer_rsrc((void*)hbase, 0, 36 * 16384, 0x00020000);
  const unsigned hv = wi * 4096 + lane * 16;
  auto put = [&](u32x4 v, int s, int kb, int i) {
    __builtin_amdgcn_raw_buffer_store_b128(v, hrs, hv + kb * 16384 + i * 1024, 0, H_AUX);
    *(LAS u32x4*)(lds + s * G2_SLOT + g * 16384 + ((wi * 4096 + kq * 1024 + lane * 16) ^ (i * 1024))) = v;
  };
#define GLU(i, a, b) glu4(acc[i][a], acc[i][b])
  if (!typeA) {
    const int kbc = 8 + ((fc - 896 * kq) >> 5);
#pragma unroll
    for (int i = 0; i < 4; ++i) {
      __builtin_amdgcn_raw_buffer_store_b64(GLU(i, 8, 9), hrs, hv + kbc * 16384 + g * 8 + i * 1024, 0, H_AUX);
      put(cat4(GLU(i, 0, 4), GLU(i, 1, 5)), 0, kba + g, i);
      put(cat4(GLU(i, 2, 6), GLU(i, 3, 7)), 1, kba + 2 + g, i);
    }
    WAIT_V(12);
  } else {
    LAS char* kim = lds + T_KV + g * KV_BYTES;
    LAS char* vim = kim + 7 * 1024;
    bf16x8 kf[4], qf[4];
#pragma unroll
    for (int i = 0; i < 4; ++i) {
      kf[i] = pack8(acc[i][2], acc[i][3]);
      *(LAS bf16x8*)(vim + ((wi * 4 + i) * 16 + fr) * 80 + fq * 16) = pack8(acc[i][4], acc[i][5]);
    }
    if (wi < 3) {
      *(LAS bf16x8*)(kim + wi * 2048 + lane * 16) = kf[2];
      *(LAS bf16x8*)(kim + wi * 2048 + 1024 + lane * 16) = kf[3];
    }
    {
      LAS char* hp = (wi < 2) ? (kim + 6 * 1024 + lane * 16 + wi * 8) : (vim + (256 + fr) * 80 + fq * 16 + (wi - 2) * 8);
      *(LAS u32x2*)hp = (u32x2){pk2(hacc[0], hacc[1]), pk2(hacc[2], hacc[3])};
    }
    WAIT_L0();
    BAR();
    asm volatile("" ::: "memory");
    const bool halo = wi == 0;
    const f32x4 bc = *(const LAS f32x4*)(lds + T_BIAS + lane * 16), bp = *(const LAS f32x4*)(lds + T_BIAS + 1024 + lane * 16);
    const f32x4 bp0 = *(const LAS f32x4*)(lds + T_BIAS + (halo ? 2048 : 1024) + lane * 16);
    const f32x4 bp1 = *(const LAS f32x4*)(lds + T_BIAS + (halo ? 3072 : 1024) + lane * 16);
    const int pf0 = halo ? 16 : wi * 4 - 2, pf1 = halo ? 16 : wi * 4 - 1;
    const bf16x8 kp0 = *(const LAS bf16x8*)(kim + (halo ? 6 : 2 * wi - 2) * 1024 + lane * 16);
    const bf16x8 kp1 = *(const LAS bf16x8*)(kim + (halo ? 6 : 2 * wi - 1) * 1024 + lane * 16);
    const LAS char* vr = vim + (4 * fq + (fr >> 2)) * 80 + (lane & 3) * 16;
    bf16x4 vt[6][2];
#pragma unroll
    for (int d = 0; d < 2; ++d) {
      vt[0][d] = trd(vr + pf0 * 1280 + d * 8); vt[1][d] = trd(vr + pf1 * 1280 + d * 8);
#pragma unroll
      for (int i = 0; i < 4; ++i) vt[2 + i][d] = trd(vr + (wi * 4 + i) * 1280 + d * 8);
    }
    f32x4 sp[4], sc[4];
#pragma unroll
    for (int i = 0; i < 4; ++i) {
      qf[i] = pack8(acc[i][0], acc[i][1]);
      sc[i] = mfma(kf[i], qf[i], bc);
    }
    sp[2] = mfma(kf[0], qf[2], bp); sp[3] = mfma(kf[1], qf[3], bp);
    sp[0] = mfma(kp0, qf[0], bp0); sp[1] = mfma(kp1, qf[1], bp1);
    const float sl = 0.17677669529663687f * 1.4426950408889634f;
    const f32x4 z4 = {0.f, 0.f, 0.f, 0.f};
    float m[4];
#pragma unroll
    for (int i = 0; i < 4; ++i)
      m[i] = fmx(fmx(fmx(fmx(sp[i][0], sp[i][1]), sp[i][2]), fmx(fmx(sp[i][3], sc[i][0]), sc[i][1])), fmx(sc[i][2], sc[i][3]));
    {
      const unsigned t = __float_as_uint(tred<true>(m[0], m[1], m[2], m[3]));
      const auto u = __builtin_amdgcn_permlane32_swap(t, t, false, false);
      const auto u0 = __builtin_amdgcn_permlane16_swap(u[0], u[0], false, false), u1 = __builtin_amdgcn_permlane16_swap(u[1], u[1], false, false);
      m[0] = __uint_as_float(u0[0]); m[1] = __uint_as_float(u0[1]); m[2] = __uint_as_float(u1[0]); m[3] = __uint_as_float(u1[1]);
    }
    const u32x4 one4 = {0x3f803f80u, 0x3f803f80u, 0x3f803f80u, 0x3f803f80u};
#pragma unroll
    for (int i = 0; i < 4; ++i) {
      const float mm = -sl * m[i];
#pragma unroll
      for (int e = 0; e < 4; ++e) {
        sp[i][e] = __builtin_amdgcn_exp2f(__builtin_fmaf(sp[i][e], sl, mm));
        sc[i][e] = __builtin_amdgcn_exp2f(__builtin_fmaf(sc[i][e], sl, mm));
      }
      const bf16x8 pfr = pack8(sp[i], sc[i]);
      const f32x4 sm = mfma(__builtin_bit_cast(bf16x8, one4), pfr, z4);
      const f32x4 o0 = mfma(cat(vt[i][0], vt[2 + i][0]), pfr, z4);
      const f32x4 o1 = mfma(cat(vt[i][1], vt[2 + i][1]), pfr, z4);
      const float inv = __builtin_amdgcn_rcpf(sm[0]);
      put(__builtin_bit_cast(u32x4, pack8(o0 * inv, o1 * inv)), 0, 2 * j + g, i);
    }
#pragma unroll
    for (int i = 0; i < 4; ++i) put(cat4(GLU(i, 6, 8), GLU(i, 7, 9)), 1, 8 + 2 * j + g, i);
    WAIT_V(8);
  }
  ST(4);

  const int lag = g ^ 1;
  GAS unsigned* hflag = (GAS unsigned*)(ws + WS_HCNT) + (mb * 4 + kq) * 256;
  const unsigned fo = (nt * 32 + (lane & 7)) * 4;
  GAS unsigned* gcnt = (GAS unsigned*)(ws + WS_GCNT) + (mb * 4 + kq) * 16;
  const int e0 = kq * 1024, e1 = ((1 ^ kq) - 1) * 1024, e2 = ((2 ^ kq) - 2) * 1024, e3 = ((3 ^ kq) - 3) * 1024;
  auto nxt = [&](int s) { ++s; if (s == own0) ++s; if (s == own1) ++s; if (s == 18) s = (own0 == 0) ? 1 : 0; return s; };
  auto wait_cnt = [&](unsigned seen, GAS unsigned* c, unsigned need) {
    if ((unsigned)__builtin_amdgcn_readfirstlane(seen) < need) {
      asm volatile("" ::: "memory");
      unsigned spins = 0;
      while ((unsigned)__builtin_amdgcn_readfirstlane(__hip_atomic_load(c, __ATOMIC_RELAXED, __HIP_MEMORY_SCOPE_AGENT)) < need)
        if (++spins > (1u << 22)) break;
    }
    asm volatile("" ::: "memory");
  };
  auto wait_flags = [&](unsigned seen, unsigned long long need) {
    if ((__builtin_amdgcn_ballot_w64(seen != 0u) & need) != need) {
      asm volatile("" ::: "memory");
      unsigned spins = 0;
      while ((__builtin_amdgcn_ballot_w64(__hip_atomic_load(hflag + (fo >> 2), __ATOMIC_RELAXED, __HIP_MEMORY_SCOPE_AGENT) != 0u) & need) != need) {
        __builtin_amdgcn_s_sleep(1);
        if (++spins > (1u << 22)) break;
      }
    }
    asm volatile("" ::: "memory");
  };
  int stp = nxt(7);
  const int mf = wi * 4 + kq;
  const unsigned orow = ((mb * 256 + 32 * (mf >> 1) + 2 * fr + (mf & 1)) * 1024 + nt * 128 + g * 64 + 16 * (fq & 1) + 8 * (fq >> 1)) * 2;
  u32x4 xr[2] = {{0u, 0u, 0u, 0u}, {0u, 0u, 0u, 0u}};
  const int rl2 = 4 * (fq & 1) + (fr >> 2);
  unsigned ao = lb + wi * 4096 + lane * 16;
  unsigned bo = lb + G2_SA + (fq >> 1) * 2048 + g * 1024 + rl2 * 128 + 32 * ((rl2 >> 1) & 3) + 8 * (lane & 3);
  f32x4 ac2[4][4];
#pragma unroll
  for (int i = 0; i < 4; ++i)
#pragma unroll
    for (int n = 0; n < 4; ++n) { ac2[i][n] = (f32x4){0.f, 0.f, 0.f, 0.f}; asm volatile("" : "+v"(ac2[i][n])); }
  ns = lb + 2 * G2_SLOT;
  const char* ca = hbase + stp * 32768 + w * 4096;
  const char* cb = brow(stp);
  int sl2 = 0;
  WAIT_V_IF0(0, lag);
  WAIT_L0();
  BAR();
  dg3 = __builtin_amdgcn_s_memrealtime(); WAIT_L0();
  ST(5);
  ST_SAVE(); ST(0); ST(1);
  unsigned seen = 0;
#define PEEK(c, o) asm volatile("global_load_dword %0, %1, %2 sc1" : "+v"(seen) : "v"(o), "s"(c) : "memory")
  if (lag) BAR();

  auto phase2 = [&](auto mode, int q) {
    constexpr int MODE = decltype(mode)::value;
    bf16x8 a[2][4], bfr[2][4];
#define RA(kk, i) a[kk][i] = *(const LAS bf16x8*)(lp(ao) + (kk) * 16384 + (i) * 1024);
#define RB(kk, n) bfr[kk][n] = cat(trd(lp(bo ^ ((n) * 32)) + (kk) * 8192), trd(lp(bo ^ ((n) * 32)) + (kk) * 8192 + 4096));
    int dlt;
    asm volatile("" ::: "memory");
    if (MODE == 1 && q == 1 && w == 4) PEEK(hflag, fo);
    if (MODE == 2 && q == 16)
      asm volatile("global_load_dwordx4 %0, %2, %3\n\tglobal_load_dwordx4 %1, %2, %3 offset:64" : "+v"(xr[0]), "+v"(xr[1]) : "v"(orow), "s"(x) : "memory");
    RA(0, 0) RA(0, 1)
    if (MODE == 0) dmc_first(ns + w * 4096, va, ca + e0);
    RA(0, 2) RA(0, 3) RB(0, 0)
    if (MODE == 0) dmc_next<1024>(va, ca + e1);
    RB(0, 1) RB(0, 2) RB(0, 3)
    if (MODE == 0) dmc_next<2048>(va, ca + e2);
    RA(1, 0) RA(1, 1) RA(1, 2)
    if (MODE == 0) dmc_next<3072>(va, ca + e3);
    RA(1, 3) RB(1, 0)
    if (MODE < 2) dma_first(ns + G2_SA + w * 2048, vb, cb);
    RB(1, 1)
    if (MODE < 2) dma_next<1024>(vb, cb - 896);
    sl2 = (sl2 == 2) ? 0 : sl2 + 1; dlt = (sl2 == 0) ? -2 * G2_SLOT : G2_SLOT;
    if (MODE < 2) { stp = nxt(stp); ca = hbase + stp * 32768 + w * 4096; cb = brow(stp); ns = (ns == lb + 2 * G2_SLOT) ? lb : ns + G2_SLOT; }
    asm volatile("" : "+s"(ca), "+s"(cb), "+s"(ns), "+s"(sl2), "+s"(dlt), "+s"(stp) :: "memory");
    RB(1, 2) RB(1, 3)
#undef RA
#undef RB
    ao += dlt; bo += dlt;
    SCHED();
    asm volatile("" : "+v"(bo), "+v"(ao));
    if (MODE == 0) WAIT_V_IF0(6, lag ^ 1); else if (MODE == 2 && q == 16) WAIT_V_IF0(2, lag ^ 1);
    if (MODE == 1 && q == 1) {
      asm volatile("s_waitcnt vmcnt(2)" : "+v"(seen) :: "memory");
      ET(2);
      if (!lag) {
        volatile LAS unsigned* go = (volatile LAS unsigned*)(lds + T_SYNC + 4);
        if (w == 4) { wait_flags(seen, 0xf0ull); *go = 1u; }
        else { unsigned spins = 0; while (__builtin_amdgcn_readfirstlane(*go) == 0u) { __builtin_amdgcn_s_sleep(1); if (++spins > (1u << 24)) break; } }
        asm volatile("" ::: "memory");
      }
      ET(3);
      const int s2 = nxt(7), s = lag ? nxt(s2) : s2;
      const char* p = hbase + s * 32768 + wi * 8192;
      const unsigned d = lb + (lag ? 0 : 2 * G2_SLOT) + wi * 8192;
      dmc_first(d, va, p + e0); dmc_next<1024>(va, p + e1); dmc_next<2048>(va, p + e2); dmc_next<3072>(va, p + e3);
      dmc_first(d + 4096, va, p + 4096 + e0); dmc_next<1024>(va, p + 4096 + e1); dmc_next<2048>(va, p + 4096 + e2); dmc_next<3072>(va, p + 4096 + e3);
      ET(4);
    }
    WAIT_L0();
    SCHED();
    BAR();
    SCHED();
    PRIO_M();
#pragma unroll
    for (int n = 0; n < 4; ++n)
#pragma unroll
      for (int i = 0; i < 4; ++i) {
        ac2[i][n] = mfma(bfr[0][n], a[0][i], ac2[i][n]);
        SCHED();
        if (n == 3 && i == 3) {
          PRIO_T();
          if (MODE == 0) WAIT_V_IF0(6, lag); else if (MODE == 2 && q == 16) WAIT_V_IF0(2, lag);
          if (MODE == 1 && q == 1) WAIT_V_IF0(0, lag);
          if (q < 17 || !lag) BAR();
          SCHED();
        }
        ac2[i][n] = mfma(bfr[1][n], a[1][i], ac2[i][n]);
        SCHED();
        if (MODE == 1 && q == 0 && n == 1 && i == 3 && lag) {
          WAIT_V(0);
          unsigned old = 0;
          if (lane == 0) old = __hip_atomic_fetch_add((LAS unsigned*)(lds + T_SYNC), 1u, __ATOMIC_RELAXED, __HIP_MEMORY_SCOPE_WORKGROUP);
          if (__builtin_amdgcn_readfirstlane(old) == 3u && lane < 8)
            __hip_atomic_store(hflag + lane * 32 + nt, 1u, __ATOMIC_RELAXED, __HIP_MEMORY_SCOPE_AGENT);
          SCHED();
        }
      }
    PRIO_E();
    SCHED();
  };
  typedef std::integral_constant<int, 0> M0; typedef std::integral_constant<int, 1> M1; typedef std::integral_constant<int, 2> M2;
  phase2(M1{}, 0);
  ET(1);
  phase2(M1{}, 1);
  ET(5);
  dg4 = __builtin_amdgcn_s_memrealtime(); WAIT_L0();
  ST(2);
#pragma clang loop unroll(disable)
  for (int q = 2; q < 6; ++q) phase2(M0{}, q);
  PEEK(hflag, fo);
  phase2(M0{}, 6);
  asm volatile("" : "+v"(seen));
  wait_flags(seen, 0x0full);
#pragma clang loop unroll(disable)
  for (int q = 7; q < 13; ++q) phase2(M0{}, q);
  if (w == 4 && lane == 0) __hip_atomic_fetch_add(gcnt, 1u, __ATOMIC_RELAXED, __HIP_MEMORY_SCOPE_AGENT);
#pragma clang loop unroll(disable)
  for (int q = 13; q < 16; ++q) phase2(M0{}, q);
  phase2(M2{}, 16);
  PEEK(gcnt, 0u);
  phase2(M2{}, 17);
  asm volatile("s_waitcnt vmcnt(0)" : "+v"(seen) :: "memory");
  wait_cnt(seen, gcnt, 8u);
  dg5 = __builtin_amdgcn_s_memrealtime(); WAIT_L0();
  ST(3);
  const int tile = mb * 8 + nt;
  const auto rs = __builtin_amdgcn_make_buffer_rsrc(ws + WS_H + (size_t)mb * 4 * 36 * 16384, 0, 4 * 36 * 16384, 0x00020000);
  auto xo = [&](int src, int i) { const int u = 15 + nt * 3 + i; return (unsigned)((src * 36 + (u >= 36 ? u - 36 : u)) * 16384 + w * 2048) + lane * 16; };
#pragma unroll
  for (int i = 1; i < 4; ++i) {
    const unsigned o = xo(kq, i);
    __builtin_amdgcn_raw_buffer_store_b128(__builtin_bit_cast(u32x4, pack8(ac2[i][0], ac2[i][1])), rs, o, 0, 16);
    __builtin_amdgcn_raw_buffer_store_b128(__builtin_bit_cast(u32x4, pack8(ac2[i][2], ac2[i][3])), rs, o + 1024, 0, 16);
  }
  WAIT_V(0);
  ST(4);
  asm volatile("" : "+v"(xr[0]), "+v"(xr[1]));
  GAS unsigned* cnt = (GAS unsigned*)(ws + WS_CNT) + (tile * 32 + w) * 16;
  if (lane >= 1 && lane < 4) __hip_atomic_fetch_add(cnt + (kq ^ lane) * 128, 1u, __ATOMIC_RELAXED, __HIP_MEMORY_SCOPE_AGENT);
  {
    unsigned spins = 0;
    while ((unsigned)__builtin_amdgcn_readfirstlane(__hip_atomic_load(cnt + kq * 128, __ATOMIC_RELAXED, __HIP_MEMORY_SCOPE_AGENT)) < 3u) {
      __builtin_amdgcn_s_sleep(2);
      if (++spins > (1u << 22)) break;
    }
  }
  asm volatile("" ::: "memory");
  ST(5);
  u32x4 p[3][2];
#pragma unroll
  for (int i = 1; i < 4; ++i) {
    const unsigned o = xo(kq ^ i, i);
    p[i - 1][0] = __builtin_amdgcn_raw_buffer_load_b128(rs, o, 0, 16);
    p[i - 1][1] = __builtin_amdgcn_raw_buffer_load_b128(rs, o + 1024, 0, 16);
  }
  const auto ors = __builtin_amdgcn_make_buffer_rsrc(out, 0, 4 << 20, 0x00020000);
#pragma unroll
  for (int h = 0; h < 2; ++h) {
    const auto x0 = __builtin_amdgcn_permlane16_swap(xr[h][0], xr[h][2], false, false), x1 = __builtin_amdgcn_permlane16_swap(xr[h][1], xr[h][3], false, false);
    const u32x4 xv = {x0[0], x1[0], x0[1], x1[1]};
    unsigned r[4];
#pragma unroll
    for (int d = 0; d < 4; ++d) {
      float lo = ac2[0][2 * h + (d >> 1)][2 * (d & 1)] + __uint_as_float(xv[d] << 16), hi = ac2[0][2 * h + (d >> 1)][2 * (d & 1) + 1] + __uint_as_float(xv[d] & 0xffff0000u);
#pragma unroll
      for (int i = 0; i < 3; ++i) { lo += __uint_as_float(p[i][h][d] << 16); hi += __uint_as_float(p[i][h][d] & 0xffff0000u); }
      r[d] = pk2(lo, hi);
    }
    const auto y0 = __builtin_amdgcn_permlane16_swap(r[0], r[2], false, false), y1 = __builtin_amdgcn_permlane16_swap(r[1], r[3], false, false);
    __builtin_amdgcn_raw_buffer_store_b128((u32x4){y0[0], y1[0], y0[1], y1[1]}, ors, orow + 64 * h, 0, OUT_AUX);
  }
  ST(6);
  if (b == 1 && w == 0) {
    WAIT_V(0);
    const unsigned long long dg6 = __builtin_amdgcn_s_memrealtime();
    const unsigned long long l0 = ((const unsigned long long*)(ws + WS_ST))[0];
    unsigned long long q = DIAG == 1 ? dg0 - l0 : DIAG == 2 ? dg2 - dg0 : DIAG == 3 ? dg3 - dg2 : DIAG == 4 ? dg4 - dg3 : DIAG == 5 ? dg5 - dg4 : DIAG == 6 ? dg6 - dg5 : DIAG == 7 ? dg6 - l0 : 0;
    if (q > 20000) q = 20000;
    while (__builtin_amdgcn_s_memrealtime() < dg6 + 500 + 4 * q) __builtin_amdgcn_s_sleep(1);
  }
  ST(7);
  ST_OUT(1, 8);
  ST_REST(); ST_OUT(0, 6);
}

extern "C" void kernel_launch(void* const* d_in, const int* in_sizes, int n_in, void* d_out, int out_size, void* d_ws,
                              size_t ws_size, hipStream_t stream) {
  const u16* x = (const u16*)d_in[0];
  const u16* wqkv = (const u16*)d_in[1];
  const u16* wo = (const u16*)d_in[2];
  const u16* wg = (const u16*)d_in[4];
  const u16* wu = (const u16*)d_in[5];
  const u16* wd = (const u16*)d_in[6];
  char* ws = (char*)d_ws;
  (void)hipFuncSetAttribute((const void*)psum_g1_kernel, hipFuncAttributeMaxDynamicSharedMemorySize, G1_LDS);
  psum_ln_kernel<<<256, 320, 0, stream>>>(x, wqkv, wg, wu, ws);
  psum_g1_kernel<<<256, 512, G1_LDS, stream>>>(x, (u16*)d_out, wqkv, wg, wu, wo, wd, ws);
}
```

```cpp
#define PRIO_L 0
#define LORD 1
#define RPOS 0
#include <hip/hip_runtime.h>
#include <hip/hip_bf16.h>
#include <type_traits>
#define EARLY 1
#define HALO_SKIP 1
#define PF_W2 1
#define PF_LOOP 0
#define PF_R0 192
#define PF_PER 2
#define PF_LN_ROWS 96
#define PRIO_M() __builtin_amdgcn_s_setprio(1)
#define PRIO_T() __builtin_amdgcn_s_setprio(3)
#define PRIO_E() __builtin_amdgcn_s_setprio(0)
#define PF_CODE 0
#define PRECLEAN 0
#define OUT_AUX 16
#define G2ORD 0
#define EARLY2 1
#define CHAIN 1
#define PF_X 0
#define XH 1
#define ACQ 0
#define EPI_PRIO 1
#define H_AUX 16

namespace {

typedef unsigned short u16;
typedef short bf16x8 __attribute__((ext_vector_type(8)));
typedef short bf16x4 __attribute__((ext_vector_type(4)));
typedef float f32x4 __attribute__((ext_vector_type(4)));
typedef float f32x2 __attribute__((ext_vector_type(2)));
typedef unsigned u32x4 __attribute__((ext_vector_type(4)));
typedef unsigned u32x2 __attribute__((ext_vector_type(2)));
typedef __bf16 bf2 __attribute__((ext_vector_type(2)));
#define LAS __attribute__((address_space(3)))
#define GAS __attribute__((address_space(1)))

#define WAIT_V(n) asm volatile("s_waitcnt vmcnt(" #n ")" ::: "memory")
#define WAIT_V_IF0(n, c) asm volatile("s_cmp_lg_u32 %0, 0\n\ts_cbranch_scc1 1f\n\ts_waitcnt vmcnt(" #n ")\n1:" :: "s"(c) : "memory", "scc")
#define BAR_IF0(c) asm volatile("s_cmp_lg_u32 %0, 0\n\ts_cbranch_scc1 1f\n\ts_barrier\n1:" :: "s"(c) : "memory", "scc")
#define WAIT_L0() do { asm volatile("" ::: "memory"); __builtin_amdgcn_s_waitcnt(0xC07F); asm volatile("" ::: "memory"); } while (0)
#define BAR() __builtin_amdgcn_s_barrier()
#define SCHED() __builtin_amdgcn_sched_barrier(0)

__device__ __forceinline__ float b2f(u16 v) { return __uint_as_float(((unsigned)v) << 16); }
__device__ __forceinline__ unsigned pk2(float lo, float hi) {
  bf2 r = {(__bf16)lo, (__bf16)hi};
  return __builtin_bit_cast(unsigned, r);
}
__device__ __forceinline__ float rbf(float f) { return (float)(__bf16)f; }
__device__ __forceinline__ bf16x8 pack8(f32x4 a, f32x4 b) {
  u32x4 r = {pk2(a[0], a[1]), pk2(a[2], a[3]), pk2(b[0], b[1]), pk2(b[2], b[3])};
  return __builtin_bit_cast(bf16x8, r);
}
__device__ __forceinline__ bf16x4 trd(LAS const char* p) {
  return __builtin_amdgcn_ds_read_tr16_b64_v4i16((LAS bf16x4*)p);
}
__device__ __forceinline__ bf16x8 cat(bf16x4 a, bf16x4 b) { return __builtin_shufflevector(a, b, 0, 1, 2, 3, 4, 5, 6, 7); }
__device__ __forceinline__ f32x4 mfma(bf16x8 a, bf16x8 b, f32x4 c) {
  return __builtin_amdgcn_mfma_f32_16x16x32_bf16(a, b, c, 0, 0, 0);
}
__device__ __forceinline__ void dma_first(unsigned lds_dst, unsigned voff, const char* b0) {
  asm volatile("s_mov_b32 m0, %0\n\ts_nop 0\n\tglobal_load_lds_dwordx4 %1, %2" :: "s"(lds_dst), "v"(voff), "s"(b0) : "memory");
}
template <int OFF>
__device__ __forceinline__ void dma_next(unsigned voff, const char* b) {
  asm volatile("global_load_lds_dwordx4 %0, %1 offset:%2" :: "v"(voff), "s"(b), "i"(OFF) : "memory");
}
__device__ __forceinline__ void dmc_first(unsigned lds_dst, unsigned voff, const char* b0) {
  asm volatile("s_mov_b32 m0, %0\n\ts_nop 0\n\tglobal_load_lds_dwordx4 %1, %2 sc1" :: "s"(lds_dst), "v"(voff), "s"(b0) : "memory");
}
template <int OFF>
__device__ __forceinline__ void dmc_next(unsigned voff, const char* b) {
  asm volatile("global_load_lds_dwordx4 %0, %1 offset:%2 sc1" :: "v"(voff), "s"(b), "i"(OFF) : "memory");
}
__device__ __forceinline__ void dma1m(unsigned lds_dst, const char* p, unsigned long long mask) {
  asm volatile("s_mov_b32 m0, %0\n\ts_mov_b64 exec, %2\n\t"
               "global_load_lds_dwordx4 %1, off\n\t"
               "s_mov_b64 exec, -1"
               :: "s"(lds_dst), "v"(p), "s"(mask) : "memory");
}
__device__ __forceinline__ void touch(unsigned lds_dst, const void* p) {
  asm volatile("s_mov_b32 m0, %0\n\ts_nop 0\n\tglobal_load_lds_dword %1, off" :: "s"(lds_dst), "v"(p) : "memory");
}
#define CODE_ADDR(dst, sym) do { unsigned lo_, hi_; asm volatile("s_getpc_b64 s[90:91]\n\ts_add_u32 %0, s90, " #sym "@rel32@lo+4\n\ts_addc_u32 %1, s91, " #sym "@rel32@hi+12" \
    : "=s"(lo_), "=s"(hi_) :: "s90", "s91", "scc"); dst = (const char*)(((unsigned long long)hi_ << 32) | lo_); } while (0)
constexpr int LN_LINES = 16, G1_LINES = 80, G2_LINES = 30;
__device__ __forceinline__ LAS char* lp(unsigned a) { return (LAS char*)(size_t)a; }
__device__ __forceinline__ float fmx(float a, float b) { return __builtin_elementwise_maximum(a, b); }
__device__ __forceinline__ float xmax(float v) {
  auto a = __builtin_amdgcn_permlane16_swap(__float_as_uint(v), __float_as_uint(v), false, false);
  v = fmaxf(__uint_as_float(a[0]), __uint_as_float(a[1]));
  auto b = __builtin_amdgcn_permlane32_swap(__float_as_uint(v), __float_as_uint(v), false, false);
  return fmaxf(__uint_as_float(b[0]), __uint_as_float(b[1]));
}
__device__ __forceinline__ float xsum(float v) {
  auto a = __builtin_amdgcn_permlane16_swap(__float_as_uint(v), __float_as_uint(v), false, false);
  v = __uint_as_float(a[0]) + __uint_as_float(a[1]);
  auto b = __builtin_amdgcn_permlane32_swap(__float_as_uint(v), __float_as_uint(v), false, false);
  return __uint_as_float(b[0]) + __uint_as_float(b[1]);
}

template <int CTRL>
__device__ __forceinline__ float dpp_add(float v) {
  return v + __uint_as_float(__builtin_amdgcn_update_dpp(0u, __float_as_uint(v), CTRL, 0xf, 0xf, true));
}
__device__ __forceinline__ float hsum(float v) {
  v = dpp_add<0xB1>(v);
  v = dpp_add<0x4E>(v);
  v = dpp_add<0x128>(v);
  return xsum(v);
}

#define DG(kid, slot)
#define ST_DECL
#define LT_DECL
#define LT(k)
#define ET(k)
#define ST(k)
#define ST_OUT(kid, n)
#define ST_SAVE()
#define ST_REST()
#define WS_BASE_MB 0
constexpr size_t WS_XN = (size_t)WS_BASE_MB << 20;
constexpr size_t WS_H = WS_XN + (size_t)4 * 1024 * 1024;
constexpr size_t H_BYTES = (size_t)8 * 4 * 36 * 16 * 1024;
constexpr size_t WS_X = WS_H + H_BYTES;
constexpr size_t X_BYTES = (size_t)64 * 4 * 3 * 8 * 2048;
constexpr size_t WS_CNT = WS_X + X_BYTES;
constexpr size_t WS_GCNT = WS_CNT + 131072;
constexpr size_t WS_HCNT = WS_GCNT + 8192;
constexpr size_t WS_ST = WS_HCNT + 32768;

}
extern "C" __global__ __launch_bounds__(320) void psum_ln_kernel(const u16* __restrict__ x, const u16* __restrict__ wqkv, const u16* __restrict__ wg,
                                                                 const u16* __restrict__ wu, char* __restrict__ ws) {
  const int lane = threadIdx.x & 63, b = blockIdx.x;
#define DGT 0
  DG(0, 0);
  if (threadIdx.x >= 256) {
    if (lane < 8) *(unsigned*)(ws + WS_CNT + (b * 8 + lane) * 64) = 0u;
    if (lane == 8 && b < 32) *(unsigned*)(ws + WS_GCNT + b * 64) = 0u;
    if (lane >= 16 && lane < 24) *(unsigned*)(ws + WS_HCNT + b * 128 + (lane - 16) * 4) = 0u;
    __shared__ __attribute__((aligned(16))) char sink[256];
    const int kq = (b & 7) >> 1, l = lane < 40 ? lane : lane - 40;
    const u16* p = l < 12 ? wqkv + (l >> 2) * 1024 + 256 * kq + (l & 3) * 64 : (l < 26 ? wg + 896 * kq + (l - 12) * 64 : wu + 896 * kq + (l - 26) * 64);
    const int ld = l < 12 ? 3072 : 3584;
#pragma unroll
    for (int i = 0; i < PF_LN_ROWS / 32; ++i) touch((unsigned)(size_t)(LAS char*)sink, p + (i * 32 + (b >> 3)) * ld);
    return;
  }
  const int hf = (b & 1) * 128 + (b >> 1);
  const int mb = hf >> 5, mf = (hf >> 1) & 15, r = (hf & 1) * 8 + (threadIdx.x >> 6) * 2 + ((lane >> 2) & 1);
  const int row = mb * 256 + 32 * (mf >> 1) + 2 * r + (mf & 1), kl = lane >> 3, ch = lane & 3;
  const u16* xr = x + row * 1024 + kl * 32 + ch * 8;
  u32x4 raw[4];
#pragma unroll
  for (int i = 0; i < 4; ++i) raw[i] = *(const u32x4*)(xr + 256 * i);
  float v[32];
#pragma unroll
  for (int i = 0; i < 16; ++i) { v[2 * i] = __uint_as_float(raw[i >> 2][i & 3] << 16); v[2 * i + 1] = __uint_as_float(raw[i >> 2][i & 3] & 0xffff0000u); }
  float s = 0.f;
#pragma unroll
  for (int i = 0; i < 32; ++i) s += v[i];
  const float mean = hsum(s) * (1.0f / 1024.0f);
  float q = 0.f;
#pragma unroll
  for (int i = 0; i < 32; ++i) { const float d = v[i] - mean; q += d * d; }
  q = hsum(q);
  const float mb16 = rbf(mean), den = rbf(sqrtf(rbf(rbf(q * (1.0f / 1024.0f)) + 1e-5f)));
  const float inv = 1.0f / den;
  const auto rs = __builtin_amdgcn_make_buffer_rsrc(ws + WS_XN, 0, 4 << 20, 0x00020000);
  const unsigned o = ((mb * 32 + kl) * 16 + mf) * 1024 + r * 64 + ((ch ^ (2 * (r >> 3))) * 16);
#pragma unroll
  for (int i = 0; i < 4; ++i) {
    u32x4 t;
#pragma unroll
    for (int e = 0; e < 4; ++e) t[e] = pk2(rbf(v[8 * i + 2 * e] - mb16) * inv, rbf(v[8 * i + 2 * e + 1] - mb16) * inv);
    __builtin_amdgcn_raw_buffer_store_b128(t, rs, o + i * 8 * 16384, 0, H_AUX);
  }
  DG(0, 1);
}
namespace {
constexpr int G1_SA = 17 * 1024, G1_SB = 20 * 1024, G1_SLOT = G1_SA + G1_SB;
constexpr int T_SCR = 159488;
constexpr int T_BIAS = T_SCR + 256;
constexpr int G1_LDS = T_BIAS + 4096;
constexpr int G2_SA = 32768, G2_SB = 16384, G2_SLOT = G2_SA + G2_SB;
constexpr int T_SYNC = T_SCR - 64;
constexpr int T_KV = 2 * G2_SLOT, KV_BYTES = 7 * 1024 + 272 * 80;

template <bool MAX>
__device__ __forceinline__ float tred(float a, float b, float c, float d) {
  const auto r1 = __builtin_amdgcn_permlane16_swap(__float_as_uint(a), __float_as_uint(b), false, false);
  const auto r2 = __builtin_amdgcn_permlane16_swap(__float_as_uint(c), __float_as_uint(d), false, false);
  const float x0 = __uint_as_float(r1[0]), x1 = __uint_as_float(r1[1]), y0 = __uint_as_float(r2[0]), y1 = __uint_as_float(r2[1]);
  const float t = MAX ? fmx(x0, x1) : x0 + x1, u = MAX ? fmx(y0, y1) : y0 + y1;
  const auto r3 = __builtin_amdgcn_permlane32_swap(__float_as_uint(t), __float_as_uint(u), false, false);
  const float z0 = __uint_as_float(r3[0]), z1 = __uint_as_float(r3[1]);
  return MAX ? fmx(z0, z1) : z0 + z1;
}
__device__ __forceinline__ u32x2 glu4(f32x4 g, f32x4 u) {
  f32x4 e = g * -1.4426950408889634f;
#pragma unroll
  for (int i = 0; i < 4; ++i) e[i] = __builtin_amdgcn_exp2f(e[i]);
  e = e + 1.0f;
#pragma unroll
  for (int i = 0; i < 4; ++i) e[i] = __builtin_amdgcn_rcpf(e[i]);
  e = e * u;
  return (u32x2){pk2(e[0], e[1]), pk2(e[2], e[3])};
}
__device__ __forceinline__ u32x4 cat4(u32x2 a, u32x2 b) { return (u32x4){a[0], a[1], b[0], b[1]}; }

template <bool typeA>
__device__ __forceinline__ void body(const u16* __restrict__ x, u16* __restrict__ out, const u16* __restrict__ wqkv, const u16* __restrict__ wg,
                                     const u16* __restrict__ wu, const u16* __restrict__ wo, const u16* __restrict__ wd, char* __restrict__ ws) {
  extern __shared__ __attribute__((aligned(16))) char smem[];
  LAS char* lds = (LAS char*)smem;
  ST_DECL LT_DECL ST(0);
  const int tid = threadIdx.x;
  int lane = tid & 63;
  const int w = __builtin_amdgcn_readfirstlane(tid >> 6), g = w >> 2, wi = w & 3;
  const int b = blockIdx.x, xcd = b & 7, idx = b >> 3;
  const int mb = (xcd & 1) * 4 + (idx & 3), kq = xcd >> 1, ntl = idx >> 2;
  const int j = ntl & 3;
  int fr = lane & 15, fq = lane >> 4;
  const int fA = 896 * kq + 64 * j;
  const int f0 = 896 * kq + 256 + 160 * j, odd = j & 1;
  const int fa = f0 + 32 * odd, fb = fa + 64, fc = odd ? f0 : f0 + 128;

  asm volatile("" : "+s"(wqkv), "+s"(wg), "+s"(wu), "+s"(ws));
  const bool isq = typeA && wi < 3;
  const int ld = isq ? 3072 : 3584;
  const u16* q1 = isq ? wqkv + (wi * 1024 + 256 * kq + 64 * j) : ((!typeA && (wi & 2)) ? wu : wg) + (typeA ? fA : ((wi & 1) ? fb : fa));
  const char* c1 = g ? (const char*)q1 : ws + WS_XN + (size_t)(mb * 32 * 16384 + wi * 4096);
  const int s1 = g ? 8 * ld - 1024 : 0, s2 = g ? 32 * ld - 2048 : 0, s3 = g ? 40 * ld - 3072 : 0;
  const int p1 = g ? 64 * ld : 16384, p2 = g ? 16384 : 64 * 3584;
  const int d1 = (g ? G1_SA : 0) + wi * 4096, d2 = g ? 16384 + wi * 256 : G1_SA + 16384 + wi * 1024;
  const unsigned long long m2 = g ? 0xffffull : ~0ull;
  const int rr = lane >> 3, pc = lane & 7, fi = (pc >> 1) ^ ((rr >> 1) & 3), kr = (rr & 3) + 8 * (rr >> 2);
  const unsigned v1 = g ? kr * ld * 2 + fi * 32 + (pc & 1) * 16 : lane * 16;
  const unsigned lb = (unsigned)(size_t)lds;
  unsigned ns = lb;
  dma_first(ns + d1, v1, c1); dma_next<1024>(v1, c1 + s1); dma_next<2048>(v1, c1 + s2); dma_next<3072>(v1, c1 + s3);
  const char* c2;
  {
    const int hr = wi * 4 + ((lane >> 2) & 3);
    const unsigned oh = (14 + (hr >> 3)) * 1024 + (8 + (hr & 7)) * 64 + (((lane & 3) ^ (2 * (hr >> 3)) ^ 2) * 16);
    const unsigned ow = (kr + 16 * (wi >> 1) + 4 * (wi & 1)) * 7168 + (typeA ? fi * 32 : (fi >> 1) * 32) + (pc & 1) * 16;
    const char* be = g ? ws + WS_XN + (size_t)((mb > 0 ? mb - 1 : 0) * 32 * 16384) : (const char*)(typeA ? wu + fA : wg + fc);
    const char* bo = g ? be : (const char*)(wu + (typeA ? fA : fc));
    c2 = ((fi & 1) ? bo : be) + (g ? oh : ow);
  }
  auto adv = [&]() { c1 += p1; c2 += p2; ns = (ns == lb + 3 * G1_SLOT) ? lb : ns + G1_SLOT; };
  auto stage = [&]() {
    dma_first(ns + d1, v1, c1); dma_next<1024>(v1, c1 + s1); dma_next<2048>(v1, c1 + s2); dma_next<3072>(v1, c1 + s3);
    dma1m(ns + d2, c2, m2);
    adv();
  };
  dma1m(ns + d2, c2, m2);
  adv();
  stage();

  f32x4 acc[4][10];
  f32x4 hacc = {0.f, 0.f, 0.f, 0.f};
#pragma unroll
  for (int i = 0; i < 4; ++i)
#pragma unroll
    for (int n = 0; n < 10; ++n) { acc[i][n] = (f32x4){0.f, 0.f, 0.f, 0.f}; asm volatile("" : "+v"(acc[i][n])); }
  const int rl = 4 * (fq & 1) + (fr >> 2);
  unsigned hoffA = lb + 16384 + fr * 64 + ((fq ^ (2 * (fr >> 3))) * 16);
  unsigned aoff = hoffA - 16384 + wi * 4096;
  unsigned boff0 = lb + G1_SA + (fq >> 1) * 2048 + rl * 128 + 32 * ((2 * g) ^ ((rl >> 1) & 3)) + 8 * (lane & 3);
  unsigned boff1 = boff0 ^ 32;
  unsigned hoffB = (boff0 ^ (32 * (wi & 1))) + (1 + (wi >> 1)) * 4096;
  asm volatile("" : "+v"(aoff), "+v"(hoffA), "+v"(boff0), "+v"(boff1), "+v"(hoffB), "+v"(hacc));
  {
    const int lo = fr + (wi == 0 ? -7 : (wi == 2 ? 1 : 9));
    const int hi = wi == 0 ? fr + 1 : ((wi >= 2 && mb == 0) ? 0 : (wi == 2 ? 8 : 16));
    f32x4 bv;
#pragma unroll
    for (int e = 0; e < 4; ++e) bv[e] = (4 * fq + e >= lo && 4 * fq + e < hi) ? 0.f : -3.0e38f;
    *(LAS f32x4*)(lds + T_BIAS + wi * 1024 + lane * 16) = bv;
    *(LAS unsigned*)(lds + T_SYNC + (lane & 1) * 4) = 0u;
  }
  ST(1);
  WAIT_V_IF0(0, g);
  BAR();
  ST(2);
  if (g == 1) { WAIT_V(0); BAR(); }

  auto step = [&](auto halo_c, auto last_c, int q) {
    constexpr bool HALO = decltype(halo_c)::value, LAST = decltype(last_c)::value;
    bf16x8 a[2][4], wn[4][2];
    asm volatile("" ::: "memory");
    auto rdq = [&](int e, int h) {
      if (e >= (HALO ? 12 : 10)) return;
      const int kk = h >> 1, t = h & 1;
      bf16x8& d = wn[e & 3][kk];
      if (e == 11) { if (t == 0) d = *(const LAS bf16x8*)(lp(hoffA) + kk * G1_SLOT); return; }
      const int n = e < 2 ? 8 + e : e - 2;
      const bf16x4 v = trd(lp(e == 10 ? hoffB : ((n & 1) ? boff1 : boff0)) + (e == 10 ? 0 : (n >> 1) * 4096) + t * 1024 + kk * G1_SLOT);
      if (t == 0) { d[0] = v[0]; d[1] = v[1]; d[2] = v[2]; d[3] = v[3]; } else { d[4] = v[0]; d[5] = v[1]; d[6] = v[2]; d[7] = v[3]; }
    };
#define RA1(kk, i) a[kk][i] = *(const LAS bf16x8*)(lp(aoff) + (kk) * G1_SLOT + (i) * 1024);
    const int dlt = (q & 1) ? -2 * G1_SLOT : 2 * G1_SLOT;
    if (!LAST) {
#pragma unroll
      for (int kk = 0; kk < 2; ++kk) {
        dma_first(ns + d1, v1, c1); RA1(kk, 0) rdq(kk, 0);
        dma_next<1024>(v1, c1 + s1); RA1(kk, 1) rdq(kk, 1);
        dma_next<2048>(v1, c1 + s2); RA1(kk, 2) rdq(kk, 2);
        dma_next<3072>(v1, c1 + s3); RA1(kk, 3) rdq(kk, 3);
        dma1m(ns + d2, c2, m2);
        adv();
      }
    } else
    {
#pragma unroll
      for (int kk = 0; kk < 2; ++kk)
#pragma unroll
        for (int i = 0; i < 4; ++i) RA1(kk, i)
#pragma unroll
      for (int h = 0; h < 8; ++h) rdq(h >> 2, h & 3);
    }
    aoff += dlt;
    SCHED();
    if (!LAST) {
    } else {
      int t = w * 64 + __builtin_amdgcn_mbcnt_hi(~0u, __builtin_amdgcn_mbcnt_lo(~0u, 0u));
      t = t < 288 ? t : t - 288;
      const int r2 = (t >> 4) * 64 + (xcd & 1) * 32 + idx;
      touch(lb + T_SCR, (r2 < 256 ? wo + (256 * kq + r2) * 1024 : wd + (896 * kq + r2 - 256) * 1024) + (t & 15) * 64);
    }
    WAIT_L0();
    SCHED();
    BAR();
    SCHED();
    PRIO_M();
    auto fin = [&]() { SCHED(); PRIO_T(); if (!LAST) WAIT_V(0); BAR(); SCHED(); };
#pragma unroll
    for (int e = 0; e < 10; ++e) {
      const int n = e < 2 ? 8 + e : e - 2;
#pragma unroll
      for (int i = 0; i < 4; ++i) {
        acc[i][n] = mfma(wn[e & 3][0], a[0][i], acc[i][n]);
        SCHED();
        if (e == 0) rdq(2, i);
        if (!HALO && e == 9 && i == 3) fin();
        SCHED();
        acc[i][n] = mfma(wn[e & 3][1], a[1][i], acc[i][n]);
        SCHED();
        rdq(e + 3, i);
        SCHED();
      }
    }
    if (HALO) {
      hacc = mfma(wn[2][0], wn[3][0], hacc);
      WAIT_L0();
      fin();
      hacc = mfma(wn[2][1], wn[3][1], hacc);
    }
    hoffA += dlt; boff0 += dlt; boff1 += dlt; hoffB += dlt;
    PRIO_E();
    SCHED();
  };
  const int nt = ntl;
  const int kba = 8 + ((fa - 896 * kq) >> 5);
  const int own0 = typeA ? j : (kba >> 1), own1 = typeA ? 4 + j : (kba >> 1) + 1;
  const char* const hbase = ws + WS_H + (size_t)(mb * 4 + kq) * 36 * 16384;
  auto brow = [&](int stp_, int w_) {
    return (const char*)(stp_ < 4 ? wo + (size_t)(256 * kq) * 1024 : wd + (size_t)(896 * kq) * 1024 - 256 * 1024) + ((stp_ * 64 + 8 * w_) * 1024 + nt * 128) * 2;
  };
  unsigned va, vb;
  auto stage1 = [&](auto halo_c) {
    constexpr bool TA = decltype(halo_c)::value;
    typedef std::integral_constant<bool, TA || !HALO_SKIP> HC;
#pragma clang loop unroll(disable)
    for (int q = 0; q < 15; ++q) step(HC{}, std::false_type{}, q);
    step(HC{}, std::true_type{}, 15);
    ST(3);
    asm volatile("" ::: "memory");
    if (g == 0) __builtin_amdgcn_s_setprio(0); else __builtin_amdgcn_s_setprio(EPI_PRIO);
    lane = __builtin_amdgcn_mbcnt_hi(~0u, __builtin_amdgcn_mbcnt_lo(~0u, 0u));
    asm volatile("" : "+v"(lane));
    fr = lane & 15; fq = lane >> 4;
    va = lane * 16;
    vb = (lane >> 3) * 2048 + ((((lane & 7) >> 1) ^ ((lane >> 4) & 3)) * 32) + (lane & 1) * 16;
    if (g == 1) {
#pragma unroll
      for (int u = 0; u < 4; ++u) {
        const int w_ = wi * 2 + (u & 1);
        const char* p0 = brow((u >> 1) ? own1 : own0, w_);
        dma_first(lb + (u >> 1) * G2_SLOT + G2_SA + w_ * 2048, vb, p0); dma_next<1024>(vb, p0 - 896);
      }
    }
    const auto hrs = __builtin_amdgcn_make_buffer_rsrc((void*)hbase, 0, 36 * 16384, 0x00020000);
    const unsigned hv = wi * 4096 + lane * 16;
    auto put = [&](u32x4 v, int s, int kb, int i) {
      __builtin_amdgcn_raw_buffer_store_b128(v, hrs, hv + kb * 16384 + i * 1024, 0, H_AUX);
      *(LAS u32x4*)(lds + s * G2_SLOT + g * 16384 + ((wi * 4096 + kq * 1024 + lane * 16) ^ (i * 1024))) = v;
    };
#define GLU(i, a, b) glu4(acc[i][a], acc[i][b])
    if (!TA) {
      const int kbc = 8 + ((fc - 896 * kq) >> 5);
#pragma unroll
      for (int i = 0; i < 4; ++i) {
        __builtin_amdgcn_raw_buffer_store_b64(GLU(i, 8, 9), hrs, hv + kbc * 16384 + g * 8 + i * 1024, 0, H_AUX);
        put(cat4(GLU(i, 0, 4), GLU(i, 1, 5)), 0, kba + g, i);
        put(cat4(GLU(i, 2, 6), GLU(i, 3, 7)), 1, kba + 2 + g, i);
        if (i == 0) { SCHED(); BAR_IF0(g); SCHED(); }
      }
    } else {
      auto glu_part = [&](int i0, int i1) {
#pragma unroll
        for (int i = i0; i < i1; ++i) put(cat4(GLU(i, 6, 8), GLU(i, 7, 9)), 1, 8 + 2 * j + g, i);
      };
      if (g == 0) { glu_part(0, 2); SCHED(); BAR(); asm volatile("" ::: "memory"); }
    LAS char* kim = lds + T_KV + g * KV_BYTES;
    LAS char* vim = kim + 7 * 1024;
    bf16x8 kf[4], qf[4];
#pragma unroll
    for (int i = 0; i < 4; ++i) {
      kf[i] = pack8(acc[i][2], acc[i][3]);
      *(LAS bf16x8*)(vim + ((wi * 4 + i) * 16 + fr) * 80 + fq * 16) = pack8(acc[i][4], acc[i][5]);
    }
    if (wi < 3) {
      *(LAS bf16x8*)(kim + wi * 2048 + lane * 16) = kf[2];
      *(LAS bf16x8*)(kim + wi * 2048 + 1024 + lane * 16) = kf[3];
    }
    {
      LAS char* hp = (wi < 2) ? (kim + 6 * 1024 + lane * 16 + wi * 8) : (vim + (256 + fr) * 80 + fq * 16 + (wi - 2) * 8);
      *(LAS u32x2*)hp = (u32x2){pk2(hacc[0], hacc[1]), pk2(hacc[2], hacc[3])};
    }
    WAIT_L0();
    BAR();
    asm volatile("" ::: "memory");
    const bool halo = wi == 0;
    const f32x4 bc = *(const LAS f32x4*)(lds + T_BIAS + lane * 16), bp = *(const LAS f32x4*)(lds + T_BIAS + 1024 + lane * 16);
    const f32x4 bp0 = *(const LAS f32x4*)(lds + T_BIAS + (halo ? 2048 : 1024) + lane * 16);
    const f32x4 bp1 = *(const LAS f32x4*)(lds + T_BIAS + (halo ? 3072 : 1024) + lane * 16);
    const int pf0 = halo ? 16 : wi * 4 - 2, pf1 = halo ? 16 : wi * 4 - 1;
    const bf16x8 kp0 = *(const LAS bf16x8*)(kim + (halo ? 6 : 2 * wi - 2) * 1024 + lane * 16);
    const bf16x8 kp1 = *(const LAS bf16x8*)(kim + (halo ? 6 : 2 * wi - 1) * 1024 + lane * 16);
    const LAS char* vr = vim + (4 * fq + (fr >> 2)) * 80 + (lane & 3) * 16;
    bf16x4 vt[6][2];
#pragma unroll
    for (int d = 0; d < 2; ++d) {
      vt[0][d] = trd(vr + pf0 * 1280 + d * 8); vt[1][d] = trd(vr + pf1 * 1280 + d * 8);
#pragma unroll
      for (int i = 0; i < 4; ++i) vt[2 + i][d] = trd(vr + (wi * 4 + i) * 1280 + d * 8);
    }
    f32x4 sp[4], sc[4];
#pragma unroll
    for (int i = 0; i < 4; ++i) {
      qf[i] = pack8(acc[i][0], acc[i][1]);
      sc[i] = mfma(kf[i], qf[i], bc);
    }
    sp[2] = mfma(kf[0], qf[2], bp); sp[3] = mfma(kf[1], qf[3], bp);
    sp[0] = mfma(kp0, qf[0], bp0); sp[1] = mfma(kp1, qf[1], bp1);
    const float sl = 0.17677669529663687f * 1.4426950408889634f;
    const f32x4 z4 = {0.f, 0.f, 0.f, 0.f};
    float m[4];
#pragma unroll
    for (int i = 0; i < 4; ++i)
      m[i] = fmx(fmx(fmx(fmx(sp[i][0], sp[i][1]), sp[i][2]), fmx(fmx(sp[i][3], sc[i][0]), sc[i][1])), fmx(sc[i][2], sc[i][3]));
    {
      const unsigned t = __float_as_uint(tred<true>(m[0], m[1], m[2], m[3]));
      const auto u = __builtin_amdgcn_permlane32_swap(t, t, false, false);
      const auto u0 = __builtin_amdgcn_permlane16_swap(u[0], u[0], false, false), u1 = __builtin_amdgcn_permlane16_swap(u[1], u[1], false, false);
      m[0] = __uint_as_float(u0[0]); m[1] = __uint_as_float(u0[1]); m[2] = __uint_as_float(u1[0]); m[3] = __uint_as_float(u1[1]);
    }
    const u32x4 one4 = {0x3f803f80u, 0x3f803f80u, 0x3f803f80u, 0x3f803f80u};
#pragma unroll
    for (int i = 0; i < 4; ++i) {
      const float mm = -sl * m[i];
#pragma unroll
      for (int e = 0; e < 4; ++e) {
        sp[i][e] = __builtin_amdgcn_exp2f(__builtin_fmaf(sp[i][e], sl, mm));
        sc[i][e] = __builtin_amdgcn_exp2f(__builtin_fmaf(sc[i][e], sl, mm));
      }
      const bf16x8 pfr = pack8(sp[i], sc[i]);
      const f32x4 sm = mfma(__builtin_bit_cast(bf16x8, one4), pfr, z4);
      const f32x4 o0 = mfma(cat(vt[i][0], vt[2 + i][0]), pfr, z4);
      const f32x4 o1 = mfma(cat(vt[i][1], vt[2 + i][1]), pfr, z4);
      const float inv = __builtin_amdgcn_rcpf(sm[0]);
      put(__builtin_bit_cast(u32x4, pack8(o0 * inv, o1 * inv)), 0, 2 * j + g, i);
    }
      glu_part(2, 4);
      if (g == 1) glu_part(0, 2);
    }
  };
  stage1(std::integral_constant<bool, typeA>{});
  ST(4);

  const int lag = g ^ 1;
  GAS unsigned* hflag = (GAS unsigned*)(ws + WS_HCNT) + (mb * 4 + kq) * 256;
  const unsigned fo = (nt * 32 + (lane & 7)) * 4;
  GAS unsigned* gcnt = (GAS unsigned*)(ws + WS_GCNT) + (mb * 4 + kq) * 16;
  const int e0 = kq * 1024, e1 = ((1 ^ kq) - 1) * 1024, e2 = ((2 ^ kq) - 2) * 1024, e3 = ((3 ^ kq) - 3) * 1024;
  auto nxt = [&](int s) { ++s; if (s == own0) ++s; if (s == own1) ++s; if (s == 18) s = (own0 == 0) ? 1 : 0; return s; };
  auto wait_cnt = [&](unsigned seen, GAS unsigned* c, unsigned need) {
    if ((unsigned)__builtin_amdgcn_readfirstlane(seen) < need) {
      asm volatile("" ::: "memory");
      unsigned spins = 0;
      while ((unsigned)__builtin_amdgcn_readfirstlane(__hip_atomic_load(c, __ATOMIC_RELAXED, __HIP_MEMORY_SCOPE_AGENT)) < need)
        if (++spins > (1u << 22)) break;
    }
    asm volatile("" ::: "memory");
  };
  auto wait_flags = [&](unsigned seen, unsigned long long need) {
    if ((__builtin_amdgcn_ballot_w64(seen != 0u) & need) != need) {
      asm volatile("" ::: "memory");
      unsigned spins = 0;
      while ((__builtin_amdgcn_ballot_w64(__hip_atomic_load(hflag + (fo >> 2), __ATOMIC_RELAXED, __HIP_MEMORY_SCOPE_AGENT) != 0u) & need) != need) {
        __builtin_amdgcn_s_sleep(1);
        if (++spins > (1u << 22)) break;
      }
    }
    asm volatile("" ::: "memory");
  };
  int stp = nxt(7);
  const int mf = wi * 4 + kq;
  const unsigned orow = ((mb * 256 + 32 * (mf >> 1) + 2 * fr + (mf & 1)) * 1024 + nt * 128 + g * 64 + 16 * (fq & 1) + 8 * (fq >> 1)) * 2;
  u32x4 xr[2] = {{0u, 0u, 0u, 0u}, {0u, 0u, 0u, 0u}};
  const int rl2 = 4 * (fq & 1) + (fr >> 2);
  unsigned ao = lb + wi * 4096 + lane * 16;
  unsigned bo = lb + G2_SA + (fq >> 1) * 2048 + g * 1024 + rl2 * 128 + 32 * ((rl2 >> 1) & 3) + 8 * (lane & 3);
  f32x4 ac2[4][4];
#pragma unroll
  for (int i = 0; i < 4; ++i)
#pragma unroll
    for (int n = 0; n < 4; ++n) { ac2[i][n] = (f32x4){0.f, 0.f, 0.f, 0.f}; asm volatile("" : "+v"(ac2[i][n])); }
  ns = lb + 2 * G2_SLOT;
  const char* ca = hbase + stp * 32768 + w * 4096;
  const char* cb = brow(stp, w);
  int sl2 = 0;
  WAIT_V_IF0(0, lag);
  WAIT_L0();
  BAR();
  ST(5);
  ST_SAVE(); ST(0); ST(1);
  unsigned seen = 0;
#define PEEK(c, o) asm volatile("global_load_dword %0, %1, %2 sc1" : "+v"(seen) : "v"(o), "s"(c) : "memory")
  if (lag) BAR();

  auto phase2 = [&](auto mode, int q) {
    constexpr int MODE = decltype(mode)::value;
    bf16x8 a[2][4], bfr[2][4];
#define RA(kk, i) a[kk][i] = *(const LAS bf16x8*)(lp(ao) + (kk) * 16384 + (i) * 1024);
#define RB(kk, n) bfr[kk][n] = cat(trd(lp(bo ^ ((n) * 32)) + (kk) * 8192), trd(lp(bo ^ ((n) * 32)) + (kk) * 8192 + 4096));
    int dlt;
    asm volatile("" ::: "memory");
    if (MODE == 1 && q == 1 && w == 4) PEEK(hflag, fo);
    if (MODE == 2 && q == 16)
      asm volatile("global_load_dwordx4 %0, %2, %3\n\tglobal_load_dwordx4 %1, %2, %3 offset:64" : "+v"(xr[0]), "+v"(xr[1]) : "v"(orow), "s"(x) : "memory");
    RA(0, 0) RA(0, 1)
    if (MODE == 0) dmc_first(ns + w * 4096, va, ca + e0);
    RA(0, 2) RA(0, 3) RB(0, 0)
    if (MODE == 0) dmc_next<1024>(va, ca + e1);
    RB(0, 1) RB(0, 2) RB(0, 3)
    if (MODE == 0) dmc_next<2048>(va, ca + e2);
    RA(1, 0) RA(1, 1) RA(1, 2)
    if (MODE == 0) dmc_next<3072>(va, ca + e3);
    RA(1, 3) RB(1, 0)
    if (MODE < 2) dma_first(ns + G2_SA + w * 2048, vb, cb);
    RB(1, 1)
    if (MODE < 2) dma_next<1024>(vb, cb - 896);
    sl2 = (sl2 == 2) ? 0 : sl2 + 1; dlt = (sl2 == 0) ? -2 * G2_SLOT : G2_SLOT;
    if (MODE < 2) { stp = nxt(stp); ca = hbase + stp * 32768 + w * 4096; cb = brow(stp, w); ns = (ns == lb + 2 * G2_SLOT) ? lb : ns + G2_SLOT; }
    asm volatile("" : "+s"(ca), "+s"(cb), "+s"(ns), "+s"(sl2), "+s"(dlt), "+s"(stp) :: "memory");
    RB(1, 2) RB(1, 3)
#undef RA
#undef RB
    ao += dlt; bo += dlt;
    SCHED();
    asm volatile("" : "+v"(bo), "+v"(ao));
    if (MODE == 0) WAIT_V_IF0(6, lag ^ 1); else if (MODE == 2 && q == 16) WAIT_V_IF0(2, lag ^ 1);
    if (MODE == 1 && q == 1) {
      asm volatile("s_waitcnt vmcnt(2)" : "+v"(seen) :: "memory");
      ET(2);
      if (!lag) {
        volatile LAS unsigned* go = (volatile LAS unsigned*)(lds + T_SYNC + 4);
        if (w == 4) { wait_flags(seen, 0xf0ull); *go = 1u; }
        else { unsigned spins = 0; while (__builtin_amdgcn_readfirstlane(*go) == 0u) { __builtin_amdgcn_s_sleep(1); if (++spins > (1u << 24)) break; } }
        asm volatile("" ::: "memory");
      }
      ET(3);
      const int s2 = nxt(7), s = lag ? nxt(s2) : s2;
      const char* p = hbase + s * 32768 + wi * 8192;
      const unsigned d = lb + (lag ? 0 : 2 * G2_SLOT) + wi * 8192;
      dmc_first(d, va, p + e0); dmc_next<1024>(va, p + e1); dmc_next<2048>(va, p + e2); dmc_next<3072>(va, p + e3);
      dmc_first(d + 4096, va, p + 4096 + e0); dmc_next<1024>(va, p + 4096 + e1); dmc_next<2048>(va, p + 4096 + e2); dmc_next<3072>(va, p + 4096 + e3);
      ET(4);
    }
    WAIT_L0();
    SCHED();
    BAR();
    SCHED();
    PRIO_M();
#pragma unroll
    for (int n = 0; n < 4; ++n)
#pragma unroll
      for (int i = 0; i < 4; ++i) {
        ac2[i][n] = mfma(bfr[0][n], a[0][i], ac2[i][n]);
        SCHED();
        if (n == 3 && i == 3) {
          PRIO_T();
          if (MODE == 0) WAIT_V_IF0(6, lag); else if (MODE == 2 && q == 16) WAIT_V_IF0(2, lag);
          if (MODE == 1 && q == 1) WAIT_V_IF0(0, lag);
          if (q < 17 || !lag) BAR();
          SCHED();
        }
        ac2[i][n] = mfma(bfr[1][n], a[1][i], ac2[i][n]);
        SCHED();
        if (MODE == 1 && q == 0 && n == 1 && i == 3 && lag) {
          WAIT_V(0);
          unsigned old = 0;
          if (lane == 0) old = __hip_atomic_fetch_add((LAS unsigned*)(lds + T_SYNC), 1u, __ATOMIC_RELAXED, __HIP_MEMORY_SCOPE_WORKGROUP);
          if (__builtin_amdgcn_readfirstlane(old) == 3u && lane < 8)
            __hip_atomic_store(hflag + lane * 32 + nt, 1u, __ATOMIC_RELAXED, __HIP_MEMORY_SCOPE_AGENT);
          SCHED();
        }
      }
    PRIO_E();
    SCHED();
  };
  typedef std::integral_constant<int, 0> M0; typedef std::integral_constant<int, 1> M1; typedef std::integral_constant<int, 2> M2;
  phase2(M1{}, 0);
  ET(1);
  phase2(M1{}, 1);
  ET(5);
  ST(2);
#pragma clang loop unroll(disable)
  for (int q = 2; q < 6; ++q) phase2(M0{}, q);
  PEEK(hflag, fo);
  phase2(M0{}, 6);
  asm volatile("" : "+v"(seen));
  wait_flags(seen, 0x0full);
#pragma clang loop unroll(disable)
  for (int q = 7; q < 13; ++q) phase2(M0{}, q);
  if (w == 4 && lane == 0) __hip_atomic_fetch_add(gcnt, 1u, __ATOMIC_RELAXED, __HIP_MEMORY_SCOPE_AGENT);
#pragma clang loop unroll(disable)
  for (int q = 13; q < 16; ++q) phase2(M0{}, q);
  phase2(M2{}, 16);
  PEEK(gcnt, 0u);
  phase2(M2{}, 17);
  asm volatile("s_waitcnt vmcnt(0)" : "+v"(seen) :: "memory");
  wait_cnt(seen, gcnt, 8u);
  ST(3);
  const int tile = mb * 8 + nt;
  const auto rs = __builtin_amdgcn_make_buffer_rsrc(ws + WS_H + (size_t)mb * 4 * 36 * 16384, 0, 4 * 36 * 16384, 0x00020000);
  auto xo = [&](int src, int i) { const int u = 15 + nt * 3 + i; return (unsigned)((src * 36 + (u >= 36 ? u - 36 : u)) * 16384 + w * 2048) + lane * 16; };
#pragma unroll
  for (int i = 1; i < 4; ++i) {
    const unsigned o = xo(kq, i);
    __builtin_amdgcn_raw_buffer_store_b128(__builtin_bit_cast(u32x4, pack8(ac2[i][0], ac2[i][1])), rs, o, 0, 16);
    __builtin_amdgcn_raw_buffer_store_b128(__builtin_bit_cast(u32x4, pack8(ac2[i][2], ac2[i][3])), rs, o + 1024, 0, 16);
  }
  WAIT_V(0);
  ST(4);
  asm volatile("" : "+v"(xr[0]), "+v"(xr[1]));
  GAS unsigned* cnt = (GAS unsigned*)(ws + WS_CNT) + (tile * 32 + w) * 16;
  if (lane >= 1 && lane < 4) __hip_atomic_fetch_add(cnt + (kq ^ lane) * 128, 1u, __ATOMIC_RELAXED, __HIP_MEMORY_SCOPE_AGENT);
  {
    unsigned spins = 0;
    while ((unsigned)__builtin_amdgcn_readfirstlane(__hip_atomic_load(cnt + kq * 128, __ATOMIC_RELAXED, __HIP_MEMORY_SCOPE_AGENT)) < 3u) {
      __builtin_amdgcn_s_sleep(2);
      if (++spins > (1u << 22)) break;
    }
  }
  asm volatile("" ::: "memory");
  ST(5);
  u32x4 p[3][2];
#pragma unroll
  for (int i = 1; i < 4; ++i) {
    const unsigned o = xo(kq ^ i, i);
    p[i - 1][0] = __builtin_amdgcn_raw_buffer_load_b128(rs, o, 0, 16);
    p[i - 1][1] = __builtin_amdgcn_raw_buffer_load_b128(rs, o + 1024, 0, 16);
  }
  const auto ors = __builtin_amdgcn_make_buffer_rsrc(out, 0, 4 << 20, 0x00020000);
#pragma unroll
  for (int h = 0; h < 2; ++h) {
    const auto x0 = __builtin_amdgcn_permlane16_swap(xr[h][0], xr[h][2], false, false), x1 = __builtin_amdgcn_permlane16_swap(xr[h][1], xr[h][3], false, false);
    const u32x4 xv = {x0[0], x1[0], x0[1], x1[1]};
    unsigned r[4];
#pragma unroll
    for (int d = 0; d < 4; ++d) {
      float lo = ac2[0][2 * h + (d >> 1)][2 * (d & 1)] + __uint_as_float(xv[d] << 16), hi = ac2[0][2 * h + (d >> 1)][2 * (d & 1) + 1] + __uint_as_float(xv[d] & 0xffff0000u);
#pragma unroll
      for (int i = 0; i < 3; ++i) { lo += __uint_as_float(p[i][h][d] << 16); hi += __uint_as_float(p[i][h][d] & 0xffff0000u); }
      r[d] = pk2(lo, hi);
    }
    const auto y0 = __builtin_amdgcn_permlane16_swap(r[0], r[2], false, false), y1 = __builtin_amdgcn_permlane16_swap(r[1], r[3], false, false);
    __builtin_amdgcn_raw_buffer_store_b128((u32x4){y0[0], y1[0], y0[1], y1[1]}, ors, orow + 64 * h, 0, OUT_AUX);
  }
  ST(6);
  ST(7);
  ST_OUT(1, 8);
  ST_REST(); ST_OUT(0, 6);
}

}

extern "C" __global__ __launch_bounds__(512, 2) void psum_g1_kernel(const u16* __restrict__ x, u16* __restrict__ out, const u16* __restrict__ wqkv,
                                                                    const u16* __restrict__ wg, const u16* __restrict__ wu,
                                                                    const u16* __restrict__ wo, const u16* __restrict__ wd, char* __restrict__ ws) {
  if (blockIdx.x < 128) body<true>(x, out, wqkv, wg, wu, wo, wd, ws);
  else body<false>(x, out, wqkv, wg, wu, wo, wd, ws);
}

extern "C" void kernel_launch(void* const* d_in, const int* in_sizes, int n_in, void* d_out, int out_size, void* d_ws,
                              size_t ws_size, hipStream_t stream) {
  const u16* x = (const u16*)d_in[0];
  const u16* wqkv = (const u16*)d_in[1];
  const u16* wo = (const u16*)d_in[2];
  const u16* wg = (const u16*)d_in[4];
  const u16* wu = (const u16*)d_in[5];
  const u16* wd = (const u16*)d_in[6];
  char* ws = (char*)d_ws;
  (void)hipFuncSetAttribute((const void*)psum_g1_kernel, hipFuncAttributeMaxDynamicSharedMemorySize, G1_LDS);
  psum_ln_kernel<<<256, 320, 0, stream>>>(x, wqkv, wg, wu, ws);
  psum_g1_kernel<<<256, 512, G1_LDS, stream>>>(x, (u16*)d_out, wqkv, wg, wu, wo, wd, ws);
}
```

```cpp
#define PFD 0
#define PF_LN_ROWS 96
#include <hip/hip_runtime.h>
#include <hip/hip_bf16.h>
#include <type_traits>
#define EARLY 1
#define HALO_SKIP 1
#define PF_W2 1
#define PF_LOOP 0
#define PF_R0 192
#define PF_PER 2
#define PRIO_L 0
#define PRIO_M() __builtin_amdgcn_s_setprio(1)
#define PRIO_T() __builtin_amdgcn_s_setprio(3)
#define PRIO_E() __builtin_amdgcn_s_setprio(0)
#define PF_CODE 0
#define PRECLEAN 0
#define OUT_AUX 16
#define G2ORD 0
#define EARLY2 1
#define CHAIN 1
#define PF_X 0
#define XH 1
#define ACQ 0
#define EPI_PRIO 1
#define LORD 1
#define RPOS 1
#define FA 4
#define FB 0
#define H_AUX 16

namespace {

typedef unsigned short u16;
typedef short bf16x8 __attribute__((ext_vector_type(8)));
typedef short bf16x4 __attribute__((ext_vector_type(4)));
typedef float f32x4 __attribute__((ext_vector_type(4)));
typedef float f32x2 __attribute__((ext_vector_type(2)));
typedef unsigned u32x4 __attribute__((ext_vector_type(4)));
typedef unsigned u32x2 __attribute__((ext_vector_type(2)));
typedef __bf16 bf2 __attribute__((ext_vector_type(2)));
#define LAS __attribute__((address_space(3)))
#define GAS __attribute__((address_space(1)))

#define WAIT_V(n) asm volatile("s_waitcnt vmcnt(" #n ")" ::: "memory")
#define WAIT_V_IF0(n, c) asm volatile("s_cmp_lg_u32 %0, 0\n\ts_cbranch_scc1 1f\n\ts_waitcnt vmcnt(" #n ")\n1:" :: "s"(c) : "memory", "scc")
#define BAR_IF0(c) asm volatile("s_cmp_lg_u32 %0, 0\n\ts_cbranch_scc1 1f\n\ts_barrier\n\ts_setprio 0\n1:" :: "s"(c) : "memory", "scc")
#define WAIT_L0() do { asm volatile("" ::: "memory"); __builtin_amdgcn_s_waitcnt(0xC07F); asm volatile("" ::: "memory"); } while (0)
#define BAR() __builtin_amdgcn_s_barrier()
#define SCHED() __builtin_amdgcn_sched_barrier(0)

__device__ __forceinline__ float b2f(u16 v) { return __uint_as_float(((unsigned)v) << 16); }
__device__ __forceinline__ unsigned pk2(float lo, float hi) {
  bf2 r = {(__bf16)lo, (__bf16)hi};
  return __builtin_bit_cast(unsigned, r);
}
__device__ __forceinline__ float rbf(float f) { return (float)(__bf16)f; }
__device__ __forceinline__ bf16x8 pack8(f32x4 a, f32x4 b) {
  u32x4 r = {pk2(a[0], a[1]), pk2(a[2], a[3]), pk2(b[0], b[1]), pk2(b[2], b[3])};
  return __builtin_bit_cast(bf16x8, r);
}
__device__ __forceinline__ bf16x4 trd(LAS const char* p) {
  return __builtin_amdgcn_ds_read_tr16_b64_v4i16((LAS bf16x4*)p);
}
__device__ __forceinline__ bf16x8 cat(bf16x4 a, bf16x4 b) { return __builtin_shufflevector(a, b, 0, 1, 2, 3, 4, 5, 6, 7); }
__device__ __forceinline__ f32x4 mfma(bf16x8 a, bf16x8 b, f32x4 c) {
  return __builtin_amdgcn_mfma_f32_16x16x32_bf16(a, b, c, 0, 0, 0);
}
__device__ __forceinline__ void dma_first(unsigned lds_dst, unsigned voff, const char* b0) {
  asm volatile("s_mov_b32 m0, %0\n\ts_nop 0\n\tglobal_load_lds_dwordx4 %1, %2" :: "s"(lds_dst), "v"(voff), "s"(b0) : "memory");
}
template <int OFF>
__device__ __forceinline__ void dma_next(unsigned voff, const char* b) {
  asm volatile("global_load_lds_dwordx4 %0, %1 offset:%2" :: "v"(voff), "s"(b), "i"(OFF) : "memory");
}
__device__ __forceinline__ void dmc_first(unsigned lds_dst, unsigned voff, const char* b0) {
  asm volatile("s_mov_b32 m0, %0\n\ts_nop 0\n\tglobal_load_lds_dwordx4 %1, %2 sc1" :: "s"(lds_dst), "v"(voff), "s"(b0) : "memory");
}
template <int OFF>
__device__ __forceinline__ void dmc_next(unsigned voff, const char* b) {
  asm volatile("global_load_lds_dwordx4 %0, %1 offset:%2 sc1" :: "v"(voff), "s"(b), "i"(OFF) : "memory");
}
__device__ __forceinline__ void dma1m(unsigned lds_dst, const char* p, unsigned long long mask) {
  asm volatile("s_mov_b32 m0, %0\n\ts_mov_b64 exec, %2\n\t"
               "global_load_lds_dwordx4 %1, off\n\t"
               "s_mov_b64 exec, -1"
               :: "s"(lds_dst), "v"(p), "s"(mask) : "memory");
}
__device__ __forceinline__ void touch(unsigned lds_dst, const void* p) {
  asm volatile("s_mov_b32 m0, %0\n\ts_nop 0\n\tglobal_load_lds_dword %1, off" :: "s"(lds_dst), "v"(p) : "memory");
}
#define CODE_ADDR(dst, sym) do { unsigned lo_, hi_; asm volatile("s_getpc_b64 s[90:91]\n\ts_add_u32 %0, s90, " #sym "@rel32@lo+4\n\ts_addc_u32 %1, s91, " #sym "@rel32@hi+12" \
    : "=s"(lo_), "=s"(hi_) :: "s90", "s91", "scc"); dst = (const char*)(((unsigned long long)hi_ << 32) | lo_); } while (0)
constexpr int LN_LINES = 16, G1_LINES = 80, G2_LINES = 30;
__device__ __forceinline__ LAS char* lp(unsigned a) { return (LAS char*)(size_t)a; }
__device__ __forceinline__ float fmx(float a, float b) { return __builtin_elementwise_maximum(a, b); }
__device__ __forceinline__ float xmax(float v) {
  auto a = __builtin_amdgcn_permlane16_swap(__float_as_uint(v), __float_as_uint(v), false, false);
  v = fmaxf(__uint_as_float(a[0]), __uint_as_float(a[1]));
  auto b = __builtin_amdgcn_permlane32_swap(__float_as_uint(v), __float_as_uint(v), false, false);
  return fmaxf(__uint_as_float(b[0]), __uint_as_float(b[1]));
}
__device__ __forceinline__ float xsum(float v) {
  auto a = __builtin_amdgcn_permlane16_swap(__float_as_uint(v), __float_as_uint(v), false, false);
  v = __uint_as_float(a[0]) + __uint_as_float(a[1]);
  auto b = __builtin_amdgcn_permlane32_swap(__float_as_uint(v), __float_as_uint(v), false, false);
  return __uint_as_float(b[0]) + __uint_as_float(b[1]);
}

template <int CTRL>
__device__ __forceinline__ float dpp_add(float v) {
  return v + __uint_as_float(__builtin_amdgcn_update_dpp(0u, __float_as_uint(v), CTRL, 0xf, 0xf, true));
}
__device__ __forceinline__ float hsum(float v) {
  v = dpp_add<0xB1>(v);
  v = dpp_add<0x4E>(v);
  v = dpp_add<0x128>(v);
  return xsum(v);
}

#define DG(kid, slot)
#define ST_DECL
#define LT_DECL
#define LT(k)
#define ET(k)
#define ST(k)
#define ST_OUT(kid, n)
#define ST_SAVE()
#define ST_REST()
#define WS_BASE_MB 0
constexpr size_t WS_XN = (size_t)WS_BASE_MB << 20;
constexpr size_t WS_H = WS_XN + (size_t)4 * 1024 * 1024;
constexpr size_t H_BYTES = (size_t)8 * 4 * 36 * 16 * 1024;
constexpr size_t WS_X = WS_H + H_BYTES;
constexpr size_t X_BYTES = (size_t)64 * 4 * 3 * 8 * 2048;
constexpr size_t WS_CNT = WS_X + X_BYTES;
constexpr size_t WS_GCNT = WS_CNT + 131072;
constexpr size_t WS_HCNT = WS_GCNT + 8192;
constexpr size_t WS_ST = WS_HCNT + 32768;

}
extern "C" __global__ __launch_bounds__(320) void psum_ln_kernel(const u16* __restrict__ x, const u16* __restrict__ wqkv, const u16* __restrict__ wg,
                                                                 const u16* __restrict__ wu, char* __restrict__ ws) {
  const int lane = threadIdx.x & 63, b = blockIdx.x;
#define DGT 0
  DG(0, 0);
  if (threadIdx.x >= 256) {
    if (lane < 8) *(unsigned*)(ws + WS_CNT + (b * 8 + lane) * 64) = 0u;
    if (lane == 8 && b < 32) *(unsigned*)(ws + WS_GCNT + b * 64) = 0u;
    if (lane >= 16 && lane < 24) *(unsigned*)(ws + WS_HCNT + b * 128 + (lane - 16) * 4) = 0u;
    __shared__ __attribute__((aligned(16))) char sink[256];
    const int kq = (b & 7) >> 1, l = lane < 40 ? lane : lane - 40;
    const u16* p = l < 12 ? wqkv + (l >> 2) * 1024 + 256 * kq + (l & 3) * 64 : (l < 26 ? wg + 896 * kq + (l - 12) * 64 : wu + 896 * kq + (l - 26) * 64);
    const int ld = l < 12 ? 3072 : 3584;
#pragma unroll
    for (int i = 0; i < PF_LN_ROWS / 32; ++i) touch((unsigned)(size_t)(LAS char*)sink, p + (i * 32 + (b >> 3)) * ld);
    return;
  }
  const int hf = (b & 1) * 128 + (b >> 1);
  const int mb = hf >> 5, mf = (hf >> 1) & 15, r = (hf & 1) * 8 + (threadIdx.x >> 6) * 2 + ((lane >> 2) & 1);
  const int row = mb * 256 + 32 * (mf >> 1) + 2 * r + (mf & 1), kl = lane >> 3, ch = lane & 3;
  const u16* xr = x + row * 1024 + kl * 32 + ch * 8;
  u32x4 raw[4];
#pragma unroll
  for (int i = 0; i < 4; ++i) raw[i] = *(const u32x4*)(xr + 256 * i);
  float v[32];
#pragma unroll
  for (int i = 0; i < 16; ++i) { v[2 * i] = __uint_as_float(raw[i >> 2][i & 3] << 16); v[2 * i + 1] = __uint_as_float(raw[i >> 2][i & 3] & 0xffff0000u); }
  float s = 0.f;
#pragma unroll
  for (int i = 0; i < 32; ++i) s += v[i];
  const float mean = hsum(s) * (1.0f / 1024.0f);
  float q = 0.f;
#pragma unroll
  for (int i = 0; i < 32; ++i) { const float d = v[i] - mean; q += d * d; }
  q = hsum(q);
  const float mb16 = rbf(mean), den = rbf(sqrtf(rbf(rbf(q * (1.0f / 1024.0f)) + 1e-5f)));
  const float inv = 1.0f / den;
  const auto rs = __builtin_amdgcn_make_buffer_rsrc(ws + WS_XN, 0, 4 << 20, 0x00020000);
  const unsigned o = ((mb * 32 + kl) * 16 + mf) * 1024 + r * 64 + ((ch ^ (2 * (r >> 3))) * 16);
#pragma unroll
  for (int i = 0; i < 4; ++i) {
    u32x4 t;
#pragma unroll
    for (int e = 0; e < 4; ++e) t[e] = pk2(rbf(v[8 * i + 2 * e] - mb16) * inv, rbf(v[8 * i + 2 * e + 1] - mb16) * inv);
    __builtin_amdgcn_raw_buffer_store_b128(t, rs, o + i * 8 * 16384, 0, H_AUX);
  }
  DG(0, 1);
}
namespace {
constexpr int G1_SA = 17 * 1024, G1_SB = 20 * 1024, G1_SLOT = G1_SA + G1_SB;
constexpr int T_SCR = 159488;
constexpr int T_BIAS = T_SCR + 256;
constexpr int G1_LDS = T_BIAS + 4096;
constexpr int G2_SA = 32768, G2_SB = 16384, G2_SLOT = G2_SA + G2_SB;
constexpr int T_SYNC = T_SCR - 64;
constexpr int T_KV = 2 * G2_SLOT, KV_BYTES = 7 * 1024 + 272 * 80;

template <bool MAX>
__device__ __forceinline__ float tred(float a, float b, float c, float d) {
  const auto r1 = __builtin_amdgcn_permlane16_swap(__float_as_uint(a), __float_as_uint(b), false, false);
  const auto r2 = __builtin_amdgcn_permlane16_swap(__float_as_uint(c), __float_as_uint(d), false, false);
  const float x0 = __uint_as_float(r1[0]), x1 = __uint_as_float(r1[1]), y0 = __uint_as_float(r2[0]), y1 = __uint_as_float(r2[1]);
  const float t = MAX ? fmx(x0, x1) : x0 + x1, u = MAX ? fmx(y0, y1) : y0 + y1;
  const auto r3 = __builtin_amdgcn_permlane32_swap(__float_as_uint(t), __float_as_uint(u), false, false);
  const float z0 = __uint_as_float(r3[0]), z1 = __uint_as_float(r3[1]);
  return MAX ? fmx(z0, z1) : z0 + z1;
}
__device__ __forceinline__ u32x2 glu4(f32x4 g, f32x4 u) {
  f32x4 e = g * -1.4426950408889634f;
#pragma unroll
  for (int i = 0; i < 4; ++i) e[i] = __builtin_amdgcn_exp2f(e[i]);
  e = e + 1.0f;
#pragma unroll
  for (int i = 0; i < 4; ++i) e[i] = __builtin_amdgcn_rcpf(e[i]);
  e = e * u;
  return (u32x2){pk2(e[0], e[1]), pk2(e[2], e[3])};
}
__device__ __forceinline__ u32x4 cat4(u32x2 a, u32x2 b) { return (u32x4){a[0], a[1], b[0], b[1]}; }

template <bool typeA>
__device__ __forceinline__ void body(const u16* __restrict__ x, u16* __restrict__ out, const u16* __restrict__ wqkv, const u16* __restrict__ wg,
                                     const u16* __restrict__ wu, const u16* __restrict__ wo, const u16* __restrict__ wd, char* __restrict__ ws) {
  extern __shared__ __attribute__((aligned(16))) char smem[];
  LAS char* lds = (LAS char*)smem;
  ST_DECL LT_DECL ST(0);
  const int tid = threadIdx.x;
  int lane = tid & 63;
  const int w = __builtin_amdgcn_readfirstlane(tid >> 6), g = w >> 2, wi = w & 3;
  const int b = blockIdx.x, xcd = b & 7, idx = b >> 3;
  const int mb = (xcd & 1) * 4 + (idx & 3), kq = xcd >> 1, ntl = idx >> 2;
  const int j = ntl & 3;
  int fr = lane & 15, fq = lane >> 4;
  const int fA = 896 * kq + 64 * j;
  const int f0 = 896 * kq + 256 + 160 * j, odd = j & 1;
  const int fa = f0 + 32 * odd, fb = fa + 64, fc = odd ? f0 : f0 + 128;

  asm volatile("" : "+s"(wqkv), "+s"(wg), "+s"(wu), "+s"(ws));
  const bool isq = typeA && wi < 3;
  const int ld = isq ? 3072 : 3584;
  const u16* q1 = isq ? wqkv + (wi * 1024 + 256 * kq + 64 * j) : ((!typeA && (wi & 2)) ? wu : wg) + (typeA ? fA : ((wi & 1) ? fb : fa));
  const char* c1 = g ? (const char*)q1 : ws + WS_XN + (size_t)(mb * 32 * 16384 + wi * 4096);
  const int s1 = g ? 8 * ld - 1024 : 0, s2 = g ? 32 * ld - 2048 : 0, s3 = g ? 40 * ld - 3072 : 0;
  const int p1 = g ? 64 * ld : 16384, p2 = g ? 16384 : 64 * 3584;
  const int d1 = (g ? G1_SA : 0) + wi * 4096, d2 = g ? 16384 + wi * 256 : G1_SA + 16384 + wi * 1024;
  const unsigned long long m2 = g ? 0xffffull : ~0ull;
  const int rr = lane >> 3, pc = lane & 7, fi = (pc >> 1) ^ ((rr >> 1) & 3), kr = (rr & 3) + 8 * (rr >> 2);
  const unsigned v1 = g ? kr * ld * 2 + fi * 32 + (pc & 1) * 16 : lane * 16;
  const unsigned lb = (unsigned)(size_t)lds;
  unsigned ns = lb;
  dma_first(ns + d1, v1, c1); dma_next<1024>(v1, c1 + s1); dma_next<2048>(v1, c1 + s2); dma_next<3072>(v1, c1 + s3);
  const char* c2;
  {
    const int hr = wi * 4 + ((lane >> 2) & 3);
    const unsigned oh = (14 + (hr >> 3)) * 1024 + (8 + (hr & 7)) * 64 + (((lane & 3) ^ (2 * (hr >> 3)) ^ 2) * 16);
    const unsigned ow = (kr + 16 * (wi >> 1) + 4 * (wi & 1)) * 7168 + (typeA ? fi * 32 : (fi >> 1) * 32) + (pc & 1) * 16;
    const char* be = g ? ws + WS_XN + (size_t)((mb > 0 ? mb - 1 : 0) * 32 * 16384) : (const char*)(typeA ? wu + fA : wg + fc);
    const char* bo = g ? be : (const char*)(wu + (typeA ? fA : fc));
    c2 = ((fi & 1) ? bo : be) + (g ? oh : ow);
  }
  auto adv = [&]() { c1 += p1; c2 += p2; ns = (ns == lb + 3 * G1_SLOT) ? lb : ns + G1_SLOT; };
  auto stage = [&]() {
    dma_first(ns + d1, v1, c1); dma_next<1024>(v1, c1 + s1); dma_next<2048>(v1, c1 + s2); dma_next<3072>(v1, c1 + s3);
    dma1m(ns + d2, c2, m2);
    adv();
  };
  dma1m(ns + d2, c2, m2);
  adv();
  stage();

  f32x4 acc[4][10];
  f32x4 hacc = {0.f, 0.f, 0.f, 0.f};
#pragma unroll
  for (int i = 0; i < 4; ++i)
#pragma unroll
    for (int n = 0; n < 10; ++n) { acc[i][n] = (f32x4){0.f, 0.f, 0.f, 0.f}; asm volatile("" : "+v"(acc[i][n])); }
  const int rl = 4 * (fq & 1) + (fr >> 2);
  unsigned hoffA = lb + 16384 + fr * 64 + ((fq ^ (2 * (fr >> 3))) * 16);
  unsigned aoff = hoffA - 16384 + wi * 4096;
  unsigned boff0 = lb + G1_SA + (fq >> 1) * 2048 + rl * 128 + 32 * ((2 * g) ^ ((rl >> 1) & 3)) + 8 * (lane & 3);
  unsigned boff1 = boff0 ^ 32;
  unsigned hoffB = (boff0 ^ (32 * (wi & 1))) + (1 + (wi >> 1)) * 4096;
  asm volatile("" : "+v"(aoff), "+v"(hoffA), "+v"(boff0), "+v"(boff1), "+v"(hoffB), "+v"(hacc));
  {
    const int lo = fr + (wi == 0 ? -7 : (wi == 2 ? 1 : 9));
    const int hi = wi == 0 ? fr + 1 : ((wi >= 2 && mb == 0) ? 0 : (wi == 2 ? 8 : 16));
    f32x4 bv;
#pragma unroll
    for (int e = 0; e < 4; ++e) bv[e] = (4 * fq + e >= lo && 4 * fq + e < hi) ? 0.f : -3.0e38f;
    *(LAS f32x4*)(lds + T_BIAS + wi * 1024 + lane * 16) = bv;
    *(LAS unsigned*)(lds + T_SYNC + (lane & 1) * 4) = 0u;
  }
  ST(1);
  WAIT_V_IF0(0, g);
  BAR();
  ST(2);
  if (g == 1) { WAIT_V(0); BAR(); }

  auto step = [&](auto halo_c, auto last_c, int q) {
    constexpr bool HALO = decltype(halo_c)::value, LAST = decltype(last_c)::value;
    bf16x8 a[2][4], wn[4][2];
    asm volatile("" ::: "memory");
    auto rdq = [&](int e, int h) {
      if (e >= (HALO ? 12 : 10)) return;
      const int kk = h >> 1, t = h & 1;
      bf16x8& d = wn[e & 3][kk];
      if (e == 11) { if (t == 0) d = *(const LAS bf16x8*)(lp(hoffA) + kk * G1_SLOT); return; }
      const int n = e < 2 ? 8 + e : e - 2;
      const bf16x4 v = trd(lp(e == 10 ? hoffB : ((n & 1) ? boff1 : boff0)) + (e == 10 ? 0 : (n >> 1) * 4096) + t * 1024 + kk * G1_SLOT);
      if (t == 0) { d[0] = v[0]; d[1] = v[1]; d[2] = v[2]; d[3] = v[3]; } else { d[4] = v[0]; d[5] = v[1]; d[6] = v[2]; d[7] = v[3]; }
    };
#define RA1(kk, i) a[kk][i] = *(const LAS bf16x8*)(lp(aoff) + (kk) * G1_SLOT + (i) * 1024);
    const int dlt = (q & 1) ? -2 * G1_SLOT : 2 * G1_SLOT;
    if (!LAST) {
#pragma unroll
      for (int kk = 0; kk < 2; ++kk) {
        dma_first(ns + d1, v1, c1); RA1(kk, 0) rdq(kk, 0);
        dma_next<1024>(v1, c1 + s1); RA1(kk, 1) rdq(kk, 1);
        dma_next<2048>(v1, c1 + s2); RA1(kk, 2) rdq(kk, 2);
        dma_next<3072>(v1, c1 + s3); RA1(kk, 3) rdq(kk, 3);
        dma1m(ns + d2, c2, m2);
        adv();
      }
    } else
    {
#pragma unroll
      for (int kk = 0; kk < 2; ++kk)
#pragma unroll
        for (int i = 0; i < 4; ++i) RA1(kk, i)
#pragma unroll
      for (int h = 0; h < 8; ++h) rdq(h >> 2, h & 3);
    }
    aoff += dlt;
    SCHED();
    if (!LAST) {
    } else {
      int t = w * 64 + __builtin_amdgcn_mbcnt_hi(~0u, __builtin_amdgcn_mbcnt_lo(~0u, 0u));
      t = t < 288 ? t : t - 288;
      const int r2 = (t >> 4) * 64 + (xcd & 1) * 32 + idx;
      touch(lb + T_SCR, (r2 < 256 ? wo + (256 * kq + r2) * 1024 : wd + (896 * kq + r2 - 256) * 1024) + (t & 15) * 64);
    }
    WAIT_L0();
    SCHED();
    BAR();
    SCHED();
    PRIO_M();
    auto fin = [&]() { SCHED(); PRIO_T(); if (!LAST) { if (PFD) WAIT_V(1); else WAIT_V(0); } BAR(); SCHED(); };
#pragma unroll
    for (int e = 0; e < 10; ++e) {
      const int n = e < 2 ? 8 + e : e - 2;
#pragma unroll
      for (int i = 0; i < 4; ++i) {
        acc[i][n] = mfma(wn[e & 3][0], a[0][i], acc[i][n]);
        SCHED();
        if (!HALO && e == 9 && i == 3) fin();
        SCHED();
        acc[i][n] = mfma(wn[e & 3][1], a[1][i], acc[i][n]);
        SCHED();
        if (e == 0) { rdq(2 + (i >> 1), 2 * (i & 1)); rdq(2 + (i >> 1), 2 * (i & 1) + 1); } else rdq(e + 3, i);
        if (i == 3 && e < 9) { asm volatile("" :: "v"(wn[(e + 1) & 3][0]), "v"(wn[(e + 1) & 3][1])); }
        SCHED();
      }
    }
    if (HALO) {
      hacc = mfma(wn[2][0], wn[3][0], hacc);
      WAIT_L0();
      fin();
      hacc = mfma(wn[2][1], wn[3][1], hacc);
    }
    hoffA += dlt; boff0 += dlt; boff1 += dlt; hoffB += dlt;
    PRIO_E();
    SCHED();
  };
  const int nt = ntl;
  const int kba = 8 + ((fa - 896 * kq) >> 5);
  const int own0 = typeA ? j : (kba >> 1), own1 = typeA ? 4 + j : (kba >> 1) + 1;
  const char* const hbase = ws + WS_H + (size_t)(mb * 4 + kq) * 36 * 16384;
  auto brow = [&](int stp_, int w_) {
    return (const char*)(stp_ < 4 ? wo + (size_t)(256 * kq) * 1024 : wd + (size_t)(896 * kq) * 1024 - 256 * 1024) + ((stp_ * 64 + 8 * w_) * 1024 + nt * 128) * 2;
  };
  unsigned va, vb;
  auto stage1 = [&](auto halo_c) {
    constexpr bool TA = decltype(halo_c)::value;
    typedef std::integral_constant<bool, TA || !HALO_SKIP> HC;
#pragma clang loop unroll(disable)
    for (int q = 0; q < 15; ++q) step(HC{}, std::false_type{}, q);
    step(HC{}, std::true_type{}, 15);
    ST(3);
    asm volatile("" ::: "memory");
    if (g == 0) __builtin_amdgcn_s_setprio(2); else __builtin_amdgcn_s_setprio(EPI_PRIO);
    lane = __builtin_amdgcn_mbcnt_hi(~0u, __builtin_amdgcn_mbcnt_lo(~0u, 0u));
    asm volatile("" : "+v"(lane));
    fr = lane & 15; fq = lane >> 4;
    va = lane * 16;
    vb = (lane >> 3) * 2048 + ((((lane & 7) >> 1) ^ ((lane >> 4) & 3)) * 32) + (lane & 1) * 16;
    if (g == 1) {
#pragma unroll
      for (int u = 0; u < 4; ++u) {
        const int w_ = wi * 2 + (u & 1);
        const char* p0 = brow((u >> 1) ? own1 : own0, w_);
        dma_first(lb + (u >> 1) * G2_SLOT + G2_SA + w_ * 2048, vb, p0); dma_next<1024>(vb, p0 - 896);
      }
    }
    const auto hrs = __builtin_amdgcn_make_buffer_rsrc((void*)hbase, 0, 36 * 16384, 0x00020000);
    const unsigned hv = wi * 4096 + lane * 16;
    auto put = [&](u32x4 v, int s, int kb, int i) {
      __builtin_amdgcn_raw_buffer_store_b128(v, hrs, hv + kb * 16384 + i * 1024, 0, H_AUX);
      *(LAS u32x4*)(lds + s * G2_SLOT + g * 16384 + ((wi * 4096 + kq * 1024 + lane * 16) ^ (i * 1024))) = v;
    };
#define GLU(i, a, b) glu4(acc[i][a], acc[i][b])
    if (!TA) {
      const int kbc = 8 + ((fc - 896 * kq) >> 5);
#pragma unroll
      for (int i = 0; i < 4; ++i) {
        __builtin_amdgcn_raw_buffer_store_b64(GLU(i, 8, 9), hrs, hv + kbc * 16384 + g * 8 + i * 1024, 0, H_AUX);
        put(cat4(GLU(i, 0, 4), GLU(i, 1, 5)), 0, kba + g, i);
        put(cat4(GLU(i, 2, 6), GLU(i, 3, 7)), 1, kba + 2 + g, i);
        if (i == FB) { SCHED(); BAR_IF0(g); SCHED(); }
      }
    } else {
      auto glu_part = [&](int i0, int i1) {
#pragma unroll
        for (int i = i0; i < i1; ++i) put(cat4(GLU(i, 6, 8), GLU(i, 7, 9)), 1, 8 + 2 * j + g, i);
      };
      if (g == 0) { glu_part(0, FA); SCHED(); BAR(); __builtin_amdgcn_s_setprio(0); asm volatile("" ::: "memory"); }
    LAS char* kim = lds + T_KV + g * KV_BYTES;
    LAS char* vim = kim + 7 * 1024;
    bf16x8 kf[4], qf[4];
#pragma unroll
    for (int i = 0; i < 4; ++i) {
      kf[i] = pack8(acc[i][2], acc[i][3]);
      *(LAS bf16x8*)(vim + ((wi * 4 + i) * 16 + fr) * 80 + fq * 16) = pack8(acc[i][4], acc[i][5]);
    }
    if (wi < 3) {
      *(LAS bf16x8*)(kim + wi * 2048 + lane * 16) = kf[2];
      *(LAS bf16x8*)(kim + wi * 2048 + 1024 + lane * 16) = kf[3];
    }
    {
      LAS char* hp = (wi < 2) ? (kim + 6 * 1024 + lane * 16 + wi * 8) : (vim + (256 + fr) * 80 + fq * 16 + (wi - 2) * 8);
      *(LAS u32x2*)hp = (u32x2){pk2(hacc[0], hacc[1]), pk2(hacc[2], hacc[3])};
    }
    WAIT_L0();
    BAR();
    asm volatile("" ::: "memory");
    const bool halo = wi == 0;
    const f32x4 bc = *(const LAS f32x4*)(lds + T_BIAS + lane * 16), bp = *(const LAS f32x4*)(lds + T_BIAS + 1024 + lane * 16);
    const f32x4 bp0 = *(const LAS f32x4*)(lds + T_BIAS + (halo ? 2048 : 1024) + lane * 16);
    const f32x4 bp1 = *(const LAS f32x4*)(lds + T_BIAS + (halo ? 3072 : 1024) + lane * 16);
    const int pf0 = halo ? 16 : wi * 4 - 2, pf1 = halo ? 16 : wi * 4 - 1;
    const bf16x8 kp0 = *(const LAS bf16x8*)(kim + (halo ? 6 : 2 * wi - 2) * 1024 + lane * 16);
    const bf16x8 kp1 = *(const LAS bf16x8*)(kim + (halo ? 6 : 2 * wi - 1) * 1024 + lane * 16);
    const LAS char* vr = vim + (4 * fq + (fr >> 2)) * 80 + (lane & 3) * 16;
    bf16x4 vt[6][2];
#pragma unroll
    for (int d = 0; d < 2; ++d) {
      vt[0][d] = trd(vr + pf0 * 1280 + d * 8); vt[1][d] = trd(vr + pf1 * 1280 + d * 8);
#pragma unroll
      for (int i = 0; i < 4; ++i) vt[2 + i][d] = trd(vr + (wi * 4 + i) * 1280 + d * 8);
    }
    f32x4 sp[4], sc[4];
#pragma unroll
    for (int i = 0; i < 4; ++i) {
      qf[i] = pack8(acc[i][0], acc[i][1]);
      sc[i] = mfma(kf[i], qf[i], bc);
    }
    sp[2] = mfma(kf[0], qf[2], bp); sp[3] = mfma(kf[1], qf[3], bp);
    sp[0] = mfma(kp0, qf[0], bp0); sp[1] = mfma(kp1, qf[1], bp1);
    const float sl = 0.17677669529663687f * 1.4426950408889634f;
    const f32x4 z4 = {0.f, 0.f, 0.f, 0.f};
    float m[4];
#pragma unroll
    for (int i = 0; i < 4; ++i)
      m[i] = fmx(fmx(fmx(fmx(sp[i][0], sp[i][1]), sp[i][2]), fmx(fmx(sp[i][3], sc[i][0]), sc[i][1])), fmx(sc[i][2], sc[i][3]));
    {
      const unsigned t = __float_as_uint(tred<true>(m[0], m[1], m[2], m[3]));
      const auto u = __builtin_amdgcn_permlane32_swap(t, t, false, false);
      const auto u0 = __builtin_amdgcn_permlane16_swap(u[0], u[0], false, false), u1 = __builtin_amdgcn_permlane16_swap(u[1], u[1], false, false);
      m[0] = __uint_as_float(u0[0]); m[1] = __uint_as_float(u0[1]); m[2] = __uint_as_float(u1[0]); m[3] = __uint_as_float(u1[1]);
    }
    const u32x4 one4 = {0x3f803f80u, 0x3f803f80u, 0x3f803f80u, 0x3f803f80u};
#pragma unroll
    for (int i = 0; i < 4; ++i) {
      const float mm = -sl * m[i];
#pragma unroll
      for (int e = 0; e < 4; ++e) {
        sp[i][e] = __builtin_amdgcn_exp2f(__builtin_fmaf(sp[i][e], sl, mm));
        sc[i][e] = __builtin_amdgcn_exp2f(__builtin_fmaf(sc[i][e], sl, mm));
      }
      const bf16x8 pfr = pack8(sp[i], sc[i]);
      const f32x4 sm = mfma(__builtin_bit_cast(bf16x8, one4), pfr, z4);
      const f32x4 o0 = mfma(cat(vt[i][0], vt[2 + i][0]), pfr, z4);
      const f32x4 o1 = mfma(cat(vt[i][1], vt[2 + i][1]), pfr, z4);
      const float inv = __builtin_amdgcn_rcpf(sm[0]);
      put(__builtin_bit_cast(u32x4, pack8(o0 * inv, o1 * inv)), 0, 2 * j + g, i);
    }
      glu_part(FA, 4);
      if (g == 1) glu_part(0, FA);
    }
  };
  stage1(std::integral_constant<bool, typeA>{});
  ST(4);

  const int lag = g ^ 1;
  GAS unsigned* hflag = (GAS unsigned*)(ws + WS_HCNT) + (mb * 4 + kq) * 256;
  const unsigned fo = (nt * 32 + (lane & 7)) * 4;
  GAS unsigned* gcnt = (GAS unsigned*)(ws + WS_GCNT) + (mb * 4 + kq) * 16;
  const int e0 = kq * 1024, e1 = ((1 ^ kq) - 1) * 1024, e2 = ((2 ^ kq) - 2) * 1024, e3 = ((3 ^ kq) - 3) * 1024;
  auto nxt = [&](int s) { ++s; if (s == own0) ++s; if (s == own1) ++s; if (s == 18) s = (own0 == 0) ? 1 : 0; return s; };
  auto wait_cnt = [&](unsigned seen, GAS unsigned* c, unsigned need) {
    if ((unsigned)__builtin_amdgcn_readfirstlane(seen) < need) {
      asm volatile("" ::: "memory");
      unsigned spins = 0;
      while ((unsigned)__builtin_amdgcn_readfirstlane(__hip_atomic_load(c, __ATOMIC_RELAXED, __HIP_MEMORY_SCOPE_AGENT)) < need)
        if (++spins > (1u << 22)) break;
    }
    asm volatile("" ::: "memory");
  };
  auto wait_flags = [&](unsigned seen, unsigned long long need) {
    if ((__builtin_amdgcn_ballot_w64(seen != 0u) & need) != need) {
      asm volatile("" ::: "memory");
      unsigned spins = 0;
      while ((__builtin_amdgcn_ballot_w64(__hip_atomic_load(hflag + (fo >> 2), __ATOMIC_RELAXED, __HIP_MEMORY_SCOPE_AGENT) != 0u) & need) != need) {
        __builtin_amdgcn_s_sleep(1);
        if (++spins > (1u << 22)) break;
      }
    }
    asm volatile("" ::: "memory");
  };
  int stp = nxt(7);
  const int mf = wi * 4 + kq;
  const unsigned orow = ((mb * 256 + 32 * (mf >> 1) + 2 * fr + (mf & 1)) * 1024 + nt * 128 + g * 64 + 16 * (fq & 1) + 8 * (fq >> 1)) * 2;
  u32x4 xr[2] = {{0u, 0u, 0u, 0u}, {0u, 0u, 0u, 0u}};
  const int rl2 = 4 * (fq & 1) + (fr >> 2);
  unsigned ao = lb + wi * 4096 + lane * 16;
  unsigned bo = lb + G2_SA + (fq >> 1) * 2048 + g * 1024 + rl2 * 128 + 32 * ((rl2 >> 1) & 3) + 8 * (lane & 3);
  f32x4 ac2[4][4];
#pragma unroll
  for (int i = 0; i < 4; ++i)
#pragma unroll
    for (int n = 0; n < 4; ++n) { ac2[i][n] = (f32x4){0.f, 0.f, 0.f, 0.f}; asm volatile("" : "+v"(ac2[i][n])); }
  ns = lb + 2 * G2_SLOT;
  const char* ca = hbase + stp * 32768 + w * 4096;
  const char* cb = brow(stp, w);
  int sl2 = 0;
  WAIT_V_IF0(0, lag);
  WAIT_L0();
  BAR();
  ST(5);
  ST_SAVE(); ST(0); ST(1);
  unsigned seen = 0;
#define PEEK(c, o) asm volatile("global_load_dword %0, %1, %2 sc1" : "+v"(seen) : "v"(o), "s"(c) : "memory")
  if (lag) BAR();

  auto phase2 = [&](auto mode, int q) {
    constexpr int MODE = decltype(mode)::value;
    bf16x8 a[2][4], bfr[2][4];
#define RA(kk, i) a[kk][i] = *(const LAS bf16x8*)(lp(ao) + (kk) * 16384 + (i) * 1024);
#define RB(kk, n) bfr[kk][n] = cat(trd(lp(bo ^ ((n) * 32)) + (kk) * 8192), trd(lp(bo ^ ((n) * 32)) + (kk) * 8192 + 4096));
    int dlt;
    asm volatile("" ::: "memory");
    if (MODE == 1 && q == 1 && w == 4) PEEK(hflag, fo);
    if (MODE == 2 && q == 16)
      asm volatile("global_load_dwordx4 %0, %2, %3\n\tglobal_load_dwordx4 %1, %2, %3 offset:64" : "+v"(xr[0]), "+v"(xr[1]) : "v"(orow), "s"(x) : "memory");
    RA(0, 0) RA(0, 1)
    if (MODE == 0) dmc_first(ns + w * 4096, va, ca + e0);
    RA(0, 2) RA(0, 3) RB(0, 0)
    if (MODE == 0) dmc_next<1024>(va, ca + e1);
    RB(0, 1) RB(0, 2) RB(0, 3)
    if (MODE == 0) dmc_next<2048>(va, ca + e2);
    RA(1, 0) RA(1, 1) RA(1, 2)
    if (MODE == 0) dmc_next<3072>(va, ca + e3);
    RA(1, 3) RB(1, 0)
    if (MODE < 2) dma_first(ns + G2_SA + w * 2048, vb, cb);
    RB(1, 1)
    if (MODE < 2) dma_next<1024>(vb, cb - 896);
    sl2 = (sl2 == 2) ? 0 : sl2 + 1; dlt = (sl2 == 0) ? -2 * G2_SLOT : G2_SLOT;
    if (MODE < 2) { stp = nxt(stp); ca = hbase + stp * 32768 + w * 4096; cb = brow(stp, w); ns = (ns == lb + 2 * G2_SLOT) ? lb : ns + G2_SLOT; }
    asm volatile("" : "+s"(ca), "+s"(cb), "+s"(ns), "+s"(sl2), "+s"(dlt), "+s"(stp) :: "memory");
    RB(1, 2) RB(1, 3)
#undef RA
#undef RB
    ao += dlt; bo += dlt;
    SCHED();
    asm volatile("" : "+v"(bo), "+v"(ao));
    if (MODE == 0) WAIT_V_IF0(6, lag ^ 1); else if (MODE == 2 && q == 16) WAIT_V_IF0(2, lag ^ 1);
    if (MODE == 1 && q == 1) {
      asm volatile("s_waitcnt vmcnt(2)" : "+v"(seen) :: "memory");
      ET(2);
      if (!lag) {
        volatile LAS unsigned* go = (volatile LAS unsigned*)(lds + T_SYNC + 4);
        if (w == 4) { wait_flags(seen, 0xf0ull); *go = 1u; }
        else { unsigned spins = 0; while (__builtin_amdgcn_readfirstlane(*go) == 0u) { __builtin_amdgcn_s_sleep(1); if (++spins > (1u << 24)) break; } }
        asm volatile("" ::: "memory");
      }
      ET(3);
      const int s2 = nxt(7), s = lag ? nxt(s2) : s2;
      const char* p = hbase + s * 32768 + wi * 8192;
      const unsigned d = lb + (lag ? 0 : 2 * G2_SLOT) + wi * 8192;
      dmc_first(d, va, p + e0); dmc_next<1024>(va, p + e1); dmc_next<2048>(va, p + e2); dmc_next<3072>(va, p + e3);
      dmc_first(d + 4096, va, p + 4096 + e0); dmc_next<1024>(va, p + 4096 + e1); dmc_next<2048>(va, p + 4096 + e2); dmc_next<3072>(va, p + 4096 + e3);
      ET(4);
    }
    WAIT_L0();
    SCHED();
    BAR();
    SCHED();
    PRIO_M();
#pragma unroll
    for (int n = 0; n < 4; ++n)
#pragma unroll
      for (int i = 0; i < 4; ++i) {
        ac2[i][n] = mfma(bfr[0][n], a[0][i], ac2[i][n]);
        SCHED();
        if (n == 3 && i == 3) {
          PRIO_T();
          if (MODE == 0) WAIT_V_IF0(6, lag); else if (MODE == 2 && q == 16) WAIT_V_IF0(2, lag);
          if (MODE == 1 && q == 1) WAIT_V_IF0(0, lag);
          if (q < 17 || !lag) BAR();
          SCHED();
        }
        ac2[i][n] = mfma(bfr[1][n], a[1][i], ac2[i][n]);
        SCHED();
        if (MODE == 1 && q == 0 && n == 1 && i == 3 && lag) {
          WAIT_V(0);
          unsigned old = 0;
          if (lane == 0) old = __hip_atomic_fetch_add((LAS unsigned*)(lds + T_SYNC), 1u, __ATOMIC_RELAXED, __HIP_MEMORY_SCOPE_WORKGROUP);
          if (__builtin_amdgcn_readfirstlane(old) == 3u && lane < 8)
            __hip_atomic_store(hflag + lane * 32 + nt, 1u, __ATOMIC_RELAXED, __HIP_MEMORY_SCOPE_AGENT);
          SCHED();
        }
      }
    PRIO_E();
    SCHED();
  };
  typedef std::integral_constant<int, 0> M0; typedef std::integral_constant<int, 1> M1; typedef std::integral_constant<int, 2> M2;
  phase2(M1{}, 0);
  ET(1);
  phase2(M1{}, 1);
  ET(5);
  ST(2);
#pragma clang loop unroll(disable)
  for (int q = 2; q < 6; ++q) phase2(M0{}, q);
  PEEK(hflag, fo);
  phase2(M0{}, 6);
  asm volatile("" : "+v"(seen));
  wait_flags(seen, 0x0full);
#pragma clang loop unroll(disable)
  for (int q = 7; q < 13; ++q) phase2(M0{}, q);
  if (w == 4 && lane == 0) __hip_atomic_fetch_add(gcnt, 1u, __ATOMIC_RELAXED, __HIP_MEMORY_SCOPE_AGENT);
#pragma clang loop unroll(disable)
  for (int q = 13; q < 16; ++q) phase2(M0{}, q);
  phase2(M2{}, 16);
  PEEK(gcnt, 0u);
  phase2(M2{}, 17);
  asm volatile("s_waitcnt vmcnt(0)" : "+v"(seen) :: "memory");
  wait_cnt(seen, gcnt, 8u);
  ST(3);
  const int tile = mb * 8 + nt;
  const auto rs = __builtin_amdgcn_make_buffer_rsrc(ws + WS_H + (size_t)mb * 4 * 36 * 16384, 0, 4 * 36 * 16384, 0x00020000);
  auto xo = [&](int src, int i) { const int u = 15 + nt * 3 + i; return (unsigned)((src * 36 + (u >= 36 ? u - 36 : u)) * 16384 + w * 2048) + lane * 16; };
#pragma unroll
  for (int i = 1; i < 4; ++i) {
    const unsigned o = xo(kq, i);
    __builtin_amdgcn_raw_buffer_store_b128(__builtin_bit_cast(u32x4, pack8(ac2[i][0], ac2[i][1])), rs, o, 0, 16);
    __builtin_amdgcn_raw_buffer_store_b128(__builtin_bit_cast(u32x4, pack8(ac2[i][2], ac2[i][3])), rs, o + 1024, 0, 16);
  }
  WAIT_V(0);
  ST(4);
  asm volatile("" : "+v"(xr[0]), "+v"(xr[1]));
  GAS unsigned* cnt = (GAS unsigned*)(ws + WS_CNT) + (tile * 32 + w) * 16;
  if (lane >= 1 && lane < 4) __hip_atomic_fetch_add(cnt + (kq ^ lane) * 128, 1u, __ATOMIC_RELAXED, __HIP_MEMORY_SCOPE_AGENT);
  {
    unsigned spins = 0;
    while ((unsigned)__builtin_amdgcn_readfirstlane(__hip_atomic_load(cnt + kq * 128, __ATOMIC_RELAXED, __HIP_MEMORY_SCOPE_AGENT)) < 3u) {
      __builtin_amdgcn_s_sleep(2);
      if (++spins > (1u << 22)) break;
    }
  }
  asm volatile("" ::: "memory");
  ST(5);
  u32x4 p[3][2];
#pragma unroll
  for (int i = 1; i < 4; ++i) {
    const unsigned o = xo(kq ^ i, i);
    p[i - 1][0] = __builtin_amdgcn_raw_buffer_load_b128(rs, o, 0, 16);
    p[i - 1][1] = __builtin_amdgcn_raw_buffer_load_b128(rs, o + 1024, 0, 16);
  }
  const auto ors = __builtin_amdgcn_make_buffer_rsrc(out, 0, 4 << 20, 0x00020000);
#pragma unroll
  for (int h = 0; h < 2; ++h) {
    const auto x0 = __builtin_amdgcn_permlane16_swap(xr[h][0], xr[h][2], false, false), x1 = __builtin_amdgcn_permlane16_swap(xr[h][1], xr[h][3], false, false);
    const u32x4 xv = {x0[0], x1[0], x0[1], x1[1]};
    unsigned r[4];
#pragma unroll
    for (int d = 0; d < 4; ++d) {
      float lo = ac2[0][2 * h + (d >> 1)][2 * (d & 1)] + __uint_as_float(xv[d] << 16), hi = ac2[0][2 * h + (d >> 1)][2 * (d & 1) + 1] + __uint_as_float(xv[d] & 0xffff0000u);
#pragma unroll
      for (int i = 0; i < 3; ++i) { lo += __uint_as_float(p[i][h][d] << 16); hi += __uint_as_float(p[i][h][d] & 0xffff0000u); }
      r[d] = pk2(lo, hi);
    }
    const auto y0 = __builtin_amdgcn_permlane16_swap(r[0], r[2], false, false), y1 = __builtin_amdgcn_permlane16_swap(r[1], r[3], false, false);
    __builtin_amdgcn_raw_buffer_store_b128((u32x4){y0[0], y1[0], y0[1], y1[1]}, ors, orow + 64 * h, 0, OUT_AUX);
  }
  ST(6);
  ST(7);
  ST_OUT(1, 8);
  ST_REST(); ST_OUT(0, 6);
}

}

extern "C" __global__ __launch_bounds__(512, 2) void psum_g1_kernel(const u16* __restrict__ x, u16* __restrict__ out, const u16* __restrict__ wqkv,
                                                                    const u16* __restrict__ wg, const u16* __restrict__ wu,
                                                                    const u16* __restrict__ wo, const u16* __restrict__ wd, char* __restrict__ ws) {
  if (blockIdx.x < 128) body<true>(x, out, wqkv, wg, wu, wo, wd, ws);
  else body<false>(x, out, wqkv, wg, wu, wo, wd, ws);
}

extern "C" void kernel_launch(void* const* d_in, const int* in_sizes, int n_in, void* d_out, int out_size, void* d_ws,
                              size_t ws_size, hipStream_t stream) {
  const u16* x = (const u16*)d_in[0];
  const u16* wqkv = (const u16*)d_in[1];
  const u16* wo = (const u16*)d_in[2];
  const u16* wg = (const u16*)d_in[4];
  const u16* wu = (const u16*)d_in[5];
  const u16* wd = (const u16*)d_in[6];
  char* ws = (char*)d_ws;
  (void)hipFuncSetAttribute((const void*)psum_g1_kernel, hipFuncAttributeMaxDynamicSharedMemorySize, G1_LDS);
  psum_ln_kernel<<<256, 320, 0, stream>>>(x, wqkv, wg, wu, ws);
  psum_g1_kernel<<<256, 512, G1_LDS, stream>>>(x, (u16*)d_out, wqkv, wg, wu, wo, wd, ws);
}
```

```cpp
#define DIAG 10
#include <hip/hip_runtime.h>
#include <hip/hip_bf16.h>
#include <type_traits>
#define EARLY 1
#define HALO_SKIP 1
#define PF_W2 1
#define PF_LOOP 0
#define PF_R0 192
#define PF_PER 2
#define PF_LN_ROWS 96
#define PRIO_L 0
#define PRIO_M() __builtin_amdgcn_s_setprio(1)
#define PRIO_T() __builtin_amdgcn_s_setprio(3)
#define PRIO_E() __builtin_amdgcn_s_setprio(0)
#define PF_CODE 0
#define PRECLEAN 0
#define OUT_AUX 16
#define G2ORD 0
#define EARLY2 1
#define CHAIN 1
#define PF_X 0
#define XH 1
#define ACQ 0
#define EPI_PRIO 1
#define LORD 1
#define RPOS 1
#define FA 4
#define FB 0
#define PFD 0
#define H_AUX 16

namespace {

typedef unsigned short u16;
typedef short bf16x8 __attribute__((ext_vector_type(8)));
typedef short bf16x4 __attribute__((ext_vector_type(4)));
typedef float f32x4 __attribute__((ext_vector_type(4)));
typedef float f32x2 __attribute__((ext_vector_type(2)));
typedef unsigned u32x4 __attribute__((ext_vector_type(4)));
typedef unsigned u32x2 __attribute__((ext_vector_type(2)));
typedef __bf16 bf2 __attribute__((ext_vector_type(2)));
#define LAS __attribute__((address_space(3)))
#define GAS __attribute__((address_space(1)))

#define WAIT_V(n) asm volatile("s_waitcnt vmcnt(" #n ")" ::: "memory")
#define WAIT_V_IF0(n, c) asm volatile("s_cmp_lg_u32 %0, 0\n\ts_cbranch_scc1 1f\n\ts_waitcnt vmcnt(" #n ")\n1:" :: "s"(c) : "memory", "scc")
#define BAR_IF0(c) asm volatile("s_cmp_lg_u32 %0, 0\n\ts_cbranch_scc1 1f\n\ts_barrier\n\ts_setprio 0\n1:" :: "s"(c) : "memory", "scc")
#define WAIT_L0() do { asm volatile("" ::: "memory"); __builtin_amdgcn_s_waitcnt(0xC07F); asm volatile("" ::: "memory"); } while (0)
#define BAR() __builtin_amdgcn_s_barrier()
#define SCHED() __builtin_amdgcn_sched_barrier(0)

__device__ __forceinline__ float b2f(u16 v) { return __uint_as_float(((unsigned)v) << 16); }
__device__ __forceinline__ unsigned pk2(float lo, float hi) {
  bf2 r = {(__bf16)lo, (__bf16)hi};
  return __builtin_bit_cast(unsigned, r);
}
__device__ __forceinline__ float rbf(float f) { return (float)(__bf16)f; }
__device__ __forceinline__ bf16x8 pack8(f32x4 a, f32x4 b) {
  u32x4 r = {pk2(a[0], a[1]), pk2(a[2], a[3]), pk2(b[0], b[1]), pk2(b[2], b[3])};
  return __builtin_bit_cast(bf16x8, r);
}
__device__ __forceinline__ bf16x4 trd(LAS const char* p) {
  return __builtin_amdgcn_ds_read_tr16_b64_v4i16((LAS bf16x4*)p);
}
__device__ __forceinline__ bf16x8 cat(bf16x4 a, bf16x4 b) { return __builtin_shufflevector(a, b, 0, 1, 2, 3, 4, 5, 6, 7); }
__device__ __forceinline__ f32x4 mfma(bf16x8 a, bf16x8 b, f32x4 c) {
  return __builtin_amdgcn_mfma_f32_16x16x32_bf16(a, b, c, 0, 0, 0);
}
__device__ __forceinline__ void dma_first(unsigned lds_dst, unsigned voff, const char* b0) {
  asm volatile("s_mov_b32 m0, %0\n\ts_nop 0\n\tglobal_load_lds_dwordx4 %1, %2" :: "s"(lds_dst), "v"(voff), "s"(b0) : "memory");
}
template <int OFF>
__device__ __forceinline__ void dma_next(unsigned voff, const char* b) {
  asm volatile("global_load_lds_dwordx4 %0, %1 offset:%2" :: "v"(voff), "s"(b), "i"(OFF) : "memory");
}
__device__ __forceinline__ void dmc_first(unsigned lds_dst, unsigned voff, const char* b0) {
  asm volatile("s_mov_b32 m0, %0\n\ts_nop 0\n\tglobal_load_lds_dwordx4 %1, %2 sc1" :: "s"(lds_dst), "v"(voff), "s"(b0) : "memory");
}
template <int OFF>
__device__ __forceinline__ void dmc_next(unsigned voff, const char* b) {
  asm volatile("global_load_lds_dwordx4 %0, %1 offset:%2 sc1" :: "v"(voff), "s"(b), "i"(OFF) : "memory");
}
__device__ __forceinline__ void dma1m(unsigned lds_dst, const char* p, unsigned long long mask) {
  asm volatile("s_mov_b32 m0, %0\n\ts_mov_b64 exec, %2\n\t"
               "global_load_lds_dwordx4 %1, off\n\t"
               "s_mov_b64 exec, -1"
               :: "s"(lds_dst), "v"(p), "s"(mask) : "memory");
}
__device__ __forceinline__ void touch(unsigned lds_dst, const void* p) {
  asm volatile("s_mov_b32 m0, %0\n\ts_nop 0\n\tglobal_load_lds_dword %1, off" :: "s"(lds_dst), "v"(p) : "memory");
}
#define CODE_ADDR(dst, sym) do { unsigned lo_, hi_; asm volatile("s_getpc_b64 s[90:91]\n\ts_add_u32 %0, s90, " #sym "@rel32@lo+4\n\ts_addc_u32 %1, s91, " #sym "@rel32@hi+12" \
    : "=s"(lo_), "=s"(hi_) :: "s90", "s91", "scc"); dst = (const char*)(((unsigned long long)hi_ << 32) | lo_); } while (0)
constexpr int LN_LINES = 16, G1_LINES = 80, G2_LINES = 30;
__device__ __forceinline__ LAS char* lp(unsigned a) { return (LAS char*)(size_t)a; }
__device__ __forceinline__ float fmx(float a, float b) { return __builtin_elementwise_maximum(a, b); }
__device__ __forceinline__ float xmax(float v) {
  auto a = __builtin_amdgcn_permlane16_swap(__float_as_uint(v), __float_as_uint(v), false, false);
  v = fmaxf(__uint_as_float(a[0]), __uint_as_float(a[1]));
  auto b = __builtin_amdgcn_permlane32_swap(__float_as_uint(v), __float_as_uint(v), false, false);
  return fmaxf(__uint_as_float(b[0]), __uint_as_float(b[1]));
}
__device__ __forceinline__ float xsum(float v) {
  auto a = __builtin_amdgcn_permlane16_swap(__float_as_uint(v), __float_as_uint(v), false, false);
  v = __uint_as_float(a[0]) + __uint_as_float(a[1]);
  auto b = __builtin_amdgcn_permlane32_swap(__float_as_uint(v), __float_as_uint(v), false, false);
  return __uint_as_float(b[0]) + __uint_as_float(b[1]);
}

template <int CTRL>
__device__ __forceinline__ float dpp_add(float v) {
  return v + __uint_as_float(__builtin_amdgcn_update_dpp(0u, __float_as_uint(v), CTRL, 0xf, 0xf, true));
}
__device__ __forceinline__ float hsum(float v) {
  v = dpp_add<0xB1>(v);
  v = dpp_add<0x4E>(v);
  v = dpp_add<0x128>(v);
  return xsum(v);
}

#define DG(kid, slot) do { if (threadIdx.x == DGT) ((unsigned long long*)(ws + WS_ST))[((kid) * 256 + blockIdx.x) * 8 + (slot)] = __builtin_amdgcn_s_memrealtime(); } while (0)
#define ST_DECL
#define LT_DECL
#define LT(k)
#define ET(k)
#define ST(k)
#define ST_OUT(kid, n)
#define ST_SAVE()
#define ST_REST()
#define WS_BASE_MB 0
constexpr size_t WS_XN = (size_t)WS_BASE_MB << 20;
constexpr size_t WS_H = WS_XN + (size_t)4 * 1024 * 1024;
constexpr size_t H_BYTES = (size_t)8 * 4 * 36 * 16 * 1024;
constexpr size_t WS_X = WS_H + H_BYTES;
constexpr size_t X_BYTES = (size_t)64 * 4 * 3 * 8 * 2048;
constexpr size_t WS_CNT = WS_X + X_BYTES;
constexpr size_t WS_GCNT = WS_CNT + 131072;
constexpr size_t WS_HCNT = WS_GCNT + 8192;
constexpr size_t WS_ST = WS_HCNT + 32768;

}
extern "C" __global__ __launch_bounds__(320) void psum_ln_kernel(const u16* __restrict__ x, const u16* __restrict__ wqkv, const u16* __restrict__ wg,
                                                                 const u16* __restrict__ wu, char* __restrict__ ws) {
  const int lane = threadIdx.x & 63, b = blockIdx.x;
#define DGT 0
  DG(0, 0);
  if (threadIdx.x >= 256) {
    if (lane < 8) *(unsigned*)(ws + WS_CNT + (b * 8 + lane) * 64) = 0u;
    if (lane == 8 && b < 32) *(unsigned*)(ws + WS_GCNT + b * 64) = 0u;
    if (lane >= 16 && lane < 24) *(unsigned*)(ws + WS_HCNT + b * 128 + (lane - 16) * 4) = 0u;
    __shared__ __attribute__((aligned(16))) char sink[256];
    const int kq = (b & 7) >> 1, l = lane < 40 ? lane : lane - 40;
    const u16* p = l < 12 ? wqkv + (l >> 2) * 1024 + 256 * kq + (l & 3) * 64 : (l < 26 ? wg + 896 * kq + (l - 12) * 64 : wu + 896 * kq + (l - 26) * 64);
    const int ld = l < 12 ? 3072 : 3584;
#pragma unroll
    for (int i = 0; i < PF_LN_ROWS / 32; ++i) touch((unsigned)(size_t)(LAS char*)sink, p + (i * 32 + (b >> 3)) * ld);
    return;
  }
  const int hf = (b & 1) * 128 + (b >> 1);
  const int mb = hf >> 5, mf = (hf >> 1) & 15, r = (hf & 1) * 8 + (threadIdx.x >> 6) * 2 + ((lane >> 2) & 1);
  const int row = mb * 256 + 32 * (mf >> 1) + 2 * r + (mf & 1), kl = lane >> 3, ch = lane & 3;
  const u16* xr = x + row * 1024 + kl * 32 + ch * 8;
  u32x4 raw[4];
#pragma unroll
  for (int i = 0; i < 4; ++i) raw[i] = *(const u32x4*)(xr + 256 * i);
  float v[32];
#pragma unroll
  for (int i = 0; i < 16; ++i) { v[2 * i] = __uint_as_float(raw[i >> 2][i & 3] << 16); v[2 * i + 1] = __uint_as_float(raw[i >> 2][i & 3] & 0xffff0000u); }
  float s = 0.f;
#pragma unroll
  for (int i = 0; i < 32; ++i) s += v[i];
  const float mean = hsum(s) * (1.0f / 1024.0f);
  float q = 0.f;
#pragma unroll
  for (int i = 0; i < 32; ++i) { const float d = v[i] - mean; q += d * d; }
  q = hsum(q);
  const float mb16 = rbf(mean), den = rbf(sqrtf(rbf(rbf(q * (1.0f / 1024.0f)) + 1e-5f)));
  const float inv = 1.0f / den;
  const auto rs = __builtin_amdgcn_make_buffer_rsrc(ws + WS_XN, 0, 4 << 20, 0x00020000);
  const unsigned o = ((mb * 32 + kl) * 16 + mf) * 1024 + r * 64 + ((ch ^ (2 * (r >> 3))) * 16);
#pragma unroll
  for (int i = 0; i < 4; ++i) {
    u32x4 t;
#pragma unroll
    for (int e = 0; e < 4; ++e) t[e] = pk2(rbf(v[8 * i + 2 * e] - mb16) * inv, rbf(v[8 * i + 2 * e + 1] - mb16) * inv);
    __builtin_amdgcn_raw_buffer_store_b128(t, rs, o + i * 8 * 16384, 0, H_AUX);
  }
  WAIT_V(0);
  DG(0, 1);
}
namespace {
constexpr int G1_SA = 17 * 1024, G1_SB = 20 * 1024, G1_SLOT = G1_SA + G1_SB;
constexpr int T_SCR = 159488;
constexpr int T_BIAS = T_SCR + 256;
constexpr int G1_LDS = T_BIAS + 4096;
constexpr int G2_SA = 32768, G2_SB = 16384, G2_SLOT = G2_SA + G2_SB;
constexpr int T_SYNC = T_SCR - 64;
constexpr int T_KV = 2 * G2_SLOT, KV_BYTES = 7 * 1024 + 272 * 80;

template <bool MAX>
__device__ __forceinline__ float tred(float a, float b, float c, float d) {
  const auto r1 = __builtin_amdgcn_permlane16_swap(__float_as_uint(a), __float_as_uint(b), false, false);
  const auto r2 = __builtin_amdgcn_permlane16_swap(__float_as_uint(c), __float_as_uint(d), false, false);
  const float x0 = __uint_as_float(r1[0]), x1 = __uint_as_float(r1[1]), y0 = __uint_as_float(r2[0]), y1 = __uint_as_float(r2[1]);
  const float t = MAX ? fmx(x0, x1) : x0 + x1, u = MAX ? fmx(y0, y1) : y0 + y1;
  const auto r3 = __builtin_amdgcn_permlane32_swap(__float_as_uint(t), __float_as_uint(u), false, false);
  const float z0 = __uint_as_float(r3[0]), z1 = __uint_as_float(r3[1]);
  return MAX ? fmx(z0, z1) : z0 + z1;
}
__device__ __forceinline__ u32x2 glu4(f32x4 g, f32x4 u) {
  f32x4 e = g * -1.4426950408889634f;
#pragma unroll
  for (int i = 0; i < 4; ++i) e[i] = __builtin_amdgcn_exp2f(e[i]);
  e = e + 1.0f;
#pragma unroll
  for (int i = 0; i < 4; ++i) e[i] = __builtin_amdgcn_rcpf(e[i]);
  e = e * u;
  return (u32x2){pk2(e[0], e[1]), pk2(e[2], e[3])};
}
__device__ __forceinline__ u32x4 cat4(u32x2 a, u32x2 b) { return (u32x4){a[0], a[1], b[0], b[1]}; }

template <bool typeA>
__device__ __forceinline__ void body(const u16* __restrict__ x, u16* __restrict__ out, const u16* __restrict__ wqkv, const u16* __restrict__ wg,
                                     const u16* __restrict__ wu, const u16* __restrict__ wo, const u16* __restrict__ wd, char* __restrict__ ws) {
  extern __shared__ __attribute__((aligned(16))) char smem[];
  LAS char* lds = (LAS char*)smem;
  ST_DECL LT_DECL ST(0);
  const unsigned long long dg0 = __builtin_amdgcn_s_memrealtime(); unsigned long long dg1, dg2, dg3, dg4, dg5, dc2; const unsigned long long dc0 = __builtin_amdgcn_s_memtime();
  const int tid = threadIdx.x;
  int lane = tid & 63;
  const int w = __builtin_amdgcn_readfirstlane(tid >> 6), g = w >> 2, wi = w & 3;
  const int b = blockIdx.x, xcd = b & 7, idx = b >> 3;
  const int mb = (xcd & 1) * 4 + (idx & 3), kq = xcd >> 1, ntl = idx >> 2;
  const int j = ntl & 3;
  int fr = lane & 15, fq = lane >> 4;
  const int fA = 896 * kq + 64 * j;
  const int f0 = 896 * kq + 256 + 160 * j, odd = j & 1;
  const int fa = f0 + 32 * odd, fb = fa + 64, fc = odd ? f0 : f0 + 128;

  asm volatile("" : "+s"(wqkv), "+s"(wg), "+s"(wu), "+s"(ws));
  const bool isq = typeA && wi < 3;
  const int ld = isq ? 3072 : 3584;
  const u16* q1 = isq ? wqkv + (wi * 1024 + 256 * kq + 64 * j) : ((!typeA && (wi & 2)) ? wu : wg) + (typeA ? fA : ((wi & 1) ? fb : fa));
  const char* c1 = g ? (const char*)q1 : ws + WS_XN + (size_t)(mb * 32 * 16384 + wi * 4096);
  const int s1 = g ? 8 * ld - 1024 : 0, s2 = g ? 32 * ld - 2048 : 0, s3 = g ? 40 * ld - 3072 : 0;
  const int p1 = g ? 64 * ld : 16384, p2 = g ? 16384 : 64 * 3584;
  const int d1 = (g ? G1_SA : 0) + wi * 4096, d2 = g ? 16384 + wi * 256 : G1_SA + 16384 + wi * 1024;
  const unsigned long long m2 = g ? 0xffffull : ~0ull;
  const int rr = lane >> 3, pc = lane & 7, fi = (pc >> 1) ^ ((rr >> 1) & 3), kr = (rr & 3) + 8 * (rr >> 2);
  const unsigned v1 = g ? kr * ld * 2 + fi * 32 + (pc & 1) * 16 : lane * 16;
  const unsigned lb = (unsigned)(size_t)lds;
  unsigned ns = lb;
  dma_first(ns + d1, v1, c1); dma_next<1024>(v1, c1 + s1); dma_next<2048>(v1, c1 + s2); dma_next<3072>(v1, c1 + s3);
  const char* c2;
  {
    const int hr = wi * 4 + ((lane >> 2) & 3);
    const unsigned oh = (14 + (hr >> 3)) * 1024 + (8 + (hr & 7)) * 64 + (((lane & 3) ^ (2 * (hr >> 3)) ^ 2) * 16);
    const unsigned ow = (kr + 16 * (wi >> 1) + 4 * (wi & 1)) * 7168 + (typeA ? fi * 32 : (fi >> 1) * 32) + (pc & 1) * 16;
    const char* be = g ? ws + WS_XN + (size_t)((mb > 0 ? mb - 1 : 0) * 32 * 16384) : (const char*)(typeA ? wu + fA : wg + fc);
    const char* bo = g ? be : (const char*)(wu + (typeA ? fA : fc));
    c2 = ((fi & 1) ? bo : be) + (g ? oh : ow);
  }
  auto adv = [&]() { c1 += p1; c2 += p2; ns = (ns == lb + 3 * G1_SLOT) ? lb : ns + G1_SLOT; };
  auto stage = [&]() {
    dma_first(ns + d1, v1, c1); dma_next<1024>(v1, c1 + s1); dma_next<2048>(v1, c1 + s2); dma_next<3072>(v1, c1 + s3);
    dma1m(ns + d2, c2, m2);
    adv();
  };
  dma1m(ns + d2, c2, m2);
  adv();
  stage();

  f32x4 acc[4][10];
  f32x4 hacc = {0.f, 0.f, 0.f, 0.f};
#pragma unroll
  for (int i = 0; i < 4; ++i)
#pragma unroll
    for (int n = 0; n < 10; ++n) { acc[i][n] = (f32x4){0.f, 0.f, 0.f, 0.f}; asm volatile("" : "+v"(acc[i][n])); }
  const int rl = 4 * (fq & 1) + (fr >> 2);
  unsigned hoffA = lb + 16384 + fr * 64 + ((fq ^ (2 * (fr >> 3))) * 16);
  unsigned aoff = hoffA - 16384 + wi * 4096;
  unsigned boff0 = lb + G1_SA + (fq >> 1) * 2048 + rl * 128 + 32 * ((2 * g) ^ ((rl >> 1) & 3)) + 8 * (lane & 3);
  unsigned boff1 = boff0 ^ 32;
  unsigned hoffB = (boff0 ^ (32 * (wi & 1))) + (1 + (wi >> 1)) * 4096;
  asm volatile("" : "+v"(aoff), "+v"(hoffA), "+v"(boff0), "+v"(boff1), "+v"(hoffB), "+v"(hacc));
  {
    const int lo = fr + (wi == 0 ? -7 : (wi == 2 ? 1 : 9));
    const int hi = wi == 0 ? fr + 1 : ((wi >= 2 && mb == 0) ? 0 : (wi == 2 ? 8 : 16));
    f32x4 bv;
#pragma unroll
    for (int e = 0; e < 4; ++e) bv[e] = (4 * fq + e >= lo && 4 * fq + e < hi) ? 0.f : -3.0e38f;
    *(LAS f32x4*)(lds + T_BIAS + wi * 1024 + lane * 16) = bv;
    *(LAS unsigned*)(lds + T_SYNC + (lane & 1) * 4) = 0u;
  }
  ST(1);
  WAIT_V_IF0(0, g);
  BAR();
  ST(2);
  if (g == 1) { WAIT_V(0); BAR(); }

  auto step = [&](auto halo_c, auto last_c, int q) {
    constexpr bool HALO = decltype(halo_c)::value, LAST = decltype(last_c)::value;
    bf16x8 a[2][4], wn[4][2];
    asm volatile("" ::: "memory");
    auto rdq = [&](int e, int h) {
      if (e >= (HALO ? 12 : 10)) return;
      const int kk = h >> 1, t = h & 1;
      bf16x8& d = wn[e & 3][kk];
      if (e == 11) { if (t == 0) d = *(const LAS bf16x8*)(lp(hoffA) + kk * G1_SLOT); return; }
      const int n = e < 2 ? 8 + e : e - 2;
      const bf16x4 v = trd(lp(e == 10 ? hoffB : ((n & 1) ? boff1 : boff0)) + (e == 10 ? 0 : (n >> 1) * 4096) + t * 1024 + kk * G1_SLOT);
      if (t == 0) { d[0] = v[0]; d[1] = v[1]; d[2] = v[2]; d[3] = v[3]; } else { d[4] = v[0]; d[5] = v[1]; d[6] = v[2]; d[7] = v[3]; }
    };
#define RA1(kk, i) a[kk][i] = *(const LAS bf16x8*)(lp(aoff) + (kk) * G1_SLOT + (i) * 1024);
    const int dlt = (q & 1) ? -2 * G1_SLOT : 2 * G1_SLOT;
    if (!LAST) {
#pragma unroll
      for (int kk = 0; kk < 2; ++kk) {
        dma_first(ns + d1, v1, c1); RA1(kk, 0) rdq(kk, 0);
        dma_next<1024>(v1, c1 + s1); RA1(kk, 1) rdq(kk, 1);
        dma_next<2048>(v1, c1 + s2); RA1(kk, 2) rdq(kk, 2);
        dma_next<3072>(v1, c1 + s3); RA1(kk, 3) rdq(kk, 3);
        dma1m(ns + d2, c2, m2);
        adv();
      }
    } else
    {
#pragma unroll
      for (int kk = 0; kk < 2; ++kk)
#pragma unroll
        for (int i = 0; i < 4; ++i) RA1(kk, i)
#pragma unroll
      for (int h = 0; h < 8; ++h) rdq(h >> 2, h & 3);
    }
    aoff += dlt;
    SCHED();
    if (!LAST) {
    } else {
      int t = w * 64 + __builtin_amdgcn_mbcnt_hi(~0u, __builtin_amdgcn_mbcnt_lo(~0u, 0u));
      t = t < 288 ? t : t - 288;
      const int r2 = (t >> 4) * 64 + (xcd & 1) * 32 + idx;
      touch(lb + T_SCR, (r2 < 256 ? wo + (256 * kq + r2) * 1024 : wd + (896 * kq + r2 - 256) * 1024) + (t & 15) * 64);
    }
    WAIT_L0();
    SCHED();
    BAR();
    SCHED();
    PRIO_M();
    auto fin = [&]() { SCHED(); PRIO_T(); if (!LAST) { if (PFD) WAIT_V(1); else WAIT_V(0); } BAR(); SCHED(); };
#pragma unroll
    for (int e = 0; e < 10; ++e) {
      const int n = e < 2 ? 8 + e : e - 2;
#pragma unroll
      for (int i = 0; i < 4; ++i) {
        acc[i][n] = mfma(wn[e & 3][0], a[0][i], acc[i][n]);
        SCHED();
        if (!HALO && e == 9 && i == 3) fin();
        SCHED();
        acc[i][n] = mfma(wn[e & 3][1], a[1][i], acc[i][n]);
        SCHED();
        if (e == 0) { rdq(2 + (i >> 1), 2 * (i & 1)); rdq(2 + (i >> 1), 2 * (i & 1) + 1); } else rdq(e + 3, i);
        if (i == 3 && e < 9) { asm volatile("" :: "v"(wn[(e + 1) & 3][0]), "v"(wn[(e + 1) & 3][1])); }
        SCHED();
      }
    }
    if (HALO) {
      hacc = mfma(wn[2][0], wn[3][0], hacc);
      WAIT_L0();
      fin();
      hacc = mfma(wn[2][1], wn[3][1], hacc);
    }
    hoffA += dlt; boff0 += dlt; boff1 += dlt; hoffB += dlt;
    PRIO_E();
    SCHED();
  };
  const int nt = ntl;
  const int kba = 8 + ((fa - 896 * kq) >> 5);
  const int own0 = typeA ? j : (kba >> 1), own1 = typeA ? 4 + j : (kba >> 1) + 1;
  const char* const hbase = ws + WS_H + (size_t)(mb * 4 + kq) * 36 * 16384;
  auto brow = [&](int stp_, int w_) {
    return (const char*)(stp_ < 4 ? wo + (size_t)(256 * kq) * 1024 : wd + (size_t)(896 * kq) * 1024 - 256 * 1024) + ((stp_ * 64 + 8 * w_) * 1024 + nt * 128) * 2;
  };
  unsigned va, vb;
  auto stage1 = [&](auto halo_c) {
    constexpr bool TA = decltype(halo_c)::value;
    typedef std::integral_constant<bool, TA || !HALO_SKIP> HC;
#pragma clang loop unroll(disable)
    for (int q = 0; q < 15; ++q) step(HC{}, std::false_type{}, q);
    step(HC{}, std::true_type{}, 15);
    dg2 = __builtin_amdgcn_s_memrealtime(); dc2 = __builtin_amdgcn_s_memtime(); WAIT_L0();
    ST(3);
    asm volatile("" ::: "memory");
    if (g == 0) __builtin_amdgcn_s_setprio(2); else __builtin_amdgcn_s_setprio(EPI_PRIO);
    lane = __builtin_amdgcn_mbcnt_hi(~0u, __builtin_amdgcn_mbcnt_lo(~0u, 0u));
    asm volatile("" : "+v"(lane));
    fr = lane & 15; fq = lane >> 4;
    va = lane * 16;
    vb = (lane >> 3) * 2048 + ((((lane & 7) >> 1) ^ ((lane >> 4) & 3)) * 32) + (lane & 1) * 16;
    if (g == 1) {
#pragma unroll
      for (int u = 0; u < 4; ++u) {
        const int w_ = wi * 2 + (u & 1);
        const char* p0 = brow((u >> 1) ? own1 : own0, w_);
        dma_first(lb + (u >> 1) * G2_SLOT + G2_SA + w_ * 2048, vb, p0); dma_next<1024>(vb, p0 - 896);
      }
    }
    const auto hrs = __builtin_amdgcn_make_buffer_rsrc((void*)hbase, 0, 36 * 16384, 0x00020000);
    const unsigned hv = wi * 4096 + lane * 16;
    auto put = [&](u32x4 v, int s, int kb, int i) {
      __builtin_amdgcn_raw_buffer_store_b128(v, hrs, hv + kb * 16384 + i * 1024, 0, H_AUX);
      *(LAS u32x4*)(lds + s * G2_SLOT + g * 16384 + ((wi * 4096 + kq * 1024 + lane * 16) ^ (i * 1024))) = v;
    };
#define GLU(i, a, b) glu4(acc[i][a], acc[i][b])
    if (!TA) {
      const int kbc = 8 + ((fc - 896 * kq) >> 5);
#pragma unroll
      for (int i = 0; i < 4; ++i) {
        __builtin_amdgcn_raw_buffer_store_b64(GLU(i, 8, 9), hrs, hv + kbc * 16384 + g * 8 + i * 1024, 0, H_AUX);
        put(cat4(GLU(i, 0, 4), GLU(i, 1, 5)), 0, kba + g, i);
        put(cat4(GLU(i, 2, 6), GLU(i, 3, 7)), 1, kba + 2 + g, i);
        if (i == FB) { SCHED(); BAR_IF0(g); SCHED(); }
      }
    } else {
      auto glu_part = [&](int i0, int i1) {
#pragma unroll
        for (int i = i0; i < i1; ++i) put(cat4(GLU(i, 6, 8), GLU(i, 7, 9)), 1, 8 + 2 * j + g, i);
      };
      if (g == 0) { glu_part(0, FA); SCHED(); BAR(); __builtin_amdgcn_s_setprio(0); asm volatile("" ::: "memory"); }
    LAS char* kim = lds + T_KV + g * KV_BYTES;
    LAS char* vim = kim + 7 * 1024;
    bf16x8 kf[4], qf[4];
#pragma unroll
    for (int i = 0; i < 4; ++i) {
      kf[i] = pack8(acc[i][2], acc[i][3]);
      *(LAS bf16x8*)(vim + ((wi * 4 + i) * 16 + fr) * 80 + fq * 16) = pack8(acc[i][4], acc[i][5]);
    }
    if (wi < 3) {
      *(LAS bf16x8*)(kim + wi * 2048 + lane * 16) = kf[2];
      *(LAS bf16x8*)(kim + wi * 2048 + 1024 + lane * 16) = kf[3];
    }
    {
      LAS char* hp = (wi < 2) ? (kim + 6 * 1024 + lane * 16 + wi * 8) : (vim + (256 + fr) * 80 + fq * 16 + (wi - 2) * 8);
      *(LAS u32x2*)hp = (u32x2){pk2(hacc[0], hacc[1]), pk2(hacc[2], hacc[3])};
    }
    WAIT_L0();
    BAR();
    asm volatile("" ::: "memory");
    const bool halo = wi == 0;
    const f32x4 bc = *(const LAS f32x4*)(lds + T_BIAS + lane * 16), bp = *(const LAS f32x4*)(lds + T_BIAS + 1024 + lane * 16);
    const f32x4 bp0 = *(const LAS f32x4*)(lds + T_BIAS + (halo ? 2048 : 1024) + lane * 16);
    const f32x4 bp1 = *(const LAS f32x4*)(lds + T_BIAS + (halo ? 3072 : 1024) + lane * 16);
    const int pf0 = halo ? 16 : wi * 4 - 2, pf1 = halo ? 16 : wi * 4 - 1;
    const bf16x8 kp0 = *(const LAS bf16x8*)(kim + (halo ? 6 : 2 * wi - 2) * 1024 + lane * 16);
    const bf16x8 kp1 = *(const LAS bf16x8*)(kim + (halo ? 6 : 2 * wi - 1) * 1024 + lane * 16);
    const LAS char* vr = vim + (4 * fq + (fr >> 2)) * 80 + (lane & 3) * 16;
    bf16x4 vt[6][2];
#pragma unroll
    for (int d = 0; d < 2; ++d) {
      vt[0][d] = trd(vr + pf0 * 1280 + d * 8); vt[1][d] = trd(vr + pf1 * 1280 + d * 8);
#pragma unroll
      for (int i = 0; i < 4; ++i) vt[2 + i][d] = trd(vr + (wi * 4 + i) * 1280 + d * 8);
    }
    f32x4 sp[4], sc[4];
#pragma unroll
    for (int i = 0; i < 4; ++i) {
      qf[i] = pack8(acc[i][0], acc[i][1]);
      sc[i] = mfma(kf[i], qf[i], bc);
    }
    sp[2] = mfma(kf[0], qf[2], bp); sp[3] = mfma(kf[1], qf[3], bp);
    sp[0] = mfma(kp0, qf[0], bp0); sp[1] = mfma(kp1, qf[1], bp1);
    const float sl = 0.17677669529663687f * 1.4426950408889634f;
    const f32x4 z4 = {0.f, 0.f, 0.f, 0.f};
    float m[4];
#pragma unroll
    for (int i = 0; i < 4; ++i)
      m[i] = fmx(fmx(fmx(fmx(sp[i][0], sp[i][1]), sp[i][2]), fmx(fmx(sp[i][3], sc[i][0]), sc[i][1])), fmx(sc[i][2], sc[i][3]));
    {
      const unsigned t = __float_as_uint(tred<true>(m[0], m[1], m[2], m[3]));
      const auto u = __builtin_amdgcn_permlane32_swap(t, t, false, false);
      const auto u0 = __builtin_amdgcn_permlane16_swap(u[0], u[0], false, false), u1 = __builtin_amdgcn_permlane16_swap(u[1], u[1], false, false);
      m[0] = __uint_as_float(u0[0]); m[1] = __uint_as_float(u0[1]); m[2] = __uint_as_float(u1[0]); m[3] = __uint_as_float(u1[1]);
    }
    const u32x4 one4 = {0x3f803f80u, 0x3f803f80u, 0x3f803f80u, 0x3f803f80u};
#pragma unroll
    for (int i = 0; i < 4; ++i) {
      const float mm = -sl * m[i];
#pragma unroll
      for (int e = 0; e < 4; ++e) {
        sp[i][e] = __builtin_amdgcn_exp2f(__builtin_fmaf(sp[i][e], sl, mm));
        sc[i][e] = __builtin_amdgcn_exp2f(__builtin_fmaf(sc[i][e], sl, mm));
      }
      const bf16x8 pfr = pack8(sp[i], sc[i]);
      const f32x4 sm = mfma(__builtin_bit_cast(bf16x8, one4), pfr, z4);
      const f32x4 o0 = mfma(cat(vt[i][0], vt[2 + i][0]), pfr, z4);
      const f32x4 o1 = mfma(cat(vt[i][1], vt[2 + i][1]), pfr, z4);
      const float inv = __builtin_amdgcn_rcpf(sm[0]);
      put(__builtin_bit_cast(u32x4, pack8(o0 * inv, o1 * inv)), 0, 2 * j + g, i);
    }
      glu_part(FA, 4);
      if (g == 1) glu_part(0, FA);
    }
  };
  stage1(std::integral_constant<bool, typeA>{});
  ST(4);

  const int lag = g ^ 1;
  GAS unsigned* hflag = (GAS unsigned*)(ws + WS_HCNT) + (mb * 4 + kq) * 256;
  const unsigned fo = (nt * 32 + (lane & 7)) * 4;
  GAS unsigned* gcnt = (GAS unsigned*)(ws + WS_GCNT) + (mb * 4 + kq) * 16;
  const int e0 = kq * 1024, e1 = ((1 ^ kq) - 1) * 1024, e2 = ((2 ^ kq) - 2) * 1024, e3 = ((3 ^ kq) - 3) * 1024;
  auto nxt = [&](int s) { ++s; if (s == own0) ++s; if (s == own1) ++s; if (s == 18) s = (own0 == 0) ? 1 : 0; return s; };
  auto wait_cnt = [&](unsigned seen, GAS unsigned* c, unsigned need) {
    if ((unsigned)__builtin_amdgcn_readfirstlane(seen) < need) {
      asm volatile("" ::: "memory");
      unsigned spins = 0;
      while ((unsigned)__builtin_amdgcn_readfirstlane(__hip_atomic_load(c, __ATOMIC_RELAXED, __HIP_MEMORY_SCOPE_AGENT)) < need)
        if (++spins > (1u << 22)) break;
    }
    asm volatile("" ::: "memory");
  };
  auto wait_flags = [&](unsigned seen, unsigned long long need) {
    if ((__builtin_amdgcn_ballot_w64(seen != 0u) & need) != need) {
      asm volatile("" ::: "memory");
      unsigned spins = 0;
      while ((__builtin_amdgcn_ballot_w64(__hip_atomic_load(hflag + (fo >> 2), __ATOMIC_RELAXED, __HIP_MEMORY_SCOPE_AGENT) != 0u) & need) != need) {
        __builtin_amdgcn_s_sleep(1);
        if (++spins > (1u << 22)) break;
      }
    }
    asm volatile("" ::: "memory");
  };
  int stp = nxt(7);
  const int mf = wi * 4 + kq;
  const unsigned orow = ((mb * 256 + 32 * (mf >> 1) + 2 * fr + (mf & 1)) * 1024 + nt * 128 + g * 64 + 16 * (fq & 1) + 8 * (fq >> 1)) * 2;
  u32x4 xr[2] = {{0u, 0u, 0u, 0u}, {0u, 0u, 0u, 0u}};
  const int rl2 = 4 * (fq & 1) + (fr >> 2);
  unsigned ao = lb + wi * 4096 + lane * 16;
  unsigned bo = lb + G2_SA + (fq >> 1) * 2048 + g * 1024 + rl2 * 128 + 32 * ((rl2 >> 1) & 3) + 8 * (lane & 3);
  f32x4 ac2[4][4];
#pragma unroll
  for (int i = 0; i < 4; ++i)
#pragma unroll
    for (int n = 0; n < 4; ++n) { ac2[i][n] = (f32x4){0.f, 0.f, 0.f, 0.f}; asm volatile("" : "+v"(ac2[i][n])); }
  ns = lb + 2 * G2_SLOT;
  const char* ca = hbase + stp * 32768 + w * 4096;
  const char* cb = brow(stp, w);
  int sl2 = 0;
  WAIT_V_IF0(0, lag);
  WAIT_L0();
  BAR();
  dg3 = __builtin_amdgcn_s_memrealtime(); WAIT_L0();
  ST(5);
  ST_SAVE(); ST(0); ST(1);
  unsigned seen = 0;
#define PEEK(c, o) asm volatile("global_load_dword %0, %1, %2 sc1" : "+v"(seen) : "v"(o), "s"(c) : "memory")
  if (lag) BAR();

  auto phase2 = [&](auto mode, int q) {
    constexpr int MODE = decltype(mode)::value;
    bf16x8 a[2][4], bfr[2][4];
#define RA(kk, i) a[kk][i] = *(const LAS bf16x8*)(lp(ao) + (kk) * 16384 + (i) * 1024);
#define RB(kk, n) bfr[kk][n] = cat(trd(lp(bo ^ ((n) * 32)) + (kk) * 8192), trd(lp(bo ^ ((n) * 32)) + (kk) * 8192 + 4096));
    int dlt;
    asm volatile("" ::: "memory");
    if (MODE == 1 && q == 1 && w == 4) PEEK(hflag, fo);
    if (MODE == 2 && q == 16)
      asm volatile("global_load_dwordx4 %0, %2, %3\n\tglobal_load_dwordx4 %1, %2, %3 offset:64" : "+v"(xr[0]), "+v"(xr[1]) : "v"(orow), "s"(x) : "memory");
    RA(0, 0) RA(0, 1)
    if (MODE == 0) dmc_first(ns + w * 4096, va, ca + e0);
    RA(0, 2) RA(0, 3) RB(0, 0)
    if (MODE == 0) dmc_next<1024>(va, ca + e1);
    RB(0, 1) RB(0, 2) RB(0, 3)
    if (MODE == 0) dmc_next<2048>(va, ca + e2);
    RA(1, 0) RA(1, 1) RA(1, 2)
    if (MODE == 0) dmc_next<3072>(va, ca + e3);
    RA(1, 3) RB(1, 0)
    if (MODE < 2) dma_first(ns + G2_SA + w * 2048, vb, cb);
    RB(1, 1)
    if (MODE < 2) dma_next<1024>(vb, cb - 896);
    sl2 = (sl2 == 2) ? 0 : sl2 + 1; dlt = (sl2 == 0) ? -2 * G2_SLOT : G2_SLOT;
    if (MODE < 2) { stp = nxt(stp); ca = hbase + stp * 32768 + w * 4096; cb = brow(stp, w); ns = (ns == lb + 2 * G2_SLOT) ? lb : ns + G2_SLOT; }
    asm volatile("" : "+s"(ca), "+s"(cb), "+s"(ns), "+s"(sl2), "+s"(dlt), "+s"(stp) :: "memory");
    RB(1, 2) RB(1, 3)
#undef RA
#undef RB
    ao += dlt; bo += dlt;
    SCHED();
    asm volatile("" : "+v"(bo), "+v"(ao));
    if (MODE == 0) WAIT_V_IF0(6, lag ^ 1); else if (MODE == 2 && q == 16) WAIT_V_IF0(2, lag ^ 1);
    if (MODE == 1 && q == 1) {
      asm volatile("s_waitcnt vmcnt(2)" : "+v"(seen) :: "memory");
      ET(2);
      if (!lag) {
        volatile LAS unsigned* go = (volatile LAS unsigned*)(lds + T_SYNC + 4);
        if (w == 4) { wait_flags(seen, 0xf0ull); *go = 1u; }
        else { unsigned spins = 0; while (__builtin_amdgcn_readfirstlane(*go) == 0u) { __builtin_amdgcn_s_sleep(1); if (++spins > (1u << 24)) break; } }
        asm volatile("" ::: "memory");
      }
      ET(3);
      const int s2 = nxt(7), s = lag ? nxt(s2) : s2;
      const char* p = hbase + s * 32768 + wi * 8192;
      const unsigned d = lb + (lag ? 0 : 2 * G2_SLOT) + wi * 8192;
      dmc_first(d, va, p + e0); dmc_next<1024>(va, p + e1); dmc_next<2048>(va, p + e2); dmc_next<3072>(va, p + e3);
      dmc_first(d + 4096, va, p + 4096 + e0); dmc_next<1024>(va, p + 4096 + e1); dmc_next<2048>(va, p + 4096 + e2); dmc_next<3072>(va, p + 4096 + e3);
      ET(4);
    }
    WAIT_L0();
    SCHED();
    BAR();
    SCHED();
    PRIO_M();
#pragma unroll
    for (int n = 0; n < 4; ++n)
#pragma unroll
      for (int i = 0; i < 4; ++i) {
        ac2[i][n] = mfma(bfr[0][n], a[0][i], ac2[i][n]);
        SCHED();
        if (n == 3 && i == 3) {
          PRIO_T();
          if (MODE == 0) WAIT_V_IF0(6, lag); else if (MODE == 2 && q == 16) WAIT_V_IF0(2, lag);
          if (MODE == 1 && q == 1) WAIT_V_IF0(0, lag);
          if (q < 17 || !lag) BAR();
          SCHED();
        }
        ac2[i][n] = mfma(bfr[1][n], a[1][i], ac2[i][n]);
        SCHED();
        if (MODE == 1 && q == 0 && n == 1 && i == 3 && lag) {
          WAIT_V(0);
          unsigned old = 0;
          if (lane == 0) old = __hip_atomic_fetch_add((LAS unsigned*)(lds + T_SYNC), 1u, __ATOMIC_RELAXED, __HIP_MEMORY_SCOPE_WORKGROUP);
          if (__builtin_amdgcn_readfirstlane(old) == 3u && lane < 8)
            __hip_atomic_store(hflag + lane * 32 + nt, 1u, __ATOMIC_RELAXED, __HIP_MEMORY_SCOPE_AGENT);
          SCHED();
        }
      }
    PRIO_E();
    SCHED();
  };
  typedef std::integral_constant<int, 0> M0; typedef std::integral_constant<int, 1> M1; typedef std::integral_constant<int, 2> M2;
  phase2(M1{}, 0);
  ET(1);
  phase2(M1{}, 1);
  ET(5);
  dg4 = __builtin_amdgcn_s_memrealtime(); WAIT_L0();
  ST(2);
#pragma clang loop unroll(disable)
  for (int q = 2; q < 6; ++q) phase2(M0{}, q);
  PEEK(hflag, fo);
  phase2(M0{}, 6);
  asm volatile("" : "+v"(seen));
  wait_flags(seen, 0x0full);
#pragma clang loop unroll(disable)
  for (int q = 7; q < 13; ++q) phase2(M0{}, q);
  if (w == 4 && lane == 0) __hip_atomic_fetch_add(gcnt, 1u, __ATOMIC_RELAXED, __HIP_MEMORY_SCOPE_AGENT);
#pragma clang loop unroll(disable)
  for (int q = 13; q < 16; ++q) phase2(M0{}, q);
  phase2(M2{}, 16);
  PEEK(gcnt, 0u);
  phase2(M2{}, 17);
  asm volatile("s_waitcnt vmcnt(0)" : "+v"(seen) :: "memory");
  wait_cnt(seen, gcnt, 8u);
  dg5 = __builtin_amdgcn_s_memrealtime(); WAIT_L0();
  ST(3);
  const int tile = mb * 8 + nt;
  const auto rs = __builtin_amdgcn_make_buffer_rsrc(ws + WS_H + (size_t)mb * 4 * 36 * 16384, 0, 4 * 36 * 16384, 0x00020000);
  auto xo = [&](int src, int i) { const int u = 15 + nt * 3 + i; return (unsigned)((src * 36 + (u >= 36 ? u - 36 : u)) * 16384 + w * 2048) + lane * 16; };
#pragma unroll
  for (int i = 1; i < 4; ++i) {
    const unsigned o = xo(kq, i);
    __builtin_amdgcn_raw_buffer_store_b128(__builtin_bit_cast(u32x4, pack8(ac2[i][0], ac2[i][1])), rs, o, 0, 16);
    __builtin_amdgcn_raw_buffer_store_b128(__builtin_bit_cast(u32x4, pack8(ac2[i][2], ac2[i][3])), rs, o + 1024, 0, 16);
  }
  WAIT_V(0);
  ST(4);
  asm volatile("" : "+v"(xr[0]), "+v"(xr[1]));
  GAS unsigned* cnt = (GAS unsigned*)(ws + WS_CNT) + (tile * 32 + w) * 16;
  if (lane >= 1 && lane < 4) __hip_atomic_fetch_add(cnt + (kq ^ lane) * 128, 1u, __ATOMIC_RELAXED, __HIP_MEMORY_SCOPE_AGENT);
  {
    unsigned spins = 0;
    while ((unsigned)__builtin_amdgcn_readfirstlane(__hip_atomic_load(cnt + kq * 128, __ATOMIC_RELAXED, __HIP_MEMORY_SCOPE_AGENT)) < 3u) {
      __builtin_amdgcn_s_sleep(2);
      if (++spins > (1u << 22)) break;
    }
  }
  asm volatile("" ::: "memory");
  ST(5);
  u32x4 p[3][2];
#pragma unroll
  for (int i = 1; i < 4; ++i) {
    const unsigned o = xo(kq ^ i, i);
    p[i - 1][0] = __builtin_amdgcn_raw_buffer_load_b128(rs, o, 0, 16);
    p[i - 1][1] = __builtin_amdgcn_raw_buffer_load_b128(rs, o + 1024, 0, 16);
  }
  const auto ors = __builtin_amdgcn_make_buffer_rsrc(out, 0, 4 << 20, 0x00020000);
#pragma unroll
  for (int h = 0; h < 2; ++h) {
    const auto x0 = __builtin_amdgcn_permlane16_swap(xr[h][0], xr[h][2], false, false), x1 = __builtin_amdgcn_permlane16_swap(xr[h][1], xr[h][3], false, false);
    const u32x4 xv = {x0[0], x1[0], x0[1], x1[1]};
    unsigned r[4];
#pragma unroll
    for (int d = 0; d < 4; ++d) {
      float lo = ac2[0][2 * h + (d >> 1)][2 * (d & 1)] + __uint_as_float(xv[d] << 16), hi = ac2[0][2 * h + (d >> 1)][2 * (d & 1) + 1] + __uint_as_float(xv[d] & 0xffff0000u);
#pragma unroll
      for (int i = 0; i < 3; ++i) { lo += __uint_as_float(p[i][h][d] << 16); hi += __uint_as_float(p[i][h][d] & 0xffff0000u); }
      r[d] = pk2(lo, hi);
    }
    const auto y0 = __builtin_amdgcn_permlane16_swap(r[0], r[2], false, false), y1 = __builtin_amdgcn_permlane16_swap(r[1], r[3], false, false);
    __builtin_amdgcn_raw_buffer_store_b128((u32x4){y0[0], y1[0], y0[1], y1[1]}, ors, orow + 64 * h, 0, OUT_AUX);
  }
  ST(6);
  if (b == 1 && w == 0) {
    WAIT_V(0);
    const unsigned long long dg6 = __builtin_amdgcn_s_memrealtime();
    const unsigned long long l0 = ((const unsigned long long*)(ws + WS_ST))[0];
    unsigned long long q = DIAG == 1 ? dg0 - l0 : DIAG == 2 ? dg2 - dg0 : DIAG == 3 ? dg3 - dg2 : DIAG == 4 ? dg4 - dg3 : DIAG == 5 ? dg5 - dg4 : DIAG == 6 ? dg6 - dg5 : DIAG == 7 ? dg6 - l0 : DIAG == 8 ? (dc2 - dc0) / 20 : DIAG == 9 ? (__builtin_amdgcn_s_memtime() - dc2) / 20 : 0;
    if (q > 20000) q = 20000;
    while (__builtin_amdgcn_s_memrealtime() < dg6 + 500 + 4 * q) __builtin_amdgcn_s_sleep(1);
  }
  ST(7);
  ST_OUT(1, 8);
  ST_REST(); ST_OUT(0, 6);
}

}

extern "C" __global__ __launch_bounds__(512, 2) void psum_g1_kernel(const u16* __restrict__ x, u16* __restrict__ out, const u16* __restrict__ wqkv,
                                                                    const u16* __restrict__ wg, const u16* __restrict__ wu,
                                                                    const u16* __restrict__ wo, const u16* __restrict__ wd, char* __restrict__ ws) {
  if (blockIdx.x < 128) body<true>(x, out, wqkv, wg, wu, wo, wd, ws);
  else body<false>(x, out, wqkv, wg, wu, wo, wd, ws);
}

extern "C" void kernel_launch(void* const* d_in, const int* in_sizes, int n_in, void* d_out, int out_size, void* d_ws,
                              size_t ws_size, hipStream_t stream) {
  const u16* x = (const u16*)d_in[0];
  const u16* wqkv = (const u16*)d_in[1];
  const u16* wo = (const u16*)d_in[2];
  const u16* wg = (const u16*)d_in[4];
  const u16* wu = (const u16*)d_in[5];
  const u16* wd = (const u16*)d_in[6];
  char* ws = (char*)d_ws;
  (void)hipFuncSetAttribute((const void*)psum_g1_kernel, hipFuncAttributeMaxDynamicSharedMemorySize, G1_LDS);
  psum_ln_kernel<<<256, 320, 0, stream>>>(x, wqkv, wg, wu, ws);
  psum_g1_kernel<<<256, 512, G1_LDS, stream>>>(x, (u16*)d_out, wqkv, wg, wu, wo, wd, ws);
}
```

```cpp
#define PFD 0
#include <hip/hip_runtime.h>
#include <hip/hip_bf16.h>
#include <type_traits>
#define EARLY 1
#define HALO_SKIP 1
#define PF_W2 1
#define PF_LOOP 0
#define PF_R0 192
#define PF_PER 2
#define PF_LN_ROWS 96
#define PRIO_L 0
#define PRIO_M() __builtin_amdgcn_s_setprio(1)
#define PRIO_T() __builtin_amdgcn_s_setprio(3)
#define PRIO_E() __builtin_amdgcn_s_setprio(0)
#define PF_CODE 0
#define PRECLEAN 0
#define OUT_AUX 16
#define G2ORD 0
#define EARLY2 1
#define CHAIN 1
#define PF_X 0
#define XH 1
#define ACQ 0
#define EPI_PRIO 1
#define LORD 1
#define RPOS 1
#define FA 4
#define FB 0
#define H_AUX 16

namespace {

typedef unsigned short u16;
typedef short bf16x8 __attribute__((ext_vector_type(8)));
typedef short bf16x4 __attribute__((ext_vector_type(4)));
typedef float f32x4 __attribute__((ext_vector_type(4)));
typedef float f32x2 __attribute__((ext_vector_type(2)));
typedef unsigned u32x4 __attribute__((ext_vector_type(4)));
typedef unsigned u32x2 __attribute__((ext_vector_type(2)));
typedef __bf16 bf2 __attribute__((ext_vector_type(2)));
#define LAS __attribute__((address_space(3)))
#define GAS __attribute__((address_space(1)))

#define WAIT_V(n) asm volatile("s_waitcnt vmcnt(" #n ")" ::: "memory")
#define WAIT_V_IF0(n, c) asm volatile("s_cmp_lg_u32 %0, 0\n\ts_cbranch_scc1 1f\n\ts_waitcnt vmcnt(" #n ")\n1:" :: "s"(c) : "memory", "scc")
#define BAR_IF0(c) asm volatile("s_cmp_lg_u32 %0, 0\n\ts_cbranch_scc1 1f\n\ts_barrier\n\ts_setprio 0\n1:" :: "s"(c) : "memory", "scc")
#define WAIT_L0() do { asm volatile("" ::: "memory"); __builtin_amdgcn_s_waitcnt(0xC07F); asm volatile("" ::: "memory"); } while (0)
#define BAR() __builtin_amdgcn_s_barrier()
#define SCHED() __builtin_amdgcn_sched_barrier(0)

__device__ __forceinline__ float b2f(u16 v) { return __uint_as_float(((unsigned)v) << 16); }
__device__ __forceinline__ unsigned pk2(float lo, float hi) {
  bf2 r = {(__bf16)lo, (__bf16)hi};
  return __builtin_bit_cast(unsigned, r);
}
__device__ __forceinline__ float rbf(float f) { return (float)(__bf16)f; }
__device__ __forceinline__ bf16x8 pack8(f32x4 a, f32x4 b) {
  u32x4 r = {pk2(a[0], a[1]), pk2(a[2], a[3]), pk2(b[0], b[1]), pk2(b[2], b[3])};
  return __builtin_bit_cast(bf16x8, r);
}
__device__ __forceinline__ bf16x4 trd(LAS const char* p) {
  return __builtin_amdgcn_ds_read_tr16_b64_v4i16((LAS bf16x4*)p);
}
__device__ __forceinline__ bf16x8 cat(bf16x4 a, bf16x4 b) { return __builtin_shufflevector(a, b, 0, 1, 2, 3, 4, 5, 6, 7); }
__device__ __forceinline__ f32x4 mfma(bf16x8 a, bf16x8 b, f32x4 c) {
  return __builtin_amdgcn_mfma_f32_16x16x32_bf16(a, b, c, 0, 0, 0);
}
__device__ __forceinline__ void dma_first(unsigned lds_dst, unsigned voff, const char* b0) {
  asm volatile("s_mov_b32 m0, %0\n\ts_nop 0\n\tglobal_load_lds_dwordx4 %1, %2" :: "s"(lds_dst), "v"(voff), "s"(b0) : "memory");
}
template <int OFF>
__device__ __forceinline__ void dma_next(unsigned voff, const char* b) {
  asm volatile("global_load_lds_dwordx4 %0, %1 offset:%2" :: "v"(voff), "s"(b), "i"(OFF) : "memory");
}
__device__ __forceinline__ void dmc_first(unsigned lds_dst, unsigned voff, const char* b0) {
  asm volatile("s_mov_b32 m0, %0\n\ts_nop 0\n\tglobal_load_lds_dwordx4 %1, %2 sc1" :: "s"(lds_dst), "v"(voff), "s"(b0) : "memory");
}
template <int OFF>
__device__ __forceinline__ void dmc_next(unsigned voff, const char* b) {
  asm volatile("global_load_lds_dwordx4 %0, %1 offset:%2 sc1" :: "v"(voff), "s"(b), "i"(OFF) : "memory");
}
__device__ __forceinline__ void dma1m(unsigned lds_dst, const char* p, unsigned long long mask) {
  asm volatile("s_mov_b32 m0, %0\n\ts_mov_b64 exec, %2\n\t"
               "global_load_lds_dwordx4 %1, off\n\t"
               "s_mov_b64 exec, -1"
               :: "s"(lds_dst), "v"(p), "s"(mask) : "memory");
}
__device__ __forceinline__ void touch(unsigned lds_dst, const void* p) {
  asm volatile("s_mov_b32 m0, %0\n\ts_nop 0\n\tglobal_load_lds_dword %1, off" :: "s"(lds_dst), "v"(p) : "memory");
}
#define CODE_ADDR(dst, sym) do { unsigned lo_, hi_; asm volatile("s_getpc_b64 s[90:91]\n\ts_add_u32 %0, s90, " #sym "@rel32@lo+4\n\ts_addc_u32 %1, s91, " #sym "@rel32@hi+12" \
    : "=s"(lo_), "=s"(hi_) :: "s90", "s91", "scc"); dst = (const char*)(((unsigned long long)hi_ << 32) | lo_); } while (0)
constexpr int LN_LINES = 16, G1_LINES = 80, G2_LINES = 30;
__device__ __forceinline__ LAS char* lp(unsigned a) { return (LAS char*)(size_t)a; }
__device__ __forceinline__ float fmx(float a, float b) { return __builtin_elementwise_maximum(a, b); }
__device__ __forceinline__ float xmax(float v) {
  auto a = __builtin_amdgcn_permlane16_swap(__float_as_uint(v), __float_as_uint(v), false, false);
  v = fmaxf(__uint_as_float(a[0]), __uint_as_float(a[1]));
  auto b = __builtin_amdgcn_permlane32_swap(__float_as_uint(v), __float_as_uint(v), false, false);
  return fmaxf(__uint_as_float(b[0]), __uint_as_float(b[1]));
}
__device__ __forceinline__ float xsum(float v) {
  auto a = __builtin_amdgcn_permlane16_swap(__float_as_uint(v), __float_as_uint(v), false, false);
  v = __uint_as_float(a[0]) + __uint_as_float(a[1]);
  auto b = __builtin_amdgcn_permlane32_swap(__float_as_uint(v), __float_as_uint(v), false, false);
  return __uint_as_float(b[0]) + __uint_as_float(b[1]);
}

template <int CTRL>
__device__ __forceinline__ float dpp_add(float v) {
  return v + __uint_as_float(__builtin_amdgcn_update_dpp(0u, __float_as_uint(v), CTRL, 0xf, 0xf, true));
}
__device__ __forceinline__ float hsum(float v) {
  v = dpp_add<0xB1>(v);
  v = dpp_add<0x4E>(v);
  v = dpp_add<0x128>(v);
  return xsum(v);
}

#define DG(kid, slot)
#define ST_DECL
#define LT_DECL
#define LT(k)
#define ET(k)
#define ST(k)
#define ST_OUT(kid, n)
#define ST_SAVE()
#define ST_REST()
#define WS_BASE_MB 0
constexpr size_t WS_XN = (size_t)WS_BASE_MB << 20;
constexpr size_t WS_H = WS_XN + (size_t)4 * 1024 * 1024;
constexpr size_t H_BYTES = (size_t)8 * 4 * 36 * 16 * 1024;
constexpr size_t WS_X = WS_H + H_BYTES;
constexpr size_t X_BYTES = (size_t)64 * 4 * 3 * 8 * 2048;
constexpr size_t WS_CNT = WS_X + X_BYTES;
constexpr size_t WS_GCNT = WS_CNT + 131072;
constexpr size_t WS_HCNT = WS_GCNT + 8192;
constexpr size_t WS_ST = WS_HCNT + 32768;

}
extern "C" __global__ __launch_bounds__(320) void psum_ln_kernel(const u16* __restrict__ x, const u16* __restrict__ wqkv, const u16* __restrict__ wg,
                                                                 const u16* __restrict__ wu, char* __restrict__ ws) {
  const int lane = threadIdx.x & 63, b = blockIdx.x;
#define DGT 0
  DG(0, 0);
  if (threadIdx.x >= 256) {
    if (lane < 8) *(unsigned*)(ws + WS_CNT + (b * 8 + lane) * 64) = 0u;
    if (lane == 8 && b < 32) *(unsigned*)(ws + WS_GCNT + b * 64) = 0u;
    if (lane >= 16 && lane < 24) *(unsigned*)(ws + WS_HCNT + b * 128 + (lane - 16) * 4) = 0u;
    __shared__ __attribute__((aligned(16))) char sink[256];
    const int kq = (b & 7) >> 1, l = lane < 40 ? lane : lane - 40;
    const u16* p = l < 12 ? wqkv + (l >> 2) * 1024 + 256 * kq + (l & 3) * 64 : (l < 26 ? wg + 896 * kq + (l - 12) * 64 : wu + 896 * kq + (l - 26) * 64);
    const int ld = l < 12 ? 3072 : 3584;
#pragma unroll
    for (int i = 0; i < PF_LN_ROWS / 32; ++i) touch((unsigned)(size_t)(LAS char*)sink, p + (i * 32 + (b >> 3)) * ld);
    return;
  }
  const int hf = (b & 1) * 128 + (b >> 1);
  const int mb = hf >> 5, mf = (hf >> 1) & 15, r = (hf & 1) * 8 + (threadIdx.x >> 6) * 2 + ((lane >> 2) & 1);
  const int row = mb * 256 + 32 * (mf >> 1) + 2 * r + (mf & 1), kl = lane >> 3, ch = lane & 3;
  const u16* xr = x + row * 1024 + kl * 32 + ch * 8;
  u32x4 raw[4];
#pragma unroll
  for (int i = 0; i < 4; ++i) raw[i] = *(const u32x4*)(xr + 256 * i);
  float v[32];
#pragma unroll
  for (int i = 0; i < 16; ++i) { v[2 * i] = __uint_as_float(raw[i >> 2][i & 3] << 16); v[2 * i + 1] = __uint_as_float(raw[i >> 2][i & 3] & 0xffff0000u); }
  float s = 0.f;
#pragma unroll
  for (int i = 0; i < 32; ++i) s += v[i];
  const float mean = hsum(s) * (1.0f / 1024.0f);
  float q = 0.f;
#pragma unroll
  for (int i = 0; i < 32; ++i) { const float d = v[i] - mean; q += d * d; }
  q = hsum(q);
  const float mb16 = rbf(mean), den = rbf(sqrtf(rbf(rbf(q * (1.0f / 1024.0f)) + 1e-5f)));
  const float inv = 1.0f / den;
  const auto rs = __builtin_amdgcn_make_buffer_rsrc(ws + WS_XN, 0, 4 << 20, 0x00020000);
  const unsigned o = ((mb * 32 + kl) * 16 + mf) * 1024 + r * 64 + ((ch ^ (2 * (r >> 3))) * 16);
#pragma unroll
  for (int i = 0; i < 4; ++i) {
    u32x4 t;
#pragma unroll
    for (int e = 0; e < 4; ++e) t[e] = pk2(rbf(v[8 * i + 2 * e] - mb16) * inv, rbf(v[8 * i + 2 * e + 1] - mb16) * inv);
    __builtin_amdgcn_raw_buffer_store_b128(t, rs, o + i * 8 * 16384, 0, H_AUX);
  }
  DG(0, 1);
}
namespace {
constexpr int G1_SA = 17 * 1024, G1_SB = 20 * 1024, G1_SLOT = G1_SA + G1_SB;
constexpr int T_SCR = 159488;
constexpr int T_BIAS = T_SCR + 256;
constexpr int G1_LDS = T_BIAS + 4096;
constexpr int G2_SA = 32768, G2_SB = 16384, G2_SLOT = G2_SA + G2_SB;
constexpr int T_SYNC = T_SCR - 64;
constexpr int T_KV = 2 * G2_SLOT, KV_BYTES = 7 * 1024 + 272 * 80;

template <bool MAX>
__device__ __forceinline__ float tred(float a, float b, float c, float d) {
  const auto r1 = __builtin_amdgcn_permlane16_swap(__float_as_uint(a), __float_as_uint(b), false, false);
  const auto r2 = __builtin_amdgcn_permlane16_swap(__float_as_uint(c), __float_as_uint(d), false, false);
  const float x0 = __uint_as_float(r1[0]), x1 = __uint_as_float(r1[1]), y0 = __uint_as_float(r2[0]), y1 = __uint_as_float(r2[1]);
  const float t = MAX ? fmx(x0, x1) : x0 + x1, u = MAX ? fmx(y0, y1) : y0 + y1;
  const auto r3 = __builtin_amdgcn_permlane32_swap(__float_as_uint(t), __float_as_uint(u), false, false);
  const float z0 = __uint_as_float(r3[0]), z1 = __uint_as_float(r3[1]);
  return MAX ? fmx(z0, z1) : z0 + z1;
}
__device__ __forceinline__ u32x2 glu4(f32x4 g, f32x4 u) {
  f32x4 e = g * -1.4426950408889634f;
#pragma unroll
  for (int i = 0; i < 4; ++i) e[i] = __builtin_amdgcn_exp2f(e[i]);
  e = e + 1.0f;
#pragma unroll
  for (int i = 0; i < 4; ++i) e[i] = __builtin_amdgcn_rcpf(e[i]);
  e = e * u;
  return (u32x2){pk2(e[0], e[1]), pk2(e[2], e[3])};
}
__device__ __forceinline__ u32x4 cat4(u32x2 a, u32x2 b) { return (u32x4){a[0], a[1], b[0], b[1]}; }

template <bool typeA>
__device__ __forceinline__ void body(const u16* __restrict__ x, u16* __restrict__ out, const u16* __restrict__ wqkv, const u16* __restrict__ wg,
                                     const u16* __restrict__ wu, const u16* __restrict__ wo, const u16* __restrict__ wd, char* __restrict__ ws) {
  extern __shared__ __attribute__((aligned(16))) char smem[];
  LAS char* lds = (LAS char*)smem;
  ST_DECL LT_DECL ST(0);
  const int tid = threadIdx.x;
  int lane = tid & 63;
  const int w = __builtin_amdgcn_readfirstlane(tid >> 6), g = w >> 2, wi = w & 3;
  const int b = blockIdx.x, xcd = b & 7, idx = b >> 3;
  const int mb = (xcd & 1) * 4 + (idx & 3), kq = xcd >> 1, ntl = idx >> 2;
  const int j = ntl & 3;
  int fr = lane & 15, fq = lane >> 4;
  const int fA = 896 * kq + 64 * j;
  const int f0 = 896 * kq + 256 + 160 * j, odd = j & 1;
  const int fa = f0 + 32 * odd, fb = fa + 64, fc = odd ? f0 : f0 + 128;

  asm volatile("" : "+s"(wqkv), "+s"(wg), "+s"(wu), "+s"(ws));
  const bool isq = typeA && wi < 3;
  const int ld = isq ? 3072 : 3584;
  const u16* q1 = isq ? wqkv + (wi * 1024 + 256 * kq + 64 * j) : ((!typeA && (wi & 2)) ? wu : wg) + (typeA ? fA : ((wi & 1) ? fb : fa));
  const char* c1 = g ? (const char*)q1 : ws + WS_XN + (size_t)(mb * 32 * 16384 + wi * 4096);
  const int s1 = g ? 8 * ld - 1024 : 0, s2 = g ? 32 * ld - 2048 : 0, s3 = g ? 40 * ld - 3072 : 0;
  const int p1 = g ? 64 * ld : 16384, p2 = g ? 16384 : 64 * 3584;
  const int d1 = (g ? G1_SA : 0) + wi * 4096, d2 = g ? 16384 + wi * 256 : G1_SA + 16384 + wi * 1024;
  const unsigned long long m2 = g ? 0xffffull : ~0ull;
  const int rr = lane >> 3, pc = lane & 7, fi = (pc >> 1) ^ ((rr >> 1) & 3), kr = (rr & 3) + 8 * (rr >> 2);
  const unsigned v1 = g ? kr * ld * 2 + fi * 32 + (pc & 1) * 16 : lane * 16;
  const unsigned lb = (unsigned)(size_t)lds;
  unsigned ns = lb;
  dma_first(ns + d1, v1, c1); dma_next<1024>(v1, c1 + s1); dma_next<2048>(v1, c1 + s2); dma_next<3072>(v1, c1 + s3);
  const char* c2;
  {
    const int hr = wi * 4 + ((lane >> 2) & 3);
    const unsigned oh = (14 + (hr >> 3)) * 1024 + (8 + (hr & 7)) * 64 + (((lane & 3) ^ (2 * (hr >> 3)) ^ 2) * 16);
    const unsigned ow = (kr + 16 * (wi >> 1) + 4 * (wi & 1)) * 7168 + (typeA ? fi * 32 : (fi >> 1) * 32) + (pc & 1) * 16;
    const char* be = g ? ws + WS_XN + (size_t)((mb > 0 ? mb - 1 : 0) * 32 * 16384) : (const char*)(typeA ? wu + fA : wg + fc);
    const char* bo = g ? be : (const char*)(wu + (typeA ? fA : fc));
    c2 = ((fi & 1) ? bo : be) + (g ? oh : ow);
  }
  auto adv = [&]() { c1 += p1; c2 += p2; ns = (ns == lb + 3 * G1_SLOT) ? lb : ns + G1_SLOT; };
  auto stage = [&]() {
    dma_first(ns + d1, v1, c1); dma_next<1024>(v1, c1 + s1); dma_next<2048>(v1, c1 + s2); dma_next<3072>(v1, c1 + s3);
    dma1m(ns + d2, c2, m2);
    adv();
  };
  dma1m(ns + d2, c2, m2);
  adv();
  stage();

  f32x4 acc[4][10];
  f32x4 hacc = {0.f, 0.f, 0.f, 0.f};
#pragma unroll
  for (int i = 0; i < 4; ++i)
#pragma unroll
    for (int n = 0; n < 10; ++n) { acc[i][n] = (f32x4){0.f, 0.f, 0.f, 0.f}; asm volatile("" : "+v"(acc[i][n])); }
  const int rl = 4 * (fq & 1) + (fr >> 2);
  unsigned hoffA = lb + 16384 + fr * 64 + ((fq ^ (2 * (fr >> 3))) * 16);
  unsigned aoff = hoffA - 16384 + wi * 4096;
  unsigned boff0 = lb + G1_SA + (fq >> 1) * 2048 + rl * 128 + 32 * ((2 * g) ^ ((rl >> 1) & 3)) + 8 * (lane & 3);
  unsigned boff1 = boff0 ^ 32;
  unsigned hoffB = (boff0 ^ (32 * (wi & 1))) + (1 + (wi >> 1)) * 4096;
  asm volatile("" : "+v"(aoff), "+v"(hoffA), "+v"(boff0), "+v"(boff1), "+v"(hoffB), "+v"(hacc));
  {
    const int lo = fr + (wi == 0 ? -7 : (wi == 2 ? 1 : 9));
    const int hi = wi == 0 ? fr + 1 : ((wi >= 2 && mb == 0) ? 0 : (wi == 2 ? 8 : 16));
    f32x4 bv;
#pragma unroll
    for (int e = 0; e < 4; ++e) bv[e] = (4 * fq + e >= lo && 4 * fq + e < hi) ? 0.f : -3.0e38f;
    *(LAS f32x4*)(lds + T_BIAS + wi * 1024 + lane * 16) = bv;
    *(LAS unsigned*)(lds + T_SYNC + (lane & 1) * 4) = 0u;
  }
  ST(1);
  WAIT_V_IF0(0, g);
  BAR();
  ST(2);
  if (g == 1) { WAIT_V(0); BAR(); }

  auto step = [&](auto halo_c, auto last_c, int q) {
    constexpr bool HALO = decltype(halo_c)::value, LAST = decltype(last_c)::value;
    bf16x8 a[2][4], wn[4][2];
    asm volatile("" ::: "memory");
    auto rdq = [&](int e, int h) {
      if (e >= (HALO ? 12 : 10)) return;
      const int kk = h >> 1, t = h & 1;
      bf16x8& d = wn[e & 3][kk];
      if (e == 11) { if (t == 0) d = *(const LAS bf16x8*)(lp(hoffA) + kk * G1_SLOT); return; }
      const int n = e < 2 ? 8 + e : e - 2;
      const bf16x4 v = trd(lp(e == 10 ? hoffB : ((n & 1) ? boff1 : boff0)) + (e == 10 ? 0 : (n >> 1) * 4096) + t * 1024 + kk * G1_SLOT);
      if (t == 0) { d[0] = v[0]; d[1] = v[1]; d[2] = v[2]; d[3] = v[3]; } else { d[4] = v[0]; d[5] = v[1]; d[6] = v[2]; d[7] = v[3]; }
    };
#define RA1(kk, i) a[kk][i] = *(const LAS bf16x8*)(lp(aoff) + (kk) * G1_SLOT + (i) * 1024);
    const int dlt = (q & 1) ? -2 * G1_SLOT : 2 * G1_SLOT;
    if (!LAST) {
#pragma unroll
      for (int kk = 0; kk < 2; ++kk) {
        dma_first(ns + d1, v1, c1); RA1(kk, 0) rdq(kk, 0);
        dma_next<1024>(v1, c1 + s1); RA1(kk, 1) rdq(kk, 1);
        dma_next<2048>(v1, c1 + s2); RA1(kk, 2) rdq(kk, 2);
        dma_next<3072>(v1, c1 + s3); RA1(kk, 3) rdq(kk, 3);
        dma1m(ns + d2, c2, m2);
        adv();
      }
    } else
    {
#pragma unroll
      for (int kk = 0; kk < 2; ++kk)
#pragma unroll
        for (int i = 0; i < 4; ++i) RA1(kk, i)
#pragma unroll
      for (int h = 0; h < 8; ++h) rdq(h >> 2, h & 3);
    }
    aoff += dlt;
    SCHED();
    if (!LAST) {
    } else {
#if PF_W2 && !(PFD & 1)
      int t = w * 64 + __builtin_amdgcn_mbcnt_hi(~0u, __builtin_amdgcn_mbcnt_lo(~0u, 0u));
      t = t < 288 ? t : t - 288;
      const int r2 = (t >> 4) * 64 + (xcd & 1) * 32 + idx;
      touch(lb + T_SCR, (r2 < 256 ? wo + (256 * kq + r2) * 1024 : wd + (896 * kq + r2 - 256) * 1024) + (t & 15) * 64);
#endif
    }
    WAIT_L0();
    SCHED();
    BAR();
    SCHED();
    PRIO_M();
    auto fin = [&]() { SCHED(); PRIO_T(); if (!LAST) { if (PFD) WAIT_V(1); else WAIT_V(0); } BAR(); SCHED(); };
#pragma unroll
    for (int e = 0; e < 10; ++e) {
      const int n = e < 2 ? 8 + e : e - 2;
#pragma unroll
      for (int i = 0; i < 4; ++i) {
        acc[i][n] = mfma(wn[e & 3][0], a[0][i], acc[i][n]);
        SCHED();
        if (!HALO && e == 9 && i == 3) fin();
        SCHED();
        acc[i][n] = mfma(wn[e & 3][1], a[1][i], acc[i][n]);
        SCHED();
        if (e == 0) { rdq(2 + (i >> 1), 2 * (i & 1)); rdq(2 + (i >> 1), 2 * (i & 1) + 1); } else rdq(e + 3, i);
        if (i == 3 && e < 9) { asm volatile("" :: "v"(wn[(e + 1) & 3][0]), "v"(wn[(e + 1) & 3][1])); }
        SCHED();
      }
    }
    if (HALO) {
      hacc = mfma(wn[2][0], wn[3][0], hacc);
      WAIT_L0();
      fin();
      hacc = mfma(wn[2][1], wn[3][1], hacc);
    }
    hoffA += dlt; boff0 += dlt; boff1 += dlt; hoffB += dlt;
    PRIO_E();
    SCHED();
  };
  const int nt = ntl;
  const int kba = 8 + ((fa - 896 * kq) >> 5);
  const int own0 = typeA ? j : (kba >> 1), own1 = typeA ? 4 + j : (kba >> 1) + 1;
  const char* const hbase = ws + WS_H + (size_t)(mb * 4 + kq) * 36 * 16384;
  auto brow = [&](int stp_, int w_) {
    return (const char*)(stp_ < 4 ? wo + (size_t)(256 * kq) * 1024 : wd + (size_t)(896 * kq) * 1024 - 256 * 1024) + ((stp_ * 64 + 8 * w_) * 1024 + nt * 128) * 2;
  };
  unsigned va, vb;
  auto stage1 = [&](auto halo_c) {
    constexpr bool TA = decltype(halo_c)::value;
    typedef std::integral_constant<bool, TA || !HALO_SKIP> HC;
#pragma clang loop unroll(disable)
    for (int q = 0; q < 15; ++q) step(HC{}, std::false_type{}, q);
    step(HC{}, std::true_type{}, 15);
    ST(3);
    asm volatile("" ::: "memory");
    if (g == 0) __builtin_amdgcn_s_setprio(2); else __builtin_amdgcn_s_setprio(EPI_PRIO);
    lane = __builtin_amdgcn_mbcnt_hi(~0u, __builtin_amdgcn_mbcnt_lo(~0u, 0u));
    asm volatile("" : "+v"(lane));
    fr = lane & 15; fq = lane >> 4;
    va = lane * 16;
    vb = (lane >> 3) * 2048 + ((((lane & 7) >> 1) ^ ((lane >> 4) & 3)) * 32) + (lane & 1) * 16;
    if (g == 1) {
#pragma unroll
      for (int u = 0; u < 4; ++u) {
        const int w_ = wi * 2 + (u & 1);
        const char* p0 = brow((u >> 1) ? own1 : own0, w_);
        dma_first(lb + (u >> 1) * G2_SLOT + G2_SA + w_ * 2048, vb, p0); dma_next<1024>(vb, p0 - 896);
      }
    }
    const auto hrs = __builtin_amdgcn_make_buffer_rsrc((void*)hbase, 0, 36 * 16384, 0x00020000);
    const unsigned hv = wi * 4096 + lane * 16;
    auto put = [&](u32x4 v, int s, int kb, int i) {
      __builtin_amdgcn_raw_buffer_store_b128(v, hrs, hv + kb * 16384 + i * 1024, 0, H_AUX);
      *(LAS u32x4*)(lds + s * G2_SLOT + g * 16384 + ((wi * 4096 + kq * 1024 + lane * 16) ^ (i * 1024))) = v;
    };
#define GLU(i, a, b) glu4(acc[i][a], acc[i][b])
    if (!TA) {
      const int kbc = 8 + ((fc - 896 * kq) >> 5);
#pragma unroll
      for (int i = 0; i < 4; ++i) {
        __builtin_amdgcn_raw_buffer_store_b64(GLU(i, 8, 9), hrs, hv + kbc * 16384 + g * 8 + i * 1024, 0, H_AUX);
        put(cat4(GLU(i, 0, 4), GLU(i, 1, 5)), 0, kba + g, i);
        put(cat4(GLU(i, 2, 6), GLU(i, 3, 7)), 1, kba + 2 + g, i);
        if (i == FB) { SCHED(); BAR_IF0(g); SCHED(); }
      }
    } else {
      auto glu_part = [&](int i0, int i1) {
#pragma unroll
        for (int i = i0; i < i1; ++i) put(cat4(GLU(i, 6, 8), GLU(i, 7, 9)), 1, 8 + 2 * j + g, i);
      };
      if (g == 0) { glu_part(0, FA); SCHED(); BAR(); __builtin_amdgcn_s_setprio(0); asm volatile("" ::: "memory"); }
    LAS char* kim = lds + T_KV + g * KV_BYTES;
    LAS char* vim = kim + 7 * 1024;
    bf16x8 kf[4], qf[4];
#pragma unroll
    for (int i = 0; i < 4; ++i) {
      kf[i] = pack8(acc[i][2], acc[i][3]);
      *(LAS bf16x8*)(vim + ((wi * 4 + i) * 16 + fr) * 80 + fq * 16) = pack8(acc[i][4], acc[i][5]);
    }
    if (wi < 3) {
      *(LAS bf16x8*)(kim + wi * 2048 + lane * 16) = kf[2];
      *(LAS bf16x8*)(kim + wi * 2048 + 1024 + lane * 16) = kf[3];
    }
    {
      LAS char* hp = (wi < 2) ? (kim + 6 * 1024 + lane * 16 + wi * 8) : (vim + (256 + fr) * 80 + fq * 16 + (wi - 2) * 8);
      *(LAS u32x2*)hp = (u32x2){pk2(hacc[0], hacc[1]), pk2(hacc[2], hacc[3])};
    }
    WAIT_L0();
    BAR();
    asm volatile("" ::: "memory");
    const bool halo = wi == 0;
    const f32x4 bc = *(const LAS f32x4*)(lds + T_BIAS + lane * 16), bp = *(const LAS f32x4*)(lds + T_BIAS + 1024 + lane * 16);
    const f32x4 bp0 = *(const LAS f32x4*)(lds + T_BIAS + (halo ? 2048 : 1024) + lane * 16);
    const f32x4 bp1 = *(const LAS f32x4*)(lds + T_BIAS + (halo ? 3072 : 1024) + lane * 16);
    const int pf0 = halo ? 16 : wi * 4 - 2, pf1 = halo ? 16 : wi * 4 - 1;
    const bf16x8 kp0 = *(const LAS bf16x8*)(kim + (halo ? 6 : 2 * wi - 2) * 1024 + lane * 16);
    const bf16x8 kp1 = *(const LAS bf16x8*)(kim + (halo ? 6 : 2 * wi - 1) * 1024 + lane * 16);
    const LAS char* vr = vim + (4 * fq + (fr >> 2)) * 80 + (lane & 3) * 16;
    bf16x4 vt[6][2];
#pragma unroll
    for (int d = 0; d < 2; ++d) {
      vt[0][d] = trd(vr + pf0 * 1280 + d * 8); vt[1][d] = trd(vr + pf1 * 1280 + d * 8);
#pragma unroll
      for (int i = 0; i < 4; ++i) vt[2 + i][d] = trd(vr + (wi * 4 + i) * 1280 + d * 8);
    }
    f32x4 sp[4], sc[4];
#pragma unroll
    for (int i = 0; i < 4; ++i) {
      qf[i] = pack8(acc[i][0], acc[i][1]);
      sc[i] = mfma(kf[i], qf[i], bc);
    }
    sp[2] = mfma(kf[0], qf[2], bp); sp[3] = mfma(kf[1], qf[3], bp);
    sp[0] = mfma(kp0, qf[0], bp0); sp[1] = mfma(kp1, qf[1], bp1);
    const float sl = 0.17677669529663687f * 1.4426950408889634f;
    const f32x4 z4 = {0.f, 0.f, 0.f, 0.f};
    float m[4];
#pragma unroll
    for (int i = 0; i < 4; ++i)
      m[i] = fmx(fmx(fmx(fmx(sp[i][0], sp[i][1]), sp[i][2]), fmx(fmx(sp[i][3], sc[i][0]), sc[i][1])), fmx(sc[i][2], sc[i][3]));
    {
      const unsigned t = __float_as_uint(tred<true>(m[0], m[1], m[2], m[3]));
      const auto u = __builtin_amdgcn_permlane32_swap(t, t, false, false);
      const auto u0 = __builtin_amdgcn_permlane16_swap(u[0], u[0], false, false), u1 = __builtin_amdgcn_permlane16_swap(u[1], u[1], false, false);
      m[0] = __uint_as_float(u0[0]); m[1] = __uint_as_float(u0[1]); m[2] = __uint_as_float(u1[0]); m[3] = __uint_as_float(u1[1]);
    }
    const u32x4 one4 = {0x3f803f80u, 0x3f803f80u, 0x3f803f80u, 0x3f803f80u};
#pragma unroll
    for (int i = 0; i < 4; ++i) {
      const float mm = -sl * m[i];
#pragma unroll
      for (int e = 0; e < 4; ++e) {
        sp[i][e] = __builtin_amdgcn_exp2f(__builtin_fmaf(sp[i][e], sl, mm));
        sc[i][e] = __builtin_amdgcn_exp2f(__builtin_fmaf(sc[i][e], sl, mm));
      }
      const bf16x8 pfr = pack8(sp[i], sc[i]);
      const f32x4 sm = mfma(__builtin_bit_cast(bf16x8, one4), pfr, z4);
      const f32x4 o0 = mfma(cat(vt[i][0], vt[2 + i][0]), pfr, z4);
      const f32x4 o1 = mfma(cat(vt[i][1], vt[2 + i][1]), pfr, z4);
      const float inv = __builtin_amdgcn_rcpf(sm[0]);
      put(__builtin_bit_cast(u32x4, pack8(o0 * inv, o1 * inv)), 0, 2 * j + g, i);
    }
      glu_part(FA, 4);
      if (g == 1) glu_part(0, FA);
    }
  };
  stage1(std::integral_constant<bool, typeA>{});
  ST(4);

  const int lag = g ^ 1;
  GAS unsigned* hflag = (GAS unsigned*)(ws + WS_HCNT) + (mb * 4 + kq) * 256;
  const unsigned fo = (nt * 32 + (lane & 7)) * 4;
  GAS unsigned* gcnt = (GAS unsigned*)(ws + WS_GCNT) + (mb * 4 + kq) * 16;
  const int e0 = kq * 1024, e1 = ((1 ^ kq) - 1) * 1024, e2 = ((2 ^ kq) - 2) * 1024, e3 = ((3 ^ kq) - 3) * 1024;
  auto nxt = [&](int s) { ++s; if (s == own0) ++s; if (s == own1) ++s; if (s == 18) s = (own0 == 0) ? 1 : 0; return s; };
  auto wait_cnt = [&](unsigned seen, GAS unsigned* c, unsigned need) {
    if ((unsigned)__builtin_amdgcn_readfirstlane(seen) < need) {
      asm volatile("" ::: "memory");
      unsigned spins = 0;
      while ((unsigned)__builtin_amdgcn_readfirstlane(__hip_atomic_load(c, __ATOMIC_RELAXED, __HIP_MEMORY_SCOPE_AGENT)) < need)
        if (++spins > (1u << 22)) break;
    }
    asm volatile("" ::: "memory");
  };
  auto wait_flags = [&](unsigned seen, unsigned long long need) {
    if ((__builtin_amdgcn_ballot_w64(seen != 0u) & need) != need) {
      asm volatile("" ::: "memory");
      unsigned spins = 0;
      while ((__builtin_amdgcn_ballot_w64(__hip_atomic_load(hflag + (fo >> 2), __ATOMIC_RELAXED, __HIP_MEMORY_SCOPE_AGENT) != 0u) & need) != need) {
        __builtin_amdgcn_s_sleep(1);
        if (++spins > (1u << 22)) break;
      }
    }
    asm volatile("" ::: "memory");
  };
  int stp = nxt(7);
  const int mf = wi * 4 + kq;
  const unsigned orow = ((mb * 256 + 32 * (mf >> 1) + 2 * fr + (mf & 1)) * 1024 + nt * 128 + g * 64 + 16 * (fq & 1) + 8 * (fq >> 1)) * 2;
  u32x4 xr[2] = {{0u, 0u, 0u, 0u}, {0u, 0u, 0u, 0u}};
  const int rl2 = 4 * (fq & 1) + (fr >> 2);
  unsigned ao = lb + wi * 4096 + lane * 16;
  unsigned bo = lb + G2_SA + (fq >> 1) * 2048 + g * 1024 + rl2 * 128 + 32 * ((rl2 >> 1) & 3) + 8 * (lane & 3);
  f32x4 ac2[4][4];
#pragma unroll
  for (int i = 0; i < 4; ++i)
#pragma unroll
    for (int n = 0; n < 4; ++n) { ac2[i][n] = (f32x4){0.f, 0.f, 0.f, 0.f}; asm volatile("" : "+v"(ac2[i][n])); }
  ns = lb + 2 * G2_SLOT;
  const char* ca = hbase + stp * 32768 + w * 4096;
  const char* cb = brow(stp, w);
  int sl2 = 0;
  WAIT_V_IF0(0, lag);
  WAIT_L0();
  BAR();
  ST(5);
  ST_SAVE(); ST(0); ST(1);
  unsigned seen = 0;
#define PEEK(c, o) asm volatile("global_load_dword %0, %1, %2 sc1" : "+v"(seen) : "v"(o), "s"(c) : "memory")
  if (lag) BAR();

  auto phase2 = [&](auto mode, int q) {
    constexpr int MODE = decltype(mode)::value;
    bf16x8 a[2][4], bfr[2][4];
#define RA(kk, i) a[kk][i] = *(const LAS bf16x8*)(lp(ao) + (kk) * 16384 + (i) * 1024);
#define RB(kk, n) bfr[kk][n] = cat(trd(lp(bo ^ ((n) * 32)) + (kk) * 8192), trd(lp(bo ^ ((n) * 32)) + (kk) * 8192 + 4096));
    int dlt;
    asm volatile("" ::: "memory");
    if (MODE == 1 && q == 1 && w == 4) PEEK(hflag, fo);
    if (MODE == 2 && q == 16)
      asm volatile("global_load_dwordx4 %0, %2, %3\n\tglobal_load_dwordx4 %1, %2, %3 offset:64" : "+v"(xr[0]), "+v"(xr[1]) : "v"(orow), "s"(x) : "memory");
    RA(0, 0) RA(0, 1)
    if (MODE == 0) dmc_first(ns + w * 4096, va, ca + e0);
    RA(0, 2) RA(0, 3) RB(0, 0)
    if (MODE == 0) dmc_next<1024>(va, ca + e1);
    RB(0, 1) RB(0, 2) RB(0, 3)
    if (MODE == 0) dmc_next<2048>(va, ca + e2);
    RA(1, 0) RA(1, 1) RA(1, 2)
    if (MODE == 0) dmc_next<3072>(va, ca + e3);
    RA(1, 3) RB(1, 0)
    if (MODE < 2) dma_first(ns + G2_SA + w * 2048, vb, cb);
    RB(1, 1)
    if (MODE < 2) dma_next<1024>(vb, cb - 896);
    sl2 = (sl2 == 2) ? 0 : sl2 + 1; dlt = (sl2 == 0) ? -2 * G2_SLOT : G2_SLOT;
    if (MODE < 2) { stp = nxt(stp); ca = hbase + stp * 32768 + w * 4096; cb = brow(stp, w); ns = (ns == lb + 2 * G2_SLOT) ? lb : ns + G2_SLOT; }
    asm volatile("" : "+s"(ca), "+s"(cb), "+s"(ns), "+s"(sl2), "+s"(dlt), "+s"(stp) :: "memory");
    RB(1, 2) RB(1, 3)
#undef RA
#undef RB
    ao += dlt; bo += dlt;
    SCHED();
    asm volatile("" : "+v"(bo), "+v"(ao));
    if (MODE == 0) WAIT_V_IF0(6, lag ^ 1); else if (MODE == 2 && q == 16) WAIT_V_IF0(2, lag ^ 1);
    if (MODE == 1 && q == 1) {
      asm volatile("s_waitcnt vmcnt(2)" : "+v"(seen) :: "memory");
      ET(2);
      if (!lag) {
        volatile LAS unsigned* go = (volatile LAS unsigned*)(lds + T_SYNC + 4);
        if (w == 4) { wait_flags(seen, 0xf0ull); *go = 1u; }
        else { unsigned spins = 0; while (__builtin_amdgcn_readfirstlane(*go) == 0u) { __builtin_amdgcn_s_sleep(1); if (++spins > (1u << 24)) break; } }
        asm volatile("" ::: "memory");
      }
      ET(3);
      const int s2 = nxt(7), s = lag ? nxt(s2) : s2;
      const char* p = hbase + s * 32768 + wi * 8192;
      const unsigned d = lb + (lag ? 0 : 2 * G2_SLOT) + wi * 8192;
      dmc_first(d, va, p + e0); dmc_next<1024>(va, p + e1); dmc_next<2048>(va, p + e2); dmc_next<3072>(va, p + e3);
      dmc_first(d + 4096, va, p + 4096 + e0); dmc_next<1024>(va, p + 4096 + e1); dmc_next<2048>(va, p + 4096 + e2); dmc_next<3072>(va, p + 4096 + e3);
      ET(4);
    }
    WAIT_L0();
    SCHED();
    BAR();
    SCHED();
    PRIO_M();
#pragma unroll
    for (int n = 0; n < 4; ++n)
#pragma unroll
      for (int i = 0; i < 4; ++i) {
        ac2[i][n] = mfma(bfr[0][n], a[0][i], ac2[i][n]);
        SCHED();
        if (n == 3 && i == 3) {
          PRIO_T();
          if (MODE == 0) WAIT_V_IF0(6, lag); else if (MODE == 2 && q == 16) WAIT_V_IF0(2, lag);
          if (MODE == 1 && q == 1) WAIT_V_IF0(0, lag);
          if (q < 17 || !lag) BAR();
          SCHED();
        }
        ac2[i][n] = mfma(bfr[1][n], a[1][i], ac2[i][n]);
        SCHED();
        if (MODE == 1 && q == 0 && n == 1 && i == 3 && lag) {
          WAIT_V(0);
          unsigned old = 0;
          if (lane == 0) old = __hip_atomic_fetch_add((LAS unsigned*)(lds + T_SYNC), 1u, __ATOMIC_RELAXED, __HIP_MEMORY_SCOPE_WORKGROUP);
          if (__builtin_amdgcn_readfirstlane(old) == 3u && lane < 8)
            __hip_atomic_store(hflag + lane * 32 + nt, 1u, __ATOMIC_RELAXED, __HIP_MEMORY_SCOPE_AGENT);
          SCHED();
        }
      }
    PRIO_E();
    SCHED();
  };
  typedef std::integral_constant<int, 0> M0; typedef std::integral_constant<int, 1> M1; typedef std::integral_constant<int, 2> M2;
  phase2(M1{}, 0);
  ET(1);
  phase2(M1{}, 1);
  ET(5);
  ST(2);
#pragma clang loop unroll(disable)
  for (int q = 2; q < 6; ++q) phase2(M0{}, q);
  PEEK(hflag, fo);
  phase2(M0{}, 6);
  asm volatile("" : "+v"(seen));
  wait_flags(seen, 0x0full);
#pragma clang loop unroll(disable)
  for (int q = 7; q < 13; ++q) phase2(M0{}, q);
  if (w == 4 && lane == 0) __hip_atomic_fetch_add(gcnt, 1u, __ATOMIC_RELAXED, __HIP_MEMORY_SCOPE_AGENT);
#pragma clang loop unroll(disable)
  for (int q = 13; q < 16; ++q) phase2(M0{}, q);
  phase2(M2{}, 16);
  PEEK(gcnt, 0u);
  phase2(M2{}, 17);
  asm volatile("s_waitcnt vmcnt(0)" : "+v"(seen) :: "memory");
  wait_cnt(seen, gcnt, 8u);
  ST(3);
  const int tile = mb * 8 + nt;
  const auto rs = __builtin_amdgcn_make_buffer_rsrc(ws + WS_H + (size_t)mb * 4 * 36 * 16384, 0, 4 * 36 * 16384, 0x00020000);
  auto xo = [&](int src, int i) { const int u = 15 + nt * 3 + i; return (unsigned)((src * 36 + (u >= 36 ? u - 36 : u)) * 16384 + w * 2048) + lane * 16; };
#pragma unroll
  for (int i = 1; i < 4; ++i) {
    const unsigned o = xo(kq, i);
    __builtin_amdgcn_raw_buffer_store_b128(__builtin_bit_cast(u32x4, pack8(ac2[i][0], ac2[i][1])), rs, o, 0, 16);
    __builtin_amdgcn_raw_buffer_store_b128(__builtin_bit_cast(u32x4, pack8(ac2[i][2], ac2[i][3])), rs, o + 1024, 0, 16);
  }
  WAIT_V(0);
  ST(4);
  asm volatile("" : "+v"(xr[0]), "+v"(xr[1]));
  GAS unsigned* cnt = (GAS unsigned*)(ws + WS_CNT) + (tile * 32 + w) * 16;
  if (lane >= 1 && lane < 4) __hip_atomic_fetch_add(cnt + (kq ^ lane) * 128, 1u, __ATOMIC_RELAXED, __HIP_MEMORY_SCOPE_AGENT);
  {
    unsigned spins = 0;
    while ((unsigned)__builtin_amdgcn_readfirstlane(__hip_atomic_load(cnt + kq * 128, __ATOMIC_RELAXED, __HIP_MEMORY_SCOPE_AGENT)) < 3u) {
      __builtin_amdgcn_s_sleep(2);
      if (++spins > (1u << 22)) break;
    }
  }
  asm volatile("" ::: "memory");
  ST(5);
  u32x4 p[3][2];
#pragma unroll
  for (int i = 1; i < 4; ++i) {
    const unsigned o = xo(kq ^ i, i);
    p[i - 1][0] = __builtin_amdgcn_raw_buffer_load_b128(rs, o, 0, 16);
    p[i - 1][1] = __builtin_amdgcn_raw_buffer_load_b128(rs, o + 1024, 0, 16);
  }
  const auto ors = __builtin_amdgcn_make_buffer_rsrc(out, 0, 4 << 20, 0x00020000);
#pragma unroll
  for (int h = 0; h < 2; ++h) {
    const auto x0 = __builtin_amdgcn_permlane16_swap(xr[h][0], xr[h][2], false, false), x1 = __builtin_amdgcn_permlane16_swap(xr[h][1], xr[h][3], false, false);
    const u32x4 xv = {x0[0], x1[0], x0[1], x1[1]};
    unsigned r[4];
#pragma unroll
    for (int d = 0; d < 4; ++d) {
      float lo = ac2[0][2 * h + (d >> 1)][2 * (d & 1)] + __uint_as_float(xv[d] << 16), hi = ac2[0][2 * h + (d >> 1)][2 * (d & 1) + 1] + __uint_as_float(xv[d] & 0xffff0000u);
#pragma unroll
      for (int i = 0; i < 3; ++i) { lo += __uint_as_float(p[i][h][d] << 16); hi += __uint_as_float(p[i][h][d] & 0xffff0000u); }
      r[d] = pk2(lo, hi);
    }
    const auto y0 = __builtin_amdgcn_permlane16_swap(r[0], r[2], false, false), y1 = __builtin_amdgcn_permlane16_swap(r[1], r[3], false, false);
    __builtin_amdgcn_raw_buffer_store_b128((u32x4){y0[0], y1[0], y0[1], y1[1]}, ors, orow + 64 * h, 0, OUT_AUX);
  }
  ST(6);
  ST(7);
  ST_OUT(1, 8);
  ST_REST(); ST_OUT(0, 6);
}

}

extern "C" __global__ __launch_bounds__(512, 2) void psum_g1_kernel(const u16* __restrict__ x, u16* __restrict__ out, const u16* __restrict__ wqkv,
                                                                    const u16* __restrict__ wg, const u16* __restrict__ wu,
                                                                    const u16* __restrict__ wo, const u16* __restrict__ wd, char* __restrict__ ws) {
  if (blockIdx.x < 128) body<true>(x, out, wqkv, wg, wu, wo, wd, ws);
  else body<false>(x, out, wqkv, wg, wu, wo, wd, ws);
}

extern "C" void kernel_launch(void* const* d_in, const int* in_sizes, int n_in, void* d_out, int out_size, void* d_ws,
                              size_t ws_size, hipStream_t stream) {
  const u16* x = (const u16*)d_in[0];
  const u16* wqkv = (const u16*)d_in[1];
  const u16* wo = (const u16*)d_in[2];
  const u16* wg = (const u16*)d_in[4];
  const u16* wu = (const u16*)d_in[5];
  const u16* wd = (const u16*)d_in[6];
  char* ws = (char*)d_ws;
  (void)hipFuncSetAttribute((const void*)psum_g1_kernel, hipFuncAttributeMaxDynamicSharedMemorySize, G1_LDS);
  psum_ln_kernel<<<256, 320, 0, stream>>>(x, wqkv, wg, wu, ws);
  psum_g1_kernel<<<256, 512, G1_LDS, stream>>>(x, (u16*)d_out, wqkv, wg, wu, wo, wd, ws);
}
```

```cpp
#define PF_CODE 0
#include <hip/hip_runtime.h>
#include <hip/hip_bf16.h>
#include <type_traits>
#define EARLY 1
#define HALO_SKIP 1
#define PF_W2 1
#define PF_LOOP 0
#define PF_R0 192
#define PF_PER 2
#define PF_LN_ROWS 96
#define PRIO_L 0
#define PRIO_M() __builtin_amdgcn_s_setprio(1)
#define PRIO_T() __builtin_amdgcn_s_setprio(3)
#define PRIO_E() __builtin_amdgcn_s_setprio(0)
#define PRECLEAN 0
#define OUT_AUX 16
#define G2ORD 0
#define EARLY2 1
#define CHAIN 1
#define PF_X 0
#define XH 1
#define ACQ 0
#define EPI_PRIO 1
#define LORD 1
#define RPOS 1
#define FA 4
#define FB 0
#define PFD 0
#define H_AUX 16

namespace {

typedef unsigned short u16;
typedef short bf16x8 __attribute__((ext_vector_type(8)));
typedef short bf16x4 __attribute__((ext_vector_type(4)));
typedef float f32x4 __attribute__((ext_vector_type(4)));
typedef float f32x2 __attribute__((ext_vector_type(2)));
typedef unsigned u32x4 __attribute__((ext_vector_type(4)));
typedef unsigned u32x2 __attribute__((ext_vector_type(2)));
typedef __bf16 bf2 __attribute__((ext_vector_type(2)));
#define LAS __attribute__((address_space(3)))
#define GAS __attribute__((address_space(1)))

#define WAIT_V(n) asm volatile("s_waitcnt vmcnt(" #n ")" ::: "memory")
#define WAIT_V_IF0(n, c) asm volatile("s_cmp_lg_u32 %0, 0\n\ts_cbranch_scc1 1f\n\ts_waitcnt vmcnt(" #n ")\n1:" :: "s"(c) : "memory", "scc")
#define BAR_IF0(c) asm volatile("s_cmp_lg_u32 %0, 0\n\ts_cbranch_scc1 1f\n\ts_barrier\n\ts_setprio 0\n1:" :: "s"(c) : "memory", "scc")
#define WAIT_L0() do { asm volatile("" ::: "memory"); __builtin_amdgcn_s_waitcnt(0xC07F); asm volatile("" ::: "memory"); } while (0)
#define BAR() __builtin_amdgcn_s_barrier()
#define SCHED() __builtin_amdgcn_sched_barrier(0)

__device__ __forceinline__ float b2f(u16 v) { return __uint_as_float(((unsigned)v) << 16); }
__device__ __forceinline__ unsigned pk2(float lo, float hi) {
  bf2 r = {(__bf16)lo, (__bf16)hi};
  return __builtin_bit_cast(unsigned, r);
}
__device__ __forceinline__ float rbf(float f) { return (float)(__bf16)f; }
__device__ __forceinline__ bf16x8 pack8(f32x4 a, f32x4 b) {
  u32x4 r = {pk2(a[0], a[1]), pk2(a[2], a[3]), pk2(b[0], b[1]), pk2(b[2], b[3])};
  return __builtin_bit_cast(bf16x8, r);
}
__device__ __forceinline__ bf16x4 trd(LAS const char* p) {
  return __builtin_amdgcn_ds_read_tr16_b64_v4i16((LAS bf16x4*)p);
}
__device__ __forceinline__ bf16x8 cat(bf16x4 a, bf16x4 b) { return __builtin_shufflevector(a, b, 0, 1, 2, 3, 4, 5, 6, 7); }
__device__ __forceinline__ f32x4 mfma(bf16x8 a, bf16x8 b, f32x4 c) {
  return __builtin_amdgcn_mfma_f32_16x16x32_bf16(a, b, c, 0, 0, 0);
}
__device__ __forceinline__ void dma_first(unsigned lds_dst, unsigned voff, const char* b0) {
  asm volatile("s_mov_b32 m0, %0\n\ts_nop 0\n\tglobal_load_lds_dwordx4 %1, %2" :: "s"(lds_dst), "v"(voff), "s"(b0) : "memory");
}
template <int OFF>
__device__ __forceinline__ void dma_next(unsigned voff, const char* b) {
  asm volatile("global_load_lds_dwordx4 %0, %1 offset:%2" :: "v"(voff), "s"(b), "i"(OFF) : "memory");
}
__device__ __forceinline__ void dmc_first(unsigned lds_dst, unsigned voff, const char* b0) {
  asm volatile("s_mov_b32 m0, %0\n\ts_nop 0\n\tglobal_load_lds_dwordx4 %1, %2 sc1" :: "s"(lds_dst), "v"(voff), "s"(b0) : "memory");
}
template <int OFF>
__device__ __forceinline__ void dmc_next(unsigned voff, const char* b) {
  asm volatile("global_load_lds_dwordx4 %0, %1 offset:%2 sc1" :: "v"(voff), "s"(b), "i"(OFF) : "memory");
}
__device__ __forceinline__ void dma1m(unsigned lds_dst, const char* p, unsigned long long mask) {
  asm volatile("s_mov_b32 m0, %0\n\ts_mov_b64 exec, %2\n\t"
               "global_load_lds_dwordx4 %1, off\n\t"
               "s_mov_b64 exec, -1"
               :: "s"(lds_dst), "v"(p), "s"(mask) : "memory");
}
__device__ __forceinline__ void touch(unsigned lds_dst, const void* p) {
  asm volatile("s_mov_b32 m0, %0\n\ts_nop 0\n\tglobal_load_lds_dword %1, off" :: "s"(lds_dst), "v"(p) : "memory");
}
#define CODE_ADDR(dst, sym) do { unsigned lo_, hi_; asm volatile("s_getpc_b64 s[90:91]\n\ts_add_u32 %0, s90, " #sym "@rel32@lo+4\n\ts_addc_u32 %1, s91, " #sym "@rel32@hi+12" \
    : "=s"(lo_), "=s"(hi_) :: "s90", "s91", "scc"); dst = (const char*)(((unsigned long long)hi_ << 32) | lo_); } while (0)
constexpr int G1_LINES = 256;
__device__ __forceinline__ LAS char* lp(unsigned a) { return (LAS char*)(size_t)a; }
__device__ __forceinline__ float fmx(float a, float b) { return __builtin_elementwise_maximum(a, b); }
__device__ __forceinline__ float xmax(float v) {
  auto a = __builtin_amdgcn_permlane16_swap(__float_as_uint(v), __float_as_uint(v), false, false);
  v = fmaxf(__uint_as_float(a[0]), __uint_as_float(a[1]));
  auto b = __builtin_amdgcn_permlane32_swap(__float_as_uint(v), __float_as_uint(v), false, false);
  return fmaxf(__uint_as_float(b[0]), __uint_as_float(b[1]));
}
__device__ __forceinline__ float xsum(float v) {
  auto a = __builtin_amdgcn_permlane16_swap(__float_as_uint(v), __float_as_uint(v), false, false);
  v = __uint_as_float(a[0]) + __uint_as_float(a[1]);
  auto b = __builtin_amdgcn_permlane32_swap(__float_as_uint(v), __float_as_uint(v), false, false);
  return __uint_as_float(b[0]) + __uint_as_float(b[1]);
}

template <int CTRL>
__device__ __forceinline__ float dpp_add(float v) {
  return v + __uint_as_float(__builtin_amdgcn_update_dpp(0u, __float_as_uint(v), CTRL, 0xf, 0xf, true));
}
__device__ __forceinline__ float hsum(float v) {
  v = dpp_add<0xB1>(v);
  v = dpp_add<0x4E>(v);
  v = dpp_add<0x128>(v);
  return xsum(v);
}

#define DG(kid, slot)
#define ST_DECL
#define LT_DECL
#define LT(k)
#define ET(k)
#define ST(k)
#define ST_OUT(kid, n)
#define ST_SAVE()
#define ST_REST()
#define WS_BASE_MB 0
constexpr size_t WS_XN = (size_t)WS_BASE_MB << 20;
constexpr size_t WS_H = WS_XN + (size_t)4 * 1024 * 1024;
constexpr size_t H_BYTES = (size_t)8 * 4 * 36 * 16 * 1024;
constexpr size_t WS_X = WS_H + H_BYTES;
constexpr size_t X_BYTES = (size_t)64 * 4 * 3 * 8 * 2048;
constexpr size_t WS_CNT = WS_X + X_BYTES;
constexpr size_t WS_GCNT = WS_CNT + 131072;
constexpr size_t WS_HCNT = WS_GCNT + 8192;
constexpr size_t WS_ST = WS_HCNT + 32768;

}
extern "C" __global__ __launch_bounds__(320) void psum_ln_kernel(const u16* __restrict__ x, const u16* __restrict__ wqkv, const u16* __restrict__ wg,
                                                                 const u16* __restrict__ wu, char* __restrict__ ws) {
  const int lane = threadIdx.x & 63, b = blockIdx.x;
#define DGT 0
  DG(0, 0);
  if (threadIdx.x >= 256) {
    if (lane < 8) *(unsigned*)(ws + WS_CNT + (b * 8 + lane) * 64) = 0u;
    if (lane == 8 && b < 32) *(unsigned*)(ws + WS_GCNT + b * 64) = 0u;
    if (lane >= 16 && lane < 24) *(unsigned*)(ws + WS_HCNT + b * 128 + (lane - 16) * 4) = 0u;
    __shared__ __attribute__((aligned(16))) char sink[256];
    const int kq = (b & 7) >> 1, l = lane < 40 ? lane : lane - 40;
    const u16* p = l < 12 ? wqkv + (l >> 2) * 1024 + 256 * kq + (l & 3) * 64 : (l < 26 ? wg + 896 * kq + (l - 12) * 64 : wu + 896 * kq + (l - 26) * 64);
    const int ld = l < 12 ? 3072 : 3584;
#pragma unroll
    for (int i = 0; i < PF_LN_ROWS / 32; ++i) touch((unsigned)(size_t)(LAS char*)sink, p + (i * 32 + (b >> 3)) * ld);
    return;
  }
  const int hf = (b & 1) * 128 + (b >> 1);
  const int mb = hf >> 5, mf = (hf >> 1) & 15, r = (hf & 1) * 8 + (threadIdx.x >> 6) * 2 + ((lane >> 2) & 1);
  const int row = mb * 256 + 32 * (mf >> 1) + 2 * r + (mf & 1), kl = lane >> 3, ch = lane & 3;
  const u16* xr = x + row * 1024 + kl * 32 + ch * 8;
  u32x4 raw[4];
#pragma unroll
  for (int i = 0; i < 4; ++i) raw[i] = *(const u32x4*)(xr + 256 * i);
  float v[32];
#pragma unroll
  for (int i = 0; i < 16; ++i) { v[2 * i] = __uint_as_float(raw[i >> 2][i & 3] << 16); v[2 * i + 1] = __uint_as_float(raw[i >> 2][i & 3] & 0xffff0000u); }
  float s = 0.f;
#pragma unroll
  for (int i = 0; i < 32; ++i) s += v[i];
  const float mean = hsum(s) * (1.0f / 1024.0f);
  float q = 0.f;
#pragma unroll
  for (int i = 0; i < 32; ++i) { const float d = v[i] - mean; q += d * d; }
  q = hsum(q);
  const float mb16 = rbf(mean), den = rbf(sqrtf(rbf(rbf(q * (1.0f / 1024.0f)) + 1e-5f)));
  const float inv = 1.0f / den;
  const auto rs = __builtin_amdgcn_make_buffer_rsrc(ws + WS_XN, 0, 4 << 20, 0x00020000);
  const unsigned o = ((mb * 32 + kl) * 16 + mf) * 1024 + r * 64 + ((ch ^ (2 * (r >> 3))) * 16);
#pragma unroll
  for (int i = 0; i < 4; ++i) {
    u32x4 t;
#pragma unroll
    for (int e = 0; e < 4; ++e) t[e] = pk2(rbf(v[8 * i + 2 * e] - mb16) * inv, rbf(v[8 * i + 2 * e + 1] - mb16) * inv);
    __builtin_amdgcn_raw_buffer_store_b128(t, rs, o + i * 8 * 16384, 0, H_AUX);
  }
  DG(0, 1);
}
namespace {
constexpr int G1_SA = 17 * 1024, G1_SB = 20 * 1024, G1_SLOT = G1_SA + G1_SB;
constexpr int T_SCR = 159488;
constexpr int T_BIAS = T_SCR + 256;
constexpr int G1_LDS = T_BIAS + 4096;
constexpr int G2_SA = 32768, G2_SB = 16384, G2_SLOT = G2_SA + G2_SB;
constexpr int T_SYNC = T_SCR - 64;
constexpr int T_KV = 2 * G2_SLOT, KV_BYTES = 7 * 1024 + 272 * 80;

template <bool MAX>
__device__ __forceinline__ float tred(float a, float b, float c, float d) {
  const auto r1 = __builtin_amdgcn_permlane16_swap(__float_as_uint(a), __float_as_uint(b), false, false);
  const auto r2 = __builtin_amdgcn_permlane16_swap(__float_as_uint(c), __float_as_uint(d), false, false);
  const float x0 = __uint_as_float(r1[0]), x1 = __uint_as_float(r1[1]), y0 = __uint_as_float(r2[0]), y1 = __uint_as_float(r2[1]);
  const float t = MAX ? fmx(x0, x1) : x0 + x1, u = MAX ? fmx(y0, y1) : y0 + y1;
  const auto r3 = __builtin_amdgcn_permlane32_swap(__float_as_uint(t), __float_as_uint(u), false, false);
  const float z0 = __uint_as_float(r3[0]), z1 = __uint_as_float(r3[1]);
  return MAX ? fmx(z0, z1) : z0 + z1;
}
__device__ __forceinline__ u32x2 glu4(f32x4 g, f32x4 u) {
  f32x4 e = g * -1.4426950408889634f;
#pragma unroll
  for (int i = 0; i < 4; ++i) e[i] = __builtin_amdgcn_exp2f(e[i]);
  e = e + 1.0f;
#pragma unroll
  for (int i = 0; i < 4; ++i) e[i] = __builtin_amdgcn_rcpf(e[i]);
  e = e * u;
  return (u32x2){pk2(e[0], e[1]), pk2(e[2], e[3])};
}
__device__ __forceinline__ u32x4 cat4(u32x2 a, u32x2 b) { return (u32x4){a[0], a[1], b[0], b[1]}; }

template <bool typeA>
__device__ __forceinline__ void body(const u16* __restrict__ x, u16* __restrict__ out, const u16* __restrict__ wqkv, const u16* __restrict__ wg,
                                     const u16* __restrict__ wu, const u16* __restrict__ wo, const u16* __restrict__ wd, char* __restrict__ ws) {
  extern __shared__ __attribute__((aligned(16))) char smem[];
  LAS char* lds = (LAS char*)smem;
  ST_DECL LT_DECL ST(0);
  const int tid = threadIdx.x;
  int lane = tid & 63;
  const int w = __builtin_amdgcn_readfirstlane(tid >> 6), g = w >> 2, wi = w & 3;
  const int b = blockIdx.x, xcd = b & 7, idx = b >> 3;
  const int mb = (xcd & 1) * 4 + (idx & 3), kq = xcd >> 1, ntl = idx >> 2;
  const int j = ntl & 3;
  int fr = lane & 15, fq = lane >> 4;
  const int fA = 896 * kq + 64 * j;
  const int f0 = 896 * kq + 256 + 160 * j, odd = j & 1;
  const int fa = f0 + 32 * odd, fb = fa + 64, fc = odd ? f0 : f0 + 128;

  asm volatile("" : "+s"(wqkv), "+s"(wg), "+s"(wu), "+s"(ws));
  const bool isq = typeA && wi < 3;
  const int ld = isq ? 3072 : 3584;
  const u16* q1 = isq ? wqkv + (wi * 1024 + 256 * kq + 64 * j) : ((!typeA && (wi & 2)) ? wu : wg) + (typeA ? fA : ((wi & 1) ? fb : fa));
  const char* c1 = g ? (const char*)q1 : ws + WS_XN + (size_t)(mb * 32 * 16384 + wi * 4096);
  const int s1 = g ? 8 * ld - 1024 : 0, s2 = g ? 32 * ld - 2048 : 0, s3 = g ? 40 * ld - 3072 : 0;
  const int p1 = g ? 64 * ld : 16384, p2 = g ? 16384 : 64 * 3584;
  const int d1 = (g ? G1_SA : 0) + wi * 4096, d2 = g ? 16384 + wi * 256 : G1_SA + 16384 + wi * 1024;
  const unsigned long long m2 = g ? 0xffffull : ~0ull;
  const int rr = lane >> 3, pc = lane & 7, fi = (pc >> 1) ^ ((rr >> 1) & 3), kr = (rr & 3) + 8 * (rr >> 2);
  const unsigned v1 = g ? kr * ld * 2 + fi * 32 + (pc & 1) * 16 : lane * 16;
  const unsigned lb = (unsigned)(size_t)lds;
  unsigned ns = lb;
  dma_first(ns + d1, v1, c1); dma_next<1024>(v1, c1 + s1); dma_next<2048>(v1, c1 + s2); dma_next<3072>(v1, c1 + s3);
  const char* c2;
  {
    const int hr = wi * 4 + ((lane >> 2) & 3);
    const unsigned oh = (14 + (hr >> 3)) * 1024 + (8 + (hr & 7)) * 64 + (((lane & 3) ^ (2 * (hr >> 3)) ^ 2) * 16);
    const unsigned ow = (kr + 16 * (wi >> 1) + 4 * (wi & 1)) * 7168 + (typeA ? fi * 32 : (fi >> 1) * 32) + (pc & 1) * 16;
    const char* be = g ? ws + WS_XN + (size_t)((mb > 0 ? mb - 1 : 0) * 32 * 16384) : (const char*)(typeA ? wu + fA : wg + fc);
    const char* bo = g ? be : (const char*)(wu + (typeA ? fA : fc));
    c2 = ((fi & 1) ? bo : be) + (g ? oh : ow);
  }
  auto adv = [&]() { c1 += p1; c2 += p2; ns = (ns == lb + 3 * G1_SLOT) ? lb : ns + G1_SLOT; };
  auto stage = [&]() {
    dma_first(ns + d1, v1, c1); dma_next<1024>(v1, c1 + s1); dma_next<2048>(v1, c1 + s2); dma_next<3072>(v1, c1 + s3);
    dma1m(ns + d2, c2, m2);
    adv();
  };
  dma1m(ns + d2, c2, m2);
  adv();
  stage();

  f32x4 acc[4][10];
  f32x4 hacc = {0.f, 0.f, 0.f, 0.f};
#pragma unroll
  for (int i = 0; i < 4; ++i)
#pragma unroll
    for (int n = 0; n < 10; ++n) { acc[i][n] = (f32x4){0.f, 0.f, 0.f, 0.f}; asm volatile("" : "+v"(acc[i][n])); }
  const int rl = 4 * (fq & 1) + (fr >> 2);
  unsigned hoffA = lb + 16384 + fr * 64 + ((fq ^ (2 * (fr >> 3))) * 16);
  unsigned aoff = hoffA - 16384 + wi * 4096;
  unsigned boff0 = lb + G1_SA + (fq >> 1) * 2048 + rl * 128 + 32 * ((2 * g) ^ ((rl >> 1) & 3)) + 8 * (lane & 3);
  unsigned boff1 = boff0 ^ 32;
  unsigned hoffB = (boff0 ^ (32 * (wi & 1))) + (1 + (wi >> 1)) * 4096;
  asm volatile("" : "+v"(aoff), "+v"(hoffA), "+v"(boff0), "+v"(boff1), "+v"(hoffB), "+v"(hacc));
  {
    const int lo = fr + (wi == 0 ? -7 : (wi == 2 ? 1 : 9));
    const int hi = wi == 0 ? fr + 1 : ((wi >= 2 && mb == 0) ? 0 : (wi == 2 ? 8 : 16));
    f32x4 bv;
#pragma unroll
    for (int e = 0; e < 4; ++e) bv[e] = (4 * fq + e >= lo && 4 * fq + e < hi) ? 0.f : -3.0e38f;
    *(LAS f32x4*)(lds + T_BIAS + wi * 1024 + lane * 16) = bv;
    *(LAS unsigned*)(lds + T_SYNC + (lane & 1) * 4) = 0u;
  }
  ST(1);
  WAIT_V_IF0(0, g);
  BAR();
  ST(2);
  if (g == 1) { WAIT_V(0); BAR(); }

  auto step = [&](auto halo_c, auto last_c, int q) {
    constexpr bool HALO = decltype(halo_c)::value, LAST = decltype(last_c)::value;
    bf16x8 a[2][4], wn[4][2];
    asm volatile("" ::: "memory");
    auto rdq = [&](int e, int h) {
      if (e >= (HALO ? 12 : 10)) return;
      const int kk = h >> 1, t = h & 1;
      bf16x8& d = wn[e & 3][kk];
      if (e == 11) { if (t == 0) d = *(const LAS bf16x8*)(lp(hoffA) + kk * G1_SLOT); return; }
      const int n = e < 2 ? 8 + e : e - 2;
      const bf16x4 v = trd(lp(e == 10 ? hoffB : ((n & 1) ? boff1 : boff0)) + (e == 10 ? 0 : (n >> 1) * 4096) + t * 1024 + kk * G1_SLOT);
      if (t == 0) { d[0] = v[0]; d[1] = v[1]; d[2] = v[2]; d[3] = v[3]; } else { d[4] = v[0]; d[5] = v[1]; d[6] = v[2]; d[7] = v[3]; }
    };
#define RA1(kk, i) a[kk][i] = *(const LAS bf16x8*)(lp(aoff) + (kk) * G1_SLOT + (i) * 1024);
    const int dlt = (q & 1) ? -2 * G1_SLOT : 2 * G1_SLOT;
    if (!LAST) {
#pragma unroll
      for (int kk = 0; kk < 2; ++kk) {
        dma_first(ns + d1, v1, c1); RA1(kk, 0) rdq(kk, 0);
        dma_next<1024>(v1, c1 + s1); RA1(kk, 1) rdq(kk, 1);
        dma_next<2048>(v1, c1 + s2); RA1(kk, 2) rdq(kk, 2);
        dma_next<3072>(v1, c1 + s3); RA1(kk, 3) rdq(kk, 3);
        dma1m(ns + d2, c2, m2);
        adv();
      }
    } else
    {
#pragma unroll
      for (int kk = 0; kk < 2; ++kk)
#pragma unroll
        for (int i = 0; i < 4; ++i) RA1(kk, i)
#pragma unroll
      for (int h = 0; h < 8; ++h) rdq(h >> 2, h & 3);
    }
    aoff += dlt;
    SCHED();
    if (!LAST) {
    } else {
#if PF_W2 && !(PFD & 1)
      int t = w * 64 + __builtin_amdgcn_mbcnt_hi(~0u, __builtin_amdgcn_mbcnt_lo(~0u, 0u));
      const int tc = t - 288;
      t = t < 288 ? t : t - 288;
      const int r2 = (t >> 4) * 64 + (xcd & 1) * 32 + idx;
      const char* tp = (const char*)((r2 < 256 ? wo + (256 * kq + r2) * 1024 : wd + (896 * kq + r2 - 256) * 1024) + (t & 15) * 64);
      touch(lb + T_SCR, tp);
#endif
    }
    WAIT_L0();
    SCHED();
    BAR();
    SCHED();
    PRIO_M();
    auto fin = [&]() { SCHED(); PRIO_T(); if (!LAST) { if (PFD) WAIT_V(1); else WAIT_V(0); } BAR(); SCHED(); };
#pragma unroll
    for (int e = 0; e < 10; ++e) {
      const int n = e < 2 ? 8 + e : e - 2;
#pragma unroll
      for (int i = 0; i < 4; ++i) {
        acc[i][n] = mfma(wn[e & 3][0], a[0][i], acc[i][n]);
        SCHED();
        if (!HALO && e == 9 && i == 3) fin();
        SCHED();
        acc[i][n] = mfma(wn[e & 3][1], a[1][i], acc[i][n]);
        SCHED();
        if (e == 0) { rdq(2 + (i >> 1), 2 * (i & 1)); rdq(2 + (i >> 1), 2 * (i & 1) + 1); } else rdq(e + 3, i);
        if (i == 3 && e < 9) { asm volatile("" :: "v"(wn[(e + 1) & 3][0]), "v"(wn[(e + 1) & 3][1])); }
        SCHED();
      }
    }
    if (HALO) {
      hacc = mfma(wn[2][0], wn[3][0], hacc);
      WAIT_L0();
      fin();
      hacc = mfma(wn[2][1], wn[3][1], hacc);
    }
    hoffA += dlt; boff0 += dlt; boff1 += dlt; hoffB += dlt;
    PRIO_E();
    SCHED();
  };
  const int nt = ntl;
  const int kba = 8 + ((fa - 896 * kq) >> 5);
  const int own0 = typeA ? j : (kba >> 1), own1 = typeA ? 4 + j : (kba >> 1) + 1;
  const char* const hbase = ws + WS_H + (size_t)(mb * 4 + kq) * 36 * 16384;
  auto brow = [&](int stp_, int w_) {
    return (const char*)(stp_ < 4 ? wo + (size_t)(256 * kq) * 1024 : wd + (size_t)(896 * kq) * 1024 - 256 * 1024) + ((stp_ * 64 + 8 * w_) * 1024 + nt * 128) * 2;
  };
  unsigned va, vb;
  auto stage1 = [&](auto halo_c) {
    constexpr bool TA = decltype(halo_c)::value;
    typedef std::integral_constant<bool, TA || !HALO_SKIP> HC;
#pragma clang loop unroll(disable)
    for (int q = 0; q < 15; ++q) step(HC{}, std::false_type{}, q);
    step(HC{}, std::true_type{}, 15);
    ST(3);
    asm volatile("" ::: "memory");
    if (g == 0) __builtin_amdgcn_s_setprio(2); else __builtin_amdgcn_s_setprio(EPI_PRIO);
    lane = __builtin_amdgcn_mbcnt_hi(~0u, __builtin_amdgcn_mbcnt_lo(~0u, 0u));
    asm volatile("" : "+v"(lane));
    fr = lane & 15; fq = lane >> 4;
    va = lane * 16;
    vb = (lane >> 3) * 2048 + ((((lane & 7) >> 1) ^ ((lane >> 4) & 3)) * 32) + (lane & 1) * 16;
    if (g == 1) {
#pragma unroll
      for (int u = 0; u < 4; ++u) {
        const int w_ = wi * 2 + (u & 1);
        const char* p0 = brow((u >> 1) ? own1 : own0, w_);
        dma_first(lb + (u >> 1) * G2_SLOT + G2_SA + w_ * 2048, vb, p0); dma_next<1024>(vb, p0 - 896);
      }
    }
    const auto hrs = __builtin_amdgcn_make_buffer_rsrc((void*)hbase, 0, 36 * 16384, 0x00020000);
    const unsigned hv = wi * 4096 + lane * 16;
    auto put = [&](u32x4 v, int s, int kb, int i) {
      __builtin_amdgcn_raw_buffer_store_b128(v, hrs, hv + kb * 16384 + i * 1024, 0, H_AUX);
      *(LAS u32x4*)(lds + s * G2_SLOT + g * 16384 + ((wi * 4096 + kq * 1024 + lane * 16) ^ (i * 1024))) = v;
    };
#define GLU(i, a, b) glu4(acc[i][a], acc[i][b])
    if (!TA) {
      const int kbc = 8 + ((fc - 896 * kq) >> 5);
#pragma unroll
      for (int i = 0; i < 4; ++i) {
        __builtin_amdgcn_raw_buffer_store_b64(GLU(i, 8, 9), hrs, hv + kbc * 16384 + g * 8 + i * 1024, 0, H_AUX);
        put(cat4(GLU(i, 0, 4), GLU(i, 1, 5)), 0, kba + g, i);
        put(cat4(GLU(i, 2, 6), GLU(i, 3, 7)), 1, kba + 2 + g, i);
        if (i == FB) { SCHED(); BAR_IF0(g); SCHED(); }
      }
    } else {
      auto glu_part = [&](int i0, int i1) {
#pragma unroll
        for (int i = i0; i < i1; ++i) put(cat4(GLU(i, 6, 8), GLU(i, 7, 9)), 1, 8 + 2 * j + g, i);
      };
      if (g == 0) { glu_part(0, FA); SCHED(); BAR(); __builtin_amdgcn_s_setprio(0); asm volatile("" ::: "memory"); }
    LAS char* kim = lds + T_KV + g * KV_BYTES;
    LAS char* vim = kim + 7 * 1024;
    bf16x8 kf[4], qf[4];
#pragma unroll
    for (int i = 0; i < 4; ++i) {
      kf[i] = pack8(acc[i][2], acc[i][3]);
      *(LAS bf16x8*)(vim + ((wi * 4 + i) * 16 + fr) * 80 + fq * 16) = pack8(acc[i][4], acc[i][5]);
    }
    if (wi < 3) {
      *(LAS bf16x8*)(kim + wi * 2048 + lane * 16) = kf[2];
      *(LAS bf16x8*)(kim + wi * 2048 + 1024 + lane * 16) = kf[3];
    }
    {
      LAS char* hp = (wi < 2) ? (kim + 6 * 1024 + lane * 16 + wi * 8) : (vim + (256 + fr) * 80 + fq * 16 + (wi - 2) * 8);
      *(LAS u32x2*)hp = (u32x2){pk2(hacc[0], hacc[1]), pk2(hacc[2], hacc[3])};
    }
    WAIT_L0();
    BAR();
    asm volatile("" ::: "memory");
    const bool halo = wi == 0;
    const f32x4 bc = *(const LAS f32x4*)(lds + T_BIAS + lane * 16), bp = *(const LAS f32x4*)(lds + T_BIAS + 1024 + lane * 16);
    const f32x4 bp0 = *(const LAS f32x4*)(lds + T_BIAS + (halo ? 2048 : 1024) + lane * 16);
    const f32x4 bp1 = *(const LAS f32x4*)(lds + T_BIAS + (halo ? 3072 : 1024) + lane * 16);
    const int pf0 = halo ? 16 : wi * 4 - 2, pf1 = halo ? 16 : wi * 4 - 1;
    const bf16x8 kp0 = *(const LAS bf16x8*)(kim + (halo ? 6 : 2 * wi - 2) * 1024 + lane * 16);
    const bf16x8 kp1 = *(const LAS bf16x8*)(kim + (halo ? 6 : 2 * wi - 1) * 1024 + lane * 16);
    const LAS char* vr = vim + (4 * fq + (fr >> 2)) * 80 + (lane & 3) * 16;
    bf16x4 vt[6][2];
#pragma unroll
    for (int d = 0; d < 2; ++d) {
      vt[0][d] = trd(vr + pf0 * 1280 + d * 8); vt[1][d] = trd(vr + pf1 * 1280 + d * 8);
#pragma unroll
      for (int i = 0; i < 4; ++i) vt[2 + i][d] = trd(vr + (wi * 4 + i) * 1280 + d * 8);
    }
    f32x4 sp[4], sc[4];
#pragma unroll
    for (int i = 0; i < 4; ++i) {
      qf[i] = pack8(acc[i][0], acc[i][1]);
      sc[i] = mfma(kf[i], qf[i], bc);
    }
    sp[2] = mfma(kf[0], qf[2], bp); sp[3] = mfma(kf[1], qf[3], bp);
    sp[0] = mfma(kp0, qf[0], bp0); sp[1] = mfma(kp1, qf[1], bp1);
    const float sl = 0.17677669529663687f * 1.4426950408889634f;
    const f32x4 z4 = {0.f, 0.f, 0.f, 0.f};
    float m[4];
#pragma unroll
    for (int i = 0; i < 4; ++i)
      m[i] = fmx(fmx(fmx(fmx(sp[i][0], sp[i][1]), sp[i][2]), fmx(fmx(sp[i][3], sc[i][0]), sc[i][1])), fmx(sc[i][2], sc[i][3]));
    {
      const unsigned t = __float_as_uint(tred<true>(m[0], m[1], m[2], m[3]));
      const auto u = __builtin_amdgcn_permlane32_swap(t, t, false, false);
      const auto u0 = __builtin_amdgcn_permlane16_swap(u[0], u[0], false, false), u1 = __builtin_amdgcn_permlane16_swap(u[1], u[1], false, false);
      m[0] = __uint_as_float(u0[0]); m[1] = __uint_as_float(u0[1]); m[2] = __uint_as_float(u1[0]); m[3] = __uint_as_float(u1[1]);
    }
    const u32x4 one4 = {0x3f803f80u, 0x3f803f80u, 0x3f803f80u, 0x3f803f80u};
#pragma unroll
    for (int i = 0; i < 4; ++i) {
      const float mm = -sl * m[i];
#pragma unroll
      for (int e = 0; e < 4; ++e) {
        sp[i][e] = __builtin_amdgcn_exp2f(__builtin_fmaf(sp[i][e], sl, mm));
        sc[i][e] = __builtin_amdgcn_exp2f(__builtin_fmaf(sc[i][e], sl, mm));
      }
      const bf16x8 pfr = pack8(sp[i], sc[i]);
      const f32x4 sm = mfma(__builtin_bit_cast(bf16x8, one4), pfr, z4);
      const f32x4 o0 = mfma(cat(vt[i][0], vt[2 + i][0]), pfr, z4);
      const f32x4 o1 = mfma(cat(vt[i][1], vt[2 + i][1]), pfr, z4);
      const float inv = __builtin_amdgcn_rcpf(sm[0]);
      put(__builtin_bit_cast(u32x4, pack8(o0 * inv, o1 * inv)), 0, 2 * j + g, i);
    }
      glu_part(FA, 4);
      if (g == 1) glu_part(0, FA);
    }
  };
  stage1(std::integral_constant<bool, typeA>{});
  ST(4);

  const int lag = g ^ 1;
  GAS unsigned* hflag = (GAS unsigned*)(ws + WS_HCNT) + (mb * 4 + kq) * 256;
  const unsigned fo = (nt * 32 + (lane & 7)) * 4;
  GAS unsigned* gcnt = (GAS unsigned*)(ws + WS_GCNT) + (mb * 4 + kq) * 16;
  const int e0 = kq * 1024, e1 = ((1 ^ kq) - 1) * 1024, e2 = ((2 ^ kq) - 2) * 1024, e3 = ((3 ^ kq) - 3) * 1024;
  auto nxt = [&](int s) { ++s; if (s == own0) ++s; if (s == own1) ++s; if (s == 18) s = (own0 == 0) ? 1 : 0; return s; };
  auto wait_cnt = [&](unsigned seen, GAS unsigned* c, unsigned need) {
    if ((unsigned)__builtin_amdgcn_readfirstlane(seen) < need) {
      asm volatile("" ::: "memory");
      unsigned spins = 0;
      while ((unsigned)__builtin_amdgcn_readfirstlane(__hip_atomic_load(c, __ATOMIC_RELAXED, __HIP_MEMORY_SCOPE_AGENT)) < need)
        if (++spins > (1u << 22)) break;
    }
    asm volatile("" ::: "memory");
  };
  auto wait_flags = [&](unsigned seen, unsigned long long need) {
    if ((__builtin_amdgcn_ballot_w64(seen != 0u) & need) != need) {
      asm volatile("" ::: "memory");
      unsigned spins = 0;
      while ((__builtin_amdgcn_ballot_w64(__hip_atomic_load(hflag + (fo >> 2), __ATOMIC_RELAXED, __HIP_MEMORY_SCOPE_AGENT) != 0u) & need) != need) {
        __builtin_amdgcn_s_sleep(1);
        if (++spins > (1u << 22)) break;
      }
    }
    asm volatile("" ::: "memory");
  };
  int stp = nxt(7);
  const int mf = wi * 4 + kq;
  const unsigned orow = ((mb * 256 + 32 * (mf >> 1) + 2 * fr + (mf & 1)) * 1024 + nt * 128 + g * 64 + 16 * (fq & 1) + 8 * (fq >> 1)) * 2;
  u32x4 xr[2] = {{0u, 0u, 0u, 0u}, {0u, 0u, 0u, 0u}};
  const int rl2 = 4 * (fq & 1) + (fr >> 2);
  unsigned ao = lb + wi * 4096 + lane * 16;
  unsigned bo = lb + G2_SA + (fq >> 1) * 2048 + g * 1024 + rl2 * 128 + 32 * ((rl2 >> 1) & 3) + 8 * (lane & 3);
  f32x4 ac2[4][4];
#pragma unroll
  for (int i = 0; i < 4; ++i)
#pragma unroll
    for (int n = 0; n < 4; ++n) { ac2[i][n] = (f32x4){0.f, 0.f, 0.f, 0.f}; asm volatile("" : "+v"(ac2[i][n])); }
  ns = lb + 2 * G2_SLOT;
  const char* ca = hbase + stp * 32768 + w * 4096;
  const char* cb = brow(stp, w);
  int sl2 = 0;
  WAIT_V_IF0(0, lag);
  WAIT_L0();
  BAR();
  ST(5);
  ST_SAVE(); ST(0); ST(1);
  unsigned seen = 0;
#define PEEK(c, o) asm volatile("global_load_dword %0, %1, %2 sc1" : "+v"(seen) : "v"(o), "s"(c) : "memory")
  if (lag) BAR();

  auto phase2 = [&](auto mode, int q) {
    constexpr int MODE = decltype(mode)::value;
    bf16x8 a[2][4], bfr[2][4];
#define RA(kk, i) a[kk][i] = *(const LAS bf16x8*)(lp(ao) + (kk) * 16384 + (i) * 1024);
#define RB(kk, n) bfr[kk][n] = cat(trd(lp(bo ^ ((n) * 32)) + (kk) * 8192), trd(lp(bo ^ ((n) * 32)) + (kk) * 8192 + 4096));
    int dlt;
    asm volatile("" ::: "memory");
    if (MODE == 1 && q == 1 && w == 4) PEEK(hflag, fo);
    if (MODE == 2 && q == 16)
      asm volatile("global_load_dwordx4 %0, %2, %3\n\tglobal_load_dwordx4 %1, %2, %3 offset:64" : "+v"(xr[0]), "+v"(xr[1]) : "v"(orow), "s"(x) : "memory");
    RA(0, 0) RA(0, 1)
    if (MODE == 0) dmc_first(ns + w * 4096, va, ca + e0);
    RA(0, 2) RA(0, 3) RB(0, 0)
    if (MODE == 0) dmc_next<1024>(va, ca + e1);
    RB(0, 1) RB(0, 2) RB(0, 3)
    if (MODE == 0) dmc_next<2048>(va, ca + e2);
    RA(1, 0) RA(1, 1) RA(1, 2)
    if (MODE == 0) dmc_next<3072>(va, ca + e3);
    RA(1, 3) RB(1, 0)
    if (MODE < 2) dma_first(ns + G2_SA + w * 2048, vb, cb);
    RB(1, 1)
    if (MODE < 2) dma_next<1024>(vb, cb - 896);
    sl2 = (sl2 == 2) ? 0 : sl2 + 1; dlt = (sl2 == 0) ? -2 * G2_SLOT : G2_SLOT;
    if (MODE < 2) { stp = nxt(stp); ca = hbase + stp * 32768 + w * 4096; cb = brow(stp, w); ns = (ns == lb + 2 * G2_SLOT) ? lb : ns + G2_SLOT; }
    asm volatile("" : "+s"(ca), "+s"(cb), "+s"(ns), "+s"(sl2), "+s"(dlt), "+s"(stp) :: "memory");
    RB(1, 2) RB(1, 3)
#undef RA
#undef RB
    ao += dlt; bo += dlt;
    SCHED();
    asm volatile("" : "+v"(bo), "+v"(ao));
    if (MODE == 0) WAIT_V_IF0(6, lag ^ 1); else if (MODE == 2 && q == 16) WAIT_V_IF0(2, lag ^ 1);
    if (MODE == 1 && q == 1) {
      asm volatile("s_waitcnt vmcnt(2)" : "+v"(seen) :: "memory");
      ET(2);
      if (!lag) {
        volatile LAS unsigned* go = (volatile LAS unsigned*)(lds + T_SYNC + 4);
        if (w == 4) { wait_flags(seen, 0xf0ull); *go = 1u; }
        else { unsigned spins = 0; while (__builtin_amdgcn_readfirstlane(*go) == 0u) { __builtin_amdgcn_s_sleep(1); if (++spins > (1u << 24)) break; } }
        asm volatile("" ::: "memory");
      }
      ET(3);
      const int s2 = nxt(7), s = lag ? nxt(s2) : s2;
      const char* p = hbase + s * 32768 + wi * 8192;
      const unsigned d = lb + (lag ? 0 : 2 * G2_SLOT) + wi * 8192;
      dmc_first(d, va, p + e0); dmc_next<1024>(va, p + e1); dmc_next<2048>(va, p + e2); dmc_next<3072>(va, p + e3);
      dmc_first(d + 4096, va, p + 4096 + e0); dmc_next<1024>(va, p + 4096 + e1); dmc_next<2048>(va, p + 4096 + e2); dmc_next<3072>(va, p + 4096 + e3);
      ET(4);
    }
    WAIT_L0();
    SCHED();
    BAR();
    SCHED();
    PRIO_M();
#pragma unroll
    for (int n = 0; n < 4; ++n)
#pragma unroll
      for (int i = 0; i < 4; ++i) {
        ac2[i][n] = mfma(bfr[0][n], a[0][i], ac2[i][n]);
        SCHED();
        if (n == 3 && i == 3) {
          PRIO_T();
          if (MODE == 0) WAIT_V_IF0(6, lag); else if (MODE == 2 && q == 16) WAIT_V_IF0(2, lag);
          if (MODE == 1 && q == 1) WAIT_V_IF0(0, lag);
          if (q < 17 || !lag) BAR();
          SCHED();
        }
        ac2[i][n] = mfma(bfr[1][n], a[1][i], ac2[i][n]);
        SCHED();
        if (MODE == 1 && q == 0 && n == 1 && i == 3 && lag) {
          WAIT_V(0);
          unsigned old = 0;
          if (lane == 0) old = __hip_atomic_fetch_add((LAS unsigned*)(lds + T_SYNC), 1u, __ATOMIC_RELAXED, __HIP_MEMORY_SCOPE_WORKGROUP);
          if (__builtin_amdgcn_readfirstlane(old) == 3u && lane < 8)
            __hip_atomic_store(hflag + lane * 32 + nt, 1u, __ATOMIC_RELAXED, __HIP_MEMORY_SCOPE_AGENT);
          SCHED();
        }
      }
    PRIO_E();
    SCHED();
  };
  typedef std::integral_constant<int, 0> M0; typedef std::integral_constant<int, 1> M1; typedef std::integral_constant<int, 2> M2;
  phase2(M1{}, 0);
  ET(1);
  phase2(M1{}, 1);
  ET(5);
  ST(2);
#pragma clang loop unroll(disable)
  for (int q = 2; q < 6; ++q) phase2(M0{}, q);
  PEEK(hflag, fo);
  phase2(M0{}, 6);
  asm volatile("" : "+v"(seen));
  wait_flags(seen, 0x0full);
#pragma clang loop unroll(disable)
  for (int q = 7; q < 13; ++q) phase2(M0{}, q);
  if (w == 4 && lane == 0) __hip_atomic_fetch_add(gcnt, 1u, __ATOMIC_RELAXED, __HIP_MEMORY_SCOPE_AGENT);
#pragma clang loop unroll(disable)
  for (int q = 13; q < 16; ++q) phase2(M0{}, q);
  phase2(M2{}, 16);
  PEEK(gcnt, 0u);
  phase2(M2{}, 17);
  asm volatile("s_waitcnt vmcnt(0)" : "+v"(seen) :: "memory");
  wait_cnt(seen, gcnt, 8u);
  ST(3);
  const int tile = mb * 8 + nt;
  const auto rs = __builtin_amdgcn_make_buffer_rsrc(ws + WS_H + (size_t)mb * 4 * 36 * 16384, 0, 4 * 36 * 16384, 0x00020000);
  auto xo = [&](int src, int i) { const int u = 15 + nt * 3 + i; return (unsigned)((src * 36 + (u >= 36 ? u - 36 : u)) * 16384 + w * 2048) + lane * 16; };
#pragma unroll
  for (int i = 1; i < 4; ++i) {
    const unsigned o = xo(kq, i);
    __builtin_amdgcn_raw_buffer_store_b128(__builtin_bit_cast(u32x4, pack8(ac2[i][0], ac2[i][1])), rs, o, 0, 16);
    __builtin_amdgcn_raw_buffer_store_b128(__builtin_bit_cast(u32x4, pack8(ac2[i][2], ac2[i][3])), rs, o + 1024, 0, 16);
  }
  WAIT_V(0);
  ST(4);
  asm volatile("" : "+v"(xr[0]), "+v"(xr[1]));
  GAS unsigned* cnt = (GAS unsigned*)(ws + WS_CNT) + (tile * 32 + w) * 16;
  if (lane >= 1 && lane < 4) __hip_atomic_fetch_add(cnt + (kq ^ lane) * 128, 1u, __ATOMIC_RELAXED, __HIP_MEMORY_SCOPE_AGENT);
  {
    unsigned spins = 0;
    while ((unsigned)__builtin_amdgcn_readfirstlane(__hip_atomic_load(cnt + kq * 128, __ATOMIC_RELAXED, __HIP_MEMORY_SCOPE_AGENT)) < 3u) {
      __builtin_amdgcn_s_sleep(2);
      if (++spins > (1u << 22)) break;
    }
  }
  asm volatile("" ::: "memory");
  ST(5);
  u32x4 p[3][2];
#pragma unroll
  for (int i = 1; i < 4; ++i) {
    const unsigned o = xo(kq ^ i, i);
    p[i - 1][0] = __builtin_amdgcn_raw_buffer_load_b128(rs, o, 0, 16);
    p[i - 1][1] = __builtin_amdgcn_raw_buffer_load_b128(rs, o + 1024, 0, 16);
  }
  const auto ors = __builtin_amdgcn_make_buffer_rsrc(out, 0, 4 << 20, 0x00020000);
#pragma unroll
  for (int h = 0; h < 2; ++h) {
    const auto x0 = __builtin_amdgcn_permlane16_swap(xr[h][0], xr[h][2], false, false), x1 = __builtin_amdgcn_permlane16_swap(xr[h][1], xr[h][3], false, false);
    const u32x4 xv = {x0[0], x1[0], x0[1], x1[1]};
    unsigned r[4];
#pragma unroll
    for (int d = 0; d < 4; ++d) {
      float lo = ac2[0][2 * h + (d >> 1)][2 * (d & 1)] + __uint_as_float(xv[d] << 16), hi = ac2[0][2 * h + (d >> 1)][2 * (d & 1) + 1] + __uint_as_float(xv[d] & 0xffff0000u);
#pragma unroll
      for (int i = 0; i < 3; ++i) { lo += __uint_as_float(p[i][h][d] << 16); hi += __uint_as_float(p[i][h][d] & 0xffff0000u); }
      r[d] = pk2(lo, hi);
    }
    const auto y0 = __builtin_amdgcn_permlane16_swap(r[0], r[2], false, false), y1 = __builtin_amdgcn_permlane16_swap(r[1], r[3], false, false);
    __builtin_amdgcn_raw_buffer_store_b128((u32x4){y0[0], y1[0], y0[1], y1[1]}, ors, orow + 64 * h, 0, OUT_AUX);
  }
  ST(6);
  ST(7);
  ST_OUT(1, 8);
  ST_REST(); ST_OUT(0, 6);
}

}

extern "C" __global__ __launch_bounds__(512, 2) void psum_g1_kernel(const u16* __restrict__ x, u16* __restrict__ out, const u16* __restrict__ wqkv,
                                                                    const u16* __restrict__ wg, const u16* __restrict__ wu,
                                                                    const u16* __restrict__ wo, const u16* __restrict__ wd, char* __restrict__ ws) {
  if (blockIdx.x < 128) body<true>(x, out, wqkv, wg, wu, wo, wd, ws);
  else body<false>(x, out, wqkv, wg, wu, wo, wd, ws);
}

extern "C" void kernel_launch(void* const* d_in, const int* in_sizes, int n_in, void* d_out, int out_size, void* d_ws,
                              size_t ws_size, hipStream_t stream) {
  const u16* x = (const u16*)d_in[0];
  const u16* wqkv = (const u16*)d_in[1];
  const u16* wo = (const u16*)d_in[2];
  const u16* wg = (const u16*)d_in[4];
  const u16* wu = (const u16*)d_in[5];
  const u16* wd = (const u16*)d_in[6];
  char* ws = (char*)d_ws;
  (void)hipFuncSetAttribute((const void*)psum_g1_kernel, hipFuncAttributeMaxDynamicSharedMemorySize, G1_LDS);
  psum_ln_kernel<<<256, 320, 0, stream>>>(x, wqkv, wg, wu, ws);
  psum_g1_kernel<<<256, 512, G1_LDS, stream>>>(x, (u16*)d_out, wqkv, wg, wu, wo, wd, ws);
}
```

```cpp
#include <hip/hip_runtime.h>
#include <hip/hip_bf16.h>
#include <type_traits>
#define EARLY 1
#define HALO_SKIP 1
#define PF_W2 1
#define PF_LOOP 0
#define PF_R0 192
#define PF_PER 2
#define PF_LN_ROWS 96
#define PRIO_L 0
#define PRIO_M() __builtin_amdgcn_s_setprio(1)
#define PRIO_T() __builtin_amdgcn_s_setprio(3)
#define PRIO_E() __builtin_amdgcn_s_setprio(0)
#define PF_CODE 0
#define PRECLEAN 0
#define OUT_AUX 16
#define G2ORD 0
#define EARLY2 1
#define CHAIN 1
#define PF_X 0
#define XH 1
#define ACQ 0
#define EPI_PRIO 1
#define LORD 1
#define RPOS 1
#define FA 4
#define FB 0
#define PFD 0
#define SGLU 0
#define H_AUX 16

namespace {

typedef unsigned short u16;
typedef short bf16x8 __attribute__((ext_vector_type(8)));
typedef short bf16x4 __attribute__((ext_vector_type(4)));
typedef float f32x4 __attribute__((ext_vector_type(4)));
typedef float f32x2 __attribute__((ext_vector_type(2)));
typedef unsigned u32x4 __attribute__((ext_vector_type(4)));
typedef unsigned u32x2 __attribute__((ext_vector_type(2)));
typedef __bf16 bf2 __attribute__((ext_vector_type(2)));
#define LAS __attribute__((address_space(3)))
#define GAS __attribute__((address_space(1)))

#define WAIT_V(n) asm volatile("s_waitcnt vmcnt(" #n ")" ::: "memory")
#define WAIT_V_IF0(n, c) asm volatile("s_cmp_lg_u32 %0, 0\n\ts_cbranch_scc1 1f\n\ts_waitcnt vmcnt(" #n ")\n1:" :: "s"(c) : "memory", "scc")
#define BAR_IF0(c) asm volatile("s_cmp_lg_u32 %0, 0\n\ts_cbranch_scc1 1f\n\ts_barrier\n\ts_setprio 0\n1:" :: "s"(c) : "memory", "scc")
#define WAIT_L0() do { asm volatile("" ::: "memory"); __builtin_amdgcn_s_waitcnt(0xC07F); asm volatile("" ::: "memory"); } while (0)
#define BAR() __builtin_amdgcn_s_barrier()
#define SCHED() __builtin_amdgcn_sched_barrier(0)

__device__ __forceinline__ float b2f(u16 v) { return __uint_as_float(((unsigned)v) << 16); }
__device__ __forceinline__ unsigned pk2(float lo, float hi) {
  bf2 r = {(__bf16)lo, (__bf16)hi};
  return __builtin_bit_cast(unsigned, r);
}
__device__ __forceinline__ float rbf(float f) { return (float)(__bf16)f; }
__device__ __forceinline__ bf16x8 pack8(f32x4 a, f32x4 b) {
  u32x4 r = {pk2(a[0], a[1]), pk2(a[2], a[3]), pk2(b[0], b[1]), pk2(b[2], b[3])};
  return __builtin_bit_cast(bf16x8, r);
}
__device__ __forceinline__ bf16x4 trd(LAS const char* p) {
  return __builtin_amdgcn_ds_read_tr16_b64_v4i16((LAS bf16x4*)p);
}
__device__ __forceinline__ bf16x8 cat(bf16x4 a, bf16x4 b) { return __builtin_shufflevector(a, b, 0, 1, 2, 3, 4, 5, 6, 7); }
__device__ __forceinline__ f32x4 mfma(bf16x8 a, bf16x8 b, f32x4 c) {
  return __builtin_amdgcn_mfma_f32_16x16x32_bf16(a, b, c, 0, 0, 0);
}
__device__ __forceinline__ void dma_first(unsigned lds_dst, unsigned voff, const char* b0) {
  asm volatile("s_mov_b32 m0, %0\n\ts_nop 0\n\tglobal_load_lds_dwordx4 %1, %2" :: "s"(lds_dst), "v"(voff), "s"(b0) : "memory");
}
template <int OFF>
__device__ __forceinline__ void dma_next(unsigned voff, const char* b) {
  asm volatile("global_load_lds_dwordx4 %0, %1 offset:%2" :: "v"(voff), "s"(b), "i"(OFF) : "memory");
}
__device__ __forceinline__ void dmc_first(unsigned lds_dst, unsigned voff, const char* b0) {
  asm volatile("s_mov_b32 m0, %0\n\ts_nop 0\n\tglobal_load_lds_dwordx4 %1, %2 sc1" :: "s"(lds_dst), "v"(voff), "s"(b0) : "memory");
}
template <int OFF>
__device__ __forceinline__ void dmc_next(unsigned voff, const char* b) {
  asm volatile("global_load_lds_dwordx4 %0, %1 offset:%2 sc1" :: "v"(voff), "s"(b), "i"(OFF) : "memory");
}
__device__ __forceinline__ void dma1m(unsigned lds_dst, const char* p, unsigned long long mask) {
  asm volatile("s_mov_b32 m0, %0\n\ts_mov_b64 exec, %2\n\t"
               "global_load_lds_dwordx4 %1, off sc1\n\t"
               "s_mov_b64 exec, -1"
               :: "s"(lds_dst), "v"(p), "s"(mask) : "memory");
}
__device__ __forceinline__ void touch(unsigned lds_dst, const void* p) {
  asm volatile("s_mov_b32 m0, %0\n\ts_nop 0\n\tglobal_load_lds_dword %1, off" :: "s"(lds_dst), "v"(p) : "memory");
}
#define CODE_ADDR(dst, sym) do { unsigned lo_, hi_; asm volatile("s_getpc_b64 s[90:91]\n\ts_add_u32 %0, s90, " #sym "@rel32@lo+4\n\ts_addc_u32 %1, s91, " #sym "@rel32@hi+12" \
    : "=s"(lo_), "=s"(hi_) :: "s90", "s91", "scc"); dst = (const char*)(((unsigned long long)hi_ << 32) | lo_); } while (0)
constexpr int LN_LINES = 16, G1_LINES = 80, G2_LINES = 30;
__device__ __forceinline__ LAS char* lp(unsigned a) { return (LAS char*)(size_t)a; }
__device__ __forceinline__ float fmx(float a, float b) { return __builtin_elementwise_maximum(a, b); }
__device__ __forceinline__ float xmax(float v) {
  auto a = __builtin_amdgcn_permlane16_swap(__float_as_uint(v), __float_as_uint(v), false, false);
  v = fmaxf(__uint_as_float(a[0]), __uint_as_float(a[1]));
  auto b = __builtin_amdgcn_permlane32_swap(__float_as_uint(v), __float_as_uint(v), false, false);
  return fmaxf(__uint_as_float(b[0]), __uint_as_float(b[1]));
}
__device__ __forceinline__ float xsum(float v) {
  auto a = __builtin_amdgcn_permlane16_swap(__float_as_uint(v), __float_as_uint(v), false, false);
  v = __uint_as_float(a[0]) + __uint_as_float(a[1]);
  auto b = __builtin_amdgcn_permlane32_swap(__float_as_uint(v), __float_as_uint(v), false, false);
  return __uint_as_float(b[0]) + __uint_as_float(b[1]);
}

template <int CTRL>
__device__ __forceinline__ float dpp_add(float v) {
  return v + __uint_as_float(__builtin_amdgcn_update_dpp(0u, __float_as_uint(v), CTRL, 0xf, 0xf, true));
}
__device__ __forceinline__ float hsum(float v) {
  v = dpp_add<0xB1>(v);
  v = dpp_add<0x4E>(v);
  v = dpp_add<0x128>(v);
  return xsum(v);
}

#define DG(kid, slot)
#define ST_DECL
#define LT_DECL
#define LT(k)
#define ET(k)
#define ST(k)
#define ST_OUT(kid, n)
#define ST_SAVE()
#define ST_REST()
constexpr int XN_STEP = 17 * 1024;
constexpr size_t WS_XN = 0;
constexpr size_t WS_H = WS_XN + (size_t)8 * 32 * XN_STEP;
constexpr size_t H_BYTES = (size_t)8 * 4 * 36 * 16 * 1024;
constexpr size_t WS_CNT = WS_H + H_BYTES;
constexpr size_t WS_GCNT = WS_CNT + 131072;
constexpr size_t WS_HCNT = WS_GCNT + 8192;
constexpr size_t WS_LNF = WS_HCNT + 32768;
constexpr size_t WS_ST = WS_LNF + 32768;

constexpr int G1_SA = 17 * 1024, G1_SB = 20 * 1024, G1_SLOT = G1_SA + G1_SB;
constexpr int T_SCR = 159488;
constexpr int T_BIAS = T_SCR + 256;
constexpr int G1_LDS = T_BIAS + 4096;
constexpr int G2_SA = 32768, G2_SB = 16384, G2_SLOT = G2_SA + G2_SB;
constexpr int T_SYNC = T_SCR - 64;
constexpr int T_KV = 2 * G2_SLOT, KV_BYTES = 7 * 1024 + 272 * 80;

template <bool MAX>
__device__ __forceinline__ float tred(float a, float b, float c, float d) {
  const auto r1 = __builtin_amdgcn_permlane16_swap(__float_as_uint(a), __float_as_uint(b), false, false);
  const auto r2 = __builtin_amdgcn_permlane16_swap(__float_as_uint(c), __float_as_uint(d), false, false);
  const float x0 = __uint_as_float(r1[0]), x1 = __uint_as_float(r1[1]), y0 = __uint_as_float(r2[0]), y1 = __uint_as_float(r2[1]);
  const float t = MAX ? fmx(x0, x1) : x0 + x1, u = MAX ? fmx(y0, y1) : y0 + y1;
  const auto r3 = __builtin_amdgcn_permlane32_swap(__float_as_uint(t), __float_as_uint(u), false, false);
  const float z0 = __uint_as_float(r3[0]), z1 = __uint_as_float(r3[1]);
  return MAX ? fmx(z0, z1) : z0 + z1;
}
__device__ __forceinline__ u32x2 glu4(f32x4 g, f32x4 u) {
  f32x4 e = g * -1.4426950408889634f;
#pragma unroll
  for (int i = 0; i < 4; ++i) e[i] = __builtin_amdgcn_exp2f(e[i]);
  e = e + 1.0f;
#pragma unroll
  for (int i = 0; i < 4; ++i) e[i] = __builtin_amdgcn_rcpf(e[i]);
  e = e * u;
  return (u32x2){pk2(e[0], e[1]), pk2(e[2], e[3])};
}
__device__ __forceinline__ u32x2 glu4s(f32x4 g, f32x4 u) {
  float e[4];
#pragma unroll
  for (int i = 0; i < 4; ++i) { e[i] = g[i] * -1.4426950408889634f; asm volatile("" : "+v"(e[i])); e[i] = __builtin_amdgcn_exp2f(e[i]); }
#pragma unroll
  for (int i = 0; i < 4; ++i) { e[i] = e[i] + 1.0f; asm volatile("" : "+v"(e[i])); e[i] = __builtin_amdgcn_rcpf(e[i]); }
#pragma unroll
  for (int i = 0; i < 4; ++i) { e[i] = e[i] * u[i]; asm volatile("" : "+v"(e[i])); }
  return (u32x2){pk2(e[0], e[1]), pk2(e[2], e[3])};
}
__device__ __forceinline__ u32x4 cat4(u32x2 a, u32x2 b) { return (u32x4){a[0], a[1], b[0], b[1]}; }

template <bool typeA>
__device__ __forceinline__ void body(const u16* __restrict__ x, u16* __restrict__ out, const u16* __restrict__ wqkv, const u16* __restrict__ wg,
                                     const u16* __restrict__ wu, const u16* __restrict__ wo, const u16* __restrict__ wd, char* __restrict__ ws) {
  extern __shared__ __attribute__((aligned(16))) char smem[];
  LAS char* lds = (LAS char*)smem;
  ST_DECL LT_DECL ST(0);
  const int tid = threadIdx.x;
  int lane = tid & 63;
  const int w = __builtin_amdgcn_readfirstlane(tid >> 6), g = w >> 2, wi = w & 3;
  const int b = blockIdx.x, xcd = b & 7, idx = b >> 3;
  const int mb = (xcd & 1) * 4 + (idx & 3), kq = xcd >> 1, ntl = idx >> 2;
  const int j = ntl & 3;
  int fr = lane & 15, fq = lane >> 4;
  const int fA = 896 * kq + 64 * j;
  const int f0 = 896 * kq + 256 + 160 * j, odd = j & 1;
  const int fa = f0 + 32 * odd, fb = fa + 64, fc = odd ? f0 : f0 + 128;

  asm volatile("" : "+s"(wqkv), "+s"(wg), "+s"(wu), "+s"(ws), "+s"(x));
  const int mi = kq * 8 + ntl;
  const bool lnh = !typeA && w == 4 && j < 2, lnw = g == 0 || lnh;
  const int lr = (lnh ? (kq * 2 + j) * 2 : (mi & 1) * 8 + wi * 2) + ((lane >> 2) & 1), lf = lnh ? 16 : mi >> 1;
  const int lrow = lnh ? (mb > 0 ? mb * 256 - 16 : 0) + 2 * (lr & 7) + (lr >> 3) : mb * 256 + 32 * (lf >> 1) + 2 * lr + (lf & 1);
  u32x4 raw[4] = {{0u, 0u, 0u, 0u}, {0u, 0u, 0u, 0u}, {0u, 0u, 0u, 0u}, {0u, 0u, 0u, 0u}};
  asm volatile("s_cmp_eq_u32 %6, 0\n\ts_cbranch_scc1 1f\n\tglobal_load_dwordx4 %0, %4, %5\n\tglobal_load_dwordx4 %1, %4, %5 offset:512\n\t"
               "global_load_dwordx4 %2, %4, %5 offset:1024\n\tglobal_load_dwordx4 %3, %4, %5 offset:1536\n1:"
               : "+v"(raw[0]), "+v"(raw[1]), "+v"(raw[2]), "+v"(raw[3])
               : "v"(lrow * 2048 + (lane >> 3) * 64 + (lane & 3) * 16), "s"(x), "s"(__builtin_amdgcn_readfirstlane(lnw ? 1 : 0)) : "memory", "scc");
  GAS unsigned* lnf = (GAS unsigned*)(ws + WS_LNF) + mb * 1024 + (lane < 32 ? mi * 32 + lane : (lane - 32) * 32 + mi);
  unsigned entry = 0;
  asm volatile("s_cmp_lg_u32 %2, 7\n\ts_cbranch_scc1 1f\n\tglobal_load_dword %0, %1, off sc1\n1:" : "+v"(entry) : "v"(lnf), "s"(w) : "memory", "scc");

  const bool isq = typeA && wi < 3;
  const int ld = isq ? 3072 : 3584;
  const u16* q1 = isq ? wqkv + (wi * 1024 + 256 * kq + 64 * j) : ((!typeA && (wi & 2)) ? wu : wg) + (typeA ? fA : ((wi & 1) ? fb : fa));
  const char* const xnb = ws + WS_XN + (size_t)mb * 32 * XN_STEP;
  const char* c1 = g ? (const char*)q1 : xnb + wi * 4096;
  const int s1 = g ? 8 * ld - 1024 : 0, s2 = g ? 32 * ld - 2048 : 0, s3 = g ? 40 * ld - 3072 : 0;
  const int p1 = g ? 64 * ld : XN_STEP, p2 = g ? XN_STEP : 64 * 3584;
  const int d1 = (g ? G1_SA : 0) + wi * 4096, d2 = g ? 16384 + wi * 256 : G1_SA + 16384 + wi * 1024;
  const unsigned long long m2 = g ? 0xffffull : ~0ull;
  const int rr = lane >> 3, pc = lane & 7, fi = (pc >> 1) ^ ((rr >> 1) & 3), kr = (rr & 3) + 8 * (rr >> 2);
  const unsigned v1 = g ? kr * ld * 2 + fi * 32 + (pc & 1) * 16 : lane * 16;
  const unsigned lb = (unsigned)(size_t)lds;
  const char* c2;
  {
    const unsigned ow = (kr + 16 * (wi >> 1) + 4 * (wi & 1)) * 7168 + (typeA ? fi * 32 : (fi >> 1) * 32) + (pc & 1) * 16;
    const char* be = g ? xnb : (const char*)(typeA ? wu + fA : wg + fc);
    const char* bo = g ? be : (const char*)(wu + (typeA ? fA : fc));
    c2 = ((fi & 1) ? bo : be) + (g ? 16384 + wi * 256 + (lane & 15) * 16 : ow);
  }
  auto chain1 = [&](unsigned dst, const char* p) { dmc_first(dst + d1, v1, p); dmc_next<1024>(v1, p + s1); dmc_next<2048>(v1, p + s2); dmc_next<3072>(v1, p + s3); };
  auto first = [&](bool one) {
    if (one) { chain1(lb, c1); chain1(lb + G1_SLOT, c1 + p1); }
    else { dma1m(lb + d2, c2, m2); dma1m(lb + G1_SLOT + d2, c2 + p2, m2); }
  };
  if (w == 3) {
    char* z = lane < 8 ? ws + WS_CNT + (((mb * 8 + ntl) * 4 + kq) * 8 + lane) * 64
            : (lane < 10 ? ws + WS_HCNT + ((mb * 4 + kq) * 8 + ntl) * 128 + (lane - 8) * 16 : ws + WS_GCNT + (mb * 4 + kq) * 64);
    if (lane < (ntl == 0 ? 11 : 10)) asm volatile("global_store_dwordx4 %0, %1, off sc1\n\ts_nop 1" :: "v"(z), "v"((u32x4){0u, 0u, 0u, 0u}) : "memory");
  }
  first(g == 1);
  if (lnw) {
    WAIT_V(8);
    WAIT_V_IF0(2, g);
    asm volatile("" : "+v"(raw[0]), "+v"(raw[1]), "+v"(raw[2]), "+v"(raw[3]));
    float v[32];
#pragma unroll
    for (int i = 0; i < 16; ++i) { v[2 * i] = __uint_as_float(raw[i >> 2][i & 3] << 16); v[2 * i + 1] = __uint_as_float(raw[i >> 2][i & 3] & 0xffff0000u); }
    float s = 0.f;
#pragma unroll
    for (int i = 0; i < 32; ++i) s += v[i];
    const float mean = hsum(s) * (1.0f / 1024.0f);
    float q = 0.f;
#pragma unroll
    for (int i = 0; i < 32; ++i) { const float d = v[i] - mean; q += d * d; }
    q = hsum(q);
    const float mb16 = rbf(mean), den = rbf(sqrtf(rbf(rbf(q * (1.0f / 1024.0f)) + 1e-5f)));
    const float inv = 1.0f / den;
    const auto xrs = __builtin_amdgcn_make_buffer_rsrc((void*)xnb, 0, 32 * XN_STEP, 0x00020000);
    const unsigned o = (lane >> 3) * XN_STEP + lf * 1024 + lr * 64 + (((lane & 3) ^ (2 * (lr >> 3))) * 16);
#pragma unroll
    for (int i = 0; i < 4; ++i) {
      u32x4 t;
#pragma unroll
      for (int e = 0; e < 4; ++e) t[e] = pk2(rbf(v[8 * i + 2 * e] - mb16) * inv, rbf(v[8 * i + 2 * e + 1] - mb16) * inv);
      __builtin_amdgcn_raw_buffer_store_b128(t, xrs, o + i * 8 * XN_STEP, 0, 16);
    }
  }

  f32x4 acc[4][10];
  f32x4 hacc = {0.f, 0.f, 0.f, 0.f};
#pragma unroll
  for (int i = 0; i < 4; ++i)
#pragma unroll
    for (int n = 0; n < 10; ++n) { acc[i][n] = (f32x4){0.f, 0.f, 0.f, 0.f}; asm volatile("" : "+v"(acc[i][n])); }
  const int rl = 4 * (fq & 1) + (fr >> 2);
  unsigned hoffA = lb + 16384 + fr * 64 + ((fq ^ (2 * (fr >> 3))) * 16);
  unsigned aoff = hoffA - 16384 + wi * 4096;
  unsigned boff0 = lb + G1_SA + (fq >> 1) * 2048 + rl * 128 + 32 * ((2 * g) ^ ((rl >> 1) & 3)) + 8 * (lane & 3);
  unsigned boff1 = boff0 ^ 32;
  unsigned hoffB = (boff0 ^ (32 * (wi & 1))) + (1 + (wi >> 1)) * 4096;
  asm volatile("" : "+v"(aoff), "+v"(hoffA), "+v"(boff0), "+v"(boff1), "+v"(hoffB), "+v"(hacc));
  {
    const int lo = fr + (wi == 0 ? -7 : (wi == 2 ? 1 : 9));
    const int hi = wi == 0 ? fr + 1 : ((wi >= 2 && mb == 0) ? 0 : (wi == 2 ? 8 : 16));
    f32x4 bv;
#pragma unroll
    for (int e = 0; e < 4; ++e) bv[e] = (4 * fq + e >= lo && 4 * fq + e < hi) ? 0.f : -3.0e38f;
    *(LAS f32x4*)(lds + T_BIAS + wi * 1024 + lane * 16) = bv;
    *(LAS unsigned*)(lds + T_SYNC + (lane & 1) * 4) = 0u;
  }
  asm volatile("s_waitcnt vmcnt(8)" : "+v"(entry) :: "memory");
  if (lnw) WAIT_V(0);
  BAR();
  ST(1);
  if (w == 7) {
    if (lane >= 32) __hip_atomic_store(lnf, entry + 1u, __ATOMIC_RELAXED, __HIP_MEMORY_SCOPE_AGENT);
    for (unsigned spins = 0; spins < (1u << 22); ++spins) {
      unsigned now = entry + 1u;
      if (lane < 32) now = __hip_atomic_load(lnf, __ATOMIC_RELAXED, __HIP_MEMORY_SCOPE_AGENT);
      if (__builtin_amdgcn_ballot_w64(now == entry) == 0ull) break;
      __builtin_amdgcn_s_sleep(1);
    }
    if (lane >= 32) __hip_atomic_store(lnf, entry + 2u, __ATOMIC_RELAXED, __HIP_MEMORY_SCOPE_AGENT);
  }
  asm volatile("" ::: "memory");
  BAR();
  first(g == 0);
  c1 += 2 * p1; c2 += 2 * p2;
  unsigned ns = lb + 2 * G1_SLOT;
  auto adv = [&]() { c1 += p1; c2 += p2; ns = (ns == lb + 3 * G1_SLOT) ? lb : ns + G1_SLOT; };
  WAIT_V_IF0(0, g);
  BAR();
  ST(2);
  if (g == 1) { WAIT_V(0); BAR(); }

  auto step = [&](auto halo_c, auto last_c, int q) {
    constexpr bool HALO = decltype(halo_c)::value, LAST = decltype(last_c)::value;
    bf16x8 a[2][4], wn[4][2];
    asm volatile("" ::: "memory");
    auto rdq = [&](int e, int h) {
      if (e >= (HALO ? 12 : 10)) return;
      const int kk = h >> 1, t = h & 1;
      bf16x8& d = wn[e & 3][kk];
      if (e == 11) { if (t == 0) d = *(const LAS bf16x8*)(lp(hoffA) + kk * G1_SLOT); return; }
      const int n = e < 2 ? 8 + e : e - 2;
      const bf16x4 v = trd(lp(e == 10 ? hoffB : ((n & 1) ? boff1 : boff0)) + (e == 10 ? 0 : (n >> 1) * 4096) + t * 1024 + kk * G1_SLOT);
      if (t == 0) { d[0] = v[0]; d[1] = v[1]; d[2] = v[2]; d[3] = v[3]; } else { d[4] = v[0]; d[5] = v[1]; d[6] = v[2]; d[7] = v[3]; }
    };
#define RA1(kk, i) a[kk][i] = *(const LAS bf16x8*)(lp(aoff) + (kk) * G1_SLOT + (i) * 1024);
    const int dlt = (q & 1) ? -2 * G1_SLOT : 2 * G1_SLOT;
    if (!LAST) {
#pragma unroll
      for (int kk = 0; kk < 2; ++kk) {
        dmc_first(ns + d1, v1, c1); RA1(kk, 0) rdq(kk, 0);
        dmc_next<1024>(v1, c1 + s1); RA1(kk, 1) rdq(kk, 1);
        dmc_next<2048>(v1, c1 + s2); RA1(kk, 2) rdq(kk, 2);
        dmc_next<3072>(v1, c1 + s3); RA1(kk, 3) rdq(kk, 3);
        dma1m(ns + d2, c2, m2);
        adv();
      }
    } else
    {
#pragma unroll
      for (int kk = 0; kk < 2; ++kk)
#pragma unroll
        for (int i = 0; i < 4; ++i) RA1(kk, i)
#pragma unroll
      for (int h = 0; h < 8; ++h) rdq(h >> 2, h & 3);
    }
    aoff += dlt;
    SCHED();
    if (!LAST) {
    } else {
#if PF_W2 && !(PFD & 1)
      int t = w * 64 + __builtin_amdgcn_mbcnt_hi(~0u, __builtin_amdgcn_mbcnt_lo(~0u, 0u));
      t = t < 288 ? t : t - 288;
      const int r2 = (t >> 4) * 64 + (xcd & 1) * 32 + idx;
      touch(lb + T_SCR, (r2 < 256 ? wo + (256 * kq + r2) * 1024 : wd + (896 * kq + r2 - 256) * 1024) + (t & 15) * 64);
#endif
    }
    WAIT_L0();
    SCHED();
    BAR();
    SCHED();
    PRIO_M();
    auto fin = [&]() { SCHED(); PRIO_T(); if (!LAST) { if (PFD) WAIT_V(1); else WAIT_V(0); } BAR(); SCHED(); };
#pragma unroll
    for (int e = 0; e < 10; ++e) {
      const int n = e < 2 ? 8 + e : e - 2;
#pragma unroll
      for (int i = 0; i < 4; ++i) {
        acc[i][n] = mfma(wn[e & 3][0], a[0][i], acc[i][n]);
        SCHED();
        if (!HALO && e == 9 && i == 3) fin();
        SCHED();
        acc[i][n] = mfma(wn[e & 3][1], a[1][i], acc[i][n]);
        SCHED();
        if (e == 0) { rdq(2 + (i >> 1), 2 * (i & 1)); rdq(2 + (i >> 1), 2 * (i & 1) + 1); } else rdq(e + 3, i);
        if (i == 3 && e < 9) { asm volatile("" :: "v"(wn[(e + 1) & 3][0]), "v"(wn[(e + 1) & 3][1])); }
        SCHED();
      }
    }
    if (HALO) {
      hacc = mfma(wn[2][0], wn[3][0], hacc);
      WAIT_L0();
      fin();
      hacc = mfma(wn[2][1], wn[3][1], hacc);
    }
    hoffA += dlt; boff0 += dlt; boff1 += dlt; hoffB += dlt;
    PRIO_E();
    SCHED();
  };
  const int nt = ntl;
  const int kba = 8 + ((fa - 896 * kq) >> 5);
  const int own0 = typeA ? j : (kba >> 1), own1 = typeA ? 4 + j : (kba >> 1) + 1;
  const char* const hbase = ws + WS_H + (size_t)(mb * 4 + kq) * 36 * 16384;
  auto brow = [&](int stp_, int w_) {
    return (const char*)(stp_ < 4 ? wo + (size_t)(256 * kq) * 1024 : wd + (size_t)(896 * kq) * 1024 - 256 * 1024) + ((stp_ * 64 + 8 * w_) * 1024 + nt * 128) * 2;
  };
  unsigned va, vb;
  auto stage1 = [&](auto halo_c) {
    constexpr bool TA = decltype(halo_c)::value;
    typedef std::integral_constant<bool, TA || !HALO_SKIP> HC;
#pragma clang loop unroll(disable)
    for (int q = 0; q < 15; ++q) step(HC{}, std::false_type{}, q);
    step(HC{}, std::true_type{}, 15);
    ST(3);
    asm volatile("" ::: "memory");
    if (g == 0) __builtin_amdgcn_s_setprio(2); else __builtin_amdgcn_s_setprio(EPI_PRIO);
    lane = __builtin_amdgcn_mbcnt_hi(~0u, __builtin_amdgcn_mbcnt_lo(~0u, 0u));
    asm volatile("" : "+v"(lane));
    fr = lane & 15; fq = lane >> 4;
    va = lane * 16;
    vb = (lane >> 3) * 2048 + ((((lane & 7) >> 1) ^ ((lane >> 4) & 3)) * 32) + (lane & 1) * 16;
    if (g == 1) {
#pragma unroll
      for (int u = 0; u < 4; ++u) {
        const int w_ = wi * 2 + (u & 1);
        const char* p0 = brow((u >> 1) ? own1 : own0, w_);
        dma_first(lb + (u >> 1) * G2_SLOT + G2_SA + w_ * 2048, vb, p0); dma_next<1024>(vb, p0 - 896);
      }
    }
    const auto hrs = __builtin_amdgcn_make_buffer_rsrc((void*)hbase, 0, 36 * 16384, 0x00020000);
    const unsigned hv = wi * 4096 + lane * 16;
    auto put = [&](u32x4 v, int s, int kb, int i) {
      __builtin_amdgcn_raw_buffer_store_b128(v, hrs, hv + kb * 16384 + i * 1024, 0, H_AUX);
      *(LAS u32x4*)(lds + s * G2_SLOT + g * 16384 + ((wi * 4096 + kq * 1024 + lane * 16) ^ (i * 1024))) = v;
    };
#define GLU(i, a, b) glu4(acc[i][a], acc[i][b])
    if (!TA) {
      const int kbc = 8 + ((fc - 896 * kq) >> 5);
#pragma unroll
      for (int i = 0; i < 4; ++i) {
        __builtin_amdgcn_raw_buffer_store_b64(GLU(i, 8, 9), hrs, hv + kbc * 16384 + g * 8 + i * 1024, 0, H_AUX);
        put(cat4(GLU(i, 0, 4), GLU(i, 1, 5)), 0, kba + g, i);
        put(cat4(GLU(i, 2, 6), GLU(i, 3, 7)), 1, kba + 2 + g, i);
        if (i == FB) { SCHED(); BAR_IF0(g); SCHED(); }
      }
    } else {
      auto glu_part = [&](int i0, int i1) {
#pragma unroll
        for (int i = i0; i < i1; ++i) put(cat4(GLU(i, 6, 8), GLU(i, 7, 9)), 1, 8 + 2 * j + g, i);
      };
      if (g == 0) { glu_part(0, FA); SCHED(); BAR(); __builtin_amdgcn_s_setprio(0); asm volatile("" ::: "memory"); }
    LAS char* kim = lds + T_KV + g * KV_BYTES;
    LAS char* vim = kim + 7 * 1024;
    bf16x8 kf[4], qf[4];
#pragma unroll
    for (int i = 0; i < 4; ++i) {
      kf[i] = pack8(acc[i][2], acc[i][3]);
      *(LAS bf16x8*)(vim + ((wi * 4 + i) * 16 + fr) * 80 + fq * 16) = pack8(acc[i][4], acc[i][5]);
    }
    if (wi < 3) {
      *(LAS bf16x8*)(kim + wi * 2048 + lane * 16) = kf[2];
      *(LAS bf16x8*)(kim + wi * 2048 + 1024 + lane * 16) = kf[3];
    }
    {
      LAS char* hp = (wi < 2) ? (kim + 6 * 1024 + lane * 16 + wi * 8) : (vim + (256 + fr) * 80 + fq * 16 + (wi - 2) * 8);
      *(LAS u32x2*)hp = (u32x2){pk2(hacc[0], hacc[1]), pk2(hacc[2], hacc[3])};
    }
    WAIT_L0();
    BAR();
    asm volatile("" ::: "memory");
    const bool halo = wi == 0;
    const f32x4 bc = *(const LAS f32x4*)(lds + T_BIAS + lane * 16), bp = *(const LAS f32x4*)(lds + T_BIAS + 1024 + lane * 16);
    const f32x4 bp0 = *(const LAS f32x4*)(lds + T_BIAS + (halo ? 2048 : 1024) + lane * 16);
    const f32x4 bp1 = *(const LAS f32x4*)(lds + T_BIAS + (halo ? 3072 : 1024) + lane * 16);
    const int pf0 = halo ? 16 : wi * 4 - 2, pf1 = halo ? 16 : wi * 4 - 1;
    const bf16x8 kp0 = *(const LAS bf16x8*)(kim + (halo ? 6 : 2 * wi - 2) * 1024 + lane * 16);
    const bf16x8 kp1 = *(const LAS bf16x8*)(kim + (halo ? 6 : 2 * wi - 1) * 1024 + lane * 16);
    const LAS char* vr = vim + (4 * fq + (fr >> 2)) * 80 + (lane & 3) * 16;
    bf16x4 vt[6][2];
#pragma unroll
    for (int d = 0; d < 2; ++d) {
      vt[0][d] = trd(vr + pf0 * 1280 + d * 8); vt[1][d] = trd(vr + pf1 * 1280 + d * 8);
#pragma unroll
      for (int i = 0; i < 4; ++i) vt[2 + i][d] = trd(vr + (wi * 4 + i) * 1280 + d * 8);
    }
    f32x4 sp[4], sc[4];
#pragma unroll
    for (int i = 0; i < 4; ++i) {
      qf[i] = pack8(acc[i][0], acc[i][1]);
      sc[i] = mfma(kf[i], qf[i], bc);
    }
    sp[2] = mfma(kf[0], qf[2], bp); sp[3] = mfma(kf[1], qf[3], bp);
    sp[0] = mfma(kp0, qf[0], bp0); sp[1] = mfma(kp1, qf[1], bp1);
    const float sl = 0.17677669529663687f * 1.4426950408889634f;
    const f32x4 z4 = {0.f, 0.f, 0.f, 0.f};
    float m[4];
#pragma unroll
    for (int i = 0; i < 4; ++i)
      m[i] = fmx(fmx(fmx(fmx(sp[i][0], sp[i][1]), sp[i][2]), fmx(fmx(sp[i][3], sc[i][0]), sc[i][1])), fmx(sc[i][2], sc[i][3]));
    {
      const unsigned t = __float_as_uint(tred<true>(m[0], m[1], m[2], m[3]));
      const auto u = __builtin_amdgcn_permlane32_swap(t, t, false, false);
      const auto u0 = __builtin_amdgcn_permlane16_swap(u[0], u[0], false, false), u1 = __builtin_amdgcn_permlane16_swap(u[1], u[1], false, false);
      m[0] = __uint_as_float(u0[0]); m[1] = __uint_as_float(u0[1]); m[2] = __uint_as_float(u1[0]); m[3] = __uint_as_float(u1[1]);
    }
    const u32x4 one4 = {0x3f803f80u, 0x3f803f80u, 0x3f803f80u, 0x3f803f80u};
#pragma unroll
    for (int i = 0; i < 4; ++i) {
      const float mm = -sl * m[i];
#pragma unroll
      for (int e = 0; e < 4; ++e) {
        sp[i][e] = __builtin_amdgcn_exp2f(__builtin_fmaf(sp[i][e], sl, mm));
        sc[i][e] = __builtin_amdgcn_exp2f(__builtin_fmaf(sc[i][e], sl, mm));
      }
      const bf16x8 pfr = pack8(sp[i], sc[i]);
      const f32x4 sm = mfma(__builtin_bit_cast(bf16x8, one4), pfr, z4);
      const f32x4 o0 = mfma(cat(vt[i][0], vt[2 + i][0]), pfr, z4);
      const f32x4 o1 = mfma(cat(vt[i][1], vt[2 + i][1]), pfr, z4);
      const float inv = __builtin_amdgcn_rcpf(sm[0]);
      put(__builtin_bit_cast(u32x4, pack8(o0 * inv, o1 * inv)), 0, 2 * j + g, i);
    }
      glu_part(FA, 4);
      if (g == 1) glu_part(0, FA);
    }
  };
  stage1(std::integral_constant<bool, typeA>{});
  ST(4);

  const int lag = g ^ 1;
  GAS unsigned* hflag = (GAS unsigned*)(ws + WS_HCNT) + (mb * 4 + kq) * 256;
  const unsigned fo = (nt * 32 + (lane & 7)) * 4;
  GAS unsigned* gcnt = (GAS unsigned*)(ws + WS_GCNT) + (mb * 4 + kq) * 16;
  const int e0 = kq * 1024, e1 = ((1 ^ kq) - 1) * 1024, e2 = ((2 ^ kq) - 2) * 1024, e3 = ((3 ^ kq) - 3) * 1024;
  auto nxt = [&](int s) { ++s; if (s == own0) ++s; if (s == own1) ++s; if (s == 18) s = (own0 == 0) ? 1 : 0; return s; };
  auto wait_cnt = [&](unsigned seen, GAS unsigned* c, unsigned need) {
    if ((unsigned)__builtin_amdgcn_readfirstlane(seen) < need) {
      asm volatile("" ::: "memory");
      unsigned spins = 0;
      while ((unsigned)__builtin_amdgcn_readfirstlane(__hip_atomic_load(c, __ATOMIC_RELAXED, __HIP_MEMORY_SCOPE_AGENT)) < need)
        if (++spins > (1u << 22)) break;
    }
    asm volatile("" ::: "memory");
  };
  auto wait_flags = [&](unsigned seen, unsigned long long need) {
    if ((__builtin_amdgcn_ballot_w64(seen != 0u) & need) != need) {
      asm volatile("" ::: "memory");
      unsigned spins = 0;
      while ((__builtin_amdgcn_ballot_w64(__hip_atomic_load(hflag + (fo >> 2), __ATOMIC_RELAXED, __HIP_MEMORY_SCOPE_AGENT) != 0u) & need) != need) {
        __builtin_amdgcn_s_sleep(1);
        if (++spins > (1u << 22)) break;
      }
    }
    asm volatile("" ::: "memory");
  };
  int stp = nxt(7);
  const int mf = wi * 4 + kq;
  const unsigned orow = ((mb * 256 + 32 * (mf >> 1) + 2 * fr + (mf & 1)) * 1024 + nt * 128 + g * 64 + 16 * (fq & 1) + 8 * (fq >> 1)) * 2;
  u32x4 xr[2] = {{0u, 0u, 0u, 0u}, {0u, 0u, 0u, 0u}};
  const int rl2 = 4 * (fq & 1) + (fr >> 2);
  unsigned ao = lb + wi * 4096 + lane * 16;
  unsigned bo = lb + G2_SA + (fq >> 1) * 2048 + g * 1024 + rl2 * 128 + 32 * ((rl2 >> 1) & 3) + 8 * (lane & 3);
  f32x4 ac2[4][4];
#pragma unroll
  for (int i = 0; i < 4; ++i)
#pragma unroll
    for (int n = 0; n < 4; ++n) { ac2[i][n] = (f32x4){0.f, 0.f, 0.f, 0.f}; asm volatile("" : "+v"(ac2[i][n])); }
  ns = lb + 2 * G2_SLOT;
  const char* ca = hbase + stp * 32768 + w * 4096;
  const char* cb = brow(stp, w);
  int sl2 = 0;
  WAIT_V_IF0(0, lag);
  WAIT_L0();
  BAR();
  ST(5);
  ST_SAVE(); ST(0); ST(1);
  unsigned seen = 0;
#define PEEK(c, o) asm volatile("global_load_dword %0, %1, %2 sc1" : "+v"(seen) : "v"(o), "s"(c) : "memory")
  if (lag) BAR();

  auto phase2 = [&](auto mode, int q) {
    constexpr int MODE = decltype(mode)::value;
    bf16x8 a[2][4], bfr[2][4];
#define RA(kk, i) a[kk][i] = *(const LAS bf16x8*)(lp(ao) + (kk) * 16384 + (i) * 1024);
#define RB(kk, n) bfr[kk][n] = cat(trd(lp(bo ^ ((n) * 32)) + (kk) * 8192), trd(lp(bo ^ ((n) * 32)) + (kk) * 8192 + 4096));
    int dlt;
    asm volatile("" ::: "memory");
    if (MODE == 1 && q == 1 && w == 4) PEEK(hflag, fo);
    if (MODE == 2 && q == 16)
      asm volatile("global_load_dwordx4 %0, %2, %3\n\tglobal_load_dwordx4 %1, %2, %3 offset:64" : "+v"(xr[0]), "+v"(xr[1]) : "v"(orow), "s"(x) : "memory");
    RA(0, 0) RA(0, 1)
    if (MODE == 0) dmc_first(ns + w * 4096, va, ca + e0);
    RA(0, 2) RA(0, 3) RB(0, 0)
    if (MODE == 0) dmc_next<1024>(va, ca + e1);
    RB(0, 1) RB(0, 2) RB(0, 3)
    if (MODE == 0) dmc_next<2048>(va, ca + e2);
    RA(1, 0) RA(1, 1) RA(1, 2)
    if (MODE == 0) dmc_next<3072>(va, ca + e3);
    RA(1, 3) RB(1, 0)
    if (MODE < 2) dma_first(ns + G2_SA + w * 2048, vb, cb);
    RB(1, 1)
    if (MODE < 2) dma_next<1024>(vb, cb - 896);
    sl2 = (sl2 == 2) ? 0 : sl2 + 1; dlt = (sl2 == 0) ? -2 * G2_SLOT : G2_SLOT;
    if (MODE < 2) { stp = nxt(stp); ca = hbase + stp * 32768 + w * 4096; cb = brow(stp, w); ns = (ns == lb + 2 * G2_SLOT) ? lb : ns + G2_SLOT; }
    asm volatile("" : "+s"(ca), "+s"(cb), "+s"(ns), "+s"(sl2), "+s"(dlt), "+s"(stp) :: "memory");
    RB(1, 2) RB(1, 3)
#undef RA
#undef RB
    ao += dlt; bo += dlt;
    SCHED();
    asm volatile("" : "+v"(bo), "+v"(ao));
    if (MODE == 0) WAIT_V_IF0(6, lag ^ 1); else if (MODE == 2 && q == 16) WAIT_V_IF0(2, lag ^ 1);
    if (MODE == 1 && q == 1) {
      asm volatile("s_waitcnt vmcnt(2)" : "+v"(seen) :: "memory");
      ET(2);
      if (!lag) {
        volatile LAS unsigned* go = (volatile LAS unsigned*)(lds + T_SYNC + 4);
        if (w == 4) { wait_flags(seen, 0xf0ull); *go = 1u; }
        else { unsigned spins = 0; while (__builtin_amdgcn_readfirstlane(*go) == 0u) { __builtin_amdgcn_s_sleep(1); if (++spins > (1u << 24)) break; } }
        asm volatile("" ::: "memory");
      }
      ET(3);
      const int s2 = nxt(7), s = lag ? nxt(s2) : s2;
      const char* p = hbase + s * 32768 + wi * 8192;
      const unsigned d = lb + (lag ? 0 : 2 * G2_SLOT) + wi * 8192;
      dmc_first(d, va, p + e0); dmc_next<1024>(va, p + e1); dmc_next<2048>(va, p + e2); dmc_next<3072>(va, p + e3);
      dmc_first(d + 4096, va, p + 4096 + e0); dmc_next<1024>(va, p + 4096 + e1); dmc_next<2048>(va, p + 4096 + e2); dmc_next<3072>(va, p + 4096 + e3);
      ET(4);
    }
    WAIT_L0();
    SCHED();
    BAR();
    SCHED();
    PRIO_M();
#pragma unroll
    for (int n = 0; n < 4; ++n)
#pragma unroll
      for (int i = 0; i < 4; ++i) {
        ac2[i][n] = mfma(bfr[0][n], a[0][i], ac2[i][n]);
        SCHED();
        if (n == 3 && i == 3) {
          PRIO_T();
          if (MODE == 0) WAIT_V_IF0(6, lag); else if (MODE == 2 && q == 16) WAIT_V_IF0(2, lag);
          if (MODE == 1 && q == 1) WAIT_V_IF0(0, lag);
          if (q < 17 || !lag) BAR();
          SCHED();
        }
        ac2[i][n] = mfma(bfr[1][n], a[1][i], ac2[i][n]);
        SCHED();
        if (MODE == 1 && q == 0 && n == 1 && i == 3 && lag) {
          WAIT_V(0);
          unsigned old = 0;
          if (lane == 0) old = __hip_atomic_fetch_add((LAS unsigned*)(lds + T_SYNC), 1u, __ATOMIC_RELAXED, __HIP_MEMORY_SCOPE_WORKGROUP);
          if (__builtin_amdgcn_readfirstlane(old) == 3u && lane < 8)
            __hip_atomic_store(hflag + lane * 32 + nt, 1u, __ATOMIC_RELAXED, __HIP_MEMORY_SCOPE_AGENT);
          SCHED();
        }
      }
    PRIO_E();
    SCHED();
  };
  typedef std::integral_constant<int, 0> M0; typedef std::integral_constant<int, 1> M1; typedef std::integral_constant<int, 2> M2;
  phase2(M1{}, 0);
  ET(1);
  phase2(M1{}, 1);
  ET(5);
  ST(2);
#pragma clang loop unroll(disable)
  for (int q = 2; q < 6; ++q) phase2(M0{}, q);
  PEEK(hflag, fo);
  phase2(M0{}, 6);
  asm volatile("" : "+v"(seen));
  wait_flags(seen, 0x0full);
#pragma clang loop unroll(disable)
  for (int q = 7; q < 13; ++q) phase2(M0{}, q);
  if (w == 4 && lane == 0) __hip_atomic_fetch_add(gcnt, 1u, __ATOMIC_RELAXED, __HIP_MEMORY_SCOPE_AGENT);
#pragma clang loop unroll(disable)
  for (int q = 13; q < 16; ++q) phase2(M0{}, q);
  phase2(M2{}, 16);
  PEEK(gcnt, 0u);
  phase2(M2{}, 17);
  asm volatile("s_waitcnt vmcnt(0)" : "+v"(seen) :: "memory");
  wait_cnt(seen, gcnt, 8u);
  ST(3);
  const int tile = mb * 8 + nt;
  const auto rs = __builtin_amdgcn_make_buffer_rsrc(ws + WS_H + (size_t)mb * 4 * 36 * 16384, 0, 4 * 36 * 16384, 0x00020000);
  auto xo = [&](int src, int i) { const int u = 15 + nt * 3 + i; return (unsigned)((src * 36 + (u >= 36 ? u - 36 : u)) * 16384 + w * 2048) + lane * 16; };
#pragma unroll
  for (int i = 1; i < 4; ++i) {
    const unsigned o = xo(kq, i);
    __builtin_amdgcn_raw_buffer_store_b128(__builtin_bit_cast(u32x4, pack8(ac2[i][0], ac2[i][1])), rs, o, 0, 16);
    __builtin_amdgcn_raw_buffer_store_b128(__builtin_bit_cast(u32x4, pack8(ac2[i][2], ac2[i][3])), rs, o + 1024, 0, 16);
  }
  GAS unsigned* cnt = (GAS unsigned*)(ws + WS_CNT) + (tile * 32 + w) * 16;
  GAS unsigned* mine = cnt + kq * 128 + (lane & 3);
  seen = __hip_atomic_load(mine, __ATOMIC_RELAXED, __HIP_MEMORY_SCOPE_AGENT);
  WAIT_V(0);
  ST(4);
  asm volatile("" : "+v"(xr[0]), "+v"(xr[1]));
  if (lane >= 1 && lane < 4) __hip_atomic_store(cnt + (kq ^ lane) * 128 + lane, 1u, __ATOMIC_RELAXED, __HIP_MEMORY_SCOPE_AGENT);
  u32x4 p[3][2];
#pragma unroll
  for (int i = 0; i < 3; ++i) p[i][0] = p[i][1] = (u32x4){0u, 0u, 0u, 0u};
  {
    unsigned done = 0;
#pragma clang loop unroll(disable)
    for (unsigned spins = 0; spins < (1u << 22); ++spins) {
      const unsigned rdy = (unsigned)__builtin_amdgcn_ballot_w64(seen != 0u) & 0xeu & ~done;
      asm volatile("" ::: "memory");
#pragma unroll
      for (int i = 1; i < 4; ++i)
        if (rdy & (1u << i)) {
          const unsigned o = xo(kq ^ i, i);
          p[i - 1][0] = __builtin_amdgcn_raw_buffer_load_b128(rs, o, 0, 16);
          p[i - 1][1] = __builtin_amdgcn_raw_buffer_load_b128(rs, o + 1024, 0, 16);
        }
      done |= rdy;
      if (done == 0xeu) break;
      if (!rdy) __builtin_amdgcn_s_sleep(1);
      seen = __hip_atomic_load(mine, __ATOMIC_RELAXED, __HIP_MEMORY_SCOPE_AGENT);
    }
  }
  ST(5);
  const auto ors = __builtin_amdgcn_make_buffer_rsrc(out, 0, 4 << 20, 0x00020000);
#pragma unroll
  for (int h = 0; h < 2; ++h) {
    const auto x0 = __builtin_amdgcn_permlane16_swap(xr[h][0], xr[h][2], false, false), x1 = __builtin_amdgcn_permlane16_swap(xr[h][1], xr[h][3], false, false);
    const u32x4 xv = {x0[0], x1[0], x0[1], x1[1]};
    unsigned r[4];
#pragma unroll
    for (int d = 0; d < 4; ++d) {
      float lo = ac2[0][2 * h + (d >> 1)][2 * (d & 1)] + __uint_as_float(xv[d] << 16), hi = ac2[0][2 * h + (d >> 1)][2 * (d & 1) + 1] + __uint_as_float(xv[d] & 0xffff0000u);
#pragma unroll
      for (int i = 0; i < 3; ++i) { lo += __uint_as_float(p[i][h][d] << 16); hi += __uint_as_float(p[i][h][d] & 0xffff0000u); }
      r[d] = pk2(lo, hi);
    }
    const auto y0 = __builtin_amdgcn_permlane16_swap(r[0], r[2], false, false), y1 = __builtin_amdgcn_permlane16_swap(r[1], r[3], false, false);
    __builtin_amdgcn_raw_buffer_store_b128((u32x4){y0[0], y1[0], y0[1], y1[1]}, ors, orow + 64 * h, 0, OUT_AUX);
  }
  ST(6);
  ST(7);
  ST_OUT(1, 8);
  ST_REST(); ST_OUT(0, 6);
}

}

extern "C" __global__ __launch_bounds__(512, 2) void psum_g1_kernel(const u16* __restrict__ x, u16* __restrict__ out, const u16* __restrict__ wqkv,
                                                                    const u16* __restrict__ wg, const u16* __restrict__ wu,
                                                                    const u16* __restrict__ wo, const u16* __restrict__ wd, char* __restrict__ ws) {
  if (blockIdx.x < 128) body<true>(x, out, wqkv, wg, wu, wo, wd, ws);
  else body<false>(x, out, wqkv, wg, wu, wo, wd, ws);
}

extern "C" void kernel_launch(void* const* d_in, const int* in_sizes, int n_in, void* d_out, int out_size, void* d_ws,
                              size_t ws_size, hipStream_t stream) {
  const u16* x = (const u16*)d_in[0];
  const u16* wqkv = (const u16*)d_in[1];
  const u16* wo = (const u16*)d_in[2];
  const u16* wg = (const u16*)d_in[4];
  const u16* wu = (const u16*)d_in[5];
  const u16* wd = (const u16*)d_in[6];
  char* ws = (char*)d_ws;
  (void)hipFuncSetAttribute((const void*)psum_g1_kernel, hipFuncAttributeMaxDynamicSharedMemorySize, G1_LDS);
  psum_g1_kernel<<<256, 512, G1_LDS, stream>>>(x, (u16*)d_out, wqkv, wg, wu, wo, wd, ws);
}
```

```cpp
#define TLBW 0
#define WDELAY 0
#define HDEFER 0
#include <hip/hip_runtime.h>
#include <hip/hip_bf16.h>
#include <type_traits>
#define EARLY 1
#define HALO_SKIP 1
#define PF_W2 1
#define PF_LOOP 0
#define PF_R0 192
#define PF_PER 2
#define PF_LN_ROWS 96
#define PRIO_L 0
#define PRIO_M() __builtin_amdgcn_s_setprio(1)
#define PRIO_T() __builtin_amdgcn_s_setprio(3)
#define PRIO_E() __builtin_amdgcn_s_setprio(0)
#define PF_CODE 0
#define PRECLEAN 0
#define OUT_AUX 16
#define G2ORD 0
#define EARLY2 1
#define CHAIN 1
#define PF_X 0
#define XH 1
#define ACQ 0
#define EPI_PRIO 1
#define LORD 1
#define RPOS 1
#define FA 4
#define FB 0
#define PFD 0
#define SGLU 0
#define H_AUX 16

namespace {

typedef unsigned short u16;
typedef short bf16x8 __attribute__((ext_vector_type(8)));
typedef short bf16x4 __attribute__((ext_vector_type(4)));
typedef float f32x4 __attribute__((ext_vector_type(4)));
typedef float f32x2 __attribute__((ext_vector_type(2)));
typedef unsigned u32x4 __attribute__((ext_vector_type(4)));
typedef unsigned u32x2 __attribute__((ext_vector_type(2)));
typedef __bf16 bf2 __attribute__((ext_vector_type(2)));
#define LAS __attribute__((address_space(3)))
#define GAS __attribute__((address_space(1)))

#define WAIT_V(n) asm volatile("s_waitcnt vmcnt(" #n ")" ::: "memory")
#define WAIT_V_IF0(n, c) asm volatile("s_cmp_lg_u32 %0, 0\n\ts_cbranch_scc1 1f\n\ts_waitcnt vmcnt(" #n ")\n1:" :: "s"(c) : "memory", "scc")
#define BAR_IF0(c) asm volatile("s_cmp_lg_u32 %0, 0\n\ts_cbranch_scc1 1f\n\ts_barrier\n\ts_setprio 0\n1:" :: "s"(c) : "memory", "scc")
#define WAIT_L0() do { asm volatile("" ::: "memory"); __builtin_amdgcn_s_waitcnt(0xC07F); asm volatile("" ::: "memory"); } while (0)
#define BAR() __builtin_amdgcn_s_barrier()
#define SCHED() __builtin_amdgcn_sched_barrier(0)

__device__ __forceinline__ float b2f(u16 v) { return __uint_as_float(((unsigned)v) << 16); }
__device__ __forceinline__ unsigned pk2(float lo, float hi) {
  bf2 r = {(__bf16)lo, (__bf16)hi};
  return __builtin_bit_cast(unsigned, r);
}
__device__ __forceinline__ float rbf(float f) { return (float)(__bf16)f; }
__device__ __forceinline__ bf16x8 pack8(f32x4 a, f32x4 b) {
  u32x4 r = {pk2(a[0], a[1]), pk2(a[2], a[3]), pk2(b[0], b[1]), pk2(b[2], b[3])};
  return __builtin_bit_cast(bf16x8, r);
}
__device__ __forceinline__ bf16x4 trd(LAS const char* p) {
  return __builtin_amdgcn_ds_read_tr16_b64_v4i16((LAS bf16x4*)p);
}
__device__ __forceinline__ bf16x8 cat(bf16x4 a, bf16x4 b) { return __builtin_shufflevector(a, b, 0, 1, 2, 3, 4, 5, 6, 7); }
__device__ __forceinline__ f32x4 mfma(bf16x8 a, bf16x8 b, f32x4 c) {
  return __builtin_amdgcn_mfma_f32_16x16x32_bf16(a, b, c, 0, 0, 0);
}
__device__ __forceinline__ void dma_first(unsigned lds_dst, unsigned voff, const char* b0) {
  asm volatile("s_mov_b32 m0, %0\n\ts_nop 0\n\tglobal_load_lds_dwordx4 %1, %2" :: "s"(lds_dst), "v"(voff), "s"(b0) : "memory");
}
template <int OFF>
__device__ __forceinline__ void dma_next(unsigned voff, const char* b) {
  asm volatile("global_load_lds_dwordx4 %0, %1 offset:%2" :: "v"(voff), "s"(b), "i"(OFF) : "memory");
}
__device__ __forceinline__ void dmc_first(unsigned lds_dst, unsigned voff, const char* b0) {
  asm volatile("s_mov_b32 m0, %0\n\ts_nop 0\n\tglobal_load_lds_dwordx4 %1, %2 sc1" :: "s"(lds_dst), "v"(voff), "s"(b0) : "memory");
}
template <int OFF>
__device__ __forceinline__ void dmc_next(unsigned voff, const char* b) {
  asm volatile("global_load_lds_dwordx4 %0, %1 offset:%2 sc1" :: "v"(voff), "s"(b), "i"(OFF) : "memory");
}
__device__ __forceinline__ void dma1m(unsigned lds_dst, const char* p, unsigned long long mask) {
  asm volatile("s_mov_b32 m0, %0\n\ts_mov_b64 exec, %2\n\t"
               "global_load_lds_dwordx4 %1, off sc1\n\t"
               "s_mov_b64 exec, -1"
               :: "s"(lds_dst), "v"(p), "s"(mask) : "memory");
}
__device__ __forceinline__ void touch(unsigned lds_dst, const void* p) {
  asm volatile("s_mov_b32 m0, %0\n\ts_nop 0\n\tglobal_load_lds_dword %1, off" :: "s"(lds_dst), "v"(p) : "memory");
}
#define CODE_ADDR(dst, sym) do { unsigned lo_, hi_; asm volatile("s_getpc_b64 s[90:91]\n\ts_add_u32 %0, s90, " #sym "@rel32@lo+4\n\ts_addc_u32 %1, s91, " #sym "@rel32@hi+12" \
    : "=s"(lo_), "=s"(hi_) :: "s90", "s91", "scc"); dst = (const char*)(((unsigned long long)hi_ << 32) | lo_); } while (0)
constexpr int LN_LINES = 16, G1_LINES = 80, G2_LINES = 30;
__device__ __forceinline__ LAS char* lp(unsigned a) { return (LAS char*)(size_t)a; }
__device__ __forceinline__ float fmx(float a, float b) { return __builtin_elementwise_maximum(a, b); }
__device__ __forceinline__ float xmax(float v) {
  auto a = __builtin_amdgcn_permlane16_swap(__float_as_uint(v), __float_as_uint(v), false, false);
  v = fmaxf(__uint_as_float(a[0]), __uint_as_float(a[1]));
  auto b = __builtin_amdgcn_permlane32_swap(__float_as_uint(v), __float_as_uint(v), false, false);
  return fmaxf(__uint_as_float(b[0]), __uint_as_float(b[1]));
}
__device__ __forceinline__ float xsum(float v) {
  auto a = __builtin_amdgcn_permlane16_swap(__float_as_uint(v), __float_as_uint(v), false, false);
  v = __uint_as_float(a[0]) + __uint_as_float(a[1]);
  auto b = __builtin_amdgcn_permlane32_swap(__float_as_uint(v), __float_as_uint(v), false, false);
  return __uint_as_float(b[0]) + __uint_as_float(b[1]);
}

template <int CTRL>
__device__ __forceinline__ float dpp_add(float v) {
  return v + __uint_as_float(__builtin_amdgcn_update_dpp(0u, __float_as_uint(v), CTRL, 0xf, 0xf, true));
}
__device__ __forceinline__ float hsum(float v) {
  v = dpp_add<0xB1>(v);
  v = dpp_add<0x4E>(v);
  v = dpp_add<0x128>(v);
  return xsum(v);
}

#define DG(kid, slot)
#define ST_DECL
#define LT_DECL
#define LT(k)
#define ET(k)
#define ST(k)
#define ST_OUT(kid, n)
#define ST_SAVE()
#define ST_REST()
constexpr int XN_STEP = 17 * 1024;
constexpr size_t WS_XN = 0;
constexpr size_t WS_H = WS_XN + (size_t)8 * 32 * XN_STEP;
constexpr size_t H_BYTES = (size_t)8 * 4 * 36 * 16 * 1024;
constexpr size_t WS_CNT = WS_H + H_BYTES;
constexpr size_t WS_GCNT = WS_CNT + 131072;
constexpr size_t WS_HCNT = WS_GCNT + 8192;
constexpr size_t WS_LNF = WS_HCNT + 32768;
constexpr size_t WS_ST = WS_LNF + 32768;

constexpr int G1_SA = 17 * 1024, G1_SB = 20 * 1024, G1_SLOT = G1_SA + G1_SB;
constexpr int T_SCR = 159488;
constexpr int T_BIAS = T_SCR + 256;
constexpr int G1_LDS = T_BIAS + 4096;
constexpr int G2_SA = 32768, G2_SB = 16384, G2_SLOT = G2_SA + G2_SB;
constexpr int T_SYNC = T_SCR - 64;
constexpr int T_KV = 2 * G2_SLOT, KV_BYTES = 7 * 1024 + 272 * 80;

template <bool MAX>
__device__ __forceinline__ float tred(float a, float b, float c, float d) {
  const auto r1 = __builtin_amdgcn_permlane16_swap(__float_as_uint(a), __float_as_uint(b), false, false);
  const auto r2 = __builtin_amdgcn_permlane16_swap(__float_as_uint(c), __float_as_uint(d), false, false);
  const float x0 = __uint_as_float(r1[0]), x1 = __uint_as_float(r1[1]), y0 = __uint_as_float(r2[0]), y1 = __uint_as_float(r2[1]);
  const float t = MAX ? fmx(x0, x1) : x0 + x1, u = MAX ? fmx(y0, y1) : y0 + y1;
  const auto r3 = __builtin_amdgcn_permlane32_swap(__float_as_uint(t), __float_as_uint(u), false, false);
  const float z0 = __uint_as_float(r3[0]), z1 = __uint_as_float(r3[1]);
  return MAX ? fmx(z0, z1) : z0 + z1;
}
__device__ __forceinline__ u32x2 glu4(f32x4 g, f32x4 u) {
  f32x4 e = g * -1.4426950408889634f;
#pragma unroll
  for (int i = 0; i < 4; ++i) e[i] = __builtin_amdgcn_exp2f(e[i]);
  e = e + 1.0f;
#pragma unroll
  for (int i = 0; i < 4; ++i) e[i] = __builtin_amdgcn_rcpf(e[i]);
  e = e * u;
  return (u32x2){pk2(e[0], e[1]), pk2(e[2], e[3])};
}
__device__ __forceinline__ u32x2 glu4s(f32x4 g, f32x4 u) {
  float e[4];
#pragma unroll
  for (int i = 0; i < 4; ++i) { e[i] = g[i] * -1.4426950408889634f; asm volatile("" : "+v"(e[i])); e[i] = __builtin_amdgcn_exp2f(e[i]); }
#pragma unroll
  for (int i = 0; i < 4; ++i) { e[i] = e[i] + 1.0f; asm volatile("" : "+v"(e[i])); e[i] = __builtin_amdgcn_rcpf(e[i]); }
#pragma unroll
  for (int i = 0; i < 4; ++i) { e[i] = e[i] * u[i]; asm volatile("" : "+v"(e[i])); }
  return (u32x2){pk2(e[0], e[1]), pk2(e[2], e[3])};
}
__device__ __forceinline__ u32x4 cat4(u32x2 a, u32x2 b) { return (u32x4){a[0], a[1], b[0], b[1]}; }

template <bool typeA>
__device__ __forceinline__ void body(const u16* __restrict__ x, u16* __restrict__ out, const u16* __restrict__ wqkv, const u16* __restrict__ wg,
                                     const u16* __restrict__ wu, const u16* __restrict__ wo, const u16* __restrict__ wd, char* __restrict__ ws) {
  extern __shared__ __attribute__((aligned(16))) char smem[];
  LAS char* lds = (LAS char*)smem;
  ST_DECL LT_DECL ST(0);
  const int tid = threadIdx.x;
  int lane = tid & 63;
  const int w = __builtin_amdgcn_readfirstlane(tid >> 6), g = w >> 2, wi = w & 3;
  const int b = blockIdx.x, xcd = b & 7, idx = b >> 3;
  const int mb = (xcd & 1) * 4 + (idx & 3), kq = xcd >> 1, ntl = idx >> 2;
  const int j = ntl & 3;
  int fr = lane & 15, fq = lane >> 4;
  const int fA = 896 * kq + 64 * j;
  const int f0 = 896 * kq + 256 + 160 * j, odd = j & 1;
  const int fa = f0 + 32 * odd, fb = fa + 64, fc = odd ? f0 : f0 + 128;

  asm volatile("" : "+s"(wqkv), "+s"(wg), "+s"(wu), "+s"(ws), "+s"(x));
  const int mi = kq * 8 + ntl;
  const bool lnh = !typeA && w == 4 && j < 2, lnw = g == 0 || lnh;
  const int lr = (lnh ? (kq * 2 + j) * 2 : (mi & 1) * 8 + wi * 2) + ((lane >> 2) & 1), lf = lnh ? 16 : mi >> 1;
  const int lrow = lnh ? (mb > 0 ? mb * 256 - 16 : 0) + 2 * (lr & 7) + (lr >> 3) : mb * 256 + 32 * (lf >> 1) + 2 * lr + (lf & 1);
  u32x4 raw[4] = {{0u, 0u, 0u, 0u}, {0u, 0u, 0u, 0u}, {0u, 0u, 0u, 0u}, {0u, 0u, 0u, 0u}};
  asm volatile("s_cmp_eq_u32 %6, 0\n\ts_cbranch_scc1 1f\n\tglobal_load_dwordx4 %0, %4, %5\n\tglobal_load_dwordx4 %1, %4, %5 offset:512\n\t"
               "global_load_dwordx4 %2, %4, %5 offset:1024\n\tglobal_load_dwordx4 %3, %4, %5 offset:1536\n1:"
               : "+v"(raw[0]), "+v"(raw[1]), "+v"(raw[2]), "+v"(raw[3])
               : "v"(lrow * 2048 + (lane >> 3) * 64 + (lane & 3) * 16), "s"(x), "s"(__builtin_amdgcn_readfirstlane(lnw ? 1 : 0)) : "memory", "scc");
  GAS unsigned* lnf = (GAS unsigned*)(ws + WS_LNF) + mb * 1024 + (lane < 32 ? mi * 32 + lane : (lane - 32) * 32 + mi);
  unsigned entry = 0;
  asm volatile("s_cmp_lg_u32 %2, 7\n\ts_cbranch_scc1 1f\n\tglobal_load_dword %0, %1, off sc1\n1:" : "+v"(entry) : "v"(lnf), "s"(w) : "memory", "scc");

  const bool isq = typeA && wi < 3;
  const int ld = isq ? 3072 : 3584;
  const u16* q1 = isq ? wqkv + (wi * 1024 + 256 * kq + 64 * j) : ((!typeA && (wi & 2)) ? wu : wg) + (typeA ? fA : ((wi & 1) ? fb : fa));
  const char* const xnb = ws + WS_XN + (size_t)mb * 32 * XN_STEP;
  const char* c1 = g ? (const char*)q1 : xnb + wi * 4096;
  const int s1 = g ? 8 * ld - 1024 : 0, s2 = g ? 32 * ld - 2048 : 0, s3 = g ? 40 * ld - 3072 : 0;
  const int p1 = g ? 64 * ld : XN_STEP, p2 = g ? XN_STEP : 64 * 3584;
  const int d1 = (g ? G1_SA : 0) + wi * 4096, d2 = g ? 16384 + wi * 256 : G1_SA + 16384 + wi * 1024;
  const unsigned long long m2 = g ? 0xffffull : ~0ull;
  const int rr = lane >> 3, pc = lane & 7, fi = (pc >> 1) ^ ((rr >> 1) & 3), kr = (rr & 3) + 8 * (rr >> 2);
  const unsigned v1 = g ? kr * ld * 2 + fi * 32 + (pc & 1) * 16 : lane * 16;
  const unsigned lb = (unsigned)(size_t)lds;
  const char* c2;
  {
    const unsigned ow = (kr + 16 * (wi >> 1) + 4 * (wi & 1)) * 7168 + (typeA ? fi * 32 : (fi >> 1) * 32) + (pc & 1) * 16;
    const char* be = g ? xnb : (const char*)(typeA ? wu + fA : wg + fc);
    const char* bo = g ? be : (const char*)(wu + (typeA ? fA : fc));
    c2 = ((fi & 1) ? bo : be) + (g ? 16384 + wi * 256 + (lane & 15) * 16 : ow);
  }
  auto chain1 = [&](unsigned dst, const char* p) { dmc_first(dst + d1, v1, p); dmc_next<1024>(v1, p + s1); dmc_next<2048>(v1, p + s2); dmc_next<3072>(v1, p + s3); };
  auto first = [&](bool one) {
    if (one) { chain1(lb, c1); chain1(lb + G1_SLOT, c1 + p1); }
    else { dma1m(lb + d2, c2, m2); dma1m(lb + G1_SLOT + d2, c2 + p2, m2); }
  };
  if (w == 3) {
    char* z = lane < 8 ? ws + WS_CNT + (((mb * 8 + ntl) * 4 + kq) * 8 + lane) * 64
            : (lane < 10 ? ws + WS_HCNT + ((mb * 4 + kq) * 8 + ntl) * 128 + (lane - 8) * 16 : ws + WS_GCNT + (mb * 4 + kq) * 64);
    if (lane < (ntl == 0 ? 11 : 10)) asm volatile("global_store_dwordx4 %0, %1, off sc1\n\ts_nop 1" :: "v"(z), "v"((u32x4){0u, 0u, 0u, 0u}) : "memory");
  }
  if (!(HDEFER && lnh)) first(g == 1);
  if (lnw) {
    WAIT_V(8);
    WAIT_V_IF0(2, g);
    if (HDEFER && lnh) WAIT_V(0);
    asm volatile("" : "+v"(raw[0]), "+v"(raw[1]), "+v"(raw[2]), "+v"(raw[3]));
    float v[32];
#pragma unroll
    for (int i = 0; i < 16; ++i) { v[2 * i] = __uint_as_float(raw[i >> 2][i & 3] << 16); v[2 * i + 1] = __uint_as_float(raw[i >> 2][i & 3] & 0xffff0000u); }
    float s = 0.f;
#pragma unroll
    for (int i = 0; i < 32; ++i) s += v[i];
    const float mean = hsum(s) * (1.0f / 1024.0f);
    float q = 0.f;
#pragma unroll
    for (int i = 0; i < 32; ++i) { const float d = v[i] - mean; q += d * d; }
    q = hsum(q);
    const float mb16 = rbf(mean), den = rbf(sqrtf(rbf(rbf(q * (1.0f / 1024.0f)) + 1e-5f)));
    const float inv = 1.0f / den;
    const auto xrs = __builtin_amdgcn_make_buffer_rsrc((void*)xnb, 0, 32 * XN_STEP, 0x00020000);
    const unsigned o = (lane >> 3) * XN_STEP + lf * 1024 + lr * 64 + (((lane & 3) ^ (2 * (lr >> 3))) * 16);
#pragma unroll
    for (int i = 0; i < 4; ++i) {
      u32x4 t;
#pragma unroll
      for (int e = 0; e < 4; ++e) t[e] = pk2(rbf(v[8 * i + 2 * e] - mb16) * inv, rbf(v[8 * i + 2 * e + 1] - mb16) * inv);
      __builtin_amdgcn_raw_buffer_store_b128(t, xrs, o + i * 8 * XN_STEP, 0, 16);
    }
  }

  f32x4 acc[4][10];
  f32x4 hacc = {0.f, 0.f, 0.f, 0.f};
#pragma unroll
  for (int i = 0; i < 4; ++i)
#pragma unroll
    for (int n = 0; n < 10; ++n) { acc[i][n] = (f32x4){0.f, 0.f, 0.f, 0.f}; asm volatile("" : "+v"(acc[i][n])); }
  const int rl = 4 * (fq & 1) + (fr >> 2);
  unsigned hoffA = lb + 16384 + fr * 64 + ((fq ^ (2 * (fr >> 3))) * 16);
  unsigned aoff = hoffA - 16384 + wi * 4096;
  unsigned boff0 = lb + G1_SA + (fq >> 1) * 2048 + rl * 128 + 32 * ((2 * g) ^ ((rl >> 1) & 3)) + 8 * (lane & 3);
  unsigned boff1 = boff0 ^ 32;
  unsigned hoffB = (boff0 ^ (32 * (wi & 1))) + (1 + (wi >> 1)) * 4096;
  asm volatile("" : "+v"(aoff), "+v"(hoffA), "+v"(boff0), "+v"(boff1), "+v"(hoffB), "+v"(hacc));
  {
    const int lo = fr + (wi == 0 ? -7 : (wi == 2 ? 1 : 9));
    const int hi = wi == 0 ? fr + 1 : ((wi >= 2 && mb == 0) ? 0 : (wi == 2 ? 8 : 16));
    f32x4 bv;
#pragma unroll
    for (int e = 0; e < 4; ++e) bv[e] = (4 * fq + e >= lo && 4 * fq + e < hi) ? 0.f : -3.0e38f;
    *(LAS f32x4*)(lds + T_BIAS + wi * 1024 + lane * 16) = bv;
    *(LAS unsigned*)(lds + T_SYNC + (lane & 1) * 4) = 0u;
  }
  asm volatile("s_waitcnt vmcnt(8)" : "+v"(entry) :: "memory");
  if (lnw) WAIT_V(0);
  BAR();
  if (HDEFER && lnh) first(true);
  ST(1);
  if (w == 7) {
    if (lane >= 32) __hip_atomic_store(lnf, entry + 1u, __ATOMIC_RELAXED, __HIP_MEMORY_SCOPE_AGENT);
    for (unsigned spins = 0; spins < (1u << 22); ++spins) {
      unsigned now = entry + 1u;
      if (lane < 32) now = __hip_atomic_load(lnf, __ATOMIC_RELAXED, __HIP_MEMORY_SCOPE_AGENT);
      if (__builtin_amdgcn_ballot_w64(now == entry) == 0ull) break;
      __builtin_amdgcn_s_sleep(1);
    }
    if (lane >= 32) __hip_atomic_store(lnf, entry + 2u, __ATOMIC_RELAXED, __HIP_MEMORY_SCOPE_AGENT);
  }
  asm volatile("" ::: "memory");
  BAR();
  first(g == 0);
  c1 += 2 * p1; c2 += 2 * p2;
  unsigned ns = lb + 2 * G1_SLOT;
  auto adv = [&]() { c1 += p1; c2 += p2; ns = (ns == lb + 3 * G1_SLOT) ? lb : ns + G1_SLOT; };
  WAIT_V_IF0(0, g);
  BAR();
  ST(2);
  if (g == 1) { WAIT_V(0); BAR(); }

  auto step = [&](auto halo_c, auto last_c, int q) {
    constexpr bool HALO = decltype(halo_c)::value, LAST = decltype(last_c)::value;
    bf16x8 a[2][4], wn[4][2];
    asm volatile("" ::: "memory");
    auto rdq = [&](int e, int h) {
      if (e >= (HALO ? 12 : 10)) return;
      const int kk = h >> 1, t = h & 1;
      bf16x8& d = wn[e & 3][kk];
      if (e == 11) { if (t == 0) d = *(const LAS bf16x8*)(lp(hoffA) + kk * G1_SLOT); return; }
      const int n = e < 2 ? 8 + e : e - 2;
      const bf16x4 v = trd(lp(e == 10 ? hoffB : ((n & 1) ? boff1 : boff0)) + (e == 10 ? 0 : (n >> 1) * 4096) + t * 1024 + kk * G1_SLOT);
      if (t == 0) { d[0] = v[0]; d[1] = v[1]; d[2] = v[2]; d[3] = v[3]; } else { d[4] = v[0]; d[5] = v[1]; d[6] = v[2]; d[7] = v[3]; }
    };
#define RA1(kk, i) a[kk][i] = *(const LAS bf16x8*)(lp(aoff) + (kk) * G1_SLOT + (i) * 1024);
    const int dlt = (q & 1) ? -2 * G1_SLOT : 2 * G1_SLOT;
    if (!LAST) {
#pragma unroll
      for (int kk = 0; kk < 2; ++kk) {
        dmc_first(ns + d1, v1, c1); RA1(kk, 0) rdq(kk, 0);
        dmc_next<1024>(v1, c1 + s1); RA1(kk, 1) rdq(kk, 1);
        dmc_next<2048>(v1, c1 + s2); RA1(kk, 2) rdq(kk, 2);
        dmc_next<3072>(v1, c1 + s3); RA1(kk, 3) rdq(kk, 3);
        dma1m(ns + d2, c2, m2);
        adv();
      }
    } else
    {
#pragma unroll
      for (int kk = 0; kk < 2; ++kk)
#pragma unroll
        for (int i = 0; i < 4; ++i) RA1(kk, i)
#pragma unroll
      for (int h = 0; h < 8; ++h) rdq(h >> 2, h & 3);
    }
    aoff += dlt;
    SCHED();
    if (!LAST) {
    } else {
#if PF_W2 && !(PFD & 1)
      int t = w * 64 + __builtin_amdgcn_mbcnt_hi(~0u, __builtin_amdgcn_mbcnt_lo(~0u, 0u));
      t = t < 288 ? t : t - 288;
      const int r2 = (t >> 4) * 64 + (xcd & 1) * 32 + idx;
      touch(lb + T_SCR, (r2 < 256 ? wo + (256 * kq + r2) * 1024 : wd + (896 * kq + r2 - 256) * 1024) + (t & 15) * 64);
#endif
    }
    WAIT_L0();
    SCHED();
    BAR();
    SCHED();
    PRIO_M();
    auto fin = [&]() { SCHED(); PRIO_T(); if (!LAST) { if (PFD) WAIT_V(1); else WAIT_V(0); } BAR(); SCHED(); };
#pragma unroll
    for (int e = 0; e < 10; ++e) {
      const int n = e < 2 ? 8 + e : e - 2;
#pragma unroll
      for (int i = 0; i < 4; ++i) {
        acc[i][n] = mfma(wn[e & 3][0], a[0][i], acc[i][n]);
        SCHED();
        if (!HALO && e == 9 && i == 3) fin();
        SCHED();
        acc[i][n] = mfma(wn[e & 3][1], a[1][i], acc[i][n]);
        SCHED();
        if (e == 0) { rdq(2 + (i >> 1), 2 * (i & 1)); rdq(2 + (i >> 1), 2 * (i & 1) + 1); } else rdq(e + 3, i);
        if (i == 3 && e < 9) { asm volatile("" :: "v"(wn[(e + 1) & 3][0]), "v"(wn[(e + 1) & 3][1])); }
        SCHED();
      }
    }
    if (HALO) {
      hacc = mfma(wn[2][0], wn[3][0], hacc);
      WAIT_L0();
      fin();
      hacc = mfma(wn[2][1], wn[3][1], hacc);
    }
    hoffA += dlt; boff0 += dlt; boff1 += dlt; hoffB += dlt;
    PRIO_E();
    SCHED();
  };
  const int nt = ntl;
  const int kba = 8 + ((fa - 896 * kq) >> 5);
  const int own0 = typeA ? j : (kba >> 1), own1 = typeA ? 4 + j : (kba >> 1) + 1;
  const char* const hbase = ws + WS_H + (size_t)(mb * 4 + kq) * 36 * 16384;
  auto brow = [&](int stp_, int w_) {
    return (const char*)(stp_ < 4 ? wo + (size_t)(256 * kq) * 1024 : wd + (size_t)(896 * kq) * 1024 - 256 * 1024) + ((stp_ * 64 + 8 * w_) * 1024 + nt * 128) * 2;
  };
  unsigned va, vb;
  auto stage1 = [&](auto halo_c) {
    constexpr bool TA = decltype(halo_c)::value;
    typedef std::integral_constant<bool, TA || !HALO_SKIP> HC;
#pragma clang loop unroll(disable)
    for (int q = 0; q < 15; ++q) step(HC{}, std::false_type{}, q);
    step(HC{}, std::true_type{}, 15);
    ST(3);
    asm volatile("" ::: "memory");
    if (g == 0) __builtin_amdgcn_s_setprio(2); else __builtin_amdgcn_s_setprio(EPI_PRIO);
    lane = __builtin_amdgcn_mbcnt_hi(~0u, __builtin_amdgcn_mbcnt_lo(~0u, 0u));
    asm volatile("" : "+v"(lane));
    fr = lane & 15; fq = lane >> 4;
    va = lane * 16;
    vb = (lane >> 3) * 2048 + ((((lane & 7) >> 1) ^ ((lane >> 4) & 3)) * 32) + (lane & 1) * 16;
    if (g == 1) {
#pragma unroll
      for (int u = 0; u < 4; ++u) {
        const int w_ = wi * 2 + (u & 1);
        const char* p0 = brow((u >> 1) ? own1 : own0, w_);
        dma_first(lb + (u >> 1) * G2_SLOT + G2_SA + w_ * 2048, vb, p0); dma_next<1024>(vb, p0 - 896);
      }
    }
    const auto hrs = __builtin_amdgcn_make_buffer_rsrc((void*)hbase, 0, 36 * 16384, 0x00020000);
    const unsigned hv = wi * 4096 + lane * 16;
    auto put = [&](u32x4 v, int s, int kb, int i) {
      __builtin_amdgcn_raw_buffer_store_b128(v, hrs, hv + kb * 16384 + i * 1024, 0, H_AUX);
      *(LAS u32x4*)(lds + s * G2_SLOT + g * 16384 + ((wi * 4096 + kq * 1024 + lane * 16) ^ (i * 1024))) = v;
    };
#define GLU(i, a, b) glu4(acc[i][a], acc[i][b])
    if (!TA) {
      const int kbc = 8 + ((fc - 896 * kq) >> 5);
#pragma unroll
      for (int i = 0; i < 4; ++i) {
        __builtin_amdgcn_raw_buffer_store_b64(GLU(i, 8, 9), hrs, hv + kbc * 16384 + g * 8 + i * 1024, 0, H_AUX);
        put(cat4(GLU(i, 0, 4), GLU(i, 1, 5)), 0, kba + g, i);
        put(cat4(GLU(i, 2, 6), GLU(i, 3, 7)), 1, kba + 2 + g, i);
        if (i == FB) { SCHED(); BAR_IF0(g); SCHED(); }
      }
    } else {
      auto glu_part = [&](int i0, int i1) {
#pragma unroll
        for (int i = i0; i < i1; ++i) put(cat4(GLU(i, 6, 8), GLU(i, 7, 9)), 1, 8 + 2 * j + g, i);
      };
      if (g == 0) { glu_part(0, FA); SCHED(); BAR(); __builtin_amdgcn_s_setprio(0); asm volatile("" ::: "memory"); }
    LAS char* kim = lds + T_KV + g * KV_BYTES;
    LAS char* vim = kim + 7 * 1024;
    bf16x8 kf[4], qf[4];
#pragma unroll
    for (int i = 0; i < 4; ++i) {
      kf[i] = pack8(acc[i][2], acc[i][3]);
      *(LAS bf16x8*)(vim + ((wi * 4 + i) * 16 + fr) * 80 + fq * 16) = pack8(acc[i][4], acc[i][5]);
    }
    if (wi < 3) {
      *(LAS bf16x8*)(kim + wi * 2048 + lane * 16) = kf[2];
      *(LAS bf16x8*)(kim + wi * 2048 + 1024 + lane * 16) = kf[3];
    }
    {
      LAS char* hp = (wi < 2) ? (kim + 6 * 1024 + lane * 16 + wi * 8) : (vim + (256 + fr) * 80 + fq * 16 + (wi - 2) * 8);
      *(LAS u32x2*)hp = (u32x2){pk2(hacc[0], hacc[1]), pk2(hacc[2], hacc[3])};
    }
    WAIT_L0();
    BAR();
    asm volatile("" ::: "memory");
    const bool halo = wi == 0;
    const f32x4 bc = *(const LAS f32x4*)(lds + T_BIAS + lane * 16), bp = *(const LAS f32x4*)(lds + T_BIAS + 1024 + lane * 16);
    const f32x4 bp0 = *(const LAS f32x4*)(lds + T_BIAS + (halo ? 2048 : 1024) + lane * 16);
    const f32x4 bp1 = *(const LAS f32x4*)(lds + T_BIAS + (halo ? 3072 : 1024) + lane * 16);
    const int pf0 = halo ? 16 : wi * 4 - 2, pf1 = halo ? 16 : wi * 4 - 1;
    const bf16x8 kp0 = *(const LAS bf16x8*)(kim + (halo ? 6 : 2 * wi - 2) * 1024 + lane * 16);
    const bf16x8 kp1 = *(const LAS bf16x8*)(kim + (halo ? 6 : 2 * wi - 1) * 1024 + lane * 16);
    const LAS char* vr = vim + (4 * fq + (fr >> 2)) * 80 + (lane & 3) * 16;
    bf16x4 vt[6][2];
#pragma unroll
    for (int d = 0; d < 2; ++d) {
      vt[0][d] = trd(vr + pf0 * 1280 + d * 8); vt[1][d] = trd(vr + pf1 * 1280 + d * 8);
#pragma unroll
      for (int i = 0; i < 4; ++i) vt[2 + i][d] = trd(vr + (wi * 4 + i) * 1280 + d * 8);
    }
    f32x4 sp[4], sc[4];
#pragma unroll
    for (int i = 0; i < 4; ++i) {
      qf[i] = pack8(acc[i][0], acc[i][1]);
      sc[i] = mfma(kf[i], qf[i], bc);
    }
    sp[2] = mfma(kf[0], qf[2], bp); sp[3] = mfma(kf[1], qf[3], bp);
    sp[0] = mfma(kp0, qf[0], bp0); sp[1] = mfma(kp1, qf[1], bp1);
    const float sl = 0.17677669529663687f * 1.4426950408889634f;
    const f32x4 z4 = {0.f, 0.f, 0.f, 0.f};
    float m[4];
#pragma unroll
    for (int i = 0; i < 4; ++i)
      m[i] = fmx(fmx(fmx(fmx(sp[i][0], sp[i][1]), sp[i][2]), fmx(fmx(sp[i][3], sc[i][0]), sc[i][1])), fmx(sc[i][2], sc[i][3]));
    {
      const unsigned t = __float_as_uint(tred<true>(m[0], m[1], m[2], m[3]));
      const auto u = __builtin_amdgcn_permlane32_swap(t, t, false, false);
      const auto u0 = __builtin_amdgcn_permlane16_swap(u[0], u[0], false, false), u1 = __builtin_amdgcn_permlane16_swap(u[1], u[1], false, false);
      m[0] = __uint_as_float(u0[0]); m[1] = __uint_as_float(u0[1]); m[2] = __uint_as_float(u1[0]); m[3] = __uint_as_float(u1[1]);
    }
    const u32x4 one4 = {0x3f803f80u, 0x3f803f80u, 0x3f803f80u, 0x3f803f80u};
#pragma unroll
    for (int i = 0; i < 4; ++i) {
      const float mm = -sl * m[i];
#pragma unroll
      for (int e = 0; e < 4; ++e) {
        sp[i][e] = __builtin_amdgcn_exp2f(__builtin_fmaf(sp[i][e], sl, mm));
        sc[i][e] = __builtin_amdgcn_exp2f(__builtin_fmaf(sc[i][e], sl, mm));
      }
      const bf16x8 pfr = pack8(sp[i], sc[i]);
      const f32x4 sm = mfma(__builtin_bit_cast(bf16x8, one4), pfr, z4);
      const f32x4 o0 = mfma(cat(vt[i][0], vt[2 + i][0]), pfr, z4);
      const f32x4 o1 = mfma(cat(vt[i][1], vt[2 + i][1]), pfr, z4);
      const float inv = __builtin_amdgcn_rcpf(sm[0]);
      put(__builtin_bit_cast(u32x4, pack8(o0 * inv, o1 * inv)), 0, 2 * j + g, i);
    }
      glu_part(FA, 4);
      if (g == 1) glu_part(0, FA);
    }
  };
  stage1(std::integral_constant<bool, typeA>{});
  ST(4);

  const int lag = g ^ 1;
  GAS unsigned* hflag = (GAS unsigned*)(ws + WS_HCNT) + (mb * 4 + kq) * 256;
  const unsigned fo = (nt * 32 + (lane & 7)) * 4;
  GAS unsigned* gcnt = (GAS unsigned*)(ws + WS_GCNT) + (mb * 4 + kq) * 16;
  const int e0 = kq * 1024, e1 = ((1 ^ kq) - 1) * 1024, e2 = ((2 ^ kq) - 2) * 1024, e3 = ((3 ^ kq) - 3) * 1024;
  auto nxt = [&](int s) { ++s; if (s == own0) ++s; if (s == own1) ++s; if (s == 18) s = (own0 == 0) ? 1 : 0; return s; };
  auto wait_cnt = [&](unsigned seen, GAS unsigned* c, unsigned need) {
    if ((unsigned)__builtin_amdgcn_readfirstlane(seen) < need) {
      asm volatile("" ::: "memory");
      unsigned spins = 0;
      while ((unsigned)__builtin_amdgcn_readfirstlane(__hip_atomic_load(c, __ATOMIC_RELAXED, __HIP_MEMORY_SCOPE_AGENT)) < need)
        if (++spins > (1u << 22)) break;
    }
    asm volatile("" ::: "memory");
  };
  auto wait_flags = [&](unsigned seen, unsigned long long need) {
    if ((__builtin_amdgcn_ballot_w64(seen != 0u) & need) != need) {
      asm volatile("" ::: "memory");
      unsigned spins = 0;
      while ((__builtin_amdgcn_ballot_w64(__hip_atomic_load(hflag + (fo >> 2), __ATOMIC_RELAXED, __HIP_MEMORY_SCOPE_AGENT) != 0u) & need) != need) {
        __builtin_amdgcn_s_sleep(1);
        if (++spins > (1u << 22)) break;
      }
    }
    asm volatile("" ::: "memory");
  };
  int stp = nxt(7);
  const int mf = wi * 4 + kq;
  const unsigned orow = ((mb * 256 + 32 * (mf >> 1) + 2 * fr + (mf & 1)) * 1024 + nt * 128 + g * 64 + 16 * (fq & 1) + 8 * (fq >> 1)) * 2;
  u32x4 xr[2] = {{0u, 0u, 0u, 0u}, {0u, 0u, 0u, 0u}};
  const int rl2 = 4 * (fq & 1) + (fr >> 2);
  unsigned ao = lb + wi * 4096 + lane * 16;
  unsigned bo = lb + G2_SA + (fq >> 1) * 2048 + g * 1024 + rl2 * 128 + 32 * ((rl2 >> 1) & 3) + 8 * (lane & 3);
  f32x4 ac2[4][4];
#pragma unroll
  for (int i = 0; i < 4; ++i)
#pragma unroll
    for (int n = 0; n < 4; ++n) { ac2[i][n] = (f32x4){0.f, 0.f, 0.f, 0.f}; asm volatile("" : "+v"(ac2[i][n])); }
  ns = lb + 2 * G2_SLOT;
  const char* ca = hbase + stp * 32768 + w * 4096;
  const char* cb = brow(stp, w);
  int sl2 = 0;
  WAIT_V_IF0(0, lag);
  WAIT_L0();
  BAR();
  ST(5);
  ST_SAVE(); ST(0); ST(1);
  unsigned seen = 0;
#define PEEK(c, o) asm volatile("global_load_dword %0, %1, %2 sc1" : "+v"(seen) : "v"(o), "s"(c) : "memory")
  if (lag) BAR();

  auto phase2 = [&](auto mode, int q) {
    constexpr int MODE = decltype(mode)::value;
    bf16x8 a[2][4], bfr[2][4];
#define RA(kk, i) a[kk][i] = *(const LAS bf16x8*)(lp(ao) + (kk) * 16384 + (i) * 1024);
#define RB(kk, n) bfr[kk][n] = cat(trd(lp(bo ^ ((n) * 32)) + (kk) * 8192), trd(lp(bo ^ ((n) * 32)) + (kk) * 8192 + 4096));
    int dlt;
    asm volatile("" ::: "memory");
    if (MODE == 1 && q == 1 && w == 4) PEEK(hflag, fo);
    if (MODE == 2 && q == 16)
      asm volatile("global_load_dwordx4 %0, %2, %3\n\tglobal_load_dwordx4 %1, %2, %3 offset:64" : "+v"(xr[0]), "+v"(xr[1]) : "v"(orow), "s"(x) : "memory");
    RA(0, 0) RA(0, 1)
    if (MODE == 0) dmc_first(ns + w * 4096, va, ca + e0);
    RA(0, 2) RA(0, 3) RB(0, 0)
    if (MODE == 0) dmc_next<1024>(va, ca + e1);
    RB(0, 1) RB(0, 2) RB(0, 3)
    if (MODE == 0) dmc_next<2048>(va, ca + e2);
    RA(1, 0) RA(1, 1) RA(1, 2)
    if (MODE == 0) dmc_next<3072>(va, ca + e3);
    RA(1, 3) RB(1, 0)
    if (MODE < 2) dma_first(ns + G2_SA + w * 2048, vb, cb);
    RB(1, 1)
    if (MODE < 2) dma_next<1024>(vb, cb - 896);
    sl2 = (sl2 == 2) ? 0 : sl2 + 1; dlt = (sl2 == 0) ? -2 * G2_SLOT : G2_SLOT;
    if (MODE < 2) { stp = nxt(stp); ca = hbase + stp * 32768 + w * 4096; cb = brow(stp, w); ns = (ns == lb + 2 * G2_SLOT) ? lb : ns + G2_SLOT; }
    asm volatile("" : "+s"(ca), "+s"(cb), "+s"(ns), "+s"(sl2), "+s"(dlt), "+s"(stp) :: "memory");
    RB(1, 2) RB(1, 3)
#undef RA
#undef RB
    ao += dlt; bo += dlt;
    SCHED();
    asm volatile("" : "+v"(bo), "+v"(ao));
    if (MODE == 0) WAIT_V_IF0(6, lag ^ 1); else if (MODE == 2 && q == 16) WAIT_V_IF0(2, lag ^ 1);
    if (MODE == 1 && q == 1) {
      asm volatile("s_waitcnt vmcnt(2)" : "+v"(seen) :: "memory");
      ET(2);
      if (!lag) {
        volatile LAS unsigned* go = (volatile LAS unsigned*)(lds + T_SYNC + 4);
        if (w == 4) { wait_flags(seen, 0xf0ull); *go = 1u; }
        else { unsigned spins = 0; while (__builtin_amdgcn_readfirstlane(*go) == 0u) { __builtin_amdgcn_s_sleep(1); if (++spins > (1u << 24)) break; } }
        asm volatile("" ::: "memory");
      }
      ET(3);
      const int s2 = nxt(7), s = lag ? nxt(s2) : s2;
      const char* p = hbase + s * 32768 + wi * 8192;
      const unsigned d = lb + (lag ? 0 : 2 * G2_SLOT) + wi * 8192;
      dmc_first(d, va, p + e0); dmc_next<1024>(va, p + e1); dmc_next<2048>(va, p + e2); dmc_next<3072>(va, p + e3);
      dmc_first(d + 4096, va, p + 4096 + e0); dmc_next<1024>(va, p + 4096 + e1); dmc_next<2048>(va, p + 4096 + e2); dmc_next<3072>(va, p + 4096 + e3);
      ET(4);
    }
    WAIT_L0();
    SCHED();
    BAR();
    SCHED();
    PRIO_M();
#pragma unroll
    for (int n = 0; n < 4; ++n)
#pragma unroll
      for (int i = 0; i < 4; ++i) {
        ac2[i][n] = mfma(bfr[0][n], a[0][i], ac2[i][n]);
        SCHED();
        if (n == 3 && i == 3) {
          PRIO_T();
          if (MODE == 0) WAIT_V_IF0(6, lag); else if (MODE == 2 && q == 16) WAIT_V_IF0(2, lag);
          if (MODE == 1 && q == 1) WAIT_V_IF0(0, lag);
          if (q < 17 || !lag) BAR();
          SCHED();
        }
        ac2[i][n] = mfma(bfr[1][n], a[1][i], ac2[i][n]);
        SCHED();
        if (MODE == 1 && q == 0 && n == 1 && i == 3 && lag) {
          WAIT_V(0);
          unsigned old = 0;
          if (lane == 0) old = __hip_atomic_fetch_add((LAS unsigned*)(lds + T_SYNC), 1u, __ATOMIC_RELAXED, __HIP_MEMORY_SCOPE_WORKGROUP);
          if (__builtin_amdgcn_readfirstlane(old) == 3u && lane < 8)
            __hip_atomic_store(hflag + lane * 32 + nt, 1u, __ATOMIC_RELAXED, __HIP_MEMORY_SCOPE_AGENT);
          SCHED();
        }
      }
    PRIO_E();
    SCHED();
  };
  typedef std::integral_constant<int, 0> M0; typedef std::integral_constant<int, 1> M1; typedef std::integral_constant<int, 2> M2;
  phase2(M1{}, 0);
  ET(1);
  phase2(M1{}, 1);
  ET(5);
  ST(2);
#pragma clang loop unroll(disable)
  for (int q = 2; q < 6; ++q) phase2(M0{}, q);
  PEEK(hflag, fo);
  phase2(M0{}, 6);
  asm volatile("" : "+v"(seen));
  wait_flags(seen, 0x0full);
#pragma clang loop unroll(disable)
  for (int q = 7; q < 13; ++q) phase2(M0{}, q);
  if (w == 4 && lane == 0) __hip_atomic_fetch_add(gcnt, 1u, __ATOMIC_RELAXED, __HIP_MEMORY_SCOPE_AGENT);
#pragma clang loop unroll(disable)
  for (int q = 13; q < 16; ++q) phase2(M0{}, q);
  phase2(M2{}, 16);
  PEEK(gcnt, 0u);
  phase2(M2{}, 17);
  asm volatile("s_waitcnt vmcnt(0)" : "+v"(seen) :: "memory");
  wait_cnt(seen, gcnt, 8u);
  ST(3);
  const int tile = mb * 8 + nt;
  const auto rs = __builtin_amdgcn_make_buffer_rsrc(ws + WS_H + (size_t)mb * 4 * 36 * 16384, 0, 4 * 36 * 16384, 0x00020000);
  auto xo = [&](int src, int i) { const int u = 15 + nt * 3 + i; return (unsigned)((src * 36 + (u >= 36 ? u - 36 : u)) * 16384 + w * 2048) + lane * 16; };
#pragma unroll
  for (int i = 1; i < 4; ++i) {
    const unsigned o = xo(kq, i);
    __builtin_amdgcn_raw_buffer_store_b128(__builtin_bit_cast(u32x4, pack8(ac2[i][0], ac2[i][1])), rs, o, 0, 16);
    __builtin_amdgcn_raw_buffer_store_b128(__builtin_bit_cast(u32x4, pack8(ac2[i][2], ac2[i][3])), rs, o + 1024, 0, 16);
  }
  GAS unsigned* cnt = (GAS unsigned*)(ws + WS_CNT) + (tile * 32 + w) * 16;
  GAS unsigned* mine = cnt + kq * 128 + (lane & 3);
  seen = __hip_atomic_load(mine, __ATOMIC_RELAXED, __HIP_MEMORY_SCOPE_AGENT);
  WAIT_V(0);
  ST(4);
  asm volatile("" : "+v"(xr[0]), "+v"(xr[1]));
  if (lane >= 1 && lane < 4) __hip_atomic_store(cnt + (kq ^ lane) * 128 + lane, 1u, __ATOMIC_RELAXED, __HIP_MEMORY_SCOPE_AGENT);
  u32x4 p[3][2];
#pragma unroll
  for (int i = 0; i < 3; ++i) p[i][0] = p[i][1] = (u32x4){0u, 0u, 0u, 0u};
  {
    unsigned done = 0;
#pragma clang loop unroll(disable)
    for (unsigned spins = 0; spins < (1u << 22); ++spins) {
      const unsigned rdy = (unsigned)__builtin_amdgcn_ballot_w64(seen != 0u) & 0xeu & ~done;
      asm volatile("" ::: "memory");
#pragma unroll
      for (int i = 1; i < 4; ++i)
        if (rdy & (1u << i)) {
          const unsigned o = xo(kq ^ i, i);
          p[i - 1][0] = __builtin_amdgcn_raw_buffer_load_b128(rs, o, 0, 16);
          p[i - 1][1] = __builtin_amdgcn_raw_buffer_load_b128(rs, o + 1024, 0, 16);
        }
      done |= rdy;
      if (done == 0xeu) break;
      if (!rdy) __builtin_amdgcn_s_sleep(1);
      seen = __hip_atomic_load(mine, __ATOMIC_RELAXED, __HIP_MEMORY_SCOPE_AGENT);
    }
  }
  ST(5);
  const auto ors = __builtin_amdgcn_make_buffer_rsrc(out, 0, 4 << 20, 0x00020000);
#pragma unroll
  for (int h = 0; h < 2; ++h) {
    const auto x0 = __builtin_amdgcn_permlane16_swap(xr[h][0], xr[h][2], false, false), x1 = __builtin_amdgcn_permlane16_swap(xr[h][1], xr[h][3], false, false);
    const u32x4 xv = {x0[0], x1[0], x0[1], x1[1]};
    unsigned r[4];
#pragma unroll
    for (int d = 0; d < 4; ++d) {
      float lo = ac2[0][2 * h + (d >> 1)][2 * (d & 1)] + __uint_as_float(xv[d] << 16), hi = ac2[0][2 * h + (d >> 1)][2 * (d & 1) + 1] + __uint_as_float(xv[d] & 0xffff0000u);
#pragma unroll
      for (int i = 0; i < 3; ++i) { lo += __uint_as_float(p[i][h][d] << 16); hi += __uint_as_float(p[i][h][d] & 0xffff0000u); }
      r[d] = pk2(lo, hi);
    }
    const auto y0 = __builtin_amdgcn_permlane16_swap(r[0], r[2], false, false), y1 = __builtin_amdgcn_permlane16_swap(r[1], r[3], false, false);
    __builtin_amdgcn_raw_buffer_store_b128((u32x4){y0[0], y1[0], y0[1], y1[1]}, ors, orow + 64 * h, 0, OUT_AUX);
  }
  ST(6);
  ST(7);
  ST_OUT(1, 8);
  ST_REST(); ST_OUT(0, 6);
}

}

extern "C" __global__ __launch_bounds__(512, 2) void psum_g1_kernel(const u16* __restrict__ x, u16* __restrict__ out, const u16* __restrict__ wqkv,
                                                                    const u16* __restrict__ wg, const u16* __restrict__ wu,
                                                                    const u16* __restrict__ wo, const u16* __restrict__ wd, char* __restrict__ ws) {
  if (blockIdx.x < 128) body<true>(x, out, wqkv, wg, wu, wo, wd, ws);
  else body<false>(x, out, wqkv, wg, wu, wo, wd, ws);
}

extern "C" void kernel_launch(void* const* d_in, const int* in_sizes, int n_in, void* d_out, int out_size, void* d_ws,
                              size_t ws_size, hipStream_t stream) {
  const u16* x = (const u16*)d_in[0];
  const u16* wqkv = (const u16*)d_in[1];
  const u16* wo = (const u16*)d_in[2];
  const u16* wg = (const u16*)d_in[4];
  const u16* wu = (const u16*)d_in[5];
  const u16* wd = (const u16*)d_in[6];
  char* ws = (char*)d_ws;
  (void)hipFuncSetAttribute((const void*)psum_g1_kernel, hipFuncAttributeMaxDynamicSharedMemorySize, G1_LDS);
  psum_g1_kernel<<<256, 512, G1_LDS, stream>>>(x, (u16*)d_out, wqkv, wg, wu, wo, wd, ws);
}
```

```cpp
#define DIAG 10
#include <hip/hip_runtime.h>
#include <hip/hip_bf16.h>
#include <type_traits>
#define EARLY 1
#define HALO_SKIP 1
#define PF_W2 1
#define PF_LOOP 0
#define PF_R0 192
#define PF_PER 2
#define PF_LN_ROWS 96
#define PRIO_L 0
#define PRIO_M() __builtin_amdgcn_s_setprio(1)
#define PRIO_T() __builtin_amdgcn_s_setprio(3)
#define PRIO_E() __builtin_amdgcn_s_setprio(0)
#define PF_CODE 0
#define PRECLEAN 0
#define OUT_AUX 16
#define G2ORD 0
#define EARLY2 1
#define CHAIN 1
#define PF_X 0
#define XH 1
#define ACQ 0
#define EPI_PRIO 1
#define LORD 1
#define RPOS 1
#define FA 4
#define FB 0
#define PFD 0
#define SGLU 0
#define TLBW 0
#define WDELAY 0
#define HDEFER 1
#define H_AUX 16

namespace {

typedef unsigned short u16;
typedef short bf16x8 __attribute__((ext_vector_type(8)));
typedef short bf16x4 __attribute__((ext_vector_type(4)));
typedef float f32x4 __attribute__((ext_vector_type(4)));
typedef float f32x2 __attribute__((ext_vector_type(2)));
typedef unsigned u32x4 __attribute__((ext_vector_type(4)));
typedef unsigned u32x2 __attribute__((ext_vector_type(2)));
typedef __bf16 bf2 __attribute__((ext_vector_type(2)));
#define LAS __attribute__((address_space(3)))
#define GAS __attribute__((address_space(1)))

#define WAIT_V(n) asm volatile("s_waitcnt vmcnt(" #n ")" ::: "memory")
#define WAIT_V_IF0(n, c) asm volatile("s_cmp_lg_u32 %0, 0\n\ts_cbranch_scc1 1f\n\ts_waitcnt vmcnt(" #n ")\n1:" :: "s"(c) : "memory", "scc")
#define BAR_IF0(c) asm volatile("s_cmp_lg_u32 %0, 0\n\ts_cbranch_scc1 1f\n\ts_barrier\n\ts_setprio 0\n1:" :: "s"(c) : "memory", "scc")
#define WAIT_L0() do { asm volatile("" ::: "memory"); __builtin_amdgcn_s_waitcnt(0xC07F); asm volatile("" ::: "memory"); } while (0)
#define BAR() __builtin_amdgcn_s_barrier()
#define SCHED() __builtin_amdgcn_sched_barrier(0)

__device__ __forceinline__ float b2f(u16 v) { return __uint_as_float(((unsigned)v) << 16); }
__device__ __forceinline__ unsigned pk2(float lo, float hi) {
  bf2 r = {(__bf16)lo, (__bf16)hi};
  return __builtin_bit_cast(unsigned, r);
}
__device__ __forceinline__ float rbf(float f) { return (float)(__bf16)f; }
__device__ __forceinline__ bf16x8 pack8(f32x4 a, f32x4 b) {
  u32x4 r = {pk2(a[0], a[1]), pk2(a[2], a[3]), pk2(b[0], b[1]), pk2(b[2], b[3])};
  return __builtin_bit_cast(bf16x8, r);
}
__device__ __forceinline__ bf16x4 trd(LAS const char* p) {
  return __builtin_amdgcn_ds_read_tr16_b64_v4i16((LAS bf16x4*)p);
}
__device__ __forceinline__ bf16x8 cat(bf16x4 a, bf16x4 b) { return __builtin_shufflevector(a, b, 0, 1, 2, 3, 4, 5, 6, 7); }
__device__ __forceinline__ f32x4 mfma(bf16x8 a, bf16x8 b, f32x4 c) {
  return __builtin_amdgcn_mfma_f32_16x16x32_bf16(a, b, c, 0, 0, 0);
}
__device__ __forceinline__ void dma_first(unsigned lds_dst, unsigned voff, const char* b0) {
  asm volatile("s_mov_b32 m0, %0\n\ts_nop 0\n\tglobal_load_lds_dwordx4 %1, %2" :: "s"(lds_dst), "v"(voff), "s"(b0) : "memory");
}
template <int OFF>
__device__ __forceinline__ void dma_next(unsigned voff, const char* b) {
  asm volatile("global_load_lds_dwordx4 %0, %1 offset:%2" :: "v"(voff), "s"(b), "i"(OFF) : "memory");
}
__device__ __forceinline__ void dmc_first(unsigned lds_dst, unsigned voff, const char* b0) {
  asm volatile("s_mov_b32 m0, %0\n\ts_nop 0\n\tglobal_load_lds_dwordx4 %1, %2 sc1" :: "s"(lds_dst), "v"(voff), "s"(b0) : "memory");
}
template <int OFF>
__device__ __forceinline__ void dmc_next(unsigned voff, const char* b) {
  asm volatile("global_load_lds_dwordx4 %0, %1 offset:%2 sc1" :: "v"(voff), "s"(b), "i"(OFF) : "memory");
}
__device__ __forceinline__ void dma1m(unsigned lds_dst, const char* p, unsigned long long mask) {
  asm volatile("s_mov_b32 m0, %0\n\ts_mov_b64 exec, %2\n\t"
               "global_load_lds_dwordx4 %1, off sc1\n\t"
               "s_mov_b64 exec, -1"
               :: "s"(lds_dst), "v"(p), "s"(mask) : "memory");
}
__device__ __forceinline__ void touch(unsigned lds_dst, const void* p) {
  asm volatile("s_mov_b32 m0, %0\n\ts_nop 0\n\tglobal_load_lds_dword %1, off" :: "s"(lds_dst), "v"(p) : "memory");
}
#define CODE_ADDR(dst, sym) do { unsigned lo_, hi_; asm volatile("s_getpc_b64 s[90:91]\n\ts_add_u32 %0, s90, " #sym "@rel32@lo+4\n\ts_addc_u32 %1, s91, " #sym "@rel32@hi+12" \
    : "=s"(lo_), "=s"(hi_) :: "s90", "s91", "scc"); dst = (const char*)(((unsigned long long)hi_ << 32) | lo_); } while (0)
constexpr int LN_LINES = 16, G1_LINES = 80, G2_LINES = 30;
__device__ __forceinline__ LAS char* lp(unsigned a) { return (LAS char*)(size_t)a; }
__device__ __forceinline__ float fmx(float a, float b) { return __builtin_elementwise_maximum(a, b); }
__device__ __forceinline__ float xmax(float v) {
  auto a = __builtin_amdgcn_permlane16_swap(__float_as_uint(v), __float_as_uint(v), false, false);
  v = fmaxf(__uint_as_float(a[0]), __uint_as_float(a[1]));
  auto b = __builtin_amdgcn_permlane32_swap(__float_as_uint(v), __float_as_uint(v), false, false);
  return fmaxf(__uint_as_float(b[0]), __uint_as_float(b[1]));
}
__device__ __forceinline__ float xsum(float v) {
  auto a = __builtin_amdgcn_permlane16_swap(__float_as_uint(v), __float_as_uint(v), false, false);
  v = __uint_as_float(a[0]) + __uint_as_float(a[1]);
  auto b = __builtin_amdgcn_permlane32_swap(__float_as_uint(v), __float_as_uint(v), false, false);
  return __uint_as_float(b[0]) + __uint_as_float(b[1]);
}

template <int CTRL>
__device__ __forceinline__ float dpp_add(float v) {
  return v + __uint_as_float(__builtin_amdgcn_update_dpp(0u, __float_as_uint(v), CTRL, 0xf, 0xf, true));
}
__device__ __forceinline__ float hsum(float v) {
  v = dpp_add<0xB1>(v);
  v = dpp_add<0x4E>(v);
  v = dpp_add<0x128>(v);
  return xsum(v);
}

#define DG(kid, slot) do { if (threadIdx.x == DGT) ((unsigned long long*)(ws + WS_ST))[((kid) * 256 + blockIdx.x) * 8 + (slot)] = __builtin_amdgcn_s_memrealtime(); } while (0)
#define ST_DECL
#define LT_DECL
#define LT(k)
#define ET(k)
#define ST(k)
#define ST_OUT(kid, n)
#define ST_SAVE()
#define ST_REST()
constexpr int XN_STEP = 17 * 1024;
constexpr size_t WS_XN = 0;
constexpr size_t WS_H = WS_XN + (size_t)8 * 32 * XN_STEP;
constexpr size_t H_BYTES = (size_t)8 * 4 * 36 * 16 * 1024;
constexpr size_t WS_CNT = WS_H + H_BYTES;
constexpr size_t WS_GCNT = WS_CNT + 131072;
constexpr size_t WS_HCNT = WS_GCNT + 8192;
constexpr size_t WS_LNF = WS_HCNT + 32768;
constexpr size_t WS_ST = WS_LNF + 32768;

constexpr int G1_SA = 17 * 1024, G1_SB = 20 * 1024, G1_SLOT = G1_SA + G1_SB;
constexpr int T_SCR = 159488;
constexpr int T_BIAS = T_SCR + 256;
constexpr int G1_LDS = T_BIAS + 4096;
constexpr int G2_SA = 32768, G2_SB = 16384, G2_SLOT = G2_SA + G2_SB;
constexpr int T_SYNC = T_SCR - 64;
constexpr int T_KV = 2 * G2_SLOT, KV_BYTES = 7 * 1024 + 272 * 80;

template <bool MAX>
__device__ __forceinline__ float tred(float a, float b, float c, float d) {
  const auto r1 = __builtin_amdgcn_permlane16_swap(__float_as_uint(a), __float_as_uint(b), false, false);
  const auto r2 = __builtin_amdgcn_permlane16_swap(__float_as_uint(c), __float_as_uint(d), false, false);
  const float x0 = __uint_as_float(r1[0]), x1 = __uint_as_float(r1[1]), y0 = __uint_as_float(r2[0]), y1 = __uint_as_float(r2[1]);
  const float t = MAX ? fmx(x0, x1) : x0 + x1, u = MAX ? fmx(y0, y1) : y0 + y1;
  const auto r3 = __builtin_amdgcn_permlane32_swap(__float_as_uint(t), __float_as_uint(u), false, false);
  const float z0 = __uint_as_float(r3[0]), z1 = __uint_as_float(r3[1]);
  return MAX ? fmx(z0, z1) : z0 + z1;
}
__device__ __forceinline__ u32x2 glu4(f32x4 g, f32x4 u) {
  f32x4 e = g * -1.4426950408889634f;
#pragma unroll
  for (int i = 0; i < 4; ++i) e[i] = __builtin_amdgcn_exp2f(e[i]);
  e = e + 1.0f;
#pragma unroll
  for (int i = 0; i < 4; ++i) e[i] = __builtin_amdgcn_rcpf(e[i]);
  e = e * u;
  return (u32x2){pk2(e[0], e[1]), pk2(e[2], e[3])};
}
__device__ __forceinline__ u32x2 glu4s(f32x4 g, f32x4 u) {
  float e[4];
#pragma unroll
  for (int i = 0; i < 4; ++i) { e[i] = g[i] * -1.4426950408889634f; asm volatile("" : "+v"(e[i])); e[i] = __builtin_amdgcn_exp2f(e[i]); }
#pragma unroll
  for (int i = 0; i < 4; ++i) { e[i] = e[i] + 1.0f; asm volatile("" : "+v"(e[i])); e[i] = __builtin_amdgcn_rcpf(e[i]); }
#pragma unroll
  for (int i = 0; i < 4; ++i) { e[i] = e[i] * u[i]; asm volatile("" : "+v"(e[i])); }
  return (u32x2){pk2(e[0], e[1]), pk2(e[2], e[3])};
}
__device__ __forceinline__ u32x4 cat4(u32x2 a, u32x2 b) { return (u32x4){a[0], a[1], b[0], b[1]}; }

template <bool typeA>
__device__ __forceinline__ void body(const u16* __restrict__ x, u16* __restrict__ out, const u16* __restrict__ wqkv, const u16* __restrict__ wg,
                                     const u16* __restrict__ wu, const u16* __restrict__ wo, const u16* __restrict__ wd, char* __restrict__ ws) {
  extern __shared__ __attribute__((aligned(16))) char smem[];
  LAS char* lds = (LAS char*)smem;
  ST_DECL LT_DECL ST(0);
  const unsigned long long dg0 = __builtin_amdgcn_s_memrealtime(); unsigned long long dgp, dg1, dg2, dg3, dg4, dg5, dc2; const unsigned long long dc0 = __builtin_amdgcn_s_memtime();
  const int tid = threadIdx.x;
  int lane = tid & 63;
  const int w = __builtin_amdgcn_readfirstlane(tid >> 6), g = w >> 2, wi = w & 3;
  const int b = blockIdx.x, xcd = b & 7, idx = b >> 3;
  const int mb = (xcd & 1) * 4 + (idx & 3), kq = xcd >> 1, ntl = idx >> 2;
  const int j = ntl & 3;
  int fr = lane & 15, fq = lane >> 4;
  const int fA = 896 * kq + 64 * j;
  const int f0 = 896 * kq + 256 + 160 * j, odd = j & 1;
  const int fa = f0 + 32 * odd, fb = fa + 64, fc = odd ? f0 : f0 + 128;

  asm volatile("" : "+s"(wqkv), "+s"(wg), "+s"(wu), "+s"(ws), "+s"(x));
  const int mi = kq * 8 + ntl;
  const bool lnh = !typeA && w == 4 && j < 2, lnw = g == 0 || lnh;
  const int lr = (lnh ? (kq * 2 + j) * 2 : (mi & 1) * 8 + wi * 2) + ((lane >> 2) & 1), lf = lnh ? 16 : mi >> 1;
  const int lrow = lnh ? (mb > 0 ? mb * 256 - 16 : 0) + 2 * (lr & 7) + (lr >> 3) : mb * 256 + 32 * (lf >> 1) + 2 * lr + (lf & 1);
  u32x4 raw[4] = {{0u, 0u, 0u, 0u}, {0u, 0u, 0u, 0u}, {0u, 0u, 0u, 0u}, {0u, 0u, 0u, 0u}};
  asm volatile("s_cmp_eq_u32 %6, 0\n\ts_cbranch_scc1 1f\n\tglobal_load_dwordx4 %0, %4, %5\n\tglobal_load_dwordx4 %1, %4, %5 offset:512\n\t"
               "global_load_dwordx4 %2, %4, %5 offset:1024\n\tglobal_load_dwordx4 %3, %4, %5 offset:1536\n1:"
               : "+v"(raw[0]), "+v"(raw[1]), "+v"(raw[2]), "+v"(raw[3])
               : "v"(lrow * 2048 + (lane >> 3) * 64 + (lane & 3) * 16), "s"(x), "s"(__builtin_amdgcn_readfirstlane(lnw ? 1 : 0)) : "memory", "scc");
  GAS unsigned* lnf = (GAS unsigned*)(ws + WS_LNF) + mb * 1024 + (lane < 32 ? mi * 32 + lane : (lane - 32) * 32 + mi);
  unsigned entry = 0;
  asm volatile("s_cmp_lg_u32 %2, 7\n\ts_cbranch_scc1 1f\n\tglobal_load_dword %0, %1, off sc1\n1:" : "+v"(entry) : "v"(lnf), "s"(w) : "memory", "scc");

  const bool isq = typeA && wi < 3;
  const int ld = isq ? 3072 : 3584;
  const u16* q1 = isq ? wqkv + (wi * 1024 + 256 * kq + 64 * j) : ((!typeA && (wi & 2)) ? wu : wg) + (typeA ? fA : ((wi & 1) ? fb : fa));
  const char* const xnb = ws + WS_XN + (size_t)mb * 32 * XN_STEP;
  const char* c1 = g ? (const char*)q1 : xnb + wi * 4096;
  const int s1 = g ? 8 * ld - 1024 : 0, s2 = g ? 32 * ld - 2048 : 0, s3 = g ? 40 * ld - 3072 : 0;
  const int p1 = g ? 64 * ld : XN_STEP, p2 = g ? XN_STEP : 64 * 3584;
  const int d1 = (g ? G1_SA : 0) + wi * 4096, d2 = g ? 16384 + wi * 256 : G1_SA + 16384 + wi * 1024;
  const unsigned long long m2 = g ? 0xffffull : ~0ull;
  const int rr = lane >> 3, pc = lane & 7, fi = (pc >> 1) ^ ((rr >> 1) & 3), kr = (rr & 3) + 8 * (rr >> 2);
  const unsigned v1 = g ? kr * ld * 2 + fi * 32 + (pc & 1) * 16 : lane * 16;
  const unsigned lb = (unsigned)(size_t)lds;
  const char* c2;
  {
    const unsigned ow = (kr + 16 * (wi >> 1) + 4 * (wi & 1)) * 7168 + (typeA ? fi * 32 : (fi >> 1) * 32) + (pc & 1) * 16;
    const char* be = g ? xnb : (const char*)(typeA ? wu + fA : wg + fc);
    const char* bo = g ? be : (const char*)(wu + (typeA ? fA : fc));
    c2 = ((fi & 1) ? bo : be) + (g ? 16384 + wi * 256 + (lane & 15) * 16 : ow);
  }
  auto chain1 = [&](unsigned dst, const char* p) { dmc_first(dst + d1, v1, p); dmc_next<1024>(v1, p + s1); dmc_next<2048>(v1, p + s2); dmc_next<3072>(v1, p + s3); };
  auto first = [&](bool one) {
    if (one) { chain1(lb, c1); chain1(lb + G1_SLOT, c1 + p1); }
    else { dma1m(lb + d2, c2, m2); dma1m(lb + G1_SLOT + d2, c2 + p2, m2); }
  };
  if (w == 3) {
    char* z = lane < 8 ? ws + WS_CNT + (((mb * 8 + ntl) * 4 + kq) * 8 + lane) * 64
            : (lane < 10 ? ws + WS_HCNT + ((mb * 4 + kq) * 8 + ntl) * 128 + (lane - 8) * 16 : ws + WS_GCNT + (mb * 4 + kq) * 64);
    if (lane < (ntl == 0 ? 11 : 10)) asm volatile("global_store_dwordx4 %0, %1, off sc1\n\ts_nop 1" :: "v"(z), "v"((u32x4){0u, 0u, 0u, 0u}) : "memory");
  }
  if (!(HDEFER && lnh)) first(g == 1);
  if (lnw) {
    WAIT_V(8);
    WAIT_V_IF0(2, g);
    if (HDEFER && lnh) WAIT_V(0);
    asm volatile("" : "+v"(raw[0]), "+v"(raw[1]), "+v"(raw[2]), "+v"(raw[3]));
    float v[32];
#pragma unroll
    for (int i = 0; i < 16; ++i) { v[2 * i] = __uint_as_float(raw[i >> 2][i & 3] << 16); v[2 * i + 1] = __uint_as_float(raw[i >> 2][i & 3] & 0xffff0000u); }
    float s = 0.f;
#pragma unroll
    for (int i = 0; i < 32; ++i) s += v[i];
    const float mean = hsum(s) * (1.0f / 1024.0f);
    float q = 0.f;
#pragma unroll
    for (int i = 0; i < 32; ++i) { const float d = v[i] - mean; q += d * d; }
    q = hsum(q);
    const float mb16 = rbf(mean), den = rbf(sqrtf(rbf(rbf(q * (1.0f / 1024.0f)) + 1e-5f)));
    const float inv = 1.0f / den;
    const auto xrs = __builtin_amdgcn_make_buffer_rsrc((void*)xnb, 0, 32 * XN_STEP, 0x00020000);
    const unsigned o = (lane >> 3) * XN_STEP + lf * 1024 + lr * 64 + (((lane & 3) ^ (2 * (lr >> 3))) * 16);
#pragma unroll
    for (int i = 0; i < 4; ++i) {
      u32x4 t;
#pragma unroll
      for (int e = 0; e < 4; ++e) t[e] = pk2(rbf(v[8 * i + 2 * e] - mb16) * inv, rbf(v[8 * i + 2 * e + 1] - mb16) * inv);
      __builtin_amdgcn_raw_buffer_store_b128(t, xrs, o + i * 8 * XN_STEP, 0, 16);
    }
  }

  f32x4 acc[4][10];
  f32x4 hacc = {0.f, 0.f, 0.f, 0.f};
#pragma unroll
  for (int i = 0; i < 4; ++i)
#pragma unroll
    for (int n = 0; n < 10; ++n) { acc[i][n] = (f32x4){0.f, 0.f, 0.f, 0.f}; asm volatile("" : "+v"(acc[i][n])); }
  const int rl = 4 * (fq & 1) + (fr >> 2);
  unsigned hoffA = lb + 16384 + fr * 64 + ((fq ^ (2 * (fr >> 3))) * 16);
  unsigned aoff = hoffA - 16384 + wi * 4096;
  unsigned boff0 = lb + G1_SA + (fq >> 1) * 2048 + rl * 128 + 32 * ((2 * g) ^ ((rl >> 1) & 3)) + 8 * (lane & 3);
  unsigned boff1 = boff0 ^ 32;
  unsigned hoffB = (boff0 ^ (32 * (wi & 1))) + (1 + (wi >> 1)) * 4096;
  asm volatile("" : "+v"(aoff), "+v"(hoffA), "+v"(boff0), "+v"(boff1), "+v"(hoffB), "+v"(hacc));
  {
    const int lo = fr + (wi == 0 ? -7 : (wi == 2 ? 1 : 9));
    const int hi = wi == 0 ? fr + 1 : ((wi >= 2 && mb == 0) ? 0 : (wi == 2 ? 8 : 16));
    f32x4 bv;
#pragma unroll
    for (int e = 0; e < 4; ++e) bv[e] = (4 * fq + e >= lo && 4 * fq + e < hi) ? 0.f : -3.0e38f;
    *(LAS f32x4*)(lds + T_BIAS + wi * 1024 + lane * 16) = bv;
    *(LAS unsigned*)(lds + T_SYNC + (lane & 1) * 4) = 0u;
  }
  asm volatile("s_waitcnt vmcnt(8)" : "+v"(entry) :: "memory");
  if (lnw) WAIT_V(0);
  BAR();
  if (HDEFER && lnh) first(true);
  dgp = __builtin_amdgcn_s_memrealtime(); WAIT_L0();
  ST(1);
  if (w == 7) {
    if (lane >= 32) __hip_atomic_store(lnf, entry + 1u, __ATOMIC_RELAXED, __HIP_MEMORY_SCOPE_AGENT);
    for (unsigned spins = 0; spins < (1u << 22); ++spins) {
      unsigned now = entry + 1u;
      if (lane < 32) now = __hip_atomic_load(lnf, __ATOMIC_RELAXED, __HIP_MEMORY_SCOPE_AGENT);
      if (__builtin_amdgcn_ballot_w64(now == entry) == 0ull) break;
      __builtin_amdgcn_s_sleep(1);
    }
    if (lane >= 32) __hip_atomic_store(lnf, entry + 2u, __ATOMIC_RELAXED, __HIP_MEMORY_SCOPE_AGENT);
  }
  asm volatile("" ::: "memory");
  BAR();
  first(g == 0);
  c1 += 2 * p1; c2 += 2 * p2;
  unsigned ns = lb + 2 * G1_SLOT;
  auto adv = [&]() { c1 += p1; c2 += p2; ns = (ns == lb + 3 * G1_SLOT) ? lb : ns + G1_SLOT; };
  WAIT_V_IF0(0, g);
  BAR();
  dg1 = __builtin_amdgcn_s_memrealtime(); WAIT_L0();
  ST(2);
  if (g == 1) { WAIT_V(0); BAR(); }

  auto step = [&](auto halo_c, auto last_c, int q) {
    constexpr bool HALO = decltype(halo_c)::value, LAST = decltype(last_c)::value;
    bf16x8 a[2][4], wn[4][2];
    asm volatile("" ::: "memory");
    auto rdq = [&](int e, int h) {
      if (e >= (HALO ? 12 : 10)) return;
      const int kk = h >> 1, t = h & 1;
      bf16x8& d = wn[e & 3][kk];
      if (e == 11) { if (t == 0) d = *(const LAS bf16x8*)(lp(hoffA) + kk * G1_SLOT); return; }
      const int n = e < 2 ? 8 + e : e - 2;
      const bf16x4 v = trd(lp(e == 10 ? hoffB : ((n & 1) ? boff1 : boff0)) + (e == 10 ? 0 : (n >> 1) * 4096) + t * 1024 + kk * G1_SLOT);
      if (t == 0) { d[0] = v[0]; d[1] = v[1]; d[2] = v[2]; d[3] = v[3]; } else { d[4] = v[0]; d[5] = v[1]; d[6] = v[2]; d[7] = v[3]; }
    };
#define RA1(kk, i) a[kk][i] = *(const LAS bf16x8*)(lp(aoff) + (kk) * G1_SLOT + (i) * 1024);
    const int dlt = (q & 1) ? -2 * G1_SLOT : 2 * G1_SLOT;
    if (!LAST) {
#pragma unroll
      for (int kk = 0; kk < 2; ++kk) {
        dmc_first(ns + d1, v1, c1); RA1(kk, 0) rdq(kk, 0);
        dmc_next<1024>(v1, c1 + s1); RA1(kk, 1) rdq(kk, 1);
        dmc_next<2048>(v1, c1 + s2); RA1(kk, 2) rdq(kk, 2);
        dmc_next<3072>(v1, c1 + s3); RA1(kk, 3) rdq(kk, 3);
        dma1m(ns + d2, c2, m2);
        adv();
      }
    } else
    {
#pragma unroll
      for (int kk = 0; kk < 2; ++kk)
#pragma unroll
        for (int i = 0; i < 4; ++i) RA1(kk, i)
#pragma unroll
      for (int h = 0; h < 8; ++h) rdq(h >> 2, h & 3);
    }
    aoff += dlt;
    SCHED();
    if (!LAST) {
    } else {
#if PF_W2 && !(PFD & 1)
      int t = w * 64 + __builtin_amdgcn_mbcnt_hi(~0u, __builtin_amdgcn_mbcnt_lo(~0u, 0u));
      t = t < 288 ? t : t - 288;
      const int r2 = (t >> 4) * 64 + (xcd & 1) * 32 + idx;
      touch(lb + T_SCR, (r2 < 256 ? wo + (256 * kq + r2) * 1024 : wd + (896 * kq + r2 - 256) * 1024) + (t & 15) * 64);
#endif
    }
    WAIT_L0();
    SCHED();
    BAR();
    SCHED();
    PRIO_M();
    auto fin = [&]() { SCHED(); PRIO_T(); if (!LAST) { if (PFD) WAIT_V(1); else WAIT_V(0); } BAR(); SCHED(); };
#pragma unroll
    for (int e = 0; e < 10; ++e) {
      const int n = e < 2 ? 8 + e : e - 2;
#pragma unroll
      for (int i = 0; i < 4; ++i) {
        acc[i][n] = mfma(wn[e & 3][0], a[0][i], acc[i][n]);
        SCHED();
        if (!HALO && e == 9 && i == 3) fin();
        SCHED();
        acc[i][n] = mfma(wn[e & 3][1], a[1][i], acc[i][n]);
        SCHED();
        if (e == 0) { rdq(2 + (i >> 1), 2 * (i & 1)); rdq(2 + (i >> 1), 2 * (i & 1) + 1); } else rdq(e + 3, i);
        if (i == 3 && e < 9) { asm volatile("" :: "v"(wn[(e + 1) & 3][0]), "v"(wn[(e + 1) & 3][1])); }
        SCHED();
      }
    }
    if (HALO) {
      hacc = mfma(wn[2][0], wn[3][0], hacc);
      WAIT_L0();
      fin();
      hacc = mfma(wn[2][1], wn[3][1], hacc);
    }
    hoffA += dlt; boff0 += dlt; boff1 += dlt; hoffB += dlt;
    PRIO_E();
    SCHED();
  };
  const int nt = ntl;
  const int kba = 8 + ((fa - 896 * kq) >> 5);
  const int own0 = typeA ? j : (kba >> 1), own1 = typeA ? 4 + j : (kba >> 1) + 1;
  const char* const hbase = ws + WS_H + (size_t)(mb * 4 + kq) * 36 * 16384;
  auto brow = [&](int stp_, int w_) {
    return (const char*)(stp_ < 4 ? wo + (size_t)(256 * kq) * 1024 : wd + (size_t)(896 * kq) * 1024 - 256 * 1024) + ((stp_ * 64 + 8 * w_) * 1024 + nt * 128) * 2;
  };
  unsigned va, vb;
  auto stage1 = [&](auto halo_c) {
    constexpr bool TA = decltype(halo_c)::value;
    typedef std::integral_constant<bool, TA || !HALO_SKIP> HC;
#pragma clang loop unroll(disable)
    for (int q = 0; q < 15; ++q) step(HC{}, std::false_type{}, q);
    step(HC{}, std::true_type{}, 15);
    dg2 = __builtin_amdgcn_s_memrealtime(); dc2 = __builtin_amdgcn_s_memtime(); WAIT_L0();
    ST(3);
    asm volatile("" ::: "memory");
    if (g == 0) __builtin_amdgcn_s_setprio(2); else __builtin_amdgcn_s_setprio(EPI_PRIO);
    lane = __builtin_amdgcn_mbcnt_hi(~0u, __builtin_amdgcn_mbcnt_lo(~0u, 0u));
    asm volatile("" : "+v"(lane));
    fr = lane & 15; fq = lane >> 4;
    va = lane * 16;
    vb = (lane >> 3) * 2048 + ((((lane & 7) >> 1) ^ ((lane >> 4) & 3)) * 32) + (lane & 1) * 16;
    if (g == 1) {
#pragma unroll
      for (int u = 0; u < 4; ++u) {
        const int w_ = wi * 2 + (u & 1);
        const char* p0 = brow((u >> 1) ? own1 : own0, w_);
        dma_first(lb + (u >> 1) * G2_SLOT + G2_SA + w_ * 2048, vb, p0); dma_next<1024>(vb, p0 - 896);
      }
    }
    const auto hrs = __builtin_amdgcn_make_buffer_rsrc((void*)hbase, 0, 36 * 16384, 0x00020000);
    const unsigned hv = wi * 4096 + lane * 16;
    auto put = [&](u32x4 v, int s, int kb, int i) {
      __builtin_amdgcn_raw_buffer_store_b128(v, hrs, hv + kb * 16384 + i * 1024, 0, H_AUX);
      *(LAS u32x4*)(lds + s * G2_SLOT + g * 16384 + ((wi * 4096 + kq * 1024 + lane * 16) ^ (i * 1024))) = v;
    };
#define GLU(i, a, b) glu4(acc[i][a], acc[i][b])
    if (!TA) {
      const int kbc = 8 + ((fc - 896 * kq) >> 5);
#pragma unroll
      for (int i = 0; i < 4; ++i) {
        __builtin_amdgcn_raw_buffer_store_b64(GLU(i, 8, 9), hrs, hv + kbc * 16384 + g * 8 + i * 1024, 0, H_AUX);
        put(cat4(GLU(i, 0, 4), GLU(i, 1, 5)), 0, kba + g, i);
        put(cat4(GLU(i, 2, 6), GLU(i, 3, 7)), 1, kba + 2 + g, i);
        if (i == FB) { SCHED(); BAR_IF0(g); SCHED(); }
      }
    } else {
      auto glu_part = [&](int i0, int i1) {
#pragma unroll
        for (int i = i0; i < i1; ++i) put(cat4(GLU(i, 6, 8), GLU(i, 7, 9)), 1, 8 + 2 * j + g, i);
      };
      if (g == 0) { glu_part(0, FA); SCHED(); BAR(); __builtin_amdgcn_s_setprio(0); asm volatile("" ::: "memory"); }
    LAS char* kim = lds + T_KV + g * KV_BYTES;
    LAS char* vim = kim + 7 * 1024;
    bf16x8 kf[4], qf[4];
#pragma unroll
    for (int i = 0; i < 4; ++i) {
      kf[i] = pack8(acc[i][2], acc[i][3]);
      *(LAS bf16x8*)(vim + ((wi * 4 + i) * 16 + fr) * 80 + fq * 16) = pack8(acc[i][4], acc[i][5]);
    }
    if (wi < 3) {
      *(LAS bf16x8*)(kim + wi * 2048 + lane * 16) = kf[2];
      *(LAS bf16x8*)(kim + wi * 2048 + 1024 + lane * 16) = kf[3];
    }
    {
      LAS char* hp = (wi < 2) ? (kim + 6 * 1024 + lane * 16 + wi * 8) : (vim + (256 + fr) * 80 + fq * 16 + (wi - 2) * 8);
      *(LAS u32x2*)hp = (u32x2){pk2(hacc[0], hacc[1]), pk2(hacc[2], hacc[3])};
    }
    WAIT_L0();
    BAR();
    asm volatile("" ::: "memory");
    const bool halo = wi == 0;
    const f32x4 bc = *(const LAS f32x4*)(lds + T_BIAS + lane * 16), bp = *(const LAS f32x4*)(lds + T_BIAS + 1024 + lane * 16);
    const f32x4 bp0 = *(const LAS f32x4*)(lds + T_BIAS + (halo ? 2048 : 1024) + lane * 16);
    const f32x4 bp1 = *(const LAS f32x4*)(lds + T_BIAS + (halo ? 3072 : 1024) + lane * 16);
    const int pf0 = halo ? 16 : wi * 4 - 2, pf1 = halo ? 16 : wi * 4 - 1;
    const bf16x8 kp0 = *(const LAS bf16x8*)(kim + (halo ? 6 : 2 * wi - 2) * 1024 + lane * 16);
    const bf16x8 kp1 = *(const LAS bf16x8*)(kim + (halo ? 6 : 2 * wi - 1) * 1024 + lane * 16);
    const LAS char* vr = vim + (4 * fq + (fr >> 2)) * 80 + (lane & 3) * 16;
    bf16x4 vt[6][2];
#pragma unroll
    for (int d = 0; d < 2; ++d) {
      vt[0][d] = trd(vr + pf0 * 1280 + d * 8); vt[1][d] = trd(vr + pf1 * 1280 + d * 8);
#pragma unroll
      for (int i = 0; i < 4; ++i) vt[2 + i][d] = trd(vr + (wi * 4 + i) * 1280 + d * 8);
    }
    f32x4 sp[4], sc[4];
#pragma unroll
    for (int i = 0; i < 4; ++i) {
      qf[i] = pack8(acc[i][0], acc[i][1]);
      sc[i] = mfma(kf[i], qf[i], bc);
    }
    sp[2] = mfma(kf[0], qf[2], bp); sp[3] = mfma(kf[1], qf[3], bp);
    sp[0] = mfma(kp0, qf[0], bp0); sp[1] = mfma(kp1, qf[1], bp1);
    const float sl = 0.17677669529663687f * 1.4426950408889634f;
    const f32x4 z4 = {0.f, 0.f, 0.f, 0.f};
    float m[4];
#pragma unroll
    for (int i = 0; i < 4; ++i)
      m[i] = fmx(fmx(fmx(fmx(sp[i][0], sp[i][1]), sp[i][2]), fmx(fmx(sp[i][3], sc[i][0]), sc[i][1])), fmx(sc[i][2], sc[i][3]));
    {
      const unsigned t = __float_as_uint(tred<true>(m[0], m[1], m[2], m[3]));
      const auto u = __builtin_amdgcn_permlane32_swap(t, t, false, false);
      const auto u0 = __builtin_amdgcn_permlane16_swap(u[0], u[0], false, false), u1 = __builtin_amdgcn_permlane16_swap(u[1], u[1], false, false);
      m[0] = __uint_as_float(u0[0]); m[1] = __uint_as_float(u0[1]); m[2] = __uint_as_float(u1[0]); m[3] = __uint_as_float(u1[1]);
    }
    const u32x4 one4 = {0x3f803f80u, 0x3f803f80u, 0x3f803f80u, 0x3f803f80u};
#pragma unroll
    for (int i = 0; i < 4; ++i) {
      const float mm = -sl * m[i];
#pragma unroll
      for (int e = 0; e < 4; ++e) {
        sp[i][e] = __builtin_amdgcn_exp2f(__builtin_fmaf(sp[i][e], sl, mm));
        sc[i][e] = __builtin_amdgcn_exp2f(__builtin_fmaf(sc[i][e], sl, mm));
      }
      const bf16x8 pfr = pack8(sp[i], sc[i]);
      const f32x4 sm = mfma(__builtin_bit_cast(bf16x8, one4), pfr, z4);
      const f32x4 o0 = mfma(cat(vt[i][0], vt[2 + i][0]), pfr, z4);
      const f32x4 o1 = mfma(cat(vt[i][1], vt[2 + i][1]), pfr, z4);
      const float inv = __builtin_amdgcn_rcpf(sm[0]);
      put(__builtin_bit_cast(u32x4, pack8(o0 * inv, o1 * inv)), 0, 2 * j + g, i);
    }
      glu_part(FA, 4);
      if (g == 1) glu_part(0, FA);
    }
  };
  stage1(std::integral_constant<bool, typeA>{});
  ST(4);

  const int lag = g ^ 1;
  GAS unsigned* hflag = (GAS unsigned*)(ws + WS_HCNT) + (mb * 4 + kq) * 256;
  const unsigned fo = (nt * 32 + (lane & 7)) * 4;
  GAS unsigned* gcnt = (GAS unsigned*)(ws + WS_GCNT) + (mb * 4 + kq) * 16;
  const int e0 = kq * 1024, e1 = ((1 ^ kq) - 1) * 1024, e2 = ((2 ^ kq) - 2) * 1024, e3 = ((3 ^ kq) - 3) * 1024;
  auto nxt = [&](int s) { ++s; if (s == own0) ++s; if (s == own1) ++s; if (s == 18) s = (own0 == 0) ? 1 : 0; return s; };
  auto wait_cnt = [&](unsigned seen, GAS unsigned* c, unsigned need) {
    if ((unsigned)__builtin_amdgcn_readfirstlane(seen) < need) {
      asm volatile("" ::: "memory");
      unsigned spins = 0;
      while ((unsigned)__builtin_amdgcn_readfirstlane(__hip_atomic_load(c, __ATOMIC_RELAXED, __HIP_MEMORY_SCOPE_AGENT)) < need)
        if (++spins > (1u << 22)) break;
    }
    asm volatile("" ::: "memory");
  };
  auto wait_flags = [&](unsigned seen, unsigned long long need) {
    if ((__builtin_amdgcn_ballot_w64(seen != 0u) & need) != need) {
      asm volatile("" ::: "memory");
      unsigned spins = 0;
      while ((__builtin_amdgcn_ballot_w64(__hip_atomic_load(hflag + (fo >> 2), __ATOMIC_RELAXED, __HIP_MEMORY_SCOPE_AGENT) != 0u) & need) != need) {
        __builtin_amdgcn_s_sleep(1);
        if (++spins > (1u << 22)) break;
      }
    }
    asm volatile("" ::: "memory");
  };
  int stp = nxt(7);
  const int mf = wi * 4 + kq;
  const unsigned orow = ((mb * 256 + 32 * (mf >> 1) + 2 * fr + (mf & 1)) * 1024 + nt * 128 + g * 64 + 16 * (fq & 1) + 8 * (fq >> 1)) * 2;
  u32x4 xr[2] = {{0u, 0u, 0u, 0u}, {0u, 0u, 0u, 0u}};
  const int rl2 = 4 * (fq & 1) + (fr >> 2);
  unsigned ao = lb + wi * 4096 + lane * 16;
  unsigned bo = lb + G2_SA + (fq >> 1) * 2048 + g * 1024 + rl2 * 128 + 32 * ((rl2 >> 1) & 3) + 8 * (lane & 3);
  f32x4 ac2[4][4];
#pragma unroll
  for (int i = 0; i < 4; ++i)
#pragma unroll
    for (int n = 0; n < 4; ++n) { ac2[i][n] = (f32x4){0.f, 0.f, 0.f, 0.f}; asm volatile("" : "+v"(ac2[i][n])); }
  ns = lb + 2 * G2_SLOT;
  const char* ca = hbase + stp * 32768 + w * 4096;
  const char* cb = brow(stp, w);
  int sl2 = 0;
  WAIT_V_IF0(0, lag);
  WAIT_L0();
  BAR();
  dg3 = __builtin_amdgcn_s_memrealtime(); WAIT_L0();
  ST(5);
  ST_SAVE(); ST(0); ST(1);
  unsigned seen = 0;
#define PEEK(c, o) asm volatile("global_load_dword %0, %1, %2 sc1" : "+v"(seen) : "v"(o), "s"(c) : "memory")
  if (lag) BAR();

  auto phase2 = [&](auto mode, int q) {
    constexpr int MODE = decltype(mode)::value;
    bf16x8 a[2][4], bfr[2][4];
#define RA(kk, i) a[kk][i] = *(const LAS bf16x8*)(lp(ao) + (kk) * 16384 + (i) * 1024);
#define RB(kk, n) bfr[kk][n] = cat(trd(lp(bo ^ ((n) * 32)) + (kk) * 8192), trd(lp(bo ^ ((n) * 32)) + (kk) * 8192 + 4096));
    int dlt;
    asm volatile("" ::: "memory");
    if (MODE == 1 && q == 1 && w == 4) PEEK(hflag, fo);
    if (MODE == 2 && q == 16)
      asm volatile("global_load_dwordx4 %0, %2, %3\n\tglobal_load_dwordx4 %1, %2, %3 offset:64" : "+v"(xr[0]), "+v"(xr[1]) : "v"(orow), "s"(x) : "memory");
    RA(0, 0) RA(0, 1)
    if (MODE == 0) dmc_first(ns + w * 4096, va, ca + e0);
    RA(0, 2) RA(0, 3) RB(0, 0)
    if (MODE == 0) dmc_next<1024>(va, ca + e1);
    RB(0, 1) RB(0, 2) RB(0, 3)
    if (MODE == 0) dmc_next<2048>(va, ca + e2);
    RA(1, 0) RA(1, 1) RA(1, 2)
    if (MODE == 0) dmc_next<3072>(va, ca + e3);
    RA(1, 3) RB(1, 0)
    if (MODE < 2) dma_first(ns + G2_SA + w * 2048, vb, cb);
    RB(1, 1)
    if (MODE < 2) dma_next<1024>(vb, cb - 896);
    sl2 = (sl2 == 2) ? 0 : sl2 + 1; dlt = (sl2 == 0) ? -2 * G2_SLOT : G2_SLOT;
    if (MODE < 2) { stp = nxt(stp); ca = hbase + stp * 32768 + w * 4096; cb = brow(stp, w); ns = (ns == lb + 2 * G2_SLOT) ? lb : ns + G2_SLOT; }
    asm volatile("" : "+s"(ca), "+s"(cb), "+s"(ns), "+s"(sl2), "+s"(dlt), "+s"(stp) :: "memory");
    RB(1, 2) RB(1, 3)
#undef RA
#undef RB
    ao += dlt; bo += dlt;
    SCHED();
    asm volatile("" : "+v"(bo), "+v"(ao));
    if (MODE == 0) WAIT_V_IF0(6, lag ^ 1); else if (MODE == 2 && q == 16) WAIT_V_IF0(2, lag ^ 1);
    if (MODE == 1 && q == 1) {
      asm volatile("s_waitcnt vmcnt(2)" : "+v"(seen) :: "memory");
      ET(2);
      if (!lag) {
        volatile LAS unsigned* go = (volatile LAS unsigned*)(lds + T_SYNC + 4);
        if (w == 4) { wait_flags(seen, 0xf0ull); *go = 1u; }
        else { unsigned spins = 0; while (__builtin_amdgcn_readfirstlane(*go) == 0u) { __builtin_amdgcn_s_sleep(1); if (++spins > (1u << 24)) break; } }
        asm volatile("" ::: "memory");
      }
      ET(3);
      const int s2 = nxt(7), s = lag ? nxt(s2) : s2;
      const char* p = hbase + s * 32768 + wi * 8192;
      const unsigned d = lb + (lag ? 0 : 2 * G2_SLOT) + wi * 8192;
      dmc_first(d, va, p + e0); dmc_next<1024>(va, p + e1); dmc_next<2048>(va, p + e2); dmc_next<3072>(va, p + e3);
      dmc_first(d + 4096, va, p + 4096 + e0); dmc_next<1024>(va, p + 4096 + e1); dmc_next<2048>(va, p + 4096 + e2); dmc_next<3072>(va, p + 4096 + e3);
      ET(4);
    }
    WAIT_L0();
    SCHED();
    BAR();
    SCHED();
    PRIO_M();
#pragma unroll
    for (int n = 0; n < 4; ++n)
#pragma unroll
      for (int i = 0; i < 4; ++i) {
        ac2[i][n] = mfma(bfr[0][n], a[0][i], ac2[i][n]);
        SCHED();
        if (n == 3 && i == 3) {
          PRIO_T();
          if (MODE == 0) WAIT_V_IF0(6, lag); else if (MODE == 2 && q == 16) WAIT_V_IF0(2, lag);
          if (MODE == 1 && q == 1) WAIT_V_IF0(0, lag);
          if (q < 17 || !lag) BAR();
          SCHED();
        }
        ac2[i][n] = mfma(bfr[1][n], a[1][i], ac2[i][n]);
        SCHED();
        if (MODE == 1 && q == 0 && n == 1 && i == 3 && lag) {
          WAIT_V(0);
          unsigned old = 0;
          if (lane == 0) old = __hip_atomic_fetch_add((LAS unsigned*)(lds + T_SYNC), 1u, __ATOMIC_RELAXED, __HIP_MEMORY_SCOPE_WORKGROUP);
          if (__builtin_amdgcn_readfirstlane(old) == 3u && lane < 8)
            __hip_atomic_store(hflag + lane * 32 + nt, 1u, __ATOMIC_RELAXED, __HIP_MEMORY_SCOPE_AGENT);
          SCHED();
        }
      }
    PRIO_E();
    SCHED();
  };
  typedef std::integral_constant<int, 0> M0; typedef std::integral_constant<int, 1> M1; typedef std::integral_constant<int, 2> M2;
  phase2(M1{}, 0);
  ET(1);
  phase2(M1{}, 1);
  ET(5);
  dg4 = __builtin_amdgcn_s_memrealtime(); WAIT_L0();
  ST(2);
#pragma clang loop unroll(disable)
  for (int q = 2; q < 6; ++q) phase2(M0{}, q);
  PEEK(hflag, fo);
  phase2(M0{}, 6);
  asm volatile("" : "+v"(seen));
  wait_flags(seen, 0x0full);
#pragma clang loop unroll(disable)
  for (int q = 7; q < 13; ++q) phase2(M0{}, q);
  if (w == 4 && lane == 0) __hip_atomic_fetch_add(gcnt, 1u, __ATOMIC_RELAXED, __HIP_MEMORY_SCOPE_AGENT);
#pragma clang loop unroll(disable)
  for (int q = 13; q < 16; ++q) phase2(M0{}, q);
  phase2(M2{}, 16);
  PEEK(gcnt, 0u);
  phase2(M2{}, 17);
  asm volatile("s_waitcnt vmcnt(0)" : "+v"(seen) :: "memory");
  wait_cnt(seen, gcnt, 8u);
  dg5 = __builtin_amdgcn_s_memrealtime(); WAIT_L0();
  ST(3);
  const int tile = mb * 8 + nt;
  const auto rs = __builtin_amdgcn_make_buffer_rsrc(ws + WS_H + (size_t)mb * 4 * 36 * 16384, 0, 4 * 36 * 16384, 0x00020000);
  auto xo = [&](int src, int i) { const int u = 15 + nt * 3 + i; return (unsigned)((src * 36 + (u >= 36 ? u - 36 : u)) * 16384 + w * 2048) + lane * 16; };
#pragma unroll
  for (int i = 1; i < 4; ++i) {
    const unsigned o = xo(kq, i);
    __builtin_amdgcn_raw_buffer_store_b128(__builtin_bit_cast(u32x4, pack8(ac2[i][0], ac2[i][1])), rs, o, 0, 16);
    __builtin_amdgcn_raw_buffer_store_b128(__builtin_bit_cast(u32x4, pack8(ac2[i][2], ac2[i][3])), rs, o + 1024, 0, 16);
  }
  GAS unsigned* cnt = (GAS unsigned*)(ws + WS_CNT) + (tile * 32 + w) * 16;
  GAS unsigned* mine = cnt + kq * 128 + (lane & 3);
  seen = __hip_atomic_load(mine, __ATOMIC_RELAXED, __HIP_MEMORY_SCOPE_AGENT);
  WAIT_V(0);
  ST(4);
  asm volatile("" : "+v"(xr[0]), "+v"(xr[1]));
  if (lane >= 1 && lane < 4) __hip_atomic_store(cnt + (kq ^ lane) * 128 + lane, 1u, __ATOMIC_RELAXED, __HIP_MEMORY_SCOPE_AGENT);
  u32x4 p[3][2];
#pragma unroll
  for (int i = 0; i < 3; ++i) p[i][0] = p[i][1] = (u32x4){0u, 0u, 0u, 0u};
  {
    unsigned done = 0;
#pragma clang loop unroll(disable)
    for (unsigned spins = 0; spins < (1u << 22); ++spins) {
      const unsigned rdy = (unsigned)__builtin_amdgcn_ballot_w64(seen != 0u) & 0xeu & ~done;
      asm volatile("" ::: "memory");
#pragma unroll
      for (int i = 1; i < 4; ++i)
        if (rdy & (1u << i)) {
          const unsigned o = xo(kq ^ i, i);
          p[i - 1][0] = __builtin_amdgcn_raw_buffer_load_b128(rs, o, 0, 16);
          p[i - 1][1] = __builtin_amdgcn_raw_buffer_load_b128(rs, o + 1024, 0, 16);
        }
      done |= rdy;
      if (done == 0xeu) break;
      if (!rdy) __builtin_amdgcn_s_sleep(1);
      seen = __hip_atomic_load(mine, __ATOMIC_RELAXED, __HIP_MEMORY_SCOPE_AGENT);
    }
  }
  ST(5);
  const auto ors = __builtin_amdgcn_make_buffer_rsrc(out, 0, 4 << 20, 0x00020000);
#pragma unroll
  for (int h = 0; h < 2; ++h) {
    const auto x0 = __builtin_amdgcn_permlane16_swap(xr[h][0], xr[h][2], false, false), x1 = __builtin_amdgcn_permlane16_swap(xr[h][1], xr[h][3], false, false);
    const u32x4 xv = {x0[0], x1[0], x0[1], x1[1]};
    unsigned r[4];
#pragma unroll
    for (int d = 0; d < 4; ++d) {
      float lo = ac2[0][2 * h + (d >> 1)][2 * (d & 1)] + __uint_as_float(xv[d] << 16), hi = ac2[0][2 * h + (d >> 1)][2 * (d & 1) + 1] + __uint_as_float(xv[d] & 0xffff0000u);
#pragma unroll
      for (int i = 0; i < 3; ++i) { lo += __uint_as_float(p[i][h][d] << 16); hi += __uint_as_float(p[i][h][d] & 0xffff0000u); }
      r[d] = pk2(lo, hi);
    }
    const auto y0 = __builtin_amdgcn_permlane16_swap(r[0], r[2], false, false), y1 = __builtin_amdgcn_permlane16_swap(r[1], r[3], false, false);
    __builtin_amdgcn_raw_buffer_store_b128((u32x4){y0[0], y1[0], y0[1], y1[1]}, ors, orow + 64 * h, 0, OUT_AUX);
  }
  ST(6);
  if (b == 1 && w == 0) {
    WAIT_V(0);
    const unsigned long long dg6 = __builtin_amdgcn_s_memrealtime();
    const unsigned long long l0 = dg0;
    unsigned long long q = DIAG == 1 ? dgp - dg0 : DIAG == 11 ? dg1 - dgp : DIAG == 2 ? dg2 - dg1 : DIAG == 3 ? dg3 - dg2 : DIAG == 4 ? dg4 - dg3 : DIAG == 5 ? dg5 - dg4 : DIAG == 6 ? dg6 - dg5 : DIAG == 7 ? dg6 - l0 : DIAG == 8 ? (dc2 - dc0) / 20 : DIAG == 9 ? (__builtin_amdgcn_s_memtime() - dc2) / 20 : 0;
    if (q > 20000) q = 20000;
    while (__builtin_amdgcn_s_memrealtime() < dg6 + 500 + 4 * q) __builtin_amdgcn_s_sleep(1);
  }
  ST(7);
  ST_OUT(1, 8);
  ST_REST(); ST_OUT(0, 6);
}

}

extern "C" __global__ __launch_bounds__(512, 2) void psum_g1_kernel(const u16* __restrict__ x, u16* __restrict__ out, const u16* __restrict__ wqkv,
                                                                    const u16* __restrict__ wg, const u16* __restrict__ wu,
                                                                    const u16* __restrict__ wo, const u16* __restrict__ wd, char* __restrict__ ws) {
  if (blockIdx.x < 128) body<true>(x, out, wqkv, wg, wu, wo, wd, ws);
  else body<false>(x, out, wqkv, wg, wu, wo, wd, ws);
}

extern "C" void kernel_launch(void* const* d_in, const int* in_sizes, int n_in, void* d_out, int out_size, void* d_ws,
                              size_t ws_size, hipStream_t stream) {
  const u16* x = (const u16*)d_in[0];
  const u16* wqkv = (const u16*)d_in[1];
  const u16* wo = (const u16*)d_in[2];
  const u16* wg = (const u16*)d_in[4];
  const u16* wu = (const u16*)d_in[5];
  const u16* wd = (const u16*)d_in[6];
  char* ws = (char*)d_ws;
  (void)hipFuncSetAttribute((const void*)psum_g1_kernel, hipFuncAttributeMaxDynamicSharedMemorySize, G1_LDS);
  psum_g1_kernel<<<256, 512, G1_LDS, stream>>>(x, (u16*)d_out, wqkv, wg, wu, wo, wd, ws);
}
```

```cpp
#include <hip/hip_runtime.h>
#include <hip/hip_bf16.h>
#include <type_traits>
#define EARLY 1
#define HALO_SKIP 1
#define PF_W2 1
#define PF_LOOP 0
#define PF_R0 192
#define PF_PER 2
#define PF_LN_ROWS 96
#define PRIO_L 0
#define PRIO_M() __builtin_amdgcn_s_setprio(1)
#define PRIO_T() __builtin_amdgcn_s_setprio(3)
#define PRIO_E() __builtin_amdgcn_s_setprio(0)
#define PF_CODE 0
#define PRECLEAN 0
#define OUT_AUX 16
#define G2ORD 0
#define EARLY2 1
#define CHAIN 1
#define PF_X 0
#define XH 1
#define ACQ 0
#define EPI_PRIO 1
#define LORD 1
#define RPOS 1
#define FA 4
#define FB 0
#define PFD 0
#define SGLU 0
#define TLBW 0
#define WDELAY 0
#define HDEFER 1
#define P2L 1
#define P2_M() __builtin_amdgcn_s_setprio(0)
#define P2_T() __builtin_amdgcn_s_setprio(1)
#define P2_E()
#define SPIN_CAP (1u << 17)
#define H_AUX 16

namespace {

typedef unsigned short u16;
typedef short bf16x8 __attribute__((ext_vector_type(8)));
typedef short bf16x4 __attribute__((ext_vector_type(4)));
typedef float f32x4 __attribute__((ext_vector_type(4)));
typedef float f32x2 __attribute__((ext_vector_type(2)));
typedef unsigned u32x4 __attribute__((ext_vector_type(4)));
typedef unsigned u32x2 __attribute__((ext_vector_type(2)));
typedef __bf16 bf2 __attribute__((ext_vector_type(2)));
#define LAS __attribute__((address_space(3)))
#define GAS __attribute__((address_space(1)))

#define WAIT_V(n) asm volatile("s_waitcnt vmcnt(" #n ")" ::: "memory")
#define WAIT_V_IF0(n, c) asm volatile("s_cmp_lg_u32 %0, 0\n\ts_cbranch_scc1 1f\n\ts_waitcnt vmcnt(" #n ")\n1:" :: "s"(c) : "memory", "scc")
#define BAR_IF0(c) asm volatile("s_cmp_lg_u32 %0, 0\n\ts_cbranch_scc1 1f\n\ts_barrier\n\ts_setprio 0\n1:" :: "s"(c) : "memory", "scc")
#define WAIT_L0() do { asm volatile("" ::: "memory"); __builtin_amdgcn_s_waitcnt(0xC07F); asm volatile("" ::: "memory"); } while (0)
#define BAR() __builtin_amdgcn_s_barrier()
#define SCHED() __builtin_amdgcn_sched_barrier(0)

__device__ __forceinline__ float b2f(u16 v) { return __uint_as_float(((unsigned)v) << 16); }
__device__ __forceinline__ unsigned pk2(float lo, float hi) {
  bf2 r = {(__bf16)lo, (__bf16)hi};
  return __builtin_bit_cast(unsigned, r);
}
__device__ __forceinline__ float rbf(float f) { return (float)(__bf16)f; }
__device__ __forceinline__ bf16x8 pack8(f32x4 a, f32x4 b) {
  u32x4 r = {pk2(a[0], a[1]), pk2(a[2], a[3]), pk2(b[0], b[1]), pk2(b[2], b[3])};
  return __builtin_bit_cast(bf16x8, r);
}
__device__ __forceinline__ bf16x4 trd(LAS const char* p) {
  return __builtin_amdgcn_ds_read_tr16_b64_v4i16((LAS bf16x4*)p);
}
__device__ __forceinline__ bf16x8 cat(bf16x4 a, bf16x4 b) { return __builtin_shufflevector(a, b, 0, 1, 2, 3, 4, 5, 6, 7); }
__device__ __forceinline__ f32x4 mfma(bf16x8 a, bf16x8 b, f32x4 c) {
  return __builtin_amdgcn_mfma_f32_16x16x32_bf16(a, b, c, 0, 0, 0);
}
__device__ __forceinline__ void dma_first(unsigned lds_dst, unsigned voff, const char* b0) {
  asm volatile("s_mov_b32 m0, %0\n\ts_nop 0\n\tglobal_load_lds_dwordx4 %1, %2" :: "s"(lds_dst), "v"(voff), "s"(b0) : "memory");
}
template <int OFF>
__device__ __forceinline__ void dma_next(unsigned voff, const char* b) {
  asm volatile("global_load_lds_dwordx4 %0, %1 offset:%2" :: "v"(voff), "s"(b), "i"(OFF) : "memory");
}
__device__ __forceinline__ void dmc_first(unsigned lds_dst, unsigned voff, const char* b0) {
  asm volatile("s_mov_b32 m0, %0\n\ts_nop 0\n\tglobal_load_lds_dwordx4 %1, %2 sc1" :: "s"(lds_dst), "v"(voff), "s"(b0) : "memory");
}
template <int OFF>
__device__ __forceinline__ void dmc_next(unsigned voff, const char* b) {
  asm volatile("global_load_lds_dwordx4 %0, %1 offset:%2 sc1" :: "v"(voff), "s"(b), "i"(OFF) : "memory");
}
__device__ __forceinline__ void dma1m(unsigned lds_dst, const char* p, unsigned long long mask) {
  asm volatile("s_mov_b32 m0, %0\n\ts_mov_b64 exec, %2\n\t"
               "global_load_lds_dwordx4 %1, off sc1\n\t"
               "s_mov_b64 exec, -1"
               :: "s"(lds_dst), "v"(p), "s"(mask) : "memory");
}
__device__ __forceinline__ void touch(unsigned lds_dst, const void* p) {
  asm volatile("s_mov_b32 m0, %0\n\ts_nop 0\n\tglobal_load_lds_dword %1, off" :: "s"(lds_dst), "v"(p) : "memory");
}
#define CODE_ADDR(dst, sym) do { unsigned lo_, hi_; asm volatile("s_getpc_b64 s[90:91]\n\ts_add_u32 %0, s90, " #sym "@rel32@lo+4\n\ts_addc_u32 %1, s91, " #sym "@rel32@hi+12" \
    : "=s"(lo_), "=s"(hi_) :: "s90", "s91", "scc"); dst = (const char*)(((unsigned long long)hi_ << 32) | lo_); } while (0)
constexpr int LN_LINES = 16, G1_LINES = 80, G2_LINES = 30;
__device__ __forceinline__ LAS char* lp(unsigned a) { return (LAS char*)(size_t)a; }
__device__ __forceinline__ float fmx(float a, float b) { return __builtin_elementwise_maximum(a, b); }
__device__ __forceinline__ float xmax(float v) {
  auto a = __builtin_amdgcn_permlane16_swap(__float_as_uint(v), __float_as_uint(v), false, false);
  v = fmaxf(__uint_as_float(a[0]), __uint_as_float(a[1]));
  auto b = __builtin_amdgcn_permlane32_swap(__float_as_uint(v), __float_as_uint(v), false, false);
  return fmaxf(__uint_as_float(b[0]), __uint_as_float(b[1]));
}
__device__ __forceinline__ float xsum(float v) {
  auto a = __builtin_amdgcn_permlane16_swap(__float_as_uint(v), __float_as_uint(v), false, false);
  v = __uint_as_float(a[0]) + __uint_as_float(a[1]);
  auto b = __builtin_amdgcn_permlane32_swap(__float_as_uint(v), __float_as_uint(v), false, false);
  return __uint_as_float(b[0]) + __uint_as_float(b[1]);
}

template <int CTRL>
__device__ __forceinline__ float dpp_add(float v) {
  return v + __uint_as_float(__builtin_amdgcn_update_dpp(0u, __float_as_uint(v), CTRL, 0xf, 0xf, true));
}
__device__ __forceinline__ float hsum(float v) {
  v = dpp_add<0xB1>(v);
  v = dpp_add<0x4E>(v);
  v = dpp_add<0x128>(v);
  return xsum(v);
}

#define DG(kid, slot)
#define ST_DECL
#define LT_DECL
#define LT(k)
#define ET(k)
#define ST(k)
#define ST_OUT(kid, n)
#define ST_SAVE()
#define ST_REST()
constexpr int XN_STEP = 17 * 1024;
constexpr size_t WS_XN = 0;
constexpr size_t WS_H = WS_XN + (size_t)8 * 32 * XN_STEP;
constexpr size_t H_BYTES = (size_t)8 * 4 * 36 * 16 * 1024;
constexpr size_t WS_CNT = WS_H + H_BYTES;
constexpr size_t WS_GCNT = WS_CNT + 131072;
constexpr size_t WS_HCNT = WS_GCNT + 8192;
constexpr size_t WS_LNF = WS_HCNT + 32768;
constexpr size_t WS_ST = WS_LNF + 32768;

constexpr int G1_SA = 17 * 1024, G1_SB = 20 * 1024, G1_SLOT = G1_SA + G1_SB;
constexpr int T_SCR = 159488;
constexpr int T_BIAS = T_SCR + 256;
constexpr int G1_LDS = T_BIAS + 4096;
constexpr int G2_SA = 32768, G2_SB = 16384, G2_SLOT = G2_SA + G2_SB;
constexpr int T_SYNC = T_SCR - 64;
constexpr int T_KV = 2 * G2_SLOT, KV_BYTES = 7 * 1024 + 272 * 80;

template <bool MAX>
__device__ __forceinline__ float tred(float a, float b, float c, float d) {
  const auto r1 = __builtin_amdgcn_permlane16_swap(__float_as_uint(a), __float_as_uint(b), false, false);
  const auto r2 = __builtin_amdgcn_permlane16_swap(__float_as_uint(c), __float_as_uint(d), false, false);
  const float x0 = __uint_as_float(r1[0]), x1 = __uint_as_float(r1[1]), y0 = __uint_as_float(r2[0]), y1 = __uint_as_float(r2[1]);
  const float t = MAX ? fmx(x0, x1) : x0 + x1, u = MAX ? fmx(y0, y1) : y0 + y1;
  const auto r3 = __builtin_amdgcn_permlane32_swap(__float_as_uint(t), __float_as_uint(u), false, false);
  const float z0 = __uint_as_float(r3[0]), z1 = __uint_as_float(r3[1]);
  return MAX ? fmx(z0, z1) : z0 + z1;
}
__device__ __forceinline__ u32x2 glu4(f32x4 g, f32x4 u) {
  f32x4 e = g * -1.4426950408889634f;
#pragma unroll
  for (int i = 0; i < 4; ++i) e[i] = __builtin_amdgcn_exp2f(e[i]);
  e = e + 1.0f;
#pragma unroll
  for (int i = 0; i < 4; ++i) e[i] = __builtin_amdgcn_rcpf(e[i]);
  e = e * u;
  return (u32x2){pk2(e[0], e[1]), pk2(e[2], e[3])};
}
__device__ __forceinline__ u32x2 glu4s(f32x4 g, f32x4 u) {
  float e[4];
#pragma unroll
  for (int i = 0; i < 4; ++i) { e[i] = g[i] * -1.4426950408889634f; asm volatile("" : "+v"(e[i])); e[i] = __builtin_amdgcn_exp2f(e[i]); }
#pragma unroll
  for (int i = 0; i < 4; ++i) { e[i] = e[i] + 1.0f; asm volatile("" : "+v"(e[i])); e[i] = __builtin_amdgcn_rcpf(e[i]); }
#pragma unroll
  for (int i = 0; i < 4; ++i) { e[i] = e[i] * u[i]; asm volatile("" : "+v"(e[i])); }
  return (u32x2){pk2(e[0], e[1]), pk2(e[2], e[3])};
}
__device__ __forceinline__ u32x4 cat4(u32x2 a, u32x2 b) { return (u32x4){a[0], a[1], b[0], b[1]}; }

constexpr unsigned long long step_order(bool ta, int j, int part) {
  const int own0 = ta ? j : (16 + 5 * j + (j & 1)) / 2, own1 = ta ? 4 + j : own0 + 1;
  unsigned long long r[2] = {0, 0};
  int p = 0;
  for (int t = 0; t < 18; ++t) {
    const int s = (t + 8) % 18;
    if (s == own0 || s == own1) continue;
    r[p / 12] |= (unsigned long long)s << (5 * (p % 12));
    ++p;
  }
  return r[part];
}

template <bool typeA>
__device__ __forceinline__ void body(const u16* __restrict__ x, u16* __restrict__ out, const u16* __restrict__ wqkv, const u16* __restrict__ wg,
                                     const u16* __restrict__ wu, const u16* __restrict__ wo, const u16* __restrict__ wd, char* __restrict__ ws) {
  extern __shared__ __attribute__((aligned(16))) char smem[];
  LAS char* lds = (LAS char*)smem;
  ST_DECL LT_DECL ST(0);
  const int tid = threadIdx.x;
  int lane = tid & 63;
  const int w = __builtin_amdgcn_readfirstlane(tid >> 6), g = w >> 2, wi = w & 3;
  const int b = blockIdx.x, xcd = b & 7, idx = b >> 3;
  const int mb = (xcd & 1) * 4 + (idx & 3), kq = xcd >> 1, ntl = idx >> 2;
  const int j = ntl & 3;
  int fr = lane & 15, fq = lane >> 4;
  const int fA = 896 * kq + 64 * j;
  const int f0 = 896 * kq + 256 + 160 * j, odd = j & 1;
  const int fa = f0 + 32 * odd, fb = fa + 64, fc = odd ? f0 : f0 + 128;

  const int mi = kq * 8 + ntl;
  const bool lnh = !typeA && w == 4 && j < 2, lnw = g == 0 || lnh;
  const char* const xnb = ws + WS_XN + (size_t)mb * 32 * XN_STEP;
  if (lnw) {
    __builtin_amdgcn_s_setprio(2);
    const int lr = (lnh ? (kq * 2 + j) * 2 : (mi & 1) * 8 + wi * 2) + ((lane >> 2) & 1), lf = lnh ? 16 : mi >> 1;
    const int lrow = lnh ? (mb > 0 ? mb * 256 - 16 : 0) + 2 * (lr & 7) + (lr >> 3) : mb * 256 + 32 * (lf >> 1) + 2 * lr + (lf & 1);
    const char* xr_ = (const char*)x + (lrow * 2048 + (lane >> 3) * 64 + (lane & 3) * 16);
    u32x4 raw[4];
#pragma unroll
    for (int i = 0; i < 4; ++i) raw[i] = *(const u32x4*)(xr_ + 512 * i);
    const auto xrs = __builtin_amdgcn_make_buffer_rsrc((void*)xnb, 0, 32 * XN_STEP, 0x00020000);
    const unsigned o = (lane >> 3) * XN_STEP + lf * 1024 + lr * 64 + (((lane & 3) ^ (2 * (lr >> 3))) * 16);
    ET(1);
    f32x2 v[16];
#pragma unroll
    for (int i = 0; i < 16; ++i) v[i] = (f32x2){__uint_as_float(raw[i >> 2][i & 3] << 16), __uint_as_float(raw[i >> 2][i & 3] & 0xffff0000u)};
    f32x2 s2 = (v[0] + v[1]) + (v[2] + v[3]);
#pragma unroll
    for (int i = 4; i < 16; i += 4) s2 += (v[i] + v[i + 1]) + (v[i + 2] + v[i + 3]);
    ET(2);
    const float mean = hsum(s2[0] + s2[1]) * (1.0f / 1024.0f);
    f32x2 qa = {0.f, 0.f}, qb = {0.f, 0.f};
#pragma unroll
    for (int i = 0; i < 16; i += 2) { const f32x2 d = v[i] - mean, e = v[i + 1] - mean; qa += d * d; qb += e * e; }
    qa += qb;
    const float var = hsum(qa[0] + qa[1]) * (1.0f / 1024.0f);
    const float inv = __builtin_amdgcn_rcpf(rbf(__builtin_amdgcn_sqrtf(rbf(rbf(var) + 1e-5f)))), sh = -rbf(mean);
#pragma unroll
    for (int i = 0; i < 4; ++i) {
      u32x4 t;
#pragma unroll
      for (int e = 0; e < 4; ++e) { const f32x2 y = (v[4 * i + e] + sh) * inv; t[e] = pk2(y[0], y[1]); }
      __builtin_amdgcn_raw_buffer_store_b128(t, xrs, o + i * 8 * XN_STEP, 0, 16);
    }
    __builtin_amdgcn_s_setprio(0);
  }
  GAS unsigned* lnf = (GAS unsigned*)(ws + WS_LNF) + mb * 1024 + (lane < 32 ? mi * 32 + lane : (lane - 32) * 32 + mi);
  unsigned entry = 0;
  asm volatile("s_cmp_lg_u32 %2, 7\n\ts_cbranch_scc1 1f\n\tglobal_load_dword %0, %1, off sc1\n1:" : "+v"(entry) : "v"(lnf), "s"(w) : "memory", "scc");
  if (w == 3) {
    char* z = lane < 8 ? ws + WS_CNT + (((mb * 8 + ntl) * 4 + kq) * 8 + lane) * 64
            : (lane < 10 ? ws + WS_HCNT + ((mb * 4 + kq) * 8 + ntl) * 128 + (lane - 8) * 16 : ws + WS_GCNT + (mb * 4 + kq) * 64);
    if (lane < (ntl == 0 ? 11 : 10)) asm volatile("global_store_dwordx4 %0, %1, off sc1\n\ts_nop 1" :: "v"(z), "v"((u32x4){0u, 0u, 0u, 0u}) : "memory");
  }

  const bool isq = typeA && wi < 3;
  const int ld = isq ? 3072 : 3584;
  const u16* q1 = isq ? wqkv + (wi * 1024 + 256 * kq + 64 * j) : ((!typeA && (wi & 2)) ? wu : wg) + (typeA ? fA : ((wi & 1) ? fb : fa));
  const char* c1 = g ? (const char*)q1 : xnb + wi * 4096;
  const int s1 = g ? 8 * ld - 1024 : 0, s2 = g ? 32 * ld - 2048 : 0, s3 = g ? 40 * ld - 3072 : 0;
  const int p1 = g ? 64 * ld : XN_STEP, p2 = g ? XN_STEP : 64 * 3584;
  const int d1 = (g ? G1_SA : 0) + wi * 4096, d2 = g ? 16384 + wi * 256 : G1_SA + 16384 + wi * 1024;
  const unsigned long long m2 = g ? 0xffffull : ~0ull;
  const int rr = lane >> 3, pc = lane & 7, fi = (pc >> 1) ^ ((rr >> 1) & 3), kr = (rr & 3) + 8 * (rr >> 2);
  const unsigned v1 = g ? kr * ld * 2 + fi * 32 + (pc & 1) * 16 : lane * 16;
  const unsigned lb = (unsigned)(size_t)lds;
  const char* c2;
  {
    const unsigned ow = (kr + 16 * (wi >> 1) + 4 * (wi & 1)) * 7168 + (typeA ? fi * 32 : (fi >> 1) * 32) + (pc & 1) * 16;
    const char* be = g ? xnb : (const char*)(typeA ? wu + fA : wg + fc);
    const char* bo = g ? be : (const char*)(wu + (typeA ? fA : fc));
    c2 = ((fi & 1) ? bo : be) + (g ? 16384 + wi * 256 + (lane & 15) * 16 : ow);
  }
  auto chain1 = [&](unsigned dst, const char* p) { dmc_first(dst + d1, v1, p); dmc_next<1024>(v1, p + s1); dmc_next<2048>(v1, p + s2); dmc_next<3072>(v1, p + s3); };
  auto first = [&](bool one) {
    if (one) { chain1(lb, c1); chain1(lb + G1_SLOT, c1 + p1); }
    else { dma1m(lb + d2, c2, m2); dma1m(lb + G1_SLOT + d2, c2 + p2, m2); }
  };
  if (!lnw) first(true);
  else WAIT_V(0);
  asm volatile("s_waitcnt vmcnt(8)" : "+v"(entry) :: "memory");
  ET(3);
  BAR();
  if (w == 7 && lane >= 32) __hip_atomic_store(lnf, entry + 1u, __ATOMIC_RELAXED, __HIP_MEMORY_SCOPE_AGENT);
  if (lnw) first(g == 1);
  ST(1);

  const int nt = ntl;
  const int kba = 8 + ((fa - 896 * kq) >> 5);
  const int own0 = typeA ? j : (kba >> 1), own1 = typeA ? 4 + j : (kba >> 1) + 1;
  f32x4 acc[4][10];
  f32x4 hacc = {0.f, 0.f, 0.f, 0.f};
#pragma unroll
  for (int i = 0; i < 4; ++i)
#pragma unroll
    for (int n = 0; n < 10; ++n) { acc[i][n] = (f32x4){0.f, 0.f, 0.f, 0.f}; asm volatile("" : "+v"(acc[i][n])); }
  const int rl = 4 * (fq & 1) + (fr >> 2);
  unsigned hoffA = lb + 16384 + fr * 64 + ((fq ^ (2 * (fr >> 3))) * 16);
  unsigned aoff = hoffA - 16384 + wi * 4096;
  unsigned boff0 = lb + G1_SA + (fq >> 1) * 2048 + rl * 128 + 32 * ((2 * g) ^ ((rl >> 1) & 3)) + 8 * (lane & 3);
  unsigned boff1 = boff0 ^ 32;
  unsigned hoffB = (boff0 ^ (32 * (wi & 1))) + (1 + (wi >> 1)) * 4096;
  asm volatile("" : "+v"(aoff), "+v"(hoffA), "+v"(boff0), "+v"(boff1), "+v"(hoffB), "+v"(hacc));
  {
    const int lo = fr + (wi == 0 ? -7 : (wi == 2 ? 1 : 9));
    const int hi = wi == 0 ? fr + 1 : ((wi >= 2 && mb == 0) ? 0 : (wi == 2 ? 8 : 16));
    f32x4 bv;
#pragma unroll
    for (int e = 0; e < 4; ++e) bv[e] = (4 * fq + e >= lo && 4 * fq + e < hi) ? 0.f : -3.0e38f;
    *(LAS f32x4*)(lds + T_BIAS + wi * 1024 + lane * 16) = bv;
    *(LAS unsigned*)(lds + T_SYNC + (lane & 1) * 4) = 0u;
  }
  ET(4);
  if (w == 7) {
    for (unsigned spins = 0; spins < SPIN_CAP; ++spins) {
      unsigned now = entry + 1u;
      if (lane < 32) now = __hip_atomic_load(lnf, __ATOMIC_RELAXED, __HIP_MEMORY_SCOPE_AGENT);
      if (__builtin_amdgcn_ballot_w64(now == entry) == 0ull) break;
      __builtin_amdgcn_s_sleep(1);
    }
    if (lane >= 32) __hip_atomic_store(lnf, entry + 2u, __ATOMIC_RELAXED, __HIP_MEMORY_SCOPE_AGENT);
  }
  WAIT_V_IF0(0, g);
  BAR();
  ET(5);
  first(g == 0);
  c1 += 2 * p1; c2 += 2 * p2;
  unsigned ns = lb + 2 * G1_SLOT;
  auto adv = [&]() { c1 += p1; c2 += p2; ns = (ns == lb + 3 * G1_SLOT) ? lb : ns + G1_SLOT; };
  WAIT_V(0);
  ST(2);
  if (g == 1) BAR();

  auto step = [&](auto halo_c, auto last_c, int q) {
    constexpr bool HALO = decltype(halo_c)::value, LAST = decltype(last_c)::value;
    bf16x8 a[2][4], wn[4][2];
    asm volatile("" ::: "memory");
    auto rdq = [&](int e, int h) {
      if (e >= (HALO ? 12 : 10)) return;
      const int kk = h >> 1, t = h & 1;
      bf16x8& d = wn[e & 3][kk];
      if (e == 11) { if (t == 0) d = *(const LAS bf16x8*)(lp(hoffA) + kk * G1_SLOT); return; }
      const int n = e < 2 ? 8 + e : e - 2;
      const bf16x4 v = trd(lp(e == 10 ? hoffB : ((n & 1) ? boff1 : boff0)) + (e == 10 ? 0 : (n >> 1) * 4096) + t * 1024 + kk * G1_SLOT);
      if (t == 0) { d[0] = v[0]; d[1] = v[1]; d[2] = v[2]; d[3] = v[3]; } else { d[4] = v[0]; d[5] = v[1]; d[6] = v[2]; d[7] = v[3]; }
    };
#define RA1(kk, i) a[kk][i] = *(const LAS bf16x8*)(lp(aoff) + (kk) * G1_SLOT + (i) * 1024);
    const int dlt = (q & 1) ? -2 * G1_SLOT : 2 * G1_SLOT;
    if (!LAST) {
#pragma unroll
      for (int kk = 0; kk < 2; ++kk) {
        dmc_first(ns + d1, v1, c1); RA1(kk, 0) rdq(kk, 0);
        dmc_next<1024>(v1, c1 + s1); RA1(kk, 1) rdq(kk, 1);
        dmc_next<2048>(v1, c1 + s2); RA1(kk, 2) rdq(kk, 2);
        dmc_next<3072>(v1, c1 + s3); RA1(kk, 3) rdq(kk, 3);
        dma1m(ns + d2, c2, m2);
        adv();
      }
    } else
    {
#pragma unroll
      for (int kk = 0; kk < 2; ++kk)
#pragma unroll
        for (int i = 0; i < 4; ++i) RA1(kk, i)
#pragma unroll
      for (int h = 0; h < 8; ++h) rdq(h >> 2, h & 3);
    }
    aoff += dlt;
    SCHED();
    if (!LAST) {
    } else {
#if PF_W2 && !(PFD & 1)
      int t = w * 64 + __builtin_amdgcn_mbcnt_hi(~0u, __builtin_amdgcn_mbcnt_lo(~0u, 0u));
      t = t < 288 ? t : t - 288;
      const int r2 = (t >> 4) * 64 + (xcd & 1) * 32 + idx;
      touch(lb + T_SCR, (r2 < 256 ? wo + (256 * kq + r2) * 1024 : wd + (896 * kq + r2 - 256) * 1024) + (t & 15) * 64);
#endif
    }
    WAIT_L0();
    SCHED();
    BAR();
    SCHED();
    PRIO_M();
    auto fin = [&]() { SCHED(); PRIO_T(); if (!LAST) { if (PFD) WAIT_V(1); else WAIT_V(0); } BAR(); SCHED(); };
#pragma unroll
    for (int e = 0; e < 10; ++e) {
      const int n = e < 2 ? 8 + e : e - 2;
#pragma unroll
      for (int i = 0; i < 4; ++i) {
        acc[i][n] = mfma(wn[e & 3][0], a[0][i], acc[i][n]);
        SCHED();
        if (!HALO && e == 9 && i == 3) fin();
        SCHED();
        acc[i][n] = mfma(wn[e & 3][1], a[1][i], acc[i][n]);
        SCHED();
        if (e == 0) { rdq(2 + (i >> 1), 2 * (i & 1)); rdq(2 + (i >> 1), 2 * (i & 1) + 1); } else rdq(e + 3, i);
        if (i == 3 && e < 9) { asm volatile("" :: "v"(wn[(e + 1) & 3][0]), "v"(wn[(e + 1) & 3][1])); }
        SCHED();
      }
    }
    if (HALO) {
      hacc = mfma(wn[2][0], wn[3][0], hacc);
      WAIT_L0();
      fin();
      hacc = mfma(wn[2][1], wn[3][1], hacc);
    }
    hoffA += dlt; boff0 += dlt; boff1 += dlt; hoffB += dlt;
    PRIO_E();
    SCHED();
  };
  const char* const hbase = ws + WS_H + (size_t)(mb * 4 + kq) * 36 * 16384;
  auto brow = [&](int stp_, int w_) {
    return (const char*)(stp_ < 4 ? wo + (size_t)(256 * kq) * 1024 : wd + (size_t)(896 * kq) * 1024 - 256 * 1024) + ((stp_ * 64 + 8 * w_) * 1024 + nt * 128) * 2;
  };
  unsigned va, vb;
  auto stage1 = [&](auto halo_c) {
    constexpr bool TA = decltype(halo_c)::value;
    typedef std::integral_constant<bool, TA || !HALO_SKIP> HC;
#pragma clang loop unroll(disable)
    for (int q = 0; q < 15; ++q) step(HC{}, std::false_type{}, q);
    step(HC{}, std::true_type{}, 15);
    ST(3);
    asm volatile("" ::: "memory");
    if (g == 0) __builtin_amdgcn_s_setprio(2); else __builtin_amdgcn_s_setprio(EPI_PRIO);
    lane = __builtin_amdgcn_mbcnt_hi(~0u, __builtin_amdgcn_mbcnt_lo(~0u, 0u));
    asm volatile("" : "+v"(lane));
    fr = lane & 15; fq = lane >> 4;
    va = lane * 16;
    vb = (lane >> 3) * 2048 + ((((lane & 7) >> 1) ^ ((lane >> 4) & 3)) * 32) + (lane & 1) * 16;
    if (g == 1) {
#pragma unroll
      for (int u = 0; u < 4; ++u) {
        const int w_ = wi * 2 + (u & 1);
        const char* p0 = brow((u >> 1) ? own1 : own0, w_);
        dma_first(lb + (u >> 1) * G2_SLOT + G2_SA + w_ * 2048, vb, p0); dma_next<1024>(vb, p0 - 896);
      }
    }
    const auto hrs = __builtin_amdgcn_make_buffer_rsrc((void*)hbase, 0, 36 * 16384, 0x00020000);
    const unsigned hv = wi * 4096 + lane * 16;
    auto put = [&](u32x4 v, int s, int kb, int i) {
      __builtin_amdgcn_raw_buffer_store_b128(v, hrs, hv + kb * 16384 + i * 1024, 0, H_AUX);
      *(LAS u32x4*)(lds + s * G2_SLOT + g * 16384 + ((wi * 4096 + kq * 1024 + lane * 16) ^ (i * 1024))) = v;
    };
#define GLU(i, a, b) glu4(acc[i][a], acc[i][b])
    if (!TA) {
      const int kbc = 8 + ((fc - 896 * kq) >> 5);
#pragma unroll
      for (int i = 0; i < 4; ++i) {
        __builtin_amdgcn_raw_buffer_store_b64(GLU(i, 8, 9), hrs, hv + kbc * 16384 + g * 8 + i * 1024, 0, H_AUX);
        put(cat4(GLU(i, 0, 4), GLU(i, 1, 5)), 0, kba + g, i);
        put(cat4(GLU(i, 2, 6), GLU(i, 3, 7)), 1, kba + 2 + g, i);
        if (i == FB) { SCHED(); BAR_IF0(g); SCHED(); }
      }
    } else {
      auto glu_part = [&](int i0, int i1) {
#pragma unroll
        for (int i = i0; i < i1; ++i) put(cat4(GLU(i, 6, 8), GLU(i, 7, 9)), 1, 8 + 2 * j + g, i);
      };
      if (g == 0) { glu_part(0, FA); SCHED(); BAR(); __builtin_amdgcn_s_setprio(0); asm volatile("" ::: "memory"); }
    LAS char* kim = lds + T_KV + g * KV_BYTES;
    LAS char* vim = kim + 7 * 1024;
    bf16x8 kf[4], qf[4];
#pragma unroll
    for (int i = 0; i < 4; ++i) {
      kf[i] = pack8(acc[i][2], acc[i][3]);
      *(LAS bf16x8*)(vim + ((wi * 4 + i) * 16 + fr) * 80 + fq * 16) = pack8(acc[i][4], acc[i][5]);
    }
    if (wi < 3) {
      *(LAS bf16x8*)(kim + wi * 2048 + lane * 16) = kf[2];
      *(LAS bf16x8*)(kim + wi * 2048 + 1024 + lane * 16) = kf[3];
    }
    {
      LAS char* hp = (wi < 2) ? (kim + 6 * 1024 + lane * 16 + wi * 8) : (vim + (256 + fr) * 80 + fq * 16 + (wi - 2) * 8);
      *(LAS u32x2*)hp = (u32x2){pk2(hacc[0], hacc[1]), pk2(hacc[2], hacc[3])};
    }
    WAIT_L0();
    BAR();
    asm volatile("" ::: "memory");
    const bool halo = wi == 0;
    const f32x4 bc = *(const LAS f32x4*)(lds + T_BIAS + lane * 16), bp = *(const LAS f32x4*)(lds + T_BIAS + 1024 + lane * 16);
    const f32x4 bp0 = *(const LAS f32x4*)(lds + T_BIAS + (halo ? 2048 : 1024) + lane * 16);
    const f32x4 bp1 = *(const LAS f32x4*)(lds + T_BIAS + (halo ? 3072 : 1024) + lane * 16);
    const int pf0 = halo ? 16 : wi * 4 - 2, pf1 = halo ? 16 : wi * 4 - 1;
    const bf16x8 kp0 = *(const LAS bf16x8*)(kim + (halo ? 6 : 2 * wi - 2) * 1024 + lane * 16);
    const bf16x8 kp1 = *(const LAS bf16x8*)(kim + (halo ? 6 : 2 * wi - 1) * 1024 + lane * 16);
    const LAS char* vr = vim + (4 * fq + (fr >> 2)) * 80 + (lane & 3) * 16;
    bf16x4 vt[6][2];
#pragma unroll
    for (int d = 0; d < 2; ++d) {
      vt[0][d] = trd(vr + pf0 * 1280 + d * 8); vt[1][d] = trd(vr + pf1 * 1280 + d * 8);
#pragma unroll
      for (int i = 0; i < 4; ++i) vt[2 + i][d] = trd(vr + (wi * 4 + i) * 1280 + d * 8);
    }
    f32x4 sp[4], sc[4];
#pragma unroll
    for (int i = 0; i < 4; ++i) {
      qf[i] = pack8(acc[i][0], acc[i][1]);
      sc[i] = mfma(kf[i], qf[i], bc);
    }
    sp[2] = mfma(kf[0], qf[2], bp); sp[3] = mfma(kf[1], qf[3], bp);
    sp[0] = mfma(kp0, qf[0], bp0); sp[1] = mfma(kp1, qf[1], bp1);
    const float sl = 0.17677669529663687f * 1.4426950408889634f;
    const f32x4 z4 = {0.f, 0.f, 0.f, 0.f};
    float m[4];
#pragma unroll
    for (int i = 0; i < 4; ++i)
      m[i] = fmx(fmx(fmx(fmx(sp[i][0], sp[i][1]), sp[i][2]), fmx(fmx(sp[i][3], sc[i][0]), sc[i][1])), fmx(sc[i][2], sc[i][3]));
    {
      const unsigned t = __float_as_uint(tred<true>(m[0], m[1], m[2], m[3]));
      const auto u = __builtin_amdgcn_permlane32_swap(t, t, false, false);
      const auto u0 = __builtin_amdgcn_permlane16_swap(u[0], u[0], false, false), u1 = __builtin_amdgcn_permlane16_swap(u[1], u[1], false, false);
      m[0] = __uint_as_float(u0[0]); m[1] = __uint_as_float(u0[1]); m[2] = __uint_as_float(u1[0]); m[3] = __uint_as_float(u1[1]);
    }
    const u32x4 one4 = {0x3f803f80u, 0x3f803f80u, 0x3f803f80u, 0x3f803f80u};
#pragma unroll
    for (int i = 0; i < 4; ++i) {
      const float mm = -sl * m[i];
#pragma unroll
      for (int e = 0; e < 4; ++e) {
        sp[i][e] = __builtin_amdgcn_exp2f(__builtin_fmaf(sp[i][e], sl, mm));
        sc[i][e] = __builtin_amdgcn_exp2f(__builtin_fmaf(sc[i][e], sl, mm));
      }
      const bf16x8 pfr = pack8(sp[i], sc[i]);
      const f32x4 sm = mfma(__builtin_bit_cast(bf16x8, one4), pfr, z4);
      const f32x4 o0 = mfma(cat(vt[i][0], vt[2 + i][0]), pfr, z4);
      const f32x4 o1 = mfma(cat(vt[i][1], vt[2 + i][1]), pfr, z4);
      const float inv = __builtin_amdgcn_rcpf(sm[0]);
      put(__builtin_bit_cast(u32x4, pack8(o0 * inv, o1 * inv)), 0, 2 * j + g, i);
    }
      glu_part(FA, 4);
      if (g == 1) glu_part(0, FA);
    }
  };
  stage1(std::integral_constant<bool, typeA>{});
  ST(4);

  const int lag = g ^ 1;
  GAS unsigned* hflag = (GAS unsigned*)(ws + WS_HCNT) + (mb * 4 + kq) * 256;
  const unsigned fo = (nt * 32 + (lane & 7)) * 4;
  GAS unsigned* gcnt = (GAS unsigned*)(ws + WS_GCNT) + (mb * 4 + kq) * 16;
  const int e0 = kq * 1024, e1 = ((1 ^ kq) - 1) * 1024, e2 = ((2 ^ kq) - 2) * 1024, e3 = ((3 ^ kq) - 3) * 1024;
  auto wait_cnt = [&](unsigned seen, GAS unsigned* c, unsigned need) {
    if ((unsigned)__builtin_amdgcn_readfirstlane(seen) < need) {
      asm volatile("" ::: "memory");
      unsigned spins = 0;
      while ((unsigned)__builtin_amdgcn_readfirstlane(__hip_atomic_load(c, __ATOMIC_RELAXED, __HIP_MEMORY_SCOPE_AGENT)) < need)
        if (++spins > SPIN_CAP) break;
    }
    asm volatile("" ::: "memory");
  };
  auto wait_flags = [&](unsigned seen, unsigned long long need) {
    if ((__builtin_amdgcn_ballot_w64(seen != 0u) & need) != need) {
      asm volatile("" ::: "memory");
      unsigned spins = 0;
      while ((__builtin_amdgcn_ballot_w64(__hip_atomic_load(hflag + (fo >> 2), __ATOMIC_RELAXED, __HIP_MEMORY_SCOPE_AGENT) != 0u) & need) != need) {
        __builtin_amdgcn_s_sleep(1);
        if (++spins > SPIN_CAP) break;
      }
    }
    asm volatile("" ::: "memory");
  };
  unsigned long long ord0 = j & 2 ? (j & 1 ? step_order(typeA, 3, 0) : step_order(typeA, 2, 0)) : (j & 1 ? step_order(typeA, 1, 0) : step_order(typeA, 0, 0));
  const unsigned ord1 = (unsigned)(j & 2 ? (j & 1 ? step_order(typeA, 3, 1) : step_order(typeA, 2, 1)) : (j & 1 ? step_order(typeA, 1, 1) : step_order(typeA, 0, 1)));
  const char* const caw = hbase + w * 4096;
  const char* const cbo = brow(0, w); const char* const cbd = brow(4, w) - 4 * 131072;
  auto pop = [&](const char*& ca_, const char*& cb_) {
    const unsigned s = (unsigned)ord0 & 31u;
    ord0 >>= 5;
    ca_ = caw + (s << 15); cb_ = (s < 4u ? cbo : cbd) + (s << 17);
  };
  const int mf = wi * 4 + kq;
  const unsigned orow = ((mb * 256 + 32 * (mf >> 1) + 2 * fr + (mf & 1)) * 1024 + nt * 128 + g * 64 + 16 * (fq & 1) + 8 * (fq >> 1)) * 2;
  u32x4 xr[2] = {{0u, 0u, 0u, 0u}, {0u, 0u, 0u, 0u}};
  const int rl2 = 4 * (fq & 1) + (fr >> 2);
  unsigned ao = lb + wi * 4096 + lane * 16;
  unsigned bo = lb + G2_SA + (fq >> 1) * 2048 + g * 1024 + rl2 * 128 + 32 * ((rl2 >> 1) & 3) + 8 * (lane & 3);
  f32x4 ac2[4][4];
#pragma unroll
  for (int i = 0; i < 4; ++i)
#pragma unroll
    for (int n = 0; n < 4; ++n) { ac2[i][n] = (f32x4){0.f, 0.f, 0.f, 0.f}; asm volatile("" : "+v"(ac2[i][n])); }
  ns = lb + 2 * G2_SLOT;
  const char *ca, *cb;
  const unsigned ordf = (unsigned)ord0;
  pop(ca, cb);
  unsigned sl2 = lb;
  WAIT_V_IF0(0, lag);
  WAIT_L0();
  BAR();
  ST(5);
  ST_SAVE(); ST(0); ST(1);
  unsigned seen = 0;
#define PEEK(c, o) asm volatile("global_load_dword %0, %1, %2 sc1" : "+v"(seen) : "v"(o), "s"(c) : "memory")
  if (lag) BAR();

  auto phase2 = [&](auto mode, int q) {
    constexpr int MODE = decltype(mode)::value;
    bf16x8 a[2][4], bfr[2][4];
#define RA(kk, i) a[kk][i] = *(const LAS bf16x8*)(lp(ao) + (kk) * 16384 + (i) * 1024);
#define RB(kk, n) bfr[kk][n] = cat(trd(lp(bo ^ ((n) * 32)) + (kk) * 8192), trd(lp(bo ^ ((n) * 32)) + (kk) * 8192 + 4096));
    int dlt;
    asm volatile("" ::: "memory");
    if (MODE == 1 && q == 1 && w == 4) PEEK(hflag, fo);
    if (MODE == 2 && q == 16)
      asm volatile("global_load_dwordx4 %0, %2, %3\n\tglobal_load_dwordx4 %1, %2, %3 offset:64" : "+v"(xr[0]), "+v"(xr[1]) : "v"(orow), "s"(x) : "memory");
    RA(0, 0) RA(0, 1)
    if (MODE == 0) dmc_first(ns + w * 4096, va, ca + e0);
    RA(0, 2) RA(0, 3) RB(0, 0)
    if (MODE == 0) dmc_next<1024>(va, ca + e1);
    RB(0, 1) RB(0, 2) RB(0, 3)
    if (MODE == 0) dmc_next<2048>(va, ca + e2);
    RA(1, 0) RA(1, 1) RA(1, 2)
    if (MODE == 0) dmc_next<3072>(va, ca + e3);
    RA(1, 3) RB(1, 0)
    if (MODE < 2) dma_first(ns + G2_SA + w * 2048, vb, cb);
    RB(1, 1)
    if (MODE < 2) dma_next<1024>(vb, cb - 896);
    dlt = (sl2 == lb + 2 * G2_SLOT) ? -2 * G2_SLOT : G2_SLOT; ns = sl2; sl2 += dlt;
    if (MODE < 2) pop(ca, cb);
    asm volatile("" : "+s"(ca), "+s"(cb), "+s"(ns), "+s"(sl2), "+s"(dlt), "+s"(ord0) :: "memory");
    RB(1, 2) RB(1, 3)
#undef RA
#undef RB
    ao += dlt; bo += dlt;
    SCHED();
    asm volatile("" : "+v"(bo), "+v"(ao));
    if (MODE == 0) WAIT_V_IF0(6, lag ^ 1); else if (MODE == 2 && q == 16) WAIT_V_IF0(2, lag ^ 1);
    if (MODE == 1 && q == 1) {
      asm volatile("s_waitcnt vmcnt(2)" : "+v"(seen) :: "memory");
      if (!lag) {
        volatile LAS unsigned* go = (volatile LAS unsigned*)(lds + T_SYNC + 4);
        if (w == 4) { wait_flags(seen, 0xf0ull); *go = 1u; }
        else { unsigned spins = 0; while (__builtin_amdgcn_readfirstlane(*go) == 0u) { __builtin_amdgcn_s_sleep(1); if (++spins > (SPIN_CAP << 3)) break; } }
        asm volatile("" ::: "memory");
      }
      const int s = (int)(ordf >> (5 * lag)) & 31;
      const char* p = hbase + s * 32768 + wi * 8192;
      const unsigned d = lb + (lag ? 0 : 2 * G2_SLOT) + wi * 8192;
      dmc_first(d, va, p + e0); dmc_next<1024>(va, p + e1); dmc_next<2048>(va, p + e2); dmc_next<3072>(va, p + e3);
      dmc_first(d + 4096, va, p + 4096 + e0); dmc_next<1024>(va, p + 4096 + e1); dmc_next<2048>(va, p + 4096 + e2); dmc_next<3072>(va, p + 4096 + e3);
    }
    WAIT_L0();
    SCHED();
    BAR();
    SCHED();
    P2_M();
#pragma unroll
    for (int n = 0; n < 4; ++n)
#pragma unroll
      for (int i = 0; i < 4; ++i) {
        ac2[i][n] = mfma(bfr[0][n], a[0][i], ac2[i][n]);
        SCHED();
        if (n == 3 && i == 3) {
          P2_T();
          if (MODE == 0) WAIT_V_IF0(6, lag); else if (MODE == 2 && q == 16) WAIT_V_IF0(2, lag);
          if (MODE == 1 && q == 1) WAIT_V_IF0(0, lag);
          if (q < 17 || !lag) BAR();
          SCHED();
        }
        ac2[i][n] = mfma(bfr[1][n], a[1][i], ac2[i][n]);
        SCHED();
        if (MODE == 1 && q == 0 && n == 1 && i == 3 && lag) {
          WAIT_V(0);
          unsigned old = 0;
          if (lane == 0) old = __hip_atomic_fetch_add((LAS unsigned*)(lds + T_SYNC), 1u, __ATOMIC_RELAXED, __HIP_MEMORY_SCOPE_WORKGROUP);
          if (__builtin_amdgcn_readfirstlane(old) == 3u && lane < 8)
            __hip_atomic_store(hflag + lane * 32 + nt, 1u, __ATOMIC_RELAXED, __HIP_MEMORY_SCOPE_AGENT);
          SCHED();
        }
      }
    P2_E();
    SCHED();
  };
  typedef std::integral_constant<int, 0> M0; typedef std::integral_constant<int, 1> M1; typedef std::integral_constant<int, 2> M2;
  phase2(M1{}, 0);
  phase2(M1{}, 1);
  ST(2);
#pragma clang loop unroll(disable)
  for (int q = 2; q < 6; ++q) phase2(M0{}, q);
  PEEK(hflag, fo);
  phase2(M0{}, 6);
  asm volatile("" : "+v"(seen));
  ord0 |= (unsigned long long)ord1 << 20;
  wait_flags(seen, 0x0full);
#pragma clang loop unroll(disable)
  for (int q = 7; q < 13; ++q) phase2(M0{}, q);
  if (w == 4 && lane == 0) __hip_atomic_fetch_add(gcnt, 1u, __ATOMIC_RELAXED, __HIP_MEMORY_SCOPE_AGENT);
#pragma clang loop unroll(disable)
  for (int q = 13; q < 16; ++q) phase2(M0{}, q);
  phase2(M2{}, 16);
  PEEK(gcnt, 0u);
  phase2(M2{}, 17);
  asm volatile("s_waitcnt vmcnt(0)" : "+v"(seen) :: "memory");
  wait_cnt(seen, gcnt, 8u);
  ST(3);
  const int tile = mb * 8 + nt;
  const auto rs = __builtin_amdgcn_make_buffer_rsrc(ws + WS_H + (size_t)mb * 4 * 36 * 16384, 0, 4 * 36 * 16384, 0x00020000);
  auto xo = [&](int src, int i) { const int u = 15 + nt * 3 + i; return (unsigned)((src * 36 + (u >= 36 ? u - 36 : u)) * 16384 + w * 2048) + lane * 16; };
#pragma unroll
  for (int i = 1; i < 4; ++i) {
    const unsigned o = xo(kq, i);
    __builtin_amdgcn_raw_buffer_store_b128(__builtin_bit_cast(u32x4, pack8(ac2[i][0], ac2[i][1])), rs, o, 0, 16);
    __builtin_amdgcn_raw_buffer_store_b128(__builtin_bit_cast(u32x4, pack8(ac2[i][2], ac2[i][3])), rs, o + 1024, 0, 16);
  }
  GAS unsigned* cnt = (GAS unsigned*)(ws + WS_CNT) + (tile * 32 + w) * 16;
  GAS unsigned* mine = cnt + kq * 128 + (lane & 3);
  seen = __hip_atomic_load(mine, __ATOMIC_RELAXED, __HIP_MEMORY_SCOPE_AGENT);
  WAIT_V(0);
  ST(4);
  asm volatile("" : "+v"(xr[0]), "+v"(xr[1]));
  if (lane >= 1 && lane < 4) __hip_atomic_store(cnt + (kq ^ lane) * 128 + lane, 1u, __ATOMIC_RELAXED, __HIP_MEMORY_SCOPE_AGENT);
  u32x4 p[3][2];
#pragma unroll
  for (int i = 0; i < 3; ++i) p[i][0] = p[i][1] = (u32x4){0u, 0u, 0u, 0u};
  {
    unsigned done = 0;
#pragma clang loop unroll(disable)
    for (unsigned spins = 0; spins < SPIN_CAP; ++spins) {
      const unsigned rdy = (unsigned)__builtin_amdgcn_ballot_w64(seen != 0u) & 0xeu & ~done;
      asm volatile("" ::: "memory");
#pragma unroll
      for (int i = 1; i < 4; ++i)
        if (rdy & (1u << i)) {
          const unsigned o = xo(kq ^ i, i);
          p[i - 1][0] = __builtin_amdgcn_raw_buffer_load_b128(rs, o, 0, 16);
          p[i - 1][1] = __builtin_amdgcn_raw_buffer_load_b128(rs, o + 1024, 0, 16);
        }
      done |= rdy;
      if (done == 0xeu) break;
      if (!rdy) __builtin_amdgcn_s_sleep(1);
      seen = __hip_atomic_load(mine, __ATOMIC_RELAXED, __HIP_MEMORY_SCOPE_AGENT);
    }
  }
  ST(5);
  const auto ors = __builtin_amdgcn_make_buffer_rsrc(out, 0, 4 << 20, 0x00020000);
#pragma unroll
  for (int h = 0; h < 2; ++h) {
    const auto x0 = __builtin_amdgcn_permlane16_swap(xr[h][0], xr[h][2], false, false), x1 = __builtin_amdgcn_permlane16_swap(xr[h][1], xr[h][3], false, false);
    const u32x4 xv = {x0[0], x1[0], x0[1], x1[1]};
    unsigned r[4];
#pragma unroll
    for (int d = 0; d < 4; ++d) {
      float lo = ac2[0][2 * h + (d >> 1)][2 * (d & 1)] + __uint_as_float(xv[d] << 16), hi = ac2[0][2 * h + (d >> 1)][2 * (d & 1) + 1] + __uint_as_float(xv[d] & 0xffff0000u);
#pragma unroll
      for (int i = 0; i < 3; ++i) { lo += __uint_as_float(p[i][h][d] << 16); hi += __uint_as_float(p[i][h][d] & 0xffff0000u); }
      r[d] = pk2(lo, hi);
    }
    const auto y0 = __builtin_amdgcn_permlane16_swap(r[0], r[2], false, false), y1 = __builtin_amdgcn_permlane16_swap(r[1], r[3], false, false);
    __builtin_amdgcn_raw_buffer_store_b128((u32x4){y0[0], y1[0], y0[1], y1[1]}, ors, orow + 64 * h, 0, OUT_AUX);
  }
  ST(6);
  ST(7);
  ST_OUT(1, 8);
  ST_REST(); ST_OUT(0, 6);
}

}

extern "C" __global__ __launch_bounds__(512, 2) void psum_g1_kernel(const u16* __restrict__ x, u16* __restrict__ out, const u16* __restrict__ wqkv,
                                                                    const u16* __restrict__ wg, const u16* __restrict__ wu,
                                                                    const u16* __restrict__ wo, const u16* __restrict__ wd, char* __restrict__ ws) {
  if (blockIdx.x < 128) body<true>(x, out, wqkv, wg, wu, wo, wd, ws);
  else body<false>(x, out, wqkv, wg, wu, wo, wd, ws);
}

extern "C" void kernel_launch(void* const* d_in, const int* in_sizes, int n_in, void* d_out, int out_size, void* d_ws,
                              size_t ws_size, hipStream_t stream) {
  const u16* x = (const u16*)d_in[0];
  const u16* wqkv = (const u16*)d_in[1];
  const u16* wo = (const u16*)d_in[2];
  const u16* wg = (const u16*)d_in[4];
  const u16* wu = (const u16*)d_in[5];
  const u16* wd = (const u16*)d_in[6];
  char* ws = (char*)d_ws;
  (void)hipFuncSetAttribute((const void*)psum_g1_kernel, hipFuncAttributeMaxDynamicSharedMemorySize, G1_LDS);
  psum_g1_kernel<<<256, 512, G1_LDS, stream>>>(x, (u16*)d_out, wqkv, wg, wu, wo, wd, ws);
}
```

```cpp
#define OUT_AUX 0
#include <hip/hip_runtime.h>
#include <hip/hip_bf16.h>
#include <type_traits>
#define EARLY 1
#define HALO_SKIP 1
#define PF_W2 1
#define PF_LOOP 0
#define PF_R0 192
#define PF_PER 2
#define PF_LN_ROWS 96
#define PRIO_L 0
#define PRIO_M() __builtin_amdgcn_s_setprio(1)
#define PRIO_T() __builtin_amdgcn_s_setprio(3)
#define PRIO_E() __builtin_amdgcn_s_setprio(0)
#define PF_CODE 0
#define PRECLEAN 0
#define G2ORD 0
#define EARLY2 1
#define CHAIN 1
#define PF_X 0
#define XH 1
#define ACQ 0
#define EPI_PRIO 1
#define LORD 1
#define RPOS 1
#define FA 4
#define FB 0
#define PFD 0
#define SGLU 0
#define TLBW 0
#define WDELAY 0
#define HDEFER 1
#define P2L 1
#define P2_M() __builtin_amdgcn_s_setprio(0)
#define P2_T() __builtin_amdgcn_s_setprio(1)
#define P2_E()
#define SPIN_CAP (1u << 17)
#define H_AUX 16

namespace {

typedef unsigned short u16;
typedef short bf16x8 __attribute__((ext_vector_type(8)));
typedef short bf16x4 __attribute__((ext_vector_type(4)));
typedef float f32x4 __attribute__((ext_vector_type(4)));
typedef float f32x2 __attribute__((ext_vector_type(2)));
typedef unsigned u32x4 __attribute__((ext_vector_type(4)));
typedef unsigned u32x2 __attribute__((ext_vector_type(2)));
typedef __bf16 bf2 __attribute__((ext_vector_type(2)));
#define LAS __attribute__((address_space(3)))
#define GAS __attribute__((address_space(1)))

#define WAIT_V(n) asm volatile("s_waitcnt vmcnt(" #n ")" ::: "memory")
#define WAIT_V_IF0(n, c) asm volatile("s_cmp_lg_u32 %0, 0\n\ts_cbranch_scc1 1f\n\ts_waitcnt vmcnt(" #n ")\n1:" :: "s"(c) : "memory", "scc")
#define BAR_IF0(c) asm volatile("s_cmp_lg_u32 %0, 0\n\ts_cbranch_scc1 1f\n\ts_barrier\n\ts_setprio 0\n1:" :: "s"(c) : "memory", "scc")
#define WAIT_L0() do { asm volatile("" ::: "memory"); __builtin_amdgcn_s_waitcnt(0xC07F); asm volatile("" ::: "memory"); } while (0)
#define BAR() do { asm volatile("" ::: "memory"); __builtin_amdgcn_s_barrier(); asm volatile("" ::: "memory"); } while (0)
#define SCHED() __builtin_amdgcn_sched_barrier(0)

__device__ __forceinline__ float b2f(u16 v) { return __uint_as_float(((unsigned)v) << 16); }
__device__ __forceinline__ unsigned pk2(float lo, float hi) {
  bf2 r = {(__bf16)lo, (__bf16)hi};
  return __builtin_bit_cast(unsigned, r);
}
__device__ __forceinline__ float rbf(float f) { return (float)(__bf16)f; }
__device__ __forceinline__ bf16x8 pack8(f32x4 a, f32x4 b) {
  u32x4 r = {pk2(a[0], a[1]), pk2(a[2], a[3]), pk2(b[0], b[1]), pk2(b[2], b[3])};
  return __builtin_bit_cast(bf16x8, r);
}
__device__ __forceinline__ bf16x4 trd(LAS const char* p) {
  return __builtin_amdgcn_ds_read_tr16_b64_v4i16((LAS bf16x4*)p);
}
__device__ __forceinline__ bf16x8 cat(bf16x4 a, bf16x4 b) { return __builtin_shufflevector(a, b, 0, 1, 2, 3, 4, 5, 6, 7); }
__device__ __forceinline__ f32x4 mfma(bf16x8 a, bf16x8 b, f32x4 c) {
  return __builtin_amdgcn_mfma_f32_16x16x32_bf16(a, b, c, 0, 0, 0);
}
__device__ __forceinline__ void dma_first(unsigned lds_dst, unsigned voff, const char* b0) {
  asm volatile("s_mov_b32 m0, %0\n\ts_nop 0\n\tglobal_load_lds_dwordx4 %1, %2" :: "s"(lds_dst), "v"(voff), "s"(b0) : "memory");
}
template <int OFF>
__device__ __forceinline__ void dma_next(unsigned voff, const char* b) {
  asm volatile("global_load_lds_dwordx4 %0, %1 offset:%2" :: "v"(voff), "s"(b), "i"(OFF) : "memory");
}
__device__ __forceinline__ void dmc_first(unsigned lds_dst, unsigned voff, const char* b0) {
  asm volatile("s_mov_b32 m0, %0\n\ts_nop 0\n\tglobal_load_lds_dwordx4 %1, %2 sc1" :: "s"(lds_dst), "v"(voff), "s"(b0) : "memory");
}
template <int OFF>
__device__ __forceinline__ void dmc_next(unsigned voff, const char* b) {
  asm volatile("global_load_lds_dwordx4 %0, %1 offset:%2 sc1" :: "v"(voff), "s"(b), "i"(OFF) : "memory");
}
__device__ __forceinline__ void dma1m(unsigned lds_dst, const char* p, unsigned long long mask) {
  asm volatile("s_mov_b32 m0, %0\n\ts_mov_b64 exec, %2\n\t"
               "global_load_lds_dwordx4 %1, off sc1\n\t"
               "s_mov_b64 exec, -1"
               :: "s"(lds_dst), "v"(p), "s"(mask) : "memory");
}
__device__ __forceinline__ void touch(unsigned lds_dst, const void* p) {
  asm volatile("s_mov_b32 m0, %0\n\ts_nop 0\n\tglobal_load_lds_dword %1, off" :: "s"(lds_dst), "v"(p) : "memory");
}
#define CODE_ADDR(dst, sym) do { unsigned lo_, hi_; asm volatile("s_getpc_b64 s[90:91]\n\ts_add_u32 %0, s90, " #sym "@rel32@lo+4\n\ts_addc_u32 %1, s91, " #sym "@rel32@hi+12" \
    : "=s"(lo_), "=s"(hi_) :: "s90", "s91", "scc"); dst = (const char*)(((unsigned long long)hi_ << 32) | lo_); } while (0)
constexpr int LN_LINES = 16, G1_LINES = 80, G2_LINES = 30;
__device__ __forceinline__ LAS char* lp(unsigned a) { return (LAS char*)(size_t)a; }
__device__ __forceinline__ float fmx(float a, float b) { return __builtin_elementwise_maximum(a, b); }
__device__ __forceinline__ float xmax(float v) {
  auto a = __builtin_amdgcn_permlane16_swap(__float_as_uint(v), __float_as_uint(v), false, false);
  v = fmaxf(__uint_as_float(a[0]), __uint_as_float(a[1]));
  auto b = __builtin_amdgcn_permlane32_swap(__float_as_uint(v), __float_as_uint(v), false, false);
  return fmaxf(__uint_as_float(b[0]), __uint_as_float(b[1]));
}
__device__ __forceinline__ float xsum(float v) {
  auto a = __builtin_amdgcn_permlane16_swap(__float_as_uint(v), __float_as_uint(v), false, false);
  v = __uint_as_float(a[0]) + __uint_as_float(a[1]);
  auto b = __builtin_amdgcn_permlane32_swap(__float_as_uint(v), __float_as_uint(v), false, false);
  return __uint_as_float(b[0]) + __uint_as_float(b[1]);
}

template <int CTRL>
__device__ __forceinline__ float dpp_add(float v) {
  return v + __uint_as_float(__builtin_amdgcn_update_dpp(0u, __float_as_uint(v), CTRL, 0xf, 0xf, true));
}
__device__ __forceinline__ float hsum(float v) {
  v = dpp_add<0xB1>(v);
  v = dpp_add<0x4E>(v);
  v = dpp_add<0x128>(v);
  return xsum(v);
}

#define DG(kid, slot)
#define ST_DECL
#define LT_DECL
#define LT(k)
#define ET(k)
#define ST(k)
#define ST_OUT(kid, n)
#define ST_SAVE()
#define ST_REST()
constexpr int XN_STEP = 17 * 1024;
constexpr size_t WS_XN = 0;
constexpr size_t WS_H = WS_XN + (size_t)8 * 32 * XN_STEP;
constexpr size_t H_BYTES = (size_t)8 * 4 * 36 * 16 * 1024;
constexpr size_t WS_CNT = WS_H + H_BYTES;
constexpr size_t WS_GCNT = WS_CNT + 131072;
constexpr size_t WS_HCNT = WS_GCNT + 8192;
constexpr size_t WS_LNF = WS_HCNT + 32768;
constexpr size_t WS_ST = WS_LNF + 32768;

constexpr int G1_SA = 17 * 1024, G1_SB = 20 * 1024, G1_SLOT = G1_SA + G1_SB;
constexpr int T_SCR = 159488;
constexpr int T_BIAS = T_SCR + 256;
constexpr int G1_LDS = T_BIAS + 4096;
constexpr int G2_SA = 32768, G2_SB = 16384, G2_SLOT = G2_SA + G2_SB;
constexpr int T_SYNC = T_SCR - 64;
constexpr int T_KV = 2 * G2_SLOT, KV_BYTES = 7 * 1024 + 272 * 80;

template <bool MAX>
__device__ __forceinline__ float tred(float a, float b, float c, float d) {
  const auto r1 = __builtin_amdgcn_permlane16_swap(__float_as_uint(a), __float_as_uint(b), false, false);
  const auto r2 = __builtin_amdgcn_permlane16_swap(__float_as_uint(c), __float_as_uint(d), false, false);
  const float x0 = __uint_as_float(r1[0]), x1 = __uint_as_float(r1[1]), y0 = __uint_as_float(r2[0]), y1 = __uint_as_float(r2[1]);
  const float t = MAX ? fmx(x0, x1) : x0 + x1, u = MAX ? fmx(y0, y1) : y0 + y1;
  const auto r3 = __builtin_amdgcn_permlane32_swap(__float_as_uint(t), __float_as_uint(u), false, false);
  const float z0 = __uint_as_float(r3[0]), z1 = __uint_as_float(r3[1]);
  return MAX ? fmx(z0, z1) : z0 + z1;
}
__device__ __forceinline__ u32x2 glu4(f32x4 g, f32x4 u) {
  f32x4 e = g * -1.4426950408889634f;
#pragma unroll
  for (int i = 0; i < 4; ++i) e[i] = __builtin_amdgcn_exp2f(e[i]);
  e = e + 1.0f;
#pragma unroll
  for (int i = 0; i < 4; ++i) e[i] = __builtin_amdgcn_rcpf(e[i]);
  e = e * u;
  return (u32x2){pk2(e[0], e[1]), pk2(e[2], e[3])};
}
__device__ __forceinline__ u32x2 glu4s(f32x4 g, f32x4 u) {
  float e[4];
#pragma unroll
  for (int i = 0; i < 4; ++i) { e[i] = g[i] * -1.4426950408889634f; asm volatile("" : "+v"(e[i])); e[i] = __builtin_amdgcn_exp2f(e[i]); }
#pragma unroll
  for (int i = 0; i < 4; ++i) { e[i] = e[i] + 1.0f; asm volatile("" : "+v"(e[i])); e[i] = __builtin_amdgcn_rcpf(e[i]); }
#pragma unroll
  for (int i = 0; i < 4; ++i) { e[i] = e[i] * u[i]; asm volatile("" : "+v"(e[i])); }
  return (u32x2){pk2(e[0], e[1]), pk2(e[2], e[3])};
}
__device__ __forceinline__ u32x4 cat4(u32x2 a, u32x2 b) { return (u32x4){a[0], a[1], b[0], b[1]}; }

constexpr unsigned long long step_order(bool ta, int j, int part) {
  const int own0 = ta ? j : (16 + 5 * j + (j & 1)) / 2, own1 = ta ? 4 + j : own0 + 1;
  unsigned long long r[2] = {0, 0};
  int p = 0;
  for (int t = 0; t < 18; ++t) {
    const int s = (t + 8) % 18;
    if (s == own0 || s == own1) continue;
    r[p / 12] |= (unsigned long long)s << (5 * (p % 12));
    ++p;
  }
  return r[part];
}

template <bool typeA>
__device__ __forceinline__ void body(const u16* __restrict__ x, u16* __restrict__ out, const u16* __restrict__ wqkv, const u16* __restrict__ wg,
                                     const u16* __restrict__ wu, const u16* __restrict__ wo, const u16* __restrict__ wd, char* __restrict__ ws) {
  extern __shared__ __attribute__((aligned(16))) char smem[];
  LAS char* lds = (LAS char*)smem;
  ST_DECL LT_DECL ST(0);
  const int tid = threadIdx.x;
  int lane = tid & 63;
  const int w = __builtin_amdgcn_readfirstlane(tid >> 6), g = w >> 2, wi = w & 3;
  const int b = blockIdx.x, xcd = b & 7, idx = b >> 3;
  const int mb = (xcd & 1) * 4 + (idx & 3), kq = xcd >> 1, ntl = idx >> 2;
  const int j = ntl & 3;
  int fr = lane & 15, fq = lane >> 4;
  const int fA = 896 * kq + 64 * j;
  const int f0 = 896 * kq + 256 + 160 * j, odd = j & 1;
  const int fa = f0 + 32 * odd, fb = fa + 64, fc = odd ? f0 : f0 + 128;

  const int mi = kq * 8 + ntl;
  const bool lnh = !typeA && w == 4 && j < 2, lnw = g == 0 || lnh;
  const char* const xnb = ws + WS_XN + (size_t)mb * 32 * XN_STEP;
  if (lnw) {
    __builtin_amdgcn_s_setprio(2);
    const int lr = (lnh ? (kq * 2 + j) * 2 : (mi & 1) * 8 + wi * 2) + ((lane >> 2) & 1), lf = lnh ? 16 : mi >> 1;
    const int lrow = lnh ? (mb > 0 ? mb * 256 - 16 : 0) + 2 * (lr & 7) + (lr >> 3) : mb * 256 + 32 * (lf >> 1) + 2 * lr + (lf & 1);
    const char* xr_ = (const char*)x + (lrow * 2048 + (lane >> 3) * 64 + (lane & 3) * 16);
    u32x4 raw[4];
#pragma unroll
    for (int i = 0; i < 4; ++i) raw[i] = *(const u32x4*)(xr_ + 512 * i);
    const auto xrs = __builtin_amdgcn_make_buffer_rsrc((void*)xnb, 0, 32 * XN_STEP, 0x00020000);
    const unsigned o = (lane >> 3) * XN_STEP + lf * 1024 + lr * 64 + (((lane & 3) ^ (2 * (lr >> 3))) * 16);
    ET(1);
    f32x2 v[16];
#pragma unroll
    for (int i = 0; i < 16; ++i) v[i] = (f32x2){__uint_as_float(raw[i >> 2][i & 3] << 16), __uint_as_float(raw[i >> 2][i & 3] & 0xffff0000u)};
    f32x2 s2 = (v[0] + v[1]) + (v[2] + v[3]);
#pragma unroll
    for (int i = 4; i < 16; i += 4) s2 += (v[i] + v[i + 1]) + (v[i + 2] + v[i + 3]);
    ET(2);
    const float mean = hsum(s2[0] + s2[1]) * (1.0f / 1024.0f);
    f32x2 qa = {0.f, 0.f}, qb = {0.f, 0.f};
#pragma unroll
    for (int i = 0; i < 16; i += 2) { const f32x2 d = v[i] - mean, e = v[i + 1] - mean; qa += d * d; qb += e * e; }
    qa += qb;
    const float var = hsum(qa[0] + qa[1]) * (1.0f / 1024.0f);
    const float inv = __builtin_amdgcn_rcpf(rbf(__builtin_amdgcn_sqrtf(rbf(rbf(var) + 1e-5f)))), sh = -rbf(mean);
#pragma unroll
    for (int i = 0; i < 4; ++i) {
      u32x4 t;
#pragma unroll
      for (int e = 0; e < 4; ++e) { const f32x2 y = (v[4 * i + e] + sh) * inv; t[e] = pk2(y[0], y[1]); }
      __builtin_amdgcn_raw_buffer_store_b128(t, xrs, o + i * 8 * XN_STEP, 0, 16);
    }
    __builtin_amdgcn_s_setprio(0);
  }
  GAS unsigned* lnf = (GAS unsigned*)(ws + WS_LNF) + mb * 1024 + (lane < 32 ? mi * 32 + lane : (lane - 32) * 32 + mi);
  unsigned entry = 0;
  asm volatile("s_cmp_lg_u32 %2, 7\n\ts_cbranch_scc1 1f\n\tglobal_load_dword %0, %1, off sc1\n1:" : "+v"(entry) : "v"(lnf), "s"(w) : "memory", "scc");
  if (w == 3) {
    char* z = lane < 8 ? ws + WS_CNT + (((mb * 8 + ntl) * 4 + kq) * 8 + lane) * 64
            : (lane < 10 ? ws + WS_HCNT + ((mb * 4 + kq) * 8 + ntl) * 128 + (lane - 8) * 16 : ws + WS_GCNT + (mb * 4 + kq) * 64);
    if (lane < (ntl == 0 ? 11 : 10)) asm volatile("global_store_dwordx4 %0, %1, off sc1\n\ts_nop 1" :: "v"(z), "v"((u32x4){0u, 0u, 0u, 0u}) : "memory");
  }

  const bool isq = typeA && wi < 3;
  const int ld = isq ? 3072 : 3584;
  const u16* q1 = isq ? wqkv + (wi * 1024 + 256 * kq + 64 * j) : ((!typeA && (wi & 2)) ? wu : wg) + (typeA ? fA : ((wi & 1) ? fb : fa));
  const char* c1 = g ? (const char*)q1 : xnb + wi * 4096;
  const int s1 = g ? 8 * ld - 1024 : 0, s2 = g ? 32 * ld - 2048 : 0, s3 = g ? 40 * ld - 3072 : 0;
  const int p1 = g ? 64 * ld : XN_STEP, p2 = g ? XN_STEP : 64 * 3584;
  const int d1 = (g ? G1_SA : 0) + wi * 4096, d2 = g ? 16384 + wi * 256 : G1_SA + 16384 + wi * 1024;
  const unsigned long long m2 = g ? 0xffffull : ~0ull;
  const int rr = lane >> 3, pc = lane & 7, fi = (pc >> 1) ^ ((rr >> 1) & 3), kr = (rr & 3) + 8 * (rr >> 2);
  const unsigned v1 = g ? kr * ld * 2 + fi * 32 + (pc & 1) * 16 : lane * 16;
  const unsigned lb = (unsigned)(size_t)lds;
  const char* c2;
  {
    const unsigned ow = (kr + 16 * (wi >> 1) + 4 * (wi & 1)) * 7168 + (typeA ? fi * 32 : (fi >> 1) * 32) + (pc & 1) * 16;
    const char* be = g ? xnb : (const char*)(typeA ? wu + fA : wg + fc);
    const char* bo = g ? be : (const char*)(wu + (typeA ? fA : fc));
    c2 = ((fi & 1) ? bo : be) + (g ? 16384 + wi * 256 + (lane & 15) * 16 : ow);
  }
  auto chain1 = [&](unsigned dst, const char* p) { dmc_first(dst + d1, v1, p); dmc_next<1024>(v1, p + s1); dmc_next<2048>(v1, p + s2); dmc_next<3072>(v1, p + s3); };
  auto first = [&](bool one) {
    if (one) { chain1(lb, c1); chain1(lb + G1_SLOT, c1 + p1); }
    else { dma1m(lb + d2, c2, m2); dma1m(lb + G1_SLOT + d2, c2 + p2, m2); }
  };
  if (!lnw) first(true);
  else WAIT_V(0);
  asm volatile("s_waitcnt vmcnt(8)" : "+v"(entry) :: "memory");
  ET(3);
  BAR();
  if (w == 7 && lane >= 32) __hip_atomic_store(lnf, entry + 1u, __ATOMIC_RELAXED, __HIP_MEMORY_SCOPE_AGENT);
  if (lnw) first(g == 1);
  ST(1);

  const int nt = ntl;
  const int kba = 8 + ((fa - 896 * kq) >> 5);
  const int own0 = typeA ? j : (kba >> 1), own1 = typeA ? 4 + j : (kba >> 1) + 1;
  f32x4 acc[4][10];
  f32x4 hacc = {0.f, 0.f, 0.f, 0.f};
#pragma unroll
  for (int i = 0; i < 4; ++i)
#pragma unroll
    for (int n = 0; n < 10; ++n) { acc[i][n] = (f32x4){0.f, 0.f, 0.f, 0.f}; asm volatile("" : "+v"(acc[i][n])); }
  const int rl = 4 * (fq & 1) + (fr >> 2);
  unsigned hoffA = lb + 16384 + fr * 64 + ((fq ^ (2 * (fr >> 3))) * 16);
  unsigned aoff = hoffA - 16384 + wi * 4096;
  unsigned boff0 = lb + G1_SA + (fq >> 1) * 2048 + rl * 128 + 32 * ((2 * g) ^ ((rl >> 1) & 3)) + 8 * (lane & 3);
  unsigned boff1 = boff0 ^ 32;
  unsigned hoffB = (boff0 ^ (32 * (wi & 1))) + (1 + (wi >> 1)) * 4096;
  asm volatile("" : "+v"(aoff), "+v"(hoffA), "+v"(boff0), "+v"(boff1), "+v"(hoffB), "+v"(hacc));
  {
    const int lo = fr + (wi == 0 ? -7 : (wi == 2 ? 1 : 9));
    const int hi = wi == 0 ? fr + 1 : ((wi >= 2 && mb == 0) ? 0 : (wi == 2 ? 8 : 16));
    f32x4 bv;
#pragma unroll
    for (int e = 0; e < 4; ++e) bv[e] = (4 * fq + e >= lo && 4 * fq + e < hi) ? 0.f : -3.0e38f;
    *(LAS f32x4*)(lds + T_BIAS + wi * 1024 + lane * 16) = bv;
    *(LAS unsigned*)(lds + T_SYNC + (lane & 1) * 4) = 0u;
  }
  ET(4);
  if (w == 7) {
    for (unsigned spins = 0; spins < SPIN_CAP; ++spins) {
      unsigned now = entry + 1u;
      if (lane < 32) now = __hip_atomic_load(lnf, __ATOMIC_RELAXED, __HIP_MEMORY_SCOPE_AGENT);
      if (__builtin_amdgcn_ballot_w64(now == entry) == 0ull) break;
      __builtin_amdgcn_s_sleep(1);
    }
    if (lane >= 32) __hip_atomic_store(lnf, entry + 2u, __ATOMIC_RELAXED, __HIP_MEMORY_SCOPE_AGENT);
  }
  WAIT_V_IF0(0, g);
  BAR();
  ET(5);
  first(g == 0);
  c1 += 2 * p1; c2 += 2 * p2;
  unsigned ns = lb + 2 * G1_SLOT;
  auto adv = [&]() { c1 += p1; c2 += p2; ns = (ns == lb + 3 * G1_SLOT) ? lb : ns + G1_SLOT; };
  WAIT_V(0);
  ST(2);
  if (g == 1) BAR();

  auto step = [&](auto halo_c, auto last_c, int q) {
    constexpr bool HALO = decltype(halo_c)::value, LAST = decltype(last_c)::value;
    bf16x8 a[2][4], wn[4][2];
    asm volatile("" ::: "memory");
    auto rdq = [&](int e, int h) {
      if (e >= (HALO ? 12 : 10)) return;
      const int kk = h >> 1, t = h & 1;
      bf16x8& d = wn[e & 3][kk];
      if (e == 11) { if (t == 0) d = *(const LAS bf16x8*)(lp(hoffA) + kk * G1_SLOT); return; }
      const int n = e < 2 ? 8 + e : e - 2;
      const bf16x4 v = trd(lp(e == 10 ? hoffB : ((n & 1) ? boff1 : boff0)) + (e == 10 ? 0 : (n >> 1) * 4096) + t * 1024 + kk * G1_SLOT);
      if (t == 0) { d[0] = v[0]; d[1] = v[1]; d[2] = v[2]; d[3] = v[3]; } else { d[4] = v[0]; d[5] = v[1]; d[6] = v[2]; d[7] = v[3]; }
    };
#define RA1(kk, i) a[kk][i] = *(const LAS bf16x8*)(lp(aoff) + (kk) * G1_SLOT + (i) * 1024);
    const int dlt = (q & 1) ? -2 * G1_SLOT : 2 * G1_SLOT;
    if (!LAST) {
#pragma unroll
      for (int kk = 0; kk < 2; ++kk) {
        dmc_first(ns + d1, v1, c1); RA1(kk, 0) rdq(kk, 0);
        dmc_next<1024>(v1, c1 + s1); RA1(kk, 1) rdq(kk, 1);
        dmc_next<2048>(v1, c1 + s2); RA1(kk, 2) rdq(kk, 2);
        dmc_next<3072>(v1, c1 + s3); RA1(kk, 3) rdq(kk, 3);
        dma1m(ns + d2, c2, m2);
        adv();
      }
    } else
    {
#pragma unroll
      for (int kk = 0; kk < 2; ++kk)
#pragma unroll
        for (int i = 0; i < 4; ++i) RA1(kk, i)
#pragma unroll
      for (int h = 0; h < 8; ++h) rdq(h >> 2, h & 3);
    }
    aoff += dlt;
    SCHED();
    if (!LAST) {
    } else {
#if PF_W2 && !(PFD & 1)
      int t = w * 64 + __builtin_amdgcn_mbcnt_hi(~0u, __builtin_amdgcn_mbcnt_lo(~0u, 0u));
      t = t < 288 ? t : t - 288;
      const int r2 = (t >> 4) * 64 + (xcd & 1) * 32 + idx;
      touch(lb + T_SCR, (r2 < 256 ? wo + (256 * kq + r2) * 1024 : wd + (896 * kq + r2 - 256) * 1024) + (t & 15) * 64);
#endif
    }
    WAIT_L0();
    SCHED();
    BAR();
    SCHED();
    PRIO_M();
    auto fin = [&]() { SCHED(); PRIO_T(); if (!LAST) { if (PFD) WAIT_V(1); else WAIT_V(0); } BAR(); SCHED(); };
#pragma unroll
    for (int e = 0; e < 10; ++e) {
      const int n = e < 2 ? 8 + e : e - 2;
#pragma unroll
      for (int i = 0; i < 4; ++i) {
        acc[i][n] = mfma(wn[e & 3][0], a[0][i], acc[i][n]);
        SCHED();
        if (!HALO && e == 9 && i == 3) fin();
        SCHED();
        acc[i][n] = mfma(wn[e & 3][1], a[1][i], acc[i][n]);
        SCHED();
        if (e == 0) { rdq(2 + (i >> 1), 2 * (i & 1)); rdq(2 + (i >> 1), 2 * (i & 1) + 1); } else rdq(e + 3, i);
        if (i == 3 && e < 9) { asm volatile("" :: "v"(wn[(e + 1) & 3][0]), "v"(wn[(e + 1) & 3][1])); }
        SCHED();
      }
    }
    if (HALO) {
      hacc = mfma(wn[2][0], wn[3][0], hacc);
      WAIT_L0();
      fin();
      hacc = mfma(wn[2][1], wn[3][1], hacc);
    }
    hoffA += dlt; boff0 += dlt; boff1 += dlt; hoffB += dlt;
    PRIO_E();
    SCHED();
  };
  const char* const hbase = ws + WS_H + (size_t)(mb * 4 + kq) * 36 * 16384;
  auto brow = [&](int stp_, int w_) {
    return (const char*)(stp_ < 4 ? wo + (size_t)(256 * kq) * 1024 : wd + (size_t)(896 * kq) * 1024 - 256 * 1024) + ((stp_ * 64 + 8 * w_) * 1024 + nt * 128) * 2;
  };
  unsigned va, vb;
  auto stage1 = [&](auto halo_c) {
    constexpr bool TA = decltype(halo_c)::value;
    typedef std::integral_constant<bool, TA || !HALO_SKIP> HC;
#pragma clang loop unroll(disable)
    for (int q = 0; q < 15; ++q) step(HC{}, std::false_type{}, q);
    step(HC{}, std::true_type{}, 15);
    ST(3);
    asm volatile("" ::: "memory");
    if (g == 0) __builtin_amdgcn_s_setprio(2); else __builtin_amdgcn_s_setprio(EPI_PRIO);
    lane = __builtin_amdgcn_mbcnt_hi(~0u, __builtin_amdgcn_mbcnt_lo(~0u, 0u));
    asm volatile("" : "+v"(lane));
    fr = lane & 15; fq = lane >> 4;
    va = lane * 16;
    vb = (lane >> 3) * 2048 + ((((lane & 7) >> 1) ^ ((lane >> 4) & 3)) * 32) + (lane & 1) * 16;
    if (g == 1) {
#pragma unroll
      for (int u = 0; u < 4; ++u) {
        const int w_ = wi * 2 + (u & 1);
        const char* p0 = brow((u >> 1) ? own1 : own0, w_);
        dma_first(lb + (u >> 1) * G2_SLOT + G2_SA + w_ * 2048, vb, p0); dma_next<1024>(vb, p0 - 896);
      }
    }
    const auto hrs = __builtin_amdgcn_make_buffer_rsrc((void*)hbase, 0, 36 * 16384, 0x00020000);
    const unsigned hv = wi * 4096 + lane * 16;
    auto put = [&](u32x4 v, int s, int kb, int i) {
      __builtin_amdgcn_raw_buffer_store_b128(v, hrs, hv + kb * 16384 + i * 1024, 0, H_AUX);
      *(LAS u32x4*)(lds + s * G2_SLOT + g * 16384 + ((wi * 4096 + kq * 1024 + lane * 16) ^ (i * 1024))) = v;
    };
#define GLU(i, a, b) glu4(acc[i][a], acc[i][b])
    if (!TA) {
      const int kbc = 8 + ((fc - 896 * kq) >> 5);
#pragma unroll
      for (int i = 0; i < 4; ++i) {
        __builtin_amdgcn_raw_buffer_store_b64(GLU(i, 8, 9), hrs, hv + kbc * 16384 + g * 8 + i * 1024, 0, H_AUX);
        put(cat4(GLU(i, 0, 4), GLU(i, 1, 5)), 0, kba + g, i);
        put(cat4(GLU(i, 2, 6), GLU(i, 3, 7)), 1, kba + 2 + g, i);
        if (i == FB) { SCHED(); BAR_IF0(g); SCHED(); }
      }
    } else {
      auto glu_part = [&](int i0, int i1) {
#pragma unroll
        for (int i = i0; i < i1; ++i) put(cat4(GLU(i, 6, 8), GLU(i, 7, 9)), 1, 8 + 2 * j + g, i);
      };
      if (g == 0) { glu_part(0, FA); SCHED(); BAR(); __builtin_amdgcn_s_setprio(0); asm volatile("" ::: "memory"); }
    LAS char* kim = lds + T_KV + g * KV_BYTES;
    LAS char* vim = kim + 7 * 1024;
    bf16x8 kf[4], qf[4];
#pragma unroll
    for (int i = 0; i < 4; ++i) {
      kf[i] = pack8(acc[i][2], acc[i][3]);
      *(LAS bf16x8*)(vim + ((wi * 4 + i) * 16 + fr) * 80 + fq * 16) = pack8(acc[i][4], acc[i][5]);
    }
    if (wi < 3) {
      *(LAS bf16x8*)(kim + wi * 2048 + lane * 16) = kf[2];
      *(LAS bf16x8*)(kim + wi * 2048 + 1024 + lane * 16) = kf[3];
    }
    {
      LAS char* hp = (wi < 2) ? (kim + 6 * 1024 + lane * 16 + wi * 8) : (vim + (256 + fr) * 80 + fq * 16 + (wi - 2) * 8);
      *(LAS u32x2*)hp = (u32x2){pk2(hacc[0], hacc[1]), pk2(hacc[2], hacc[3])};
    }
    WAIT_L0();
    BAR();
    asm volatile("" ::: "memory");
    const bool halo = wi == 0;
    const f32x4 bc = *(const LAS f32x4*)(lds + T_BIAS + lane * 16), bp = *(const LAS f32x4*)(lds + T_BIAS + 1024 + lane * 16);
    const f32x4 bp0 = *(const LAS f32x4*)(lds + T_BIAS + (halo ? 2048 : 1024) + lane * 16);
    const f32x4 bp1 = *(const LAS f32x4*)(lds + T_BIAS + (halo ? 3072 : 1024) + lane * 16);
    const int pf0 = halo ? 16 : wi * 4 - 2, pf1 = halo ? 16 : wi * 4 - 1;
    const bf16x8 kp0 = *(const LAS bf16x8*)(kim + (halo ? 6 : 2 * wi - 2) * 1024 + lane * 16);
    const bf16x8 kp1 = *(const LAS bf16x8*)(kim + (halo ? 6 : 2 * wi - 1) * 1024 + lane * 16);
    const LAS char* vr = vim + (4 * fq + (fr >> 2)) * 80 + (lane & 3) * 16;
    bf16x4 vt[6][2];
#pragma unroll
    for (int d = 0; d < 2; ++d) {
      vt[0][d] = trd(vr + pf0 * 1280 + d * 8); vt[1][d] = trd(vr + pf1 * 1280 + d * 8);
#pragma unroll
      for (int i = 0; i < 4; ++i) vt[2 + i][d] = trd(vr + (wi * 4 + i) * 1280 + d * 8);
    }
    f32x4 sp[4], sc[4];
#pragma unroll
    for (int i = 0; i < 4; ++i) {
      qf[i] = pack8(acc[i][0], acc[i][1]);
      sc[i] = mfma(kf[i], qf[i], bc);
    }
    sp[2] = mfma(kf[0], qf[2], bp); sp[3] = mfma(kf[1], qf[3], bp);
    sp[0] = mfma(kp0, qf[0], bp0); sp[1] = mfma(kp1, qf[1], bp1);
    const float sl = 0.17677669529663687f * 1.4426950408889634f;
    const f32x4 z4 = {0.f, 0.f, 0.f, 0.f};
    float m[4];
#pragma unroll
    for (int i = 0; i < 4; ++i)
      m[i] = fmx(fmx(fmx(fmx(sp[i][0], sp[i][1]), sp[i][2]), fmx(fmx(sp[i][3], sc[i][0]), sc[i][1])), fmx(sc[i][2], sc[i][3]));
    {
      const unsigned t = __float_as_uint(tred<true>(m[0], m[1], m[2], m[3]));
      const auto u = __builtin_amdgcn_permlane32_swap(t, t, false, false);
      const auto u0 = __builtin_amdgcn_permlane16_swap(u[0], u[0], false, false), u1 = __builtin_amdgcn_permlane16_swap(u[1], u[1], false, false);
      m[0] = __uint_as_float(u0[0]); m[1] = __uint_as_float(u0[1]); m[2] = __uint_as_float(u1[0]); m[3] = __uint_as_float(u1[1]);
    }
    const u32x4 one4 = {0x3f803f80u, 0x3f803f80u, 0x3f803f80u, 0x3f803f80u};
#pragma unroll
    for (int i = 0; i < 4; ++i) {
      const float mm = -sl * m[i];
#pragma unroll
      for (int e = 0; e < 4; ++e) {
        sp[i][e] = __builtin_amdgcn_exp2f(__builtin_fmaf(sp[i][e], sl, mm));
        sc[i][e] = __builtin_amdgcn_exp2f(__builtin_fmaf(sc[i][e], sl, mm));
      }
      const bf16x8 pfr = pack8(sp[i], sc[i]);
      const f32x4 sm = mfma(__builtin_bit_cast(bf16x8, one4), pfr, z4);
      const f32x4 o0 = mfma(cat(vt[i][0], vt[2 + i][0]), pfr, z4);
      const f32x4 o1 = mfma(cat(vt[i][1], vt[2 + i][1]), pfr, z4);
      const float inv = __builtin_amdgcn_rcpf(sm[0]);
      put(__builtin_bit_cast(u32x4, pack8(o0 * inv, o1 * inv)), 0, 2 * j + g, i);
    }
      glu_part(FA, 4);
      if (g == 1) glu_part(0, FA);
    }
  };
  stage1(std::integral_constant<bool, typeA>{});
  ST(4);

  const int lag = g ^ 1;
  GAS unsigned* hflag = (GAS unsigned*)(ws + WS_HCNT) + (mb * 4 + kq) * 256;
  const unsigned fo = (nt * 32 + (lane & 7)) * 4;
  GAS unsigned* gcnt = (GAS unsigned*)(ws + WS_GCNT) + (mb * 4 + kq) * 16;
  const int e0 = kq * 1024, e1 = ((1 ^ kq) - 1) * 1024, e2 = ((2 ^ kq) - 2) * 1024, e3 = ((3 ^ kq) - 3) * 1024;
  auto wait_cnt = [&](unsigned seen, GAS unsigned* c, unsigned need) {
    if ((unsigned)__builtin_amdgcn_readfirstlane(seen) < need) {
      asm volatile("" ::: "memory");
      unsigned spins = 0;
      while ((unsigned)__builtin_amdgcn_readfirstlane(__hip_atomic_load(c, __ATOMIC_RELAXED, __HIP_MEMORY_SCOPE_AGENT)) < need)
        if (++spins > SPIN_CAP) break;
    }
    asm volatile("" ::: "memory");
  };
  auto wait_flags = [&](unsigned seen, unsigned long long need) {
    if ((__builtin_amdgcn_ballot_w64(seen != 0u) & need) != need) {
      asm volatile("" ::: "memory");
      unsigned spins = 0;
      while ((__builtin_amdgcn_ballot_w64(__hip_atomic_load(hflag + (fo >> 2), __ATOMIC_RELAXED, __HIP_MEMORY_SCOPE_AGENT) != 0u) & need) != need) {
        __builtin_amdgcn_s_sleep(1);
        if (++spins > SPIN_CAP) break;
      }
    }
    asm volatile("" ::: "memory");
  };
  unsigned long long ord0 = j & 2 ? (j & 1 ? step_order(typeA, 3, 0) : step_order(typeA, 2, 0)) : (j & 1 ? step_order(typeA, 1, 0) : step_order(typeA, 0, 0));
  const unsigned ord1 = (unsigned)(j & 2 ? (j & 1 ? step_order(typeA, 3, 1) : step_order(typeA, 2, 1)) : (j & 1 ? step_order(typeA, 1, 1) : step_order(typeA, 0, 1)));
  const char* const caw = hbase + w * 4096;
  const char* const cbo = brow(0, w); const char* const cbd = brow(4, w) - 4 * 131072;
  auto pop = [&](const char*& ca_, const char*& cb_) {
    const unsigned s = (unsigned)ord0 & 31u;
    ord0 >>= 5;
    ca_ = caw + (s << 15); cb_ = (s < 4u ? cbo : cbd) + (s << 17);
  };
  const int mf = wi * 4 + kq;
  const unsigned orow = ((mb * 256 + 32 * (mf >> 1) + 2 * fr + (mf & 1)) * 1024 + nt * 128 + g * 64 + 16 * (fq & 1) + 8 * (fq >> 1)) * 2;
  u32x4 xr[2] = {{0u, 0u, 0u, 0u}, {0u, 0u, 0u, 0u}};
  const int rl2 = 4 * (fq & 1) + (fr >> 2);
  unsigned ao = lb + wi * 4096 + lane * 16;
  unsigned bo = lb + G2_SA + (fq >> 1) * 2048 + g * 1024 + rl2 * 128 + 32 * ((rl2 >> 1) & 3) + 8 * (lane & 3);
  f32x4 ac2[4][4];
#pragma unroll
  for (int i = 0; i < 4; ++i)
#pragma unroll
    for (int n = 0; n < 4; ++n) { ac2[i][n] = (f32x4){0.f, 0.f, 0.f, 0.f}; asm volatile("" : "+v"(ac2[i][n])); }
  ns = lb + 2 * G2_SLOT;
  const char *ca, *cb;
  const unsigned ordf = (unsigned)ord0;
  pop(ca, cb);
  unsigned sl2 = lb;
  WAIT_V_IF0(0, lag);
  WAIT_L0();
  BAR();
  ST(5);
  ST_SAVE(); ST(0); ST(1);
  unsigned seen = 0;
#define PEEK(c, o) asm volatile("global_load_dword %0, %1, %2 sc1" : "+v"(seen) : "v"(o), "s"(c) : "memory")
  if (lag) BAR();

  bf16x8 ka[2][2], kb[2][2][4];
  auto phase2 = [&](auto mode, int q) {
    constexpr int MODE = decltype(mode)::value;
    bf16x8 a[2][4], bfr[2][4];
#define RA(kk, i) a[kk][i] = *(const LAS bf16x8*)(lp(ao) + (kk) * 16384 + (i) * 1024);
#define RB(kk, n) bfr[kk][n] = cat(trd(lp(bo ^ ((n) * 32)) + (kk) * 8192), trd(lp(bo ^ ((n) * 32)) + (kk) * 8192 + 4096));
    int dlt;
    asm volatile("" ::: "memory");
    if (MODE == 1 && q == 1 && w == 4) PEEK(hflag, fo);
    if (MODE == 2 && q == 16)
      asm volatile("global_load_dwordx4 %0, %2, %3\n\tglobal_load_dwordx4 %1, %2, %3 offset:64" : "+v"(xr[0]), "+v"(xr[1]) : "v"(orow), "s"(x) : "memory");
    RA(0, 0) RA(0, 1)
    if (MODE == 0) dmc_first(ns + w * 4096, va, ca + e0);
    RA(0, 2) RA(0, 3) RB(0, 0)
    if (MODE == 0) dmc_next<1024>(va, ca + e1);
    RB(0, 1) RB(0, 2) RB(0, 3)
    if (MODE == 0) dmc_next<2048>(va, ca + e2);
    RA(1, 0) RA(1, 1) RA(1, 2)
    if (MODE == 0) dmc_next<3072>(va, ca + e3);
    RA(1, 3) RB(1, 0)
    if (MODE < 2) dma_first(ns + G2_SA + w * 2048, vb, cb);
    RB(1, 1)
    if (MODE < 2) dma_next<1024>(vb, cb - 896);
    dlt = (sl2 == lb + 2 * G2_SLOT) ? -2 * G2_SLOT : G2_SLOT; ns = sl2; sl2 += dlt;
    if (MODE < 2) pop(ca, cb);
    asm volatile("" : "+s"(ca), "+s"(cb), "+s"(ns), "+s"(sl2), "+s"(dlt), "+s"(ord0) :: "memory");
    RB(1, 2) RB(1, 3)
#undef RA
#undef RB
    ao += dlt; bo += dlt;
    SCHED();
    asm volatile("" : "+v"(bo), "+v"(ao));
    if (MODE == 0) WAIT_V_IF0(6, lag ^ 1); else if (MODE == 2 && q == 16) WAIT_V_IF0(2, lag ^ 1);
    if (MODE == 1 && q == 1) {
      asm volatile("s_waitcnt vmcnt(2)" : "+v"(seen) :: "memory");
      if (!lag) {
        volatile LAS unsigned* go = (volatile LAS unsigned*)(lds + T_SYNC + 4);
        if (w == 4) { wait_flags(seen, 0xf0ull); *go = 1u; }
        else { unsigned spins = 0; while (__builtin_amdgcn_readfirstlane(*go) == 0u) { __builtin_amdgcn_s_sleep(1); if (++spins > (SPIN_CAP << 3)) break; } }
        asm volatile("" ::: "memory");
      }
      const int s = (int)(ordf >> (5 * lag)) & 31;
      const char* p = hbase + s * 32768 + wi * 8192;
      const unsigned d = lb + (lag ? 0 : 2 * G2_SLOT) + wi * 8192;
      dmc_first(d, va, p + e0); dmc_next<1024>(va, p + e1); dmc_next<2048>(va, p + e2); dmc_next<3072>(va, p + e3);
      dmc_first(d + 4096, va, p + 4096 + e0); dmc_next<1024>(va, p + 4096 + e1); dmc_next<2048>(va, p + 4096 + e2); dmc_next<3072>(va, p + 4096 + e3);
    }
    WAIT_L0();
    SCHED();
    BAR();
    SCHED();
    P2_M();
#pragma unroll
    for (int n = 0; n < 4; ++n)
#pragma unroll
      for (int i = (MODE == 2 ? 1 : 0); i < 4; ++i) {
        ac2[i][n] = mfma(bfr[0][n], a[0][i], ac2[i][n]);
        SCHED();
        if (n == 3 && i == 3) {
          P2_T();
          if (MODE == 0) WAIT_V_IF0(6, lag); else if (MODE == 2 && q == 16) WAIT_V_IF0(2, lag);
          if (MODE == 1 && q == 1) WAIT_V_IF0(0, lag);
          if (q < 17 || !lag) BAR();
          SCHED();
        }
        ac2[i][n] = mfma(bfr[1][n], a[1][i], ac2[i][n]);
        SCHED();
        if (MODE == 1 && q == 0 && n == 1 && i == 3 && lag) {
          WAIT_V(0);
          unsigned old = 0;
          if (lane == 0) old = __hip_atomic_fetch_add((LAS unsigned*)(lds + T_SYNC), 1u, __ATOMIC_RELAXED, __HIP_MEMORY_SCOPE_WORKGROUP);
          if (__builtin_amdgcn_readfirstlane(old) == 3u && lane < 8)
            __hip_atomic_store(hflag + lane * 32 + nt, 1u, __ATOMIC_RELAXED, __HIP_MEMORY_SCOPE_AGENT);
          SCHED();
        }
      }
    if (MODE == 2) {
#pragma unroll
      for (int kk = 0; kk < 2; ++kk) {
        ka[q & 1][kk] = a[kk][0];
#pragma unroll
        for (int n = 0; n < 4; ++n) kb[q & 1][kk][n] = bfr[kk][n];
      }
    }
    P2_E();
    SCHED();
  };
  typedef std::integral_constant<int, 0> M0; typedef std::integral_constant<int, 1> M1; typedef std::integral_constant<int, 2> M2;
  phase2(M1{}, 0);
  phase2(M1{}, 1);
  ST(2);
#pragma clang loop unroll(disable)
  for (int q = 2; q < 6; ++q) phase2(M0{}, q);
  PEEK(hflag, fo);
  phase2(M0{}, 6);
  asm volatile("" : "+v"(seen));
  ord0 |= (unsigned long long)ord1 << 20;
  wait_flags(seen, 0x0full);
#pragma clang loop unroll(disable)
  for (int q = 7; q < 13; ++q) phase2(M0{}, q);
  if (w == 4 && lane == 0) __hip_atomic_fetch_add(gcnt, 1u, __ATOMIC_RELAXED, __HIP_MEMORY_SCOPE_AGENT);
#pragma clang loop unroll(disable)
  for (int q = 13; q < 16; ++q) phase2(M0{}, q);
  phase2(M2{}, 16);
  PEEK(gcnt, 0u);
  phase2(M2{}, 17);
  asm volatile("s_waitcnt vmcnt(0)" : "+v"(seen) :: "memory");
  wait_cnt(seen, gcnt, 8u);
  ST(3);
  const int tile = mb * 8 + nt;
  const auto rs = __builtin_amdgcn_make_buffer_rsrc(ws + WS_H + (size_t)mb * 4 * 36 * 16384, 0, 4 * 36 * 16384, 0x00020000);
  auto xo = [&](int src, int i) { const int u = 15 + nt * 3 + i; return (unsigned)((src * 36 + (u >= 36 ? u - 36 : u)) * 16384 + w * 2048) + lane * 16; };
#pragma unroll
  for (int i = 1; i < 4; ++i) {
    const unsigned o = xo(kq, i);
    __builtin_amdgcn_raw_buffer_store_b128(__builtin_bit_cast(u32x4, pack8(ac2[i][0], ac2[i][1])), rs, o, 0, 16);
    __builtin_amdgcn_raw_buffer_store_b128(__builtin_bit_cast(u32x4, pack8(ac2[i][2], ac2[i][3])), rs, o + 1024, 0, 16);
  }
  GAS unsigned* cnt = (GAS unsigned*)(ws + WS_CNT) + (tile * 32 + w) * 16;
  GAS unsigned* mine = cnt + kq * 128 + (lane & 3);
  seen = __hip_atomic_load(mine, __ATOMIC_RELAXED, __HIP_MEMORY_SCOPE_AGENT);
  SCHED();
#pragma unroll
  for (int n = 0; n < 4; ++n)
#pragma unroll
    for (int u = 0; u < 4; ++u) ac2[0][n] = mfma(kb[u >> 1][u & 1][n], ka[u >> 1][u & 1], ac2[0][n]);
  SCHED();
  WAIT_V(0);
  ST(4);
  asm volatile("" : "+v"(xr[0]), "+v"(xr[1]));
  if (lane >= 1 && lane < 4) __hip_atomic_store(cnt + (kq ^ lane) * 128 + lane, 1u, __ATOMIC_RELAXED, __HIP_MEMORY_SCOPE_AGENT);
  u32x4 p[3][2];
#pragma unroll
  for (int i = 0; i < 3; ++i) p[i][0] = p[i][1] = (u32x4){0u, 0u, 0u, 0u};
  {
    unsigned done = 0;
#pragma clang loop unroll(disable)
    for (unsigned spins = 0; spins < SPIN_CAP; ++spins) {
      const unsigned rdy = (unsigned)__builtin_amdgcn_ballot_w64(seen != 0u) & 0xeu & ~done;
      asm volatile("" ::: "memory");
#pragma unroll
      for (int i = 1; i < 4; ++i)
        if (rdy & (1u << i)) {
          const unsigned o = xo(kq ^ i, i);
          p[i - 1][0] = __builtin_amdgcn_raw_buffer_load_b128(rs, o, 0, 16);
          p[i - 1][1] = __builtin_amdgcn_raw_buffer_load_b128(rs, o + 1024, 0, 16);
        }
      done |= rdy;
      if (done == 0xeu) break;
      if (!rdy) __builtin_amdgcn_s_sleep(1);
      seen = __hip_atomic_load(mine, __ATOMIC_RELAXED, __HIP_MEMORY_SCOPE_AGENT);
    }
  }
  ST(5);
  const auto ors = __builtin_amdgcn_make_buffer_rsrc(out, 0, 4 << 20, 0x00020000);
#pragma unroll
  for (int h = 0; h < 2; ++h) {
    const auto x0 = __builtin_amdgcn_permlane16_swap(xr[h][0], xr[h][2], false, false), x1 = __builtin_amdgcn_permlane16_swap(xr[h][1], xr[h][3], false, false);
    const u32x4 xv = {x0[0], x1[0], x0[1], x1[1]};
    unsigned r[4];
#pragma unroll
    for (int d = 0; d < 4; ++d) {
      float lo = ac2[0][2 * h + (d >> 1)][2 * (d & 1)] + __uint_as_float(xv[d] << 16), hi = ac2[0][2 * h + (d >> 1)][2 * (d & 1) + 1] + __uint_as_float(xv[d] & 0xffff0000u);
#pragma unroll
      for (int i = 0; i < 3; ++i) { lo += __uint_as_float(p[i][h][d] << 16); hi += __uint_as_float(p[i][h][d] & 0xffff0000u); }
      r[d] = pk2(lo, hi);
    }
    const auto y0 = __builtin_amdgcn_permlane16_swap(r[0], r[2], false, false), y1 = __builtin_amdgcn_permlane16_swap(r[1], r[3], false, false);
    __builtin_amdgcn_raw_buffer_store_b128((u32x4){y0[0], y1[0], y0[1], y1[1]}, ors, orow + 64 * h, 0, OUT_AUX);
  }
  ST(6);
  ST(7);
  ST_OUT(1, 8);
  ST_REST(); ST_OUT(0, 6);
}

}

extern "C" __global__ __launch_bounds__(512, 2) void psum_g1_kernel(const u16* __restrict__ x, u16* __restrict__ out, const u16* __restrict__ wqkv,
                                                                    const u16* __restrict__ wg, const u16* __restrict__ wu,
                                                                    const u16* __restrict__ wo, const u16* __restrict__ wd, char* __restrict__ ws) {
  if (blockIdx.x < 128) body<true>(x, out, wqkv, wg, wu, wo, wd, ws);
  else body<false>(x, out, wqkv, wg, wu, wo, wd, ws);
}

extern "C" void kernel_launch(void* const* d_in, const int* in_sizes, int n_in, void* d_out, int out_size, void* d_ws,
                              size_t ws_size, hipStream_t stream) {
  const u16* x = (const u16*)d_in[0];
  const u16* wqkv = (const u16*)d_in[1];
  const u16* wo = (const u16*)d_in[2];
  const u16* wg = (const u16*)d_in[4];
  const u16* wu = (const u16*)d_in[5];
  const u16* wd = (const u16*)d_in[6];
  char* ws = (char*)d_ws;
  (void)hipFuncSetAttribute((const void*)psum_g1_kernel, hipFuncAttributeMaxDynamicSharedMemorySize, G1_LDS);
  psum_g1_kernel<<<256, 512, G1_LDS, stream>>>(x, (u16*)d_out, wqkv, wg, wu, wo, wd, ws);
}
```

```cpp
#define DIAG 20
#include <hip/hip_runtime.h>
#include <hip/hip_bf16.h>
#include <type_traits>
#define EARLY 1
#define HALO_SKIP 1
#define PF_W2 1
#define PF_LOOP 0
#define PF_R0 192
#define PF_PER 2
#define PF_LN_ROWS 96
#define PRIO_L 0
#define PRIO_M() __builtin_amdgcn_s_setprio(1)
#define PRIO_T() __builtin_amdgcn_s_setprio(3)
#define PRIO_E() __builtin_amdgcn_s_setprio(0)
#define PF_CODE 0
#define PRECLEAN 0
#define OUT_AUX 16
#define G2ORD 0
#define EARLY2 1
#define CHAIN 1
#define PF_X 0
#define XH 1
#define ACQ 0
#define EPI_PRIO 1
#define LORD 1
#define RPOS 1
#define FA 4
#define FB 0
#define PFD 0
#define SGLU 0
#define TLBW 0
#define WDELAY 0
#define HDEFER 1
#define P2L 1
#define P2_M() __builtin_amdgcn_s_setprio(0)
#define P2_T() __builtin_amdgcn_s_setprio(1)
#define P2_E()
#define SPIN_CAP (1u << 17)
#define H_AUX 16

namespace {

typedef unsigned short u16;
typedef short bf16x8 __attribute__((ext_vector_type(8)));
typedef short bf16x4 __attribute__((ext_vector_type(4)));
typedef float f32x4 __attribute__((ext_vector_type(4)));
typedef float f32x2 __attribute__((ext_vector_type(2)));
typedef unsigned u32x4 __attribute__((ext_vector_type(4)));
typedef unsigned u32x2 __attribute__((ext_vector_type(2)));
typedef __bf16 bf2 __attribute__((ext_vector_type(2)));
#define LAS __attribute__((address_space(3)))
#define GAS __attribute__((address_space(1)))

#define WAIT_V(n) asm volatile("s_waitcnt vmcnt(" #n ")" ::: "memory")
#define WAIT_V_IF0(n, c) asm volatile("s_cmp_lg_u32 %0, 0\n\ts_cbranch_scc1 1f\n\ts_waitcnt vmcnt(" #n ")\n1:" :: "s"(c) : "memory", "scc")
#define BAR_IF0(c) asm volatile("s_cmp_lg_u32 %0, 0\n\ts_cbranch_scc1 1f\n\ts_barrier\n\ts_setprio 0\n1:" :: "s"(c) : "memory", "scc")
#define WAIT_L0() do { asm volatile("" ::: "memory"); __builtin_amdgcn_s_waitcnt(0xC07F); asm volatile("" ::: "memory"); } while (0)
#define BAR() do { asm volatile("" ::: "memory"); __builtin_amdgcn_s_barrier(); asm volatile("" ::: "memory"); } while (0)
#define SCHED() __builtin_amdgcn_sched_barrier(0)

__device__ __forceinline__ float b2f(u16 v) { return __uint_as_float(((unsigned)v) << 16); }
__device__ __forceinline__ unsigned pk2(float lo, float hi) {
  bf2 r = {(__bf16)lo, (__bf16)hi};
  return __builtin_bit_cast(unsigned, r);
}
__device__ __forceinline__ float rbf(float f) { return (float)(__bf16)f; }
__device__ __forceinline__ bf16x8 pack8(f32x4 a, f32x4 b) {
  u32x4 r = {pk2(a[0], a[1]), pk2(a[2], a[3]), pk2(b[0], b[1]), pk2(b[2], b[3])};
  return __builtin_bit_cast(bf16x8, r);
}
__device__ __forceinline__ bf16x4 trd(LAS const char* p) {
  return __builtin_amdgcn_ds_read_tr16_b64_v4i16((LAS bf16x4*)p);
}
__device__ __forceinline__ bf16x8 cat(bf16x4 a, bf16x4 b) { return __builtin_shufflevector(a, b, 0, 1, 2, 3, 4, 5, 6, 7); }
__device__ __forceinline__ f32x4 mfma(bf16x8 a, bf16x8 b, f32x4 c) {
  return __builtin_amdgcn_mfma_f32_16x16x32_bf16(a, b, c, 0, 0, 0);
}
__device__ __forceinline__ void dma_first(unsigned lds_dst, unsigned voff, const char* b0) {
  asm volatile("s_mov_b32 m0, %0\n\ts_nop 0\n\tglobal_load_lds_dwordx4 %1, %2" :: "s"(lds_dst), "v"(voff), "s"(b0) : "memory");
}
template <int OFF>
__device__ __forceinline__ void dma_next(unsigned voff, const char* b) {
  asm volatile("global_load_lds_dwordx4 %0, %1 offset:%2" :: "v"(voff), "s"(b), "i"(OFF) : "memory");
}
__device__ __forceinline__ void dmc_first(unsigned lds_dst, unsigned voff, const char* b0) {
  asm volatile("s_mov_b32 m0, %0\n\ts_nop 0\n\tglobal_load_lds_dwordx4 %1, %2 sc1" :: "s"(lds_dst), "v"(voff), "s"(b0) : "memory");
}
template <int OFF>
__device__ __forceinline__ void dmc_next(unsigned voff, const char* b) {
  asm volatile("global_load_lds_dwordx4 %0, %1 offset:%2 sc1" :: "v"(voff), "s"(b), "i"(OFF) : "memory");
}
__device__ __forceinline__ void dma1m(unsigned lds_dst, const char* p, unsigned long long mask) {
  asm volatile("s_mov_b32 m0, %0\n\ts_mov_b64 exec, %2\n\t"
               "global_load_lds_dwordx4 %1, off sc1\n\t"
               "s_mov_b64 exec, -1"
               :: "s"(lds_dst), "v"(p), "s"(mask) : "memory");
}
__device__ __forceinline__ void touch(unsigned lds_dst, const void* p) {
  asm volatile("s_mov_b32 m0, %0\n\ts_nop 0\n\tglobal_load_lds_dword %1, off" :: "s"(lds_dst), "v"(p) : "memory");
}
#define CODE_ADDR(dst, sym) do { unsigned lo_, hi_; asm volatile("s_getpc_b64 s[90:91]\n\ts_add_u32 %0, s90, " #sym "@rel32@lo+4\n\ts_addc_u32 %1, s91, " #sym "@rel32@hi+12" \
    : "=s"(lo_), "=s"(hi_) :: "s90", "s91", "scc"); dst = (const char*)(((unsigned long long)hi_ << 32) | lo_); } while (0)
constexpr int LN_LINES = 16, G1_LINES = 80, G2_LINES = 30;
__device__ __forceinline__ LAS char* lp(unsigned a) { return (LAS char*)(size_t)a; }
__device__ __forceinline__ float fmx(float a, float b) { return __builtin_elementwise_maximum(a, b); }
__device__ __forceinline__ float xmax(float v) {
  auto a = __builtin_amdgcn_permlane16_swap(__float_as_uint(v), __float_as_uint(v), false, false);
  v = fmaxf(__uint_as_float(a[0]), __uint_as_float(a[1]));
  auto b = __builtin_amdgcn_permlane32_swap(__float_as_uint(v), __float_as_uint(v), false, false);
  return fmaxf(__uint_as_float(b[0]), __uint_as_float(b[1]));
}
__device__ __forceinline__ float xsum(float v) {
  auto a = __builtin_amdgcn_permlane16_swap(__float_as_uint(v), __float_as_uint(v), false, false);
  v = __uint_as_float(a[0]) + __uint_as_float(a[1]);
  auto b = __builtin_amdgcn_permlane32_swap(__float_as_uint(v), __float_as_uint(v), false, false);
  return __uint_as_float(b[0]) + __uint_as_float(b[1]);
}

template <int CTRL>
__device__ __forceinline__ float dpp_add(float v) {
  return v + __uint_as_float(__builtin_amdgcn_update_dpp(0u, __float_as_uint(v), CTRL, 0xf, 0xf, true));
}
__device__ __forceinline__ float hsum(float v) {
  v = dpp_add<0xB1>(v);
  v = dpp_add<0x4E>(v);
  v = dpp_add<0x128>(v);
  return xsum(v);
}

#define DG(kid, slot) do { if (threadIdx.x == DGT) ((unsigned long long*)(ws + WS_ST))[((kid) * 256 + blockIdx.x) * 8 + (slot)] = __builtin_amdgcn_s_memrealtime(); } while (0)
#define ST_DECL
#define LT_DECL
#define LT(k)
#define ET(k)
#define ST(k)
#define ST_OUT(kid, n)
#define ST_SAVE()
#define ST_REST()
constexpr int XN_STEP = 17 * 1024;
constexpr size_t WS_XN = 0;
constexpr size_t WS_H = WS_XN + (size_t)8 * 32 * XN_STEP;
constexpr size_t H_BYTES = (size_t)8 * 4 * 36 * 16 * 1024;
constexpr size_t WS_CNT = WS_H + H_BYTES;
constexpr size_t WS_GCNT = WS_CNT + 131072;
constexpr size_t WS_HCNT = WS_GCNT + 8192;
constexpr size_t WS_LNF = WS_HCNT + 32768;
constexpr size_t WS_ST = WS_LNF + 32768;

constexpr int G1_SA = 17 * 1024, G1_SB = 20 * 1024, G1_SLOT = G1_SA + G1_SB;
constexpr int T_SCR = 159488;
constexpr int T_BIAS = T_SCR + 256;
constexpr int G1_LDS = T_BIAS + 4096;
constexpr int G2_SA = 32768, G2_SB = 16384, G2_SLOT = G2_SA + G2_SB;
constexpr int T_SYNC = T_SCR - 64;
constexpr int T_KV = 2 * G2_SLOT, KV_BYTES = 7 * 1024 + 272 * 80;

template <bool MAX>
__device__ __forceinline__ float tred(float a, float b, float c, float d) {
  const auto r1 = __builtin_amdgcn_permlane16_swap(__float_as_uint(a), __float_as_uint(b), false, false);
  const auto r2 = __builtin_amdgcn_permlane16_swap(__float_as_uint(c), __float_as_uint(d), false, false);
  const float x0 = __uint_as_float(r1[0]), x1 = __uint_as_float(r1[1]), y0 = __uint_as_float(r2[0]), y1 = __uint_as_float(r2[1]);
  const float t = MAX ? fmx(x0, x1) : x0 + x1, u = MAX ? fmx(y0, y1) : y0 + y1;
  const auto r3 = __builtin_amdgcn_permlane32_swap(__float_as_uint(t), __float_as_uint(u), false, false);
  const float z0 = __uint_as_float(r3[0]), z1 = __uint_as_float(r3[1]);
  return MAX ? fmx(z0, z1) : z0 + z1;
}
__device__ __forceinline__ u32x2 glu4(f32x4 g, f32x4 u) {
  f32x4 e = g * -1.4426950408889634f;
#pragma unroll
  for (int i = 0; i < 4; ++i) e[i] = __builtin_amdgcn_exp2f(e[i]);
  e = e + 1.0f;
#pragma unroll
  for (int i = 0; i < 4; ++i) e[i] = __builtin_amdgcn_rcpf(e[i]);
  e = e * u;
  return (u32x2){pk2(e[0], e[1]), pk2(e[2], e[3])};
}
__device__ __forceinline__ u32x2 glu4s(f32x4 g, f32x4 u) {
  float e[4];
#pragma unroll
  for (int i = 0; i < 4; ++i) { e[i] = g[i] * -1.4426950408889634f; asm volatile("" : "+v"(e[i])); e[i] = __builtin_amdgcn_exp2f(e[i]); }
#pragma unroll
  for (int i = 0; i < 4; ++i) { e[i] = e[i] + 1.0f; asm volatile("" : "+v"(e[i])); e[i] = __builtin_amdgcn_rcpf(e[i]); }
#pragma unroll
  for (int i = 0; i < 4; ++i) { e[i] = e[i] * u[i]; asm volatile("" : "+v"(e[i])); }
  return (u32x2){pk2(e[0], e[1]), pk2(e[2], e[3])};
}
__device__ __forceinline__ u32x4 cat4(u32x2 a, u32x2 b) { return (u32x4){a[0], a[1], b[0], b[1]}; }

constexpr unsigned long long step_order(bool ta, int j, int part) {
  const int own0 = ta ? j : (16 + 5 * j + (j & 1)) / 2, own1 = ta ? 4 + j : own0 + 1;
  unsigned long long r[2] = {0, 0};
  int p = 0;
  for (int t = 0; t < 18; ++t) {
    const int s = (t + 8) % 18;
    if (s == own0 || s == own1) continue;
    r[p / 12] |= (unsigned long long)s << (5 * (p % 12));
    ++p;
  }
  return r[part];
}

template <bool typeA>
__device__ __forceinline__ void body(const u16* __restrict__ x, u16* __restrict__ out, const u16* __restrict__ wqkv, const u16* __restrict__ wg,
                                     const u16* __restrict__ wu, const u16* __restrict__ wo, const u16* __restrict__ wd, char* __restrict__ ws) {
  extern __shared__ __attribute__((aligned(16))) char smem[];
  LAS char* lds = (LAS char*)smem;
  ST_DECL LT_DECL ST(0);
  const unsigned long long dg0 = __builtin_amdgcn_s_memrealtime(); unsigned long long dgp, dg1, dg2, dg3, dg4, dg5, dc2; const unsigned long long dc0 = __builtin_amdgcn_s_memtime();
  const int tid = threadIdx.x;
  int lane = tid & 63;
  const int w = __builtin_amdgcn_readfirstlane(tid >> 6), g = w >> 2, wi = w & 3;
  const int b = blockIdx.x, xcd = b & 7, idx = b >> 3;
  const int mb = (xcd & 1) * 4 + (idx & 3), kq = xcd >> 1, ntl = idx >> 2;
  const int j = ntl & 3;
  int fr = lane & 15, fq = lane >> 4;
  const int fA = 896 * kq + 64 * j;
  const int f0 = 896 * kq + 256 + 160 * j, odd = j & 1;
  const int fa = f0 + 32 * odd, fb = fa + 64, fc = odd ? f0 : f0 + 128;

  const int mi = kq * 8 + ntl;
  const bool lnh = !typeA && w == 4 && j < 2, lnw = g == 0 || lnh;
  const char* const xnb = ws + WS_XN + (size_t)mb * 32 * XN_STEP;
  if (lnw) {
    __builtin_amdgcn_s_setprio(2);
    const int lr = (lnh ? (kq * 2 + j) * 2 : (mi & 1) * 8 + wi * 2) + ((lane >> 2) & 1), lf = lnh ? 16 : mi >> 1;
    const int lrow = lnh ? (mb > 0 ? mb * 256 - 16 : 0) + 2 * (lr & 7) + (lr >> 3) : mb * 256 + 32 * (lf >> 1) + 2 * lr + (lf & 1);
    const char* xr_ = (const char*)x + (lrow * 2048 + (lane >> 3) * 64 + (lane & 3) * 16);
    u32x4 raw[4];
#pragma unroll
    for (int i = 0; i < 4; ++i) raw[i] = *(const u32x4*)(xr_ + 512 * i);
    const auto xrs = __builtin_amdgcn_make_buffer_rsrc((void*)xnb, 0, 32 * XN_STEP, 0x00020000);
    const unsigned o = (lane >> 3) * XN_STEP + lf * 1024 + lr * 64 + (((lane & 3) ^ (2 * (lr >> 3))) * 16);
    ET(1);
    f32x2 v[16];
#pragma unroll
    for (int i = 0; i < 16; ++i) v[i] = (f32x2){__uint_as_float(raw[i >> 2][i & 3] << 16), __uint_as_float(raw[i >> 2][i & 3] & 0xffff0000u)};
    f32x2 s2 = (v[0] + v[1]) + (v[2] + v[3]);
#pragma unroll
    for (int i = 4; i < 16; i += 4) s2 += (v[i] + v[i + 1]) + (v[i + 2] + v[i + 3]);
    ET(2);
    const float mean = hsum(s2[0] + s2[1]) * (1.0f / 1024.0f);
    f32x2 qa = {0.f, 0.f}, qb = {0.f, 0.f};
#pragma unroll
    for (int i = 0; i < 16; i += 2) { const f32x2 d = v[i] - mean, e = v[i + 1] - mean; qa += d * d; qb += e * e; }
    qa += qb;
    const float var = hsum(qa[0] + qa[1]) * (1.0f / 1024.0f);
    const float inv = __builtin_amdgcn_rcpf(rbf(__builtin_amdgcn_sqrtf(rbf(rbf(var) + 1e-5f)))), sh = -rbf(mean);
#pragma unroll
    for (int i = 0; i < 4; ++i) {
      u32x4 t;
#pragma unroll
      for (int e = 0; e < 4; ++e) { const f32x2 y = (v[4 * i + e] + sh) * inv; t[e] = pk2(y[0], y[1]); }
      __builtin_amdgcn_raw_buffer_store_b128(t, xrs, o + i * 8 * XN_STEP, 0, 16);
    }
    __builtin_amdgcn_s_setprio(0);
  }
  GAS unsigned* lnf = (GAS unsigned*)(ws + WS_LNF) + mb * 1024 + (lane < 32 ? mi * 32 + lane : (lane - 32) * 32 + mi);
  unsigned entry = 0;
  asm volatile("s_cmp_lg_u32 %2, 7\n\ts_cbranch_scc1 1f\n\tglobal_load_dword %0, %1, off sc1\n1:" : "+v"(entry) : "v"(lnf), "s"(w) : "memory", "scc");
  if (w == 3) {
    char* z = lane < 8 ? ws + WS_CNT + (((mb * 8 + ntl) * 4 + kq) * 8 + lane) * 64
            : (lane < 10 ? ws + WS_HCNT + ((mb * 4 + kq) * 8 + ntl) * 128 + (lane - 8) * 16 : ws + WS_GCNT + (mb * 4 + kq) * 64);
    if (lane < (ntl == 0 ? 11 : 10)) asm volatile("global_store_dwordx4 %0, %1, off sc1\n\ts_nop 1" :: "v"(z), "v"((u32x4){0u, 0u, 0u, 0u}) : "memory");
  }

  const bool isq = typeA && wi < 3;
  const int ld = isq ? 3072 : 3584;
  const u16* q1 = isq ? wqkv + (wi * 1024 + 256 * kq + 64 * j) : ((!typeA && (wi & 2)) ? wu : wg) + (typeA ? fA : ((wi & 1) ? fb : fa));
  const char* c1 = g ? (const char*)q1 : xnb + wi * 4096;
  const int s1 = g ? 8 * ld - 1024 : 0, s2 = g ? 32 * ld - 2048 : 0, s3 = g ? 40 * ld - 3072 : 0;
  const int p1 = g ? 64 * ld : XN_STEP, p2 = g ? XN_STEP : 64 * 3584;
  const int d1 = (g ? G1_SA : 0) + wi * 4096, d2 = g ? 16384 + wi * 256 : G1_SA + 16384 + wi * 1024;
  const unsigned long long m2 = g ? 0xffffull : ~0ull;
  const int rr = lane >> 3, pc = lane & 7, fi = (pc >> 1) ^ ((rr >> 1) & 3), kr = (rr & 3) + 8 * (rr >> 2);
  const unsigned v1 = g ? kr * ld * 2 + fi * 32 + (pc & 1) * 16 : lane * 16;
  const unsigned lb = (unsigned)(size_t)lds;
  const char* c2;
  {
    const unsigned ow = (kr + 16 * (wi >> 1) + 4 * (wi & 1)) * 7168 + (typeA ? fi * 32 : (fi >> 1) * 32) + (pc & 1) * 16;
    const char* be = g ? xnb : (const char*)(typeA ? wu + fA : wg + fc);
    const char* bo = g ? be : (const char*)(wu + (typeA ? fA : fc));
    c2 = ((fi & 1) ? bo : be) + (g ? 16384 + wi * 256 + (lane & 15) * 16 : ow);
  }
  auto chain1 = [&](unsigned dst, const char* p) { dmc_first(dst + d1, v1, p); dmc_next<1024>(v1, p + s1); dmc_next<2048>(v1, p + s2); dmc_next<3072>(v1, p + s3); };
  auto first = [&](bool one) {
    if (one) { chain1(lb, c1); chain1(lb + G1_SLOT, c1 + p1); }
    else { dma1m(lb + d2, c2, m2); dma1m(lb + G1_SLOT + d2, c2 + p2, m2); }
  };
  if (!lnw) first(true);
  else WAIT_V(0);
  asm volatile("s_waitcnt vmcnt(8)" : "+v"(entry) :: "memory");
  ET(3);
  BAR();
  if (w == 7 && lane >= 32) __hip_atomic_store(lnf, entry + 1u, __ATOMIC_RELAXED, __HIP_MEMORY_SCOPE_AGENT);
  if (lnw) first(g == 1);
  dgp = __builtin_amdgcn_s_memrealtime(); WAIT_L0();
  ST(1);

  const int nt = ntl;
  const int kba = 8 + ((fa - 896 * kq) >> 5);
  const int own0 = typeA ? j : (kba >> 1), own1 = typeA ? 4 + j : (kba >> 1) + 1;
  f32x4 acc[4][10];
  f32x4 hacc = {0.f, 0.f, 0.f, 0.f};
#pragma unroll
  for (int i = 0; i < 4; ++i)
#pragma unroll
    for (int n = 0; n < 10; ++n) { acc[i][n] = (f32x4){0.f, 0.f, 0.f, 0.f}; asm volatile("" : "+v"(acc[i][n])); }
  const int rl = 4 * (fq & 1) + (fr >> 2);
  unsigned hoffA = lb + 16384 + fr * 64 + ((fq ^ (2 * (fr >> 3))) * 16);
  unsigned aoff = hoffA - 16384 + wi * 4096;
  unsigned boff0 = lb + G1_SA + (fq >> 1) * 2048 + rl * 128 + 32 * ((2 * g) ^ ((rl >> 1) & 3)) + 8 * (lane & 3);
  unsigned boff1 = boff0 ^ 32;
  unsigned hoffB = (boff0 ^ (32 * (wi & 1))) + (1 + (wi >> 1)) * 4096;
  asm volatile("" : "+v"(aoff), "+v"(hoffA), "+v"(boff0), "+v"(boff1), "+v"(hoffB), "+v"(hacc));
  {
    const int lo = fr + (wi == 0 ? -7 : (wi == 2 ? 1 : 9));
    const int hi = wi == 0 ? fr + 1 : ((wi >= 2 && mb == 0) ? 0 : (wi == 2 ? 8 : 16));
    f32x4 bv;
#pragma unroll
    for (int e = 0; e < 4; ++e) bv[e] = (4 * fq + e >= lo && 4 * fq + e < hi) ? 0.f : -3.0e38f;
    *(LAS f32x4*)(lds + T_BIAS + wi * 1024 + lane * 16) = bv;
    *(LAS unsigned*)(lds + T_SYNC + (lane & 1) * 4) = 0u;
  }
  ET(4);
  if (w == 7) {
    for (unsigned spins = 0; spins < SPIN_CAP; ++spins) {
      unsigned now = entry + 1u;
      if (lane < 32) now = __hip_atomic_load(lnf, __ATOMIC_RELAXED, __HIP_MEMORY_SCOPE_AGENT);
      if (__builtin_amdgcn_ballot_w64(now == entry) == 0ull) break;
      __builtin_amdgcn_s_sleep(1);
    }
    if (lane >= 32) __hip_atomic_store(lnf, entry + 2u, __ATOMIC_RELAXED, __HIP_MEMORY_SCOPE_AGENT);
  }
  WAIT_V_IF0(0, g);
  BAR();
  ET(5);
  first(g == 0);
  c1 += 2 * p1; c2 += 2 * p2;
  unsigned ns = lb + 2 * G1_SLOT;
  auto adv = [&]() { c1 += p1; c2 += p2; ns = (ns == lb + 3 * G1_SLOT) ? lb : ns + G1_SLOT; };
  WAIT_V(0);
  dg1 = __builtin_amdgcn_s_memrealtime(); WAIT_L0();
  ST(2);
  if (g == 1) BAR();

  auto step = [&](auto halo_c, auto last_c, int q) {
    constexpr bool HALO = decltype(halo_c)::value, LAST = decltype(last_c)::value;
    bf16x8 a[2][4], wn[4][2];
    asm volatile("" ::: "memory");
    auto rdq = [&](int e, int h) {
      if (e >= (HALO ? 12 : 10)) return;
      const int kk = h >> 1, t = h & 1;
      bf16x8& d = wn[e & 3][kk];
      if (e == 11) { if (t == 0) d = *(const LAS bf16x8*)(lp(hoffA) + kk * G1_SLOT); return; }
      const int n = e < 2 ? 8 + e : e - 2;
      const bf16x4 v = trd(lp(e == 10 ? hoffB : ((n & 1) ? boff1 : boff0)) + (e == 10 ? 0 : (n >> 1) * 4096) + t * 1024 + kk * G1_SLOT);
      if (t == 0) { d[0] = v[0]; d[1] = v[1]; d[2] = v[2]; d[3] = v[3]; } else { d[4] = v[0]; d[5] = v[1]; d[6] = v[2]; d[7] = v[3]; }
    };
#define RA1(kk, i) a[kk][i] = *(const LAS bf16x8*)(lp(aoff) + (kk) * G1_SLOT + (i) * 1024);
    const int dlt = (q & 1) ? -2 * G1_SLOT : 2 * G1_SLOT;
    if (!LAST) {
#pragma unroll
      for (int kk = 0; kk < 2; ++kk) {
        dmc_first(ns + d1, v1, c1); RA1(kk, 0) rdq(kk, 0);
        dmc_next<1024>(v1, c1 + s1); RA1(kk, 1) rdq(kk, 1);
        dmc_next<2048>(v1, c1 + s2); RA1(kk, 2) rdq(kk, 2);
        dmc_next<3072>(v1, c1 + s3); RA1(kk, 3) rdq(kk, 3);
        dma1m(ns + d2, c2, m2);
        adv();
      }
    } else
    {
#pragma unroll
      for (int kk = 0; kk < 2; ++kk)
#pragma unroll
        for (int i = 0; i < 4; ++i) RA1(kk, i)
#pragma unroll
      for (int h = 0; h < 8; ++h) rdq(h >> 2, h & 3);
    }
    aoff += dlt;
    SCHED();
    if (!LAST) {
    } else {
#if PF_W2 && !(PFD & 1)
      int t = w * 64 + __builtin_amdgcn_mbcnt_hi(~0u, __builtin_amdgcn_mbcnt_lo(~0u, 0u));
      t = t < 288 ? t : t - 288;
      const int r2 = (t >> 4) * 64 + (xcd & 1) * 32 + idx;
      touch(lb + T_SCR, (r2 < 256 ? wo + (256 * kq + r2) * 1024 : wd + (896 * kq + r2 - 256) * 1024) + (t & 15) * 64);
#endif
    }
    WAIT_L0();
    SCHED();
    BAR();
    SCHED();
    PRIO_M();
    auto fin = [&]() { SCHED(); PRIO_T(); if (!LAST) { if (PFD) WAIT_V(1); else WAIT_V(0); } BAR(); SCHED(); };
#pragma unroll
    for (int e = 0; e < 10; ++e) {
      const int n = e < 2 ? 8 + e : e - 2;
#pragma unroll
      for (int i = 0; i < 4; ++i) {
        acc[i][n] = mfma(wn[e & 3][0], a[0][i], acc[i][n]);
        SCHED();
        if (!HALO && e == 9 && i == 3) fin();
        SCHED();
        acc[i][n] = mfma(wn[e & 3][1], a[1][i], acc[i][n]);
        SCHED();
        if (e == 0) { rdq(2 + (i >> 1), 2 * (i & 1)); rdq(2 + (i >> 1), 2 * (i & 1) + 1); } else rdq(e + 3, i);
        if (i == 3 && e < 9) { asm volatile("" :: "v"(wn[(e + 1) & 3][0]), "v"(wn[(e + 1) & 3][1])); }
        SCHED();
      }
    }
    if (HALO) {
      hacc = mfma(wn[2][0], wn[3][0], hacc);
      WAIT_L0();
      fin();
      hacc = mfma(wn[2][1], wn[3][1], hacc);
    }
    hoffA += dlt; boff0 += dlt; boff1 += dlt; hoffB += dlt;
    PRIO_E();
    SCHED();
  };
  const char* const hbase = ws + WS_H + (size_t)(mb * 4 + kq) * 36 * 16384;
  auto brow = [&](int stp_, int w_) {
    return (const char*)(stp_ < 4 ? wo + (size_t)(256 * kq) * 1024 : wd + (size_t)(896 * kq) * 1024 - 256 * 1024) + ((stp_ * 64 + 8 * w_) * 1024 + nt * 128) * 2;
  };
  unsigned va, vb;
  auto stage1 = [&](auto halo_c) {
    constexpr bool TA = decltype(halo_c)::value;
    typedef std::integral_constant<bool, TA || !HALO_SKIP> HC;
#pragma clang loop unroll(disable)
    for (int q = 0; q < 15; ++q) step(HC{}, std::false_type{}, q);
    step(HC{}, std::true_type{}, 15);
    dg2 = __builtin_amdgcn_s_memrealtime(); dc2 = __builtin_amdgcn_s_memtime(); WAIT_L0();
    ST(3);
    asm volatile("" ::: "memory");
    if (g == 0) __builtin_amdgcn_s_setprio(2); else __builtin_amdgcn_s_setprio(EPI_PRIO);
    lane = __builtin_amdgcn_mbcnt_hi(~0u, __builtin_amdgcn_mbcnt_lo(~0u, 0u));
    asm volatile("" : "+v"(lane));
    fr = lane & 15; fq = lane >> 4;
    va = lane * 16;
    vb = (lane >> 3) * 2048 + ((((lane & 7) >> 1) ^ ((lane >> 4) & 3)) * 32) + (lane & 1) * 16;
    if (g == 1) {
#pragma unroll
      for (int u = 0; u < 4; ++u) {
        const int w_ = wi * 2 + (u & 1);
        const char* p0 = brow((u >> 1) ? own1 : own0, w_);
        dma_first(lb + (u >> 1) * G2_SLOT + G2_SA + w_ * 2048, vb, p0); dma_next<1024>(vb, p0 - 896);
      }
    }
    const auto hrs = __builtin_amdgcn_make_buffer_rsrc((void*)hbase, 0, 36 * 16384, 0x00020000);
    const unsigned hv = wi * 4096 + lane * 16;
    auto put = [&](u32x4 v, int s, int kb, int i) {
      __builtin_amdgcn_raw_buffer_store_b128(v, hrs, hv + kb * 16384 + i * 1024, 0, H_AUX);
      *(LAS u32x4*)(lds + s * G2_SLOT + g * 16384 + ((wi * 4096 + kq * 1024 + lane * 16) ^ (i * 1024))) = v;
    };
#define GLU(i, a, b) glu4(acc[i][a], acc[i][b])
    if (!TA) {
      const int kbc = 8 + ((fc - 896 * kq) >> 5);
#pragma unroll
      for (int i = 0; i < 4; ++i) {
        __builtin_amdgcn_raw_buffer_store_b64(GLU(i, 8, 9), hrs, hv + kbc * 16384 + g * 8 + i * 1024, 0, H_AUX);
        put(cat4(GLU(i, 0, 4), GLU(i, 1, 5)), 0, kba + g, i);
        put(cat4(GLU(i, 2, 6), GLU(i, 3, 7)), 1, kba + 2 + g, i);
        if (i == FB) { SCHED(); BAR_IF0(g); SCHED(); }
      }
    } else {
      auto glu_part = [&](int i0, int i1) {
#pragma unroll
        for (int i = i0; i < i1; ++i) put(cat4(GLU(i, 6, 8), GLU(i, 7, 9)), 1, 8 + 2 * j + g, i);
      };
      if (g == 0) { glu_part(0, FA); SCHED(); BAR(); __builtin_amdgcn_s_setprio(0); asm volatile("" ::: "memory"); }
    LAS char* kim = lds + T_KV + g * KV_BYTES;
    LAS char* vim = kim + 7 * 1024;
    bf16x8 kf[4], qf[4];
#pragma unroll
    for (int i = 0; i < 4; ++i) {
      kf[i] = pack8(acc[i][2], acc[i][3]);
      *(LAS bf16x8*)(vim + ((wi * 4 + i) * 16 + fr) * 80 + fq * 16) = pack8(acc[i][4], acc[i][5]);
    }
    if (wi < 3) {
      *(LAS bf16x8*)(kim + wi * 2048 + lane * 16) = kf[2];
      *(LAS bf16x8*)(kim + wi * 2048 + 1024 + lane * 16) = kf[3];
    }
    {
      LAS char* hp = (wi < 2) ? (kim + 6 * 1024 + lane * 16 + wi * 8) : (vim + (256 + fr) * 80 + fq * 16 + (wi - 2) * 8);
      *(LAS u32x2*)hp = (u32x2){pk2(hacc[0], hacc[1]), pk2(hacc[2], hacc[3])};
    }
    WAIT_L0();
    BAR();
    asm volatile("" ::: "memory");
    const bool halo = wi == 0;
    const f32x4 bc = *(const LAS f32x4*)(lds + T_BIAS + lane * 16), bp = *(const LAS f32x4*)(lds + T_BIAS + 1024 + lane * 16);
    const f32x4 bp0 = *(const LAS f32x4*)(lds + T_BIAS + (halo ? 2048 : 1024) + lane * 16);
    const f32x4 bp1 = *(const LAS f32x4*)(lds + T_BIAS + (halo ? 3072 : 1024) + lane * 16);
    const int pf0 = halo ? 16 : wi * 4 - 2, pf1 = halo ? 16 : wi * 4 - 1;
    const bf16x8 kp0 = *(const LAS bf16x8*)(kim + (halo ? 6 : 2 * wi - 2) * 1024 + lane * 16);
    const bf16x8 kp1 = *(const LAS bf16x8*)(kim + (halo ? 6 : 2 * wi - 1) * 1024 + lane * 16);
    const LAS char* vr = vim + (4 * fq + (fr >> 2)) * 80 + (lane & 3) * 16;
    bf16x4 vt[6][2];
#pragma unroll
    for (int d = 0; d < 2; ++d) {
      vt[0][d] = trd(vr + pf0 * 1280 + d * 8); vt[1][d] = trd(vr + pf1 * 1280 + d * 8);
#pragma unroll
      for (int i = 0; i < 4; ++i) vt[2 + i][d] = trd(vr + (wi * 4 + i) * 1280 + d * 8);
    }
    f32x4 sp[4], sc[4];
#pragma unroll
    for (int i = 0; i < 4; ++i) {
      qf[i] = pack8(acc[i][0], acc[i][1]);
      sc[i] = mfma(kf[i], qf[i], bc);
    }
    sp[2] = mfma(kf[0], qf[2], bp); sp[3] = mfma(kf[1], qf[3], bp);
    sp[0] = mfma(kp0, qf[0], bp0); sp[1] = mfma(kp1, qf[1], bp1);
    const float sl = 0.17677669529663687f * 1.4426950408889634f;
    const f32x4 z4 = {0.f, 0.f, 0.f, 0.f};
    float m[4];
#pragma unroll
    for (int i = 0; i < 4; ++i)
      m[i] = fmx(fmx(fmx(fmx(sp[i][0], sp[i][1]), sp[i][2]), fmx(fmx(sp[i][3], sc[i][0]), sc[i][1])), fmx(sc[i][2], sc[i][3]));
    {
      const unsigned t = __float_as_uint(tred<true>(m[0], m[1], m[2], m[3]));
      const auto u = __builtin_amdgcn_permlane32_swap(t, t, false, false);
      const auto u0 = __builtin_amdgcn_permlane16_swap(u[0], u[0], false, false), u1 = __builtin_amdgcn_permlane16_swap(u[1], u[1], false, false);
      m[0] = __uint_as_float(u0[0]); m[1] = __uint_as_float(u0[1]); m[2] = __uint_as_float(u1[0]); m[3] = __uint_as_float(u1[1]);
    }
    const u32x4 one4 = {0x3f803f80u, 0x3f803f80u, 0x3f803f80u, 0x3f803f80u};
#pragma unroll
    for (int i = 0; i < 4; ++i) {
      const float mm = -sl * m[i];
#pragma unroll
      for (int e = 0; e < 4; ++e) {
        sp[i][e] = __builtin_amdgcn_exp2f(__builtin_fmaf(sp[i][e], sl, mm));
        sc[i][e] = __builtin_amdgcn_exp2f(__builtin_fmaf(sc[i][e], sl, mm));
      }
      const bf16x8 pfr = pack8(sp[i], sc[i]);
      const f32x4 sm = mfma(__builtin_bit_cast(bf16x8, one4), pfr, z4);
      const f32x4 o0 = mfma(cat(vt[i][0], vt[2 + i][0]), pfr, z4);
      const f32x4 o1 = mfma(cat(vt[i][1], vt[2 + i][1]), pfr, z4);
      const float inv = __builtin_amdgcn_rcpf(sm[0]);
      put(__builtin_bit_cast(u32x4, pack8(o0 * inv, o1 * inv)), 0, 2 * j + g, i);
    }
      glu_part(FA, 4);
      if (g == 1) glu_part(0, FA);
    }
  };
  stage1(std::integral_constant<bool, typeA>{});
  ST(4);

  const int lag = g ^ 1;
  GAS unsigned* hflag = (GAS unsigned*)(ws + WS_HCNT) + (mb * 4 + kq) * 256;
  const unsigned fo = (nt * 32 + (lane & 7)) * 4;
  GAS unsigned* gcnt = (GAS unsigned*)(ws + WS_GCNT) + (mb * 4 + kq) * 16;
  const int e0 = kq * 1024, e1 = ((1 ^ kq) - 1) * 1024, e2 = ((2 ^ kq) - 2) * 1024, e3 = ((3 ^ kq) - 3) * 1024;
  auto wait_cnt = [&](unsigned seen, GAS unsigned* c, unsigned need) {
    if ((unsigned)__builtin_amdgcn_readfirstlane(seen) < need) {
      asm volatile("" ::: "memory");
      unsigned spins = 0;
      while ((unsigned)__builtin_amdgcn_readfirstlane(__hip_atomic_load(c, __ATOMIC_RELAXED, __HIP_MEMORY_SCOPE_AGENT)) < need)
        if (++spins > SPIN_CAP) break;
    }
    asm volatile("" ::: "memory");
  };
  auto wait_flags = [&](unsigned seen, unsigned long long need) {
    if ((__builtin_amdgcn_ballot_w64(seen != 0u) & need) != need) {
      asm volatile("" ::: "memory");
      unsigned spins = 0;
      while ((__builtin_amdgcn_ballot_w64(__hip_atomic_load(hflag + (fo >> 2), __ATOMIC_RELAXED, __HIP_MEMORY_SCOPE_AGENT) != 0u) & need) != need) {
        __builtin_amdgcn_s_sleep(1);
        if (++spins > SPIN_CAP) break;
      }
    }
    asm volatile("" ::: "memory");
  };
  unsigned long long ord0 = j & 2 ? (j & 1 ? step_order(typeA, 3, 0) : step_order(typeA, 2, 0)) : (j & 1 ? step_order(typeA, 1, 0) : step_order(typeA, 0, 0));
  const unsigned ord1 = (unsigned)(j & 2 ? (j & 1 ? step_order(typeA, 3, 1) : step_order(typeA, 2, 1)) : (j & 1 ? step_order(typeA, 1, 1) : step_order(typeA, 0, 1)));
  const char* const caw = hbase + w * 4096;
  const char* const cbo = brow(0, w); const char* const cbd = brow(4, w) - 4 * 131072;
  auto pop = [&](const char*& ca_, const char*& cb_) {
    const unsigned s = (unsigned)ord0 & 31u;
    ord0 >>= 5;
    ca_ = caw + (s << 15); cb_ = (s < 4u ? cbo : cbd) + (s << 17);
  };
  const int mf = wi * 4 + kq;
  const unsigned orow = ((mb * 256 + 32 * (mf >> 1) + 2 * fr + (mf & 1)) * 1024 + nt * 128 + g * 64 + 16 * (fq & 1) + 8 * (fq >> 1)) * 2;
  u32x4 xr[2] = {{0u, 0u, 0u, 0u}, {0u, 0u, 0u, 0u}};
  const int rl2 = 4 * (fq & 1) + (fr >> 2);
  unsigned ao = lb + wi * 4096 + lane * 16;
  unsigned bo = lb + G2_SA + (fq >> 1) * 2048 + g * 1024 + rl2 * 128 + 32 * ((rl2 >> 1) & 3) + 8 * (lane & 3);
  f32x4 ac2[4][4];
#pragma unroll
  for (int i = 0; i < 4; ++i)
#pragma unroll
    for (int n = 0; n < 4; ++n) { ac2[i][n] = (f32x4){0.f, 0.f, 0.f, 0.f}; asm volatile("" : "+v"(ac2[i][n])); }
  ns = lb + 2 * G2_SLOT;
  const char *ca, *cb;
  const unsigned ordf = (unsigned)ord0;
  pop(ca, cb);
  unsigned sl2 = lb;
  WAIT_V_IF0(0, lag);
  WAIT_L0();
  BAR();
  dg3 = __builtin_amdgcn_s_memrealtime(); WAIT_L0();
  ST(5);
  ST_SAVE(); ST(0); ST(1);
  unsigned seen = 0;
#define PEEK(c, o) asm volatile("global_load_dword %0, %1, %2 sc1" : "+v"(seen) : "v"(o), "s"(c) : "memory")
  if (lag) BAR();

  bf16x8 ka[2][2], kb[2][2][4];
  auto phase2 = [&](auto mode, int q) {
    constexpr int MODE = decltype(mode)::value;
    bf16x8 a[2][4], bfr[2][4];
#define RA(kk, i) a[kk][i] = *(const LAS bf16x8*)(lp(ao) + (kk) * 16384 + (i) * 1024);
#define RB(kk, n) bfr[kk][n] = cat(trd(lp(bo ^ ((n) * 32)) + (kk) * 8192), trd(lp(bo ^ ((n) * 32)) + (kk) * 8192 + 4096));
    int dlt;
    asm volatile("" ::: "memory");
    if (MODE == 1 && q == 1 && w == 4) PEEK(hflag, fo);
    if (MODE == 2 && q == 16)
      asm volatile("global_load_dwordx4 %0, %2, %3\n\tglobal_load_dwordx4 %1, %2, %3 offset:64" : "+v"(xr[0]), "+v"(xr[1]) : "v"(orow), "s"(x) : "memory");
    RA(0, 0) RA(0, 1)
    if (MODE == 0) dmc_first(ns + w * 4096, va, ca + e0);
    RA(0, 2) RA(0, 3) RB(0, 0)
    if (MODE == 0) dmc_next<1024>(va, ca + e1);
    RB(0, 1) RB(0, 2) RB(0, 3)
    if (MODE == 0) dmc_next<2048>(va, ca + e2);
    RA(1, 0) RA(1, 1) RA(1, 2)
    if (MODE == 0) dmc_next<3072>(va, ca + e3);
    RA(1, 3) RB(1, 0)
    if (MODE < 2) dma_first(ns + G2_SA + w * 2048, vb, cb);
    RB(1, 1)
    if (MODE < 2) dma_next<1024>(vb, cb - 896);
    dlt = (sl2 == lb + 2 * G2_SLOT) ? -2 * G2_SLOT : G2_SLOT; ns = sl2; sl2 += dlt;
    if (MODE < 2) pop(ca, cb);
    asm volatile("" : "+s"(ca), "+s"(cb), "+s"(ns), "+s"(sl2), "+s"(dlt), "+s"(ord0) :: "memory");
    RB(1, 2) RB(1, 3)
#undef RA
#undef RB
    ao += dlt; bo += dlt;
    SCHED();
    asm volatile("" : "+v"(bo), "+v"(ao));
    if (MODE == 0) WAIT_V_IF0(6, lag ^ 1); else if (MODE == 2 && q == 16) WAIT_V_IF0(2, lag ^ 1);
    if (MODE == 1 && q == 1) {
      asm volatile("s_waitcnt vmcnt(2)" : "+v"(seen) :: "memory");
      if (!lag) {
        volatile LAS unsigned* go = (volatile LAS unsigned*)(lds + T_SYNC + 4);
        if (w == 4) { wait_flags(seen, 0xf0ull); *go = 1u; }
        else { unsigned spins = 0; while (__builtin_amdgcn_readfirstlane(*go) == 0u) { __builtin_amdgcn_s_sleep(1); if (++spins > (SPIN_CAP << 3)) break; } }
        asm volatile("" ::: "memory");
      }
      const int s = (int)(ordf >> (5 * lag)) & 31;
      const char* p = hbase + s * 32768 + wi * 8192;
      const unsigned d = lb + (lag ? 0 : 2 * G2_SLOT) + wi * 8192;
      dmc_first(d, va, p + e0); dmc_next<1024>(va, p + e1); dmc_next<2048>(va, p + e2); dmc_next<3072>(va, p + e3);
      dmc_first(d + 4096, va, p + 4096 + e0); dmc_next<1024>(va, p + 4096 + e1); dmc_next<2048>(va, p + 4096 + e2); dmc_next<3072>(va, p + 4096 + e3);
    }
    WAIT_L0();
    SCHED();
    BAR();
    SCHED();
    P2_M();
#pragma unroll
    for (int n = 0; n < 4; ++n)
#pragma unroll
      for (int i = (MODE == 2 ? 1 : 0); i < 4; ++i) {
        ac2[i][n] = mfma(bfr[0][n], a[0][i], ac2[i][n]);
        SCHED();
        if (n == 3 && i == 3) {
          P2_T();
          if (MODE == 0) WAIT_V_IF0(6, lag); else if (MODE == 2 && q == 16) WAIT_V_IF0(2, lag);
          if (MODE == 1 && q == 1) WAIT_V_IF0(0, lag);
          if (q < 17 || !lag) BAR();
          SCHED();
        }
        ac2[i][n] = mfma(bfr[1][n], a[1][i], ac2[i][n]);
        SCHED();
        if (MODE == 1 && q == 0 && n == 1 && i == 3 && lag) {
          WAIT_V(0);
          unsigned old = 0;
          if (lane == 0) old = __hip_atomic_fetch_add((LAS unsigned*)(lds + T_SYNC), 1u, __ATOMIC_RELAXED, __HIP_MEMORY_SCOPE_WORKGROUP);
          if (__builtin_amdgcn_readfirstlane(old) == 3u && lane < 8)
            __hip_atomic_store(hflag + lane * 32 + nt, 1u, __ATOMIC_RELAXED, __HIP_MEMORY_SCOPE_AGENT);
          SCHED();
        }
      }
    if (MODE == 2) {
#pragma unroll
      for (int kk = 0; kk < 2; ++kk) {
        ka[q & 1][kk] = a[kk][0];
#pragma unroll
        for (int n = 0; n < 4; ++n) kb[q & 1][kk][n] = bfr[kk][n];
      }
    }
    P2_E();
    SCHED();
  };
  typedef std::integral_constant<int, 0> M0; typedef std::integral_constant<int, 1> M1; typedef std::integral_constant<int, 2> M2;
  phase2(M1{}, 0);
  phase2(M1{}, 1);
  dg4 = __builtin_amdgcn_s_memrealtime(); WAIT_L0();
  ST(2);
#pragma clang loop unroll(disable)
  for (int q = 2; q < 6; ++q) phase2(M0{}, q);
  PEEK(hflag, fo);
  phase2(M0{}, 6);
  asm volatile("" : "+v"(seen));
  ord0 |= (unsigned long long)ord1 << 20;
  wait_flags(seen, 0x0full);
#pragma clang loop unroll(disable)
  for (int q = 7; q < 13; ++q) phase2(M0{}, q);
  if (w == 4 && lane == 0) __hip_atomic_fetch_add(gcnt, 1u, __ATOMIC_RELAXED, __HIP_MEMORY_SCOPE_AGENT);
#pragma clang loop unroll(disable)
  for (int q = 13; q < 16; ++q) phase2(M0{}, q);
  phase2(M2{}, 16);
  PEEK(gcnt, 0u);
  phase2(M2{}, 17);
  asm volatile("s_waitcnt vmcnt(0)" : "+v"(seen) :: "memory");
  wait_cnt(seen, gcnt, 8u);
  dg5 = __builtin_amdgcn_s_memrealtime(); WAIT_L0();
  ST(3);
  const int tile = mb * 8 + nt;
  const auto rs = __builtin_amdgcn_make_buffer_rsrc(ws + WS_H + (size_t)mb * 4 * 36 * 16384, 0, 4 * 36 * 16384, 0x00020000);
  auto xo = [&](int src, int i) { const int u = 15 + nt * 3 + i; return (unsigned)((src * 36 + (u >= 36 ? u - 36 : u)) * 16384 + w * 2048) + lane * 16; };
#pragma unroll
  for (int i = 1; i < 4; ++i) {
    const unsigned o = xo(kq, i);
    __builtin_amdgcn_raw_buffer_store_b128(__builtin_bit_cast(u32x4, pack8(ac2[i][0], ac2[i][1])), rs, o, 0, 16);
    __builtin_amdgcn_raw_buffer_store_b128(__builtin_bit_cast(u32x4, pack8(ac2[i][2], ac2[i][3])), rs, o + 1024, 0, 16);
  }
  GAS unsigned* cnt = (GAS unsigned*)(ws + WS_CNT) + (tile * 32 + w) * 16;
  GAS unsigned* mine = cnt + kq * 128 + (lane & 3);
  seen = __hip_atomic_load(mine, __ATOMIC_RELAXED, __HIP_MEMORY_SCOPE_AGENT);
  SCHED();
#pragma unroll
  for (int n = 0; n < 4; ++n)
#pragma unroll
    for (int u = 0; u < 4; ++u) ac2[0][n] = mfma(kb[u >> 1][u & 1][n], ka[u >> 1][u & 1], ac2[0][n]);
  SCHED();
  WAIT_V(0);
  ST(4);
  asm volatile("" : "+v"(xr[0]), "+v"(xr[1]));
  if (lane >= 1 && lane < 4) __hip_atomic_store(cnt + (kq ^ lane) * 128 + lane, 1u, __ATOMIC_RELAXED, __HIP_MEMORY_SCOPE_AGENT);
  u32x4 p[3][2];
#pragma unroll
  for (int i = 0; i < 3; ++i) p[i][0] = p[i][1] = (u32x4){0u, 0u, 0u, 0u};
  {
    unsigned done = 0;
#pragma clang loop unroll(disable)
    for (unsigned spins = 0; spins < SPIN_CAP; ++spins) {
      const unsigned rdy = (unsigned)__builtin_amdgcn_ballot_w64(seen != 0u) & 0xeu & ~done;
      asm volatile("" ::: "memory");
#pragma unroll
      for (int i = 1; i < 4; ++i)
        if (rdy & (1u << i)) {
          const unsigned o = xo(kq ^ i, i);
          p[i - 1][0] = __builtin_amdgcn_raw_buffer_load_b128(rs, o, 0, 16);
          p[i - 1][1] = __builtin_amdgcn_raw_buffer_load_b128(rs, o + 1024, 0, 16);
        }
      done |= rdy;
      if (done == 0xeu) break;
      if (!rdy) __builtin_amdgcn_s_sleep(1);
      seen = __hip_atomic_load(mine, __ATOMIC_RELAXED, __HIP_MEMORY_SCOPE_AGENT);
    }
  }
  ST(5);
  const auto ors = __builtin_amdgcn_make_buffer_rsrc(out, 0, 4 << 20, 0x00020000);
#pragma unroll
  for (int h = 0; h < 2; ++h) {
    const auto x0 = __builtin_amdgcn_permlane16_swap(xr[h][0], xr[h][2], false, false), x1 = __builtin_amdgcn_permlane16_swap(xr[h][1], xr[h][3], false, false);
    const u32x4 xv = {x0[0], x1[0], x0[1], x1[1]};
    unsigned r[4];
#pragma unroll
    for (int d = 0; d < 4; ++d) {
      float lo = ac2[0][2 * h + (d >> 1)][2 * (d & 1)] + __uint_as_float(xv[d] << 16), hi = ac2[0][2 * h + (d >> 1)][2 * (d & 1) + 1] + __uint_as_float(xv[d] & 0xffff0000u);
#pragma unroll
      for (int i = 0; i < 3; ++i) { lo += __uint_as_float(p[i][h][d] << 16); hi += __uint_as_float(p[i][h][d] & 0xffff0000u); }
      r[d] = pk2(lo, hi);
    }
    const auto y0 = __builtin_amdgcn_permlane16_swap(r[0], r[2], false, false), y1 = __builtin_amdgcn_permlane16_swap(r[1], r[3], false, false);
    __builtin_amdgcn_raw_buffer_store_b128((u32x4){y0[0], y1[0], y0[1], y1[1]}, ors, orow + 64 * h, 0, OUT_AUX);
  }
  ST(6);
  if (w == 0 && lane == 0) {
    GAS unsigned long long* o = (GAS unsigned long long*)(ws + WS_ST) + b * 8;
    const unsigned long long vv[6] = {dg0, dg1, dg2, dg3, dg5, dc2 - dc0};
    for (int k = 0; k < 6; ++k) __hip_atomic_store(o + k, vv[k], __ATOMIC_RELAXED, __HIP_MEMORY_SCOPE_AGENT);
  }
  if (b == 1 && w == 0) {
    WAIT_V(0);
    const unsigned long long dg6 = __builtin_amdgcn_s_memrealtime();
    while (__builtin_amdgcn_s_memrealtime() < dg6 + 800) __builtin_amdgcn_s_sleep(1);
    GAS unsigned long long* s = (GAS unsigned long long*)(ws + WS_ST);
    const int k = (DIAG >> 1);
    unsigned long long s0 = ~0ull, mx = 0, mn = ~0ull;
    for (int i = lane; i < 256; i += 64) {
      const unsigned long long a0 = __hip_atomic_load(s + i * 8, __ATOMIC_RELAXED, __HIP_MEMORY_SCOPE_AGENT), v = __hip_atomic_load(s + i * 8 + (k > 5 ? 0 : k), __ATOMIC_RELAXED, __HIP_MEMORY_SCOPE_AGENT);
      s0 = min(s0, a0); mx = max(mx, v); mn = min(mn, v);
    }
    for (int o = 1; o < 64; o <<= 1) {
      s0 = min(s0, (unsigned long long)__shfl_xor((long long)s0, o)); mx = max(mx, (unsigned long long)__shfl_xor((long long)mx, o)); mn = min(mn, (unsigned long long)__shfl_xor((long long)mn, o));
    }
    unsigned long long q = k > 5 ? 0 : (k == 5 ? ((DIAG & 1) ? mx : mn) / 20 : ((DIAG & 1) ? mx : mn) - s0);
    if (q > 20000) q = 20000;
    while (__builtin_amdgcn_s_memrealtime() < dg6 + 1200 + 4 * q) __builtin_amdgcn_s_sleep(1);
  }
  ST(7);
  ST_OUT(1, 8);
  ST_REST(); ST_OUT(0, 6);
}

}

extern "C" __global__ __launch_bounds__(512, 2) void psum_g1_kernel(const u16* __restrict__ x, u16* __restrict__ out, const u16* __restrict__ wqkv,
                                                                    const u16* __restrict__ wg, const u16* __restrict__ wu,
                                                                    const u16* __restrict__ wo, const u16* __restrict__ wd, char* __restrict__ ws) {
  if (blockIdx.x < 128) body<true>(x, out, wqkv, wg, wu, wo, wd, ws);
  else body<false>(x, out, wqkv, wg, wu, wo, wd, ws);
}

extern "C" void kernel_launch(void* const* d_in, const int* in_sizes, int n_in, void* d_out, int out_size, void* d_ws,
                              size_t ws_size, hipStream_t stream) {
  const u16* x = (const u16*)d_in[0];
  const u16* wqkv = (const u16*)d_in[1];
  const u16* wo = (const u16*)d_in[2];
  const u16* wg = (const u16*)d_in[4];
  const u16* wu = (const u16*)d_in[5];
  const u16* wd = (const u16*)d_in[6];
  char* ws = (char*)d_ws;
  (void)hipFuncSetAttribute((const void*)psum_g1_kernel, hipFuncAttributeMaxDynamicSharedMemorySize, G1_LDS);
  psum_g1_kernel<<<256, 512, G1_LDS, stream>>>(x, (u16*)d_out, wqkv, wg, wu, wo, wd, ws);
}
```

```cpp
#include <hip/hip_runtime.h>
#include <hip/hip_bf16.h>
#include <type_traits>
#define EARLY 1
#define HALO_SKIP 1
#define PF_W2 1
#define PF_LOOP 0
#define PF_R0 192
#define PF_PER 2
#define PF_LN_ROWS 96
#define PRIO_L 0
#define PRIO_M() __builtin_amdgcn_s_setprio(1)
#define PRIO_T() __builtin_amdgcn_s_setprio(3)
#define PRIO_E() __builtin_amdgcn_s_setprio(0)
#define PF_CODE 0
#define PRECLEAN 0
#define OUT_AUX 16
#define G2ORD 0
#define EARLY2 1
#define CHAIN 1
#define PF_X 0
#define XH 1
#define ACQ 0
#define EPI_PRIO 1
#define LORD 1
#define RPOS 1
#define FA 4
#define FB 0
#define PFD 0
#define SGLU 0
#define TLBW 0
#define WDELAY 0
#define HDEFER 1
#define P2L 1
#define P2_M() __builtin_amdgcn_s_setprio(0)
#define P2_T() __builtin_amdgcn_s_setprio(1)
#define P2_E()
#define SPIN_CAP (1u << 17)
#define H_AUX 16

namespace {

typedef unsigned short u16;
typedef short bf16x8 __attribute__((ext_vector_type(8)));
typedef short bf16x4 __attribute__((ext_vector_type(4)));
typedef float f32x4 __attribute__((ext_vector_type(4)));
typedef float f32x2 __attribute__((ext_vector_type(2)));
typedef unsigned u32x4 __attribute__((ext_vector_type(4)));
typedef unsigned u32x2 __attribute__((ext_vector_type(2)));
typedef __bf16 bf2 __attribute__((ext_vector_type(2)));
#define LAS __attribute__((address_space(3)))
#define GAS __attribute__((address_space(1)))

#define WAIT_V(n) asm volatile("s_waitcnt vmcnt(" #n ")" ::: "memory")
#define WAIT_V_IF0(n, c) asm volatile("s_cmp_lg_u32 %0, 0\n\ts_cbranch_scc1 1f\n\ts_waitcnt vmcnt(" #n ")\n1:" :: "s"(c) : "memory", "scc")
#define BAR_IF0(c) asm volatile("s_cmp_lg_u32 %0, 0\n\ts_cbranch_scc1 1f\n\ts_barrier\n\ts_setprio 0\n1:" :: "s"(c) : "memory", "scc")
#define WAIT_L0() do { asm volatile("" ::: "memory"); __builtin_amdgcn_s_waitcnt(0xC07F); asm volatile("" ::: "memory"); } while (0)
#define BAR() do { asm volatile("" ::: "memory"); __builtin_amdgcn_s_barrier(); asm volatile("" ::: "memory"); } while (0)
#define SCHED() __builtin_amdgcn_sched_barrier(0)

__device__ __forceinline__ float b2f(u16 v) { return __uint_as_float(((unsigned)v) << 16); }
__device__ __forceinline__ unsigned pk2(float lo, float hi) {
  bf2 r = {(__bf16)lo, (__bf16)hi};
  return __builtin_bit_cast(unsigned, r);
}
__device__ __forceinline__ float rbf(float f) { return (float)(__bf16)f; }
__device__ __forceinline__ bf16x8 pack8(f32x4 a, f32x4 b) {
  u32x4 r = {pk2(a[0], a[1]), pk2(a[2], a[3]), pk2(b[0], b[1]), pk2(b[2], b[3])};
  return __builtin_bit_cast(bf16x8, r);
}
__device__ __forceinline__ bf16x4 trd(LAS const char* p) {
  return __builtin_amdgcn_ds_read_tr16_b64_v4i16((LAS bf16x4*)p);
}
__device__ __forceinline__ bf16x8 cat(bf16x4 a, bf16x4 b) { return __builtin_shufflevector(a, b, 0, 1, 2, 3, 4, 5, 6, 7); }
__device__ __forceinline__ f32x4 mfma(bf16x8 a, bf16x8 b, f32x4 c) {
  return __builtin_amdgcn_mfma_f32_16x16x32_bf16(a, b, c, 0, 0, 0);
}
__device__ __forceinline__ void dma_first(unsigned lds_dst, unsigned voff, const char* b0) {
  asm volatile("s_mov_b32 m0, %0\n\ts_nop 0\n\tglobal_load_lds_dwordx4 %1, %2" :: "s"(lds_dst), "v"(voff), "s"(b0) : "memory");
}
template <int OFF>
__device__ __forceinline__ void dma_next(unsigned voff, const char* b) {
  asm volatile("global_load_lds_dwordx4 %0, %1 offset:%2" :: "v"(voff), "s"(b), "i"(OFF) : "memory");
}
__device__ __forceinline__ void dmc_first(unsigned lds_dst, unsigned voff, const char* b0) {
  asm volatile("s_mov_b32 m0, %0\n\ts_nop 0\n\tglobal_load_lds_dwordx4 %1, %2 sc1" :: "s"(lds_dst), "v"(voff), "s"(b0) : "memory");
}
template <int OFF>
__device__ __forceinline__ void dmc_next(unsigned voff, const char* b) {
  asm volatile("global_load_lds_dwordx4 %0, %1 offset:%2 sc1" :: "v"(voff), "s"(b), "i"(OFF) : "memory");
}
__device__ __forceinline__ void dma1m(unsigned lds_dst, const char* p, unsigned long long mask) {
  asm volatile("s_mov_b32 m0, %0\n\ts_mov_b64 exec, %2\n\t"
               "global_load_lds_dwordx4 %1, off sc1\n\t"
               "s_mov_b64 exec, -1"
               :: "s"(lds_dst), "v"(p), "s"(mask) : "memory");
}
__device__ __forceinline__ void touch(unsigned lds_dst, const void* p) {
  asm volatile("s_mov_b32 m0, %0\n\ts_nop 0\n\tglobal_load_lds_dword %1, off" :: "s"(lds_dst), "v"(p) : "memory");
}
#define CODE_ADDR(dst, sym) do { unsigned lo_, hi_; asm volatile("s_getpc_b64 s[90:91]\n\ts_add_u32 %0, s90, " #sym "@rel32@lo+4\n\ts_addc_u32 %1, s91, " #sym "@rel32@hi+12" \
    : "=s"(lo_), "=s"(hi_) :: "s90", "s91", "scc"); dst = (const char*)(((unsigned long long)hi_ << 32) | lo_); } while (0)
constexpr int LN_LINES = 16, G1_LINES = 80, G2_LINES = 30;
__device__ __forceinline__ LAS char* lp(unsigned a) { return (LAS char*)(size_t)a; }
__device__ __forceinline__ float fmx(float a, float b) { return __builtin_elementwise_maximum(a, b); }
__device__ __forceinline__ float xmax(float v) {
  auto a = __builtin_amdgcn_permlane16_swap(__float_as_uint(v), __float_as_uint(v), false, false);
  v = fmaxf(__uint_as_float(a[0]), __uint_as_float(a[1]));
  auto b = __builtin_amdgcn_permlane32_swap(__float_as_uint(v), __float_as_uint(v), false, false);
  return fmaxf(__uint_as_float(b[0]), __uint_as_float(b[1]));
}
__device__ __forceinline__ float xsum(float v) {
  auto a = __builtin_amdgcn_permlane16_swap(__float_as_uint(v), __float_as_uint(v), false, false);
  v = __uint_as_float(a[0]) + __uint_as_float(a[1]);
  auto b = __builtin_amdgcn_permlane32_swap(__float_as_uint(v), __float_as_uint(v), false, false);
  return __uint_as_float(b[0]) + __uint_as_float(b[1]);
}

template <int CTRL>
__device__ __forceinline__ float dpp_add(float v) {
  return v + __uint_as_float(__builtin_amdgcn_update_dpp(0u, __float_as_uint(v), CTRL, 0xf, 0xf, true));
}
__device__ __forceinline__ float hsum(float v) {
  v = dpp_add<0xB1>(v);
  v = dpp_add<0x4E>(v);
  v = dpp_add<0x128>(v);
  return xsum(v);
}

#define DG(kid, slot)
#define ST_DECL
#define LT_DECL
#define LT(k)
#define ET(k)
#define ST(k)
#define ST_OUT(kid, n)
#define ST_SAVE()
#define ST_REST()
constexpr int XN_STEP = 17 * 1024;
constexpr size_t WS_XN = 0;
constexpr size_t WS_H = WS_XN + (size_t)8 * 32 * XN_STEP;
constexpr size_t H_BYTES = (size_t)8 * 4 * 36 * 16 * 1024;
constexpr size_t WS_CNT = WS_H + H_BYTES;
constexpr size_t WS_GCNT = WS_CNT + 131072;
constexpr size_t WS_HCNT = WS_GCNT + 8192;
constexpr size_t WS_LNF = WS_HCNT + 32768;
constexpr size_t WS_ST = WS_LNF + 32768;

constexpr int G1_SA = 17 * 1024, G1_SB = 20 * 1024, G1_SLOT = G1_SA + G1_SB;
constexpr int T_SCR = 159488;
constexpr int T_BIAS = T_SCR + 256;
constexpr int G1_LDS = T_BIAS + 4096;
constexpr int G2_SA = 32768, G2_SB = 16384, G2_SLOT = G2_SA + G2_SB;
constexpr int T_SYNC = T_SCR - 64;
constexpr int T_KV = 2 * G2_SLOT, KV_BYTES = 7 * 1024 + 272 * 80;

template <bool MAX>
__device__ __forceinline__ float tred(float a, float b, float c, float d) {
  const auto r1 = __builtin_amdgcn_permlane16_swap(__float_as_uint(a), __float_as_uint(b), false, false);
  const auto r2 = __builtin_amdgcn_permlane16_swap(__float_as_uint(c), __float_as_uint(d), false, false);
  const float x0 = __uint_as_float(r1[0]), x1 = __uint_as_float(r1[1]), y0 = __uint_as_float(r2[0]), y1 = __uint_as_float(r2[1]);
  const float t = MAX ? fmx(x0, x1) : x0 + x1, u = MAX ? fmx(y0, y1) : y0 + y1;
  const auto r3 = __builtin_amdgcn_permlane32_swap(__float_as_uint(t), __float_as_uint(u), false, false);
  const float z0 = __uint_as_float(r3[0]), z1 = __uint_as_float(r3[1]);
  return MAX ? fmx(z0, z1) : z0 + z1;
}
__device__ __forceinline__ u32x2 glu4(f32x4 g, f32x4 u) {
  f32x4 e = g * -1.4426950408889634f;
#pragma unroll
  for (int i = 0; i < 4; ++i) e[i] = __builtin_amdgcn_exp2f(e[i]);
  e = e + 1.0f;
#pragma unroll
  for (int i = 0; i < 4; ++i) e[i] = __builtin_amdgcn_rcpf(e[i]);
  e = e * u;
  return (u32x2){pk2(e[0], e[1]), pk2(e[2], e[3])};
}
__device__ __forceinline__ u32x2 glu4s(f32x4 g, f32x4 u) {
  float e[4];
#pragma unroll
  for (int i = 0; i < 4; ++i) { e[i] = g[i] * -1.4426950408889634f; asm volatile("" : "+v"(e[i])); e[i] = __builtin_amdgcn_exp2f(e[i]); }
#pragma unroll
  for (int i = 0; i < 4; ++i) { e[i] = e[i] + 1.0f; asm volatile("" : "+v"(e[i])); e[i] = __builtin_amdgcn_rcpf(e[i]); }
#pragma unroll
  for (int i = 0; i < 4; ++i) { e[i] = e[i] * u[i]; asm volatile("" : "+v"(e[i])); }
  return (u32x2){pk2(e[0], e[1]), pk2(e[2], e[3])};
}
__device__ __forceinline__ u32x4 cat4(u32x2 a, u32x2 b) { return (u32x4){a[0], a[1], b[0], b[1]}; }

constexpr unsigned long long step_order(bool ta, int j, int part) {
  const int own0 = ta ? j : (16 + 5 * j + (j & 1)) / 2, own1 = ta ? 4 + j : own0 + 1;
  unsigned long long r[2] = {0, 0};
  int p = 0;
  for (int t = 0; t < 18; ++t) {
    const int s = (t + 8) % 18;
    if (s == own0 || s == own1) continue;
    r[p / 12] |= (unsigned long long)s << (5 * (p % 12));
    ++p;
  }
  return r[part];
}

template <bool typeA>
__device__ __forceinline__ void body(const u16* __restrict__ x, u16* __restrict__ out, const u16* __restrict__ wqkv, const u16* __restrict__ wg,
                                     const u16* __restrict__ wu, const u16* __restrict__ wo, const u16* __restrict__ wd, char* __restrict__ ws) {
  extern __shared__ __attribute__((aligned(16))) char smem[];
  LAS char* lds = (LAS char*)smem;
  ST_DECL LT_DECL ST(0);
  const int tid = threadIdx.x;
  int lane = tid & 63;
  const int w = __builtin_amdgcn_readfirstlane(tid >> 6), g = w >> 2, wi = w & 3;
  const int b = blockIdx.x, xcd = b & 7, idx = b >> 3;
  const int mb = (xcd & 1) * 4 + (idx & 3), kq = xcd >> 1, ntl = idx >> 2;
  const int j = ntl & 3;
  int fr = lane & 15, fq = lane >> 4;
  const int fA = 896 * kq + 64 * j;
  const int f0 = 896 * kq + 256 + 160 * j, odd = j & 1;
  const int fa = f0 + 32 * odd, fb = fa + 64, fc = odd ? f0 : f0 + 128;

  const int mi = kq * 8 + ntl;
  const bool lnh = !typeA && w == 4 && j < 2, lnw = g == 0 || lnh;
  const char* const xnb = ws + WS_XN + (size_t)mb * 32 * XN_STEP;
  if (lnw) {
    __builtin_amdgcn_s_setprio(2);
    const int lr = (lnh ? (kq * 2 + j) * 2 : (mi & 1) * 8 + wi * 2) + ((lane >> 2) & 1), lf = lnh ? 16 : mi >> 1;
    const int lrow = lnh ? (mb > 0 ? mb * 256 - 16 : 0) + 2 * (lr & 7) + (lr >> 3) : mb * 256 + 32 * (lf >> 1) + 2 * lr + (lf & 1);
    const char* xr_ = (const char*)x + (lrow * 2048 + (lane >> 3) * 64 + (lane & 3) * 16);
    u32x4 raw[4];
#pragma unroll
    for (int i = 0; i < 4; ++i) raw[i] = *(const u32x4*)(xr_ + 512 * i);
    const auto xrs = __builtin_amdgcn_make_buffer_rsrc((void*)xnb, 0, 32 * XN_STEP, 0x00020000);
    const unsigned o = (lane >> 3) * XN_STEP + lf * 1024 + lr * 64 + (((lane & 3) ^ (2 * (lr >> 3))) * 16);
    ET(1);
    f32x2 v[16];
#pragma unroll
    for (int i = 0; i < 16; ++i) v[i] = (f32x2){__uint_as_float(raw[i >> 2][i & 3] << 16), __uint_as_float(raw[i >> 2][i & 3] & 0xffff0000u)};
    f32x2 s2 = (v[0] + v[1]) + (v[2] + v[3]);
#pragma unroll
    for (int i = 4; i < 16; i += 4) s2 += (v[i] + v[i + 1]) + (v[i + 2] + v[i + 3]);
    ET(2);
    const float mean = hsum(s2[0] + s2[1]) * (1.0f / 1024.0f);
    f32x2 qa = {0.f, 0.f}, qb = {0.f, 0.f};
#pragma unroll
    for (int i = 0; i < 16; i += 2) { const f32x2 d = v[i] - mean, e = v[i + 1] - mean; qa += d * d; qb += e * e; }
    qa += qb;
    const float var = hsum(qa[0] + qa[1]) * (1.0f / 1024.0f);
    const float inv = __builtin_amdgcn_rcpf(rbf(__builtin_amdgcn_sqrtf(rbf(rbf(var) + 1e-5f)))), sh = -rbf(mean);
#pragma unroll
    for (int i = 0; i < 4; ++i) {
      u32x4 t;
#pragma unroll
      for (int e = 0; e < 4; ++e) { const f32x2 y = (v[4 * i + e] + sh) * inv; t[e] = pk2(y[0], y[1]); }
      __builtin_amdgcn_raw_buffer_store_b128(t, xrs, o + i * 8 * XN_STEP, 0, 16);
    }
    __builtin_amdgcn_s_setprio(0);
  }
  GAS unsigned* lnf = (GAS unsigned*)(ws + WS_LNF) + mb * 1024 + (lane < 32 ? mi * 32 + lane : (lane - 32) * 32 + mi);
  unsigned entry = 0;
  asm volatile("s_cmp_lg_u32 %2, 7\n\ts_cbranch_scc1 1f\n\tglobal_load_dword %0, %1, off sc1\n1:" : "+v"(entry) : "v"(lnf), "s"(w) : "memory", "scc");
  if (w == 3) {
    char* z = lane < 8 ? ws + WS_CNT + (((mb * 8 + ntl) * 4 + kq) * 8 + lane) * 64
            : (lane < 10 ? ws + WS_HCNT + ((mb * 4 + kq) * 8 + ntl) * 128 + (lane - 8) * 16 : ws + WS_GCNT + (mb * 4 + kq) * 64);
    if (lane < (ntl == 0 ? 11 : 10)) asm volatile("global_store_dwordx4 %0, %1, off sc1\n\ts_nop 1" :: "v"(z), "v"((u32x4){0u, 0u, 0u, 0u}) : "memory");
  }

  const bool isq = typeA && wi < 3;
  const int ld = isq ? 3072 : 3584;
  const u16* q1 = isq ? wqkv + (wi * 1024 + 256 * kq + 64 * j) : ((!typeA && (wi & 2)) ? wu : wg) + (typeA ? fA : ((wi & 1) ? fb : fa));
  const char* c1 = g ? (const char*)q1 : xnb + wi * 4096;
  const int s1 = g ? 8 * ld - 1024 : 0, s2 = g ? 32 * ld - 2048 : 0, s3 = g ? 40 * ld - 3072 : 0;
  const int p1 = g ? 64 * ld : XN_STEP, p2 = g ? XN_STEP : 64 * 3584;
  const int d1 = (g ? G1_SA : 0) + wi * 4096, d2 = g ? 16384 + wi * 256 : G1_SA + 16384 + wi * 1024;
  const unsigned long long m2 = g ? 0xffffull : ~0ull;
  const int rr = lane >> 3, pc = lane & 7, fi = (pc >> 1) ^ ((rr >> 1) & 3), kr = (rr & 3) + 8 * (rr >> 2);
  const unsigned v1 = g ? kr * ld * 2 + fi * 32 + (pc & 1) * 16 : lane * 16;
  const unsigned lb = (unsigned)(size_t)lds;
  const char* c2;
  {
    const unsigned ow = (kr + 16 * (wi >> 1) + 4 * (wi & 1)) * 7168 + (typeA ? fi * 32 : (fi >> 1) * 32) + (pc & 1) * 16;
    const char* be = g ? xnb : (const char*)(typeA ? wu + fA : wg + fc);
    const char* bo = g ? be : (const char*)(wu + (typeA ? fA : fc));
    c2 = ((fi & 1) ? bo : be) + (g ? 16384 + wi * 256 + (lane & 15) * 16 : ow);
  }
  auto chain1 = [&](unsigned dst, const char* p) { dmc_first(dst + d1, v1, p); dmc_next<1024>(v1, p + s1); dmc_next<2048>(v1, p + s2); dmc_next<3072>(v1, p + s3); };
  auto first = [&](bool one) {
    if (one) { chain1(lb, c1); chain1(lb + G1_SLOT, c1 + p1); }
    else { dma1m(lb + d2, c2, m2); dma1m(lb + G1_SLOT + d2, c2 + p2, m2); }
  };
  if (!lnw) first(true);
  else WAIT_V(0);
  asm volatile("s_waitcnt vmcnt(8)" : "+v"(entry) :: "memory");
  ET(3);
  BAR();
  if (w == 7 && lane >= 32) __hip_atomic_store(lnf, entry + 1u, __ATOMIC_RELAXED, __HIP_MEMORY_SCOPE_AGENT);
  if (lnw) first(g == 1);
  ST(1);

  const int nt = ntl;
  const int kba = 8 + ((fa - 896 * kq) >> 5);
  const int own0 = typeA ? j : (kba >> 1), own1 = typeA ? 4 + j : (kba >> 1) + 1;
  f32x4 acc[4][10];
  f32x4 hacc = {0.f, 0.f, 0.f, 0.f};
#pragma unroll
  for (int i = 0; i < 4; ++i)
#pragma unroll
    for (int n = 0; n < 10; ++n) { acc[i][n] = (f32x4){0.f, 0.f, 0.f, 0.f}; asm volatile("" : "+v"(acc[i][n])); }
  const int rl = 4 * (fq & 1) + (fr >> 2);
  unsigned hoffA = lb + 16384 + fr * 64 + ((fq ^ (2 * (fr >> 3))) * 16);
  unsigned aoff = hoffA - 16384 + wi * 4096;
  unsigned boff0 = lb + G1_SA + (fq >> 1) * 2048 + rl * 128 + 32 * ((2 * g) ^ ((rl >> 1) & 3)) + 8 * (lane & 3);
  unsigned boff1 = boff0 ^ 32;
  unsigned hoffB = (boff0 ^ (32 * (wi & 1))) + (1 + (wi >> 1)) * 4096;
  asm volatile("" : "+v"(aoff), "+v"(hoffA), "+v"(boff0), "+v"(boff1), "+v"(hoffB), "+v"(hacc));
  {
    const int lo = fr + (wi == 0 ? -7 : (wi == 2 ? 1 : 9));
    const int hi = wi == 0 ? fr + 1 : ((wi >= 2 && mb == 0) ? 0 : (wi == 2 ? 8 : 16));
    f32x4 bv;
#pragma unroll
    for (int e = 0; e < 4; ++e) bv[e] = (4 * fq + e >= lo && 4 * fq + e < hi) ? 0.f : -3.0e38f;
    *(LAS f32x4*)(lds + T_BIAS + wi * 1024 + lane * 16) = bv;
    *(LAS unsigned*)(lds + T_SYNC + (lane & 1) * 4) = 0u;
  }
  ET(4);
  if (w == 7) {
    for (unsigned spins = 0; spins < SPIN_CAP; ++spins) {
      unsigned now = entry + 1u;
      if (lane < 32) now = __hip_atomic_load(lnf, __ATOMIC_RELAXED, __HIP_MEMORY_SCOPE_AGENT);
      if (__builtin_amdgcn_ballot_w64(now == entry) == 0ull) break;
      __builtin_amdgcn_s_sleep(1);
    }
    if (lane >= 32) __hip_atomic_store(lnf, entry + 2u, __ATOMIC_RELAXED, __HIP_MEMORY_SCOPE_AGENT);
  }
  WAIT_V_IF0(0, g);
  BAR();
  ET(5);
  first(g == 0);
  c1 += 2 * p1; c2 += 2 * p2;
  unsigned ns = lb + 2 * G1_SLOT;
  auto adv = [&]() { c1 += p1; c2 += p2; ns = (ns == lb + 3 * G1_SLOT) ? lb : ns + G1_SLOT; };
  WAIT_V(0);
  ST(2);
  if (g == 1) BAR();

  auto step = [&](auto halo_c, auto last_c, int q) {
    constexpr bool HALO = decltype(halo_c)::value, LAST = decltype(last_c)::value;
    bf16x8 a[2][4], wn[4][2];
    asm volatile("" ::: "memory");
    auto rdq = [&](int e, int h) {
      if (e >= (HALO ? 12 : 10)) return;
      const int kk = h >> 1, t = h & 1;
      bf16x8& d = wn[e & 3][kk];
      if (e == 11) { if (t == 0) d = *(const LAS bf16x8*)(lp(hoffA) + kk * G1_SLOT); return; }
      const int n = e < 2 ? 8 + e : e - 2;
      const bf16x4 v = trd(lp(e == 10 ? hoffB : ((n & 1) ? boff1 : boff0)) + (e == 10 ? 0 : (n >> 1) * 4096) + t * 1024 + kk * G1_SLOT);
      if (t == 0) { d[0] = v[0]; d[1] = v[1]; d[2] = v[2]; d[3] = v[3]; } else { d[4] = v[0]; d[5] = v[1]; d[6] = v[2]; d[7] = v[3]; }
    };
#define RA1(kk, i) a[kk][i] = *(const LAS bf16x8*)(lp(aoff) + (kk) * G1_SLOT + (i) * 1024);
    const int dlt = (q & 1) ? -2 * G1_SLOT : 2 * G1_SLOT;
    if (!LAST) {
#pragma unroll
      for (int kk = 0; kk < 2; ++kk) {
        dmc_first(ns + d1, v1, c1); RA1(kk, 0) rdq(kk, 0);
        dmc_next<1024>(v1, c1 + s1); RA1(kk, 1) rdq(kk, 1);
        dmc_next<2048>(v1, c1 + s2); RA1(kk, 2) rdq(kk, 2);
        dmc_next<3072>(v1, c1 + s3); RA1(kk, 3) rdq(kk, 3);
        dma1m(ns + d2, c2, m2);
        adv();
      }
    } else
    {
#pragma unroll
      for (int kk = 0; kk < 2; ++kk)
#pragma unroll
        for (int i = 0; i < 4; ++i) RA1(kk, i)
#pragma unroll
      for (int h = 0; h < 8; ++h) rdq(h >> 2, h & 3);
    }
    aoff += dlt;
    SCHED();
    if (!LAST) {
    } else {
#if PF_W2 && !(PFD & 1)
      int t = w * 64 + __builtin_amdgcn_mbcnt_hi(~0u, __builtin_amdgcn_mbcnt_lo(~0u, 0u));
      t = t < 288 ? t : t - 288;
      const int r2 = (t >> 4) * 64 + (xcd & 1) * 32 + idx;
      touch(lb + T_SCR, (r2 < 256 ? wo + (256 * kq + r2) * 1024 : wd + (896 * kq + r2 - 256) * 1024) + (t & 15) * 64);
#endif
    }
    WAIT_L0();
    SCHED();
    BAR();
    SCHED();
    PRIO_M();
    auto fin = [&]() { SCHED(); PRIO_T(); if (!LAST) { if (PFD) WAIT_V(1); else WAIT_V(0); } BAR(); SCHED(); };
#pragma unroll
    for (int e = 0; e < 10; ++e) {
      const int n = e < 2 ? 8 + e : e - 2;
#pragma unroll
      for (int i = 0; i < 4; ++i) {
        acc[i][n] = mfma(wn[e & 3][0], a[0][i], acc[i][n]);
        SCHED();
        if (!HALO && e == 9 && i == 3) fin();
        SCHED();
        acc[i][n] = mfma(wn[e & 3][1], a[1][i], acc[i][n]);
        SCHED();
        if (e == 0) { rdq(2 + (i >> 1), 2 * (i & 1)); rdq(2 + (i >> 1), 2 * (i & 1) + 1); } else rdq(e + 3, i);
        if (i == 3 && e < 9) { asm volatile("" :: "v"(wn[(e + 1) & 3][0]), "v"(wn[(e + 1) & 3][1])); }
        SCHED();
      }
    }
    if (HALO) {
      hacc = mfma(wn[2][0], wn[3][0], hacc);
      WAIT_L0();
      fin();
      hacc = mfma(wn[2][1], wn[3][1], hacc);
    }
    hoffA += dlt; boff0 += dlt; boff1 += dlt; hoffB += dlt;
    PRIO_E();
    SCHED();
  };
  const char* const hbase = ws + WS_H + (size_t)(mb * 4 + kq) * 36 * 16384;
  auto brow = [&](int stp_, int w_) {
    return (const char*)(stp_ < 4 ? wo + (size_t)(256 * kq) * 1024 : wd + (size_t)(896 * kq) * 1024 - 256 * 1024) + ((stp_ * 64 + 8 * w_) * 1024 + nt * 128) * 2;
  };
  unsigned va, vb;
  auto stage1 = [&](auto halo_c) {
    constexpr bool TA = decltype(halo_c)::value;
    typedef std::integral_constant<bool, TA || !HALO_SKIP> HC;
#pragma clang loop unroll(disable)
    for (int q = 0; q < 15; ++q) step(HC{}, std::false_type{}, q);
    step(HC{}, std::true_type{}, 15);
    ST(3);
    asm volatile("" ::: "memory");
    if (g == 0) __builtin_amdgcn_s_setprio(2); else __builtin_amdgcn_s_setprio(EPI_PRIO);
    lane = __builtin_amdgcn_mbcnt_hi(~0u, __builtin_amdgcn_mbcnt_lo(~0u, 0u));
    asm volatile("" : "+v"(lane));
    fr = lane & 15; fq = lane >> 4;
    va = lane * 16;
    vb = (lane >> 3) * 2048 + ((((lane & 7) >> 1) ^ ((lane >> 4) & 3)) * 32) + (lane & 1) * 16;
    if (g == 1) {
#pragma unroll
      for (int u = 0; u < 4; ++u) {
        const int w_ = wi * 2 + (u & 1);
        const char* p0 = brow((u >> 1) ? own1 : own0, w_);
        dma_first(lb + (u >> 1) * G2_SLOT + G2_SA + w_ * 2048, vb, p0); dma_next<1024>(vb, p0 - 896);
      }
    }
    const auto hrs = __builtin_amdgcn_make_buffer_rsrc((void*)hbase, 0, 36 * 16384, 0x00020000);
    const unsigned hv = wi * 4096 + lane * 16;
    auto put = [&](u32x4 v, int s, int kb, int i) {
      __builtin_amdgcn_raw_buffer_store_b128(v, hrs, hv + kb * 16384 + i * 1024, 0, H_AUX);
      *(LAS u32x4*)(lds + s * G2_SLOT + g * 16384 + ((wi * 4096 + kq * 1024 + lane * 16) ^ (i * 1024))) = v;
    };
#define GLU(i, a, b) glu4(acc[i][a], acc[i][b])
    if (!TA) {
      const int kbc = 8 + ((fc - 896 * kq) >> 5);
#pragma unroll
      for (int i = 0; i < 4; ++i) {
        __builtin_amdgcn_raw_buffer_store_b64(GLU(i, 8, 9), hrs, hv + kbc * 16384 + g * 8 + i * 1024, 0, H_AUX);
        put(cat4(GLU(i, 0, 4), GLU(i, 1, 5)), 0, kba + g, i);
        put(cat4(GLU(i, 2, 6), GLU(i, 3, 7)), 1, kba + 2 + g, i);
        if (i == FB) { SCHED(); BAR_IF0(g); SCHED(); }
      }
    } else {
      auto glu_part = [&](int i0, int i1) {
#pragma unroll
        for (int i = i0; i < i1; ++i) put(cat4(GLU(i, 6, 8), GLU(i, 7, 9)), 1, 8 + 2 * j + g, i);
      };
      if (g == 0) { glu_part(0, FA); SCHED(); BAR(); __builtin_amdgcn_s_setprio(0); asm volatile("" ::: "memory"); }
    LAS char* kim = lds + T_KV + g * KV_BYTES;
    LAS char* vim = kim + 7 * 1024;
    bf16x8 kf[4], qf[4];
#pragma unroll
    for (int i = 0; i < 4; ++i) {
      kf[i] = pack8(acc[i][2], acc[i][3]);
      *(LAS bf16x8*)(vim + ((wi * 4 + i) * 16 + fr) * 80 + fq * 16) = pack8(acc[i][4], acc[i][5]);
    }
    if (wi < 3) {
      *(LAS bf16x8*)(kim + wi * 2048 + lane * 16) = kf[2];
      *(LAS bf16x8*)(kim + wi * 2048 + 1024 + lane * 16) = kf[3];
    }
    {
      LAS char* hp = (wi < 2) ? (kim + 6 * 1024 + lane * 16 + wi * 8) : (vim + (256 + fr) * 80 + fq * 16 + (wi - 2) * 8);
      *(LAS u32x2*)hp = (u32x2){pk2(hacc[0], hacc[1]), pk2(hacc[2], hacc[3])};
    }
    WAIT_L0();
    BAR();
    asm volatile("" ::: "memory");
    const bool halo = wi == 0;
    const f32x4 bc = *(const LAS f32x4*)(lds + T_BIAS + lane * 16), bp = *(const LAS f32x4*)(lds + T_BIAS + 1024 + lane * 16);
    const f32x4 bp0 = *(const LAS f32x4*)(lds + T_BIAS + (halo ? 2048 : 1024) + lane * 16);
    const f32x4 bp1 = *(const LAS f32x4*)(lds + T_BIAS + (halo ? 3072 : 1024) + lane * 16);
    const int pf0 = halo ? 16 : wi * 4 - 2, pf1 = halo ? 16 : wi * 4 - 1;
    const bf16x8 kp0 = *(const LAS bf16x8*)(kim + (halo ? 6 : 2 * wi - 2) * 1024 + lane * 16);
    const bf16x8 kp1 = *(const LAS bf16x8*)(kim + (halo ? 6 : 2 * wi - 1) * 1024 + lane * 16);
    const LAS char* vr = vim + (4 * fq + (fr >> 2)) * 80 + (lane & 3) * 16;
    bf16x4 vt[6][2];
#pragma unroll
    for (int d = 0; d < 2; ++d) {
      vt[0][d] = trd(vr + pf0 * 1280 + d * 8); vt[1][d] = trd(vr + pf1 * 1280 + d * 8);
#pragma unroll
      for (int i = 0; i < 4; ++i) vt[2 + i][d] = trd(vr + (wi * 4 + i) * 1280 + d * 8);
    }
    f32x4 sp[4], sc[4];
#pragma unroll
    for (int i = 0; i < 4; ++i) {
      qf[i] = pack8(acc[i][0], acc[i][1]);
      sc[i] = mfma(kf[i], qf[i], bc);
    }
    sp[2] = mfma(kf[0], qf[2], bp); sp[3] = mfma(kf[1], qf[3], bp);
    sp[0] = mfma(kp0, qf[0], bp0); sp[1] = mfma(kp1, qf[1], bp1);
    const float sl = 0.17677669529663687f * 1.4426950408889634f;
    const f32x4 z4 = {0.f, 0.f, 0.f, 0.f};
    float m[4];
#pragma unroll
    for (int i = 0; i < 4; ++i)
      m[i] = fmx(fmx(fmx(fmx(sp[i][0], sp[i][1]), sp[i][2]), fmx(fmx(sp[i][3], sc[i][0]), sc[i][1])), fmx(sc[i][2], sc[i][3]));
    {
      const unsigned t = __float_as_uint(tred<true>(m[0], m[1], m[2], m[3]));
      const auto u = __builtin_amdgcn_permlane32_swap(t, t, false, false);
      const auto u0 = __builtin_amdgcn_permlane16_swap(u[0], u[0], false, false), u1 = __builtin_amdgcn_permlane16_swap(u[1], u[1], false, false);
      m[0] = __uint_as_float(u0[0]); m[1] = __uint_as_float(u0[1]); m[2] = __uint_as_float(u1[0]); m[3] = __uint_as_float(u1[1]);
    }
    const u32x4 one4 = {0x3f803f80u, 0x3f803f80u, 0x3f803f80u, 0x3f803f80u};
#pragma unroll
    for (int i = 0; i < 4; ++i) {
      const float mm = -sl * m[i];
#pragma unroll
      for (int e = 0; e < 4; ++e) {
        sp[i][e] = __builtin_amdgcn_exp2f(__builtin_fmaf(sp[i][e], sl, mm));
        sc[i][e] = __builtin_amdgcn_exp2f(__builtin_fmaf(sc[i][e], sl, mm));
      }
      const bf16x8 pfr = pack8(sp[i], sc[i]);
      const f32x4 sm = mfma(__builtin_bit_cast(bf16x8, one4), pfr, z4);
      const f32x4 o0 = mfma(cat(vt[i][0], vt[2 + i][0]), pfr, z4);
      const f32x4 o1 = mfma(cat(vt[i][1], vt[2 + i][1]), pfr, z4);
      const float inv = __builtin_amdgcn_rcpf(sm[0]);
      put(__builtin_bit_cast(u32x4, pack8(o0 * inv, o1 * inv)), 0, 2 * j + g, i);
    }
      glu_part(FA, 4);
      if (g == 1) glu_part(0, FA);
    }
  };
  stage1(std::integral_constant<bool, typeA>{});
  ST(4);

  const int lag = g ^ 1;
  GAS unsigned* hflag = (GAS unsigned*)(ws + WS_HCNT) + (mb * 4 + kq) * 256;
  const unsigned fo = (nt * 32 + (lane & 7)) * 4;
  GAS unsigned* gcnt = (GAS unsigned*)(ws + WS_GCNT) + (mb * 4 + kq) * 16;
  const int e0 = kq * 1024, e1 = ((1 ^ kq) - 1) * 1024, e2 = ((2 ^ kq) - 2) * 1024, e3 = ((3 ^ kq) - 3) * 1024;
  auto wait_cnt = [&](unsigned seen, GAS unsigned* c, unsigned need) {
    if ((unsigned)__builtin_amdgcn_readfirstlane(seen) < need) {
      asm volatile("" ::: "memory");
      unsigned spins = 0;
      while ((unsigned)__builtin_amdgcn_readfirstlane(__hip_atomic_load(c, __ATOMIC_RELAXED, __HIP_MEMORY_SCOPE_AGENT)) < need)
        if (++spins > SPIN_CAP) break;
    }
    asm volatile("" ::: "memory");
  };
  auto wait_flags = [&](unsigned seen, unsigned long long need) {
    if ((__builtin_amdgcn_ballot_w64(seen != 0u) & need) != need) {
      asm volatile("" ::: "memory");
      unsigned spins = 0;
      while ((__builtin_amdgcn_ballot_w64(__hip_atomic_load(hflag + (fo >> 2), __ATOMIC_RELAXED, __HIP_MEMORY_SCOPE_AGENT) != 0u) & need) != need) {
        __builtin_amdgcn_s_sleep(1);
        if (++spins > SPIN_CAP) break;
      }
    }
    asm volatile("" ::: "memory");
  };
  unsigned long long ord0 = j & 2 ? (j & 1 ? step_order(typeA, 3, 0) : step_order(typeA, 2, 0)) : (j & 1 ? step_order(typeA, 1, 0) : step_order(typeA, 0, 0));
  const unsigned ord1 = (unsigned)(j & 2 ? (j & 1 ? step_order(typeA, 3, 1) : step_order(typeA, 2, 1)) : (j & 1 ? step_order(typeA, 1, 1) : step_order(typeA, 0, 1)));
  const char* const caw = hbase + w * 4096;
  const char* const cbo = brow(0, w); const char* const cbd = brow(4, w) - 4 * 131072;
  auto pop = [&](const char*& ca_, const char*& cb_) {
    const unsigned s = (unsigned)ord0 & 31u;
    ord0 >>= 5;
    ca_ = caw + (s << 15); cb_ = (s < 4u ? cbo : cbd) + (s << 17);
  };
  const int mf = wi * 4 + kq;
  const unsigned orow = ((mb * 256 + 32 * (mf >> 1) + 2 * fr + (mf & 1)) * 1024 + nt * 128 + g * 64 + 16 * (fq & 1) + 8 * (fq >> 1)) * 2;
  u32x4 xr[2] = {{0u, 0u, 0u, 0u}, {0u, 0u, 0u, 0u}};
  const int rl2 = 4 * (fq & 1) + (fr >> 2);
  unsigned ao = lb + wi * 4096 + lane * 16;
  unsigned bo = lb + G2_SA + (fq >> 1) * 2048 + g * 1024 + rl2 * 128 + 32 * ((rl2 >> 1) & 3) + 8 * (lane & 3);
  f32x4 ac2[4][4];
#pragma unroll
  for (int i = 0; i < 4; ++i)
#pragma unroll
    for (int n = 0; n < 4; ++n) { ac2[i][n] = (f32x4){0.f, 0.f, 0.f, 0.f}; asm volatile("" : "+v"(ac2[i][n])); }
  ns = lb + 2 * G2_SLOT;
  const char *ca, *cb;
  const unsigned ordf = (unsigned)ord0;
  pop(ca, cb);
  unsigned sl2 = lb;
  WAIT_V_IF0(0, lag);
  WAIT_L0();
  BAR();
  ST(5);
  ST_SAVE(); ST(0); ST(1);
  unsigned seen = 0;
#define PEEK(c, o) asm volatile("global_load_dword %0, %1, %2 sc1" : "+v"(seen) : "v"(o), "s"(c) : "memory")
  if (lag) BAR();

  bf16x8 ka[2][2], kb[2][2][4];
  auto phase2 = [&](auto mode, int q) {
    constexpr int MODE = decltype(mode)::value;
    bf16x8 a[2][4], bfr[2][4];
#define RA(kk, i) a[kk][i] = *(const LAS bf16x8*)(lp(ao) + (kk) * 16384 + (i) * 1024);
#define RB(kk, n) bfr[kk][n] = cat(trd(lp(bo ^ ((n) * 32)) + (kk) * 8192), trd(lp(bo ^ ((n) * 32)) + (kk) * 8192 + 4096));
    int dlt;
    asm volatile("" ::: "memory");
    if (MODE == 1 && q == 1 && w == 4) PEEK(hflag, fo);
    if (MODE == 2 && q == 16)
      asm volatile("global_load_dwordx4 %0, %2, %3\n\tglobal_load_dwordx4 %1, %2, %3 offset:64" : "+v"(xr[0]), "+v"(xr[1]) : "v"(orow), "s"(x) : "memory");
    RA(0, 0) RA(0, 1)
    if (MODE == 0) dmc_first(ns + w * 4096, va, ca + e0);
    RA(0, 2) RA(0, 3) RB(0, 0)
    if (MODE == 0) dmc_next<1024>(va, ca + e1);
    RB(0, 1) RB(0, 2) RB(0, 3)
    if (MODE == 0) dmc_next<2048>(va, ca + e2);
    RA(1, 0) RA(1, 1) RA(1, 2)
    if (MODE == 0) dmc_next<3072>(va, ca + e3);
    RA(1, 3) RB(1, 0)
    if (MODE < 2) dma_first(ns + G2_SA + w * 2048, vb, cb);
    RB(1, 1)
    if (MODE < 2) dma_next<1024>(vb, cb - 896);
    dlt = (sl2 == lb + 2 * G2_SLOT) ? -2 * G2_SLOT : G2_SLOT; ns = sl2; sl2 += dlt;
    if (MODE < 2) pop(ca, cb);
    asm volatile("" : "+s"(ca), "+s"(cb), "+s"(ns), "+s"(sl2), "+s"(dlt), "+s"(ord0) :: "memory");
    RB(1, 2) RB(1, 3)
#undef RA
#undef RB
    ao += dlt; bo += dlt;
    SCHED();
    asm volatile("" : "+v"(bo), "+v"(ao));
    if (MODE == 0) WAIT_V_IF0(6, lag ^ 1); else if (MODE == 2 && q == 16) WAIT_V_IF0(2, lag ^ 1);
    if (MODE == 1 && q == 1) {
      asm volatile("s_waitcnt vmcnt(2)" : "+v"(seen) :: "memory");
      if (!lag) {
        volatile LAS unsigned* go = (volatile LAS unsigned*)(lds + T_SYNC + 4);
        if (w == 4) { wait_flags(seen, 0xf0ull); *go = 1u; }
        else { unsigned spins = 0; while (__builtin_amdgcn_readfirstlane(*go) == 0u) { __builtin_amdgcn_s_sleep(1); if (++spins > (SPIN_CAP << 3)) break; } }
        asm volatile("" ::: "memory");
      }
      const int s = (int)(ordf >> (5 * lag)) & 31;
      const char* p = hbase + s * 32768 + wi * 8192;
      const unsigned d = lb + (lag ? 0 : 2 * G2_SLOT) + wi * 8192;
      dmc_first(d, va, p + e0); dmc_next<1024>(va, p + e1); dmc_next<2048>(va, p + e2); dmc_next<3072>(va, p + e3);
      dmc_first(d + 4096, va, p + 4096 + e0); dmc_next<1024>(va, p + 4096 + e1); dmc_next<2048>(va, p + 4096 + e2); dmc_next<3072>(va, p + 4096 + e3);
    }
    WAIT_L0();
    SCHED();
    BAR();
    SCHED();
    P2_M();
#pragma unroll
    for (int n = 0; n < 4; ++n)
#pragma unroll
      for (int i = (MODE == 2 ? 1 : 0); i < 4; ++i) {
        ac2[i][n] = mfma(bfr[0][n], a[0][i], ac2[i][n]);
        SCHED();
        if (n == 3 && i == 3) {
          P2_T();
          if (MODE == 0) WAIT_V_IF0(6, lag); else if (MODE == 2 && q == 16) WAIT_V_IF0(2, lag);
          if (MODE == 1 && q == 1) WAIT_V_IF0(0, lag);
          if (q < 17 || !lag) BAR();
          SCHED();
        }
        ac2[i][n] = mfma(bfr[1][n], a[1][i], ac2[i][n]);
        SCHED();
        if (MODE == 1 && q == 0 && n == 1 && i == 3 && lag) {
          WAIT_V(0);
          unsigned old = 0;
          if (lane == 0) old = __hip_atomic_fetch_add((LAS unsigned*)(lds + T_SYNC), 1u, __ATOMIC_RELAXED, __HIP_MEMORY_SCOPE_WORKGROUP);
          if (__builtin_amdgcn_readfirstlane(old) == 3u && lane < 8)
            __hip_atomic_store(hflag + lane * 32 + nt, 1u, __ATOMIC_RELAXED, __HIP_MEMORY_SCOPE_AGENT);
          SCHED();
        }
      }
    if (MODE == 2) {
#pragma unroll
      for (int kk = 0; kk < 2; ++kk) {
        ka[q & 1][kk] = a[kk][0];
#pragma unroll
        for (int n = 0; n < 4; ++n) kb[q & 1][kk][n] = bfr[kk][n];
      }
    }
    P2_E();
    SCHED();
  };
  typedef std::integral_constant<int, 0> M0; typedef std::integral_constant<int, 1> M1; typedef std::integral_constant<int, 2> M2;
  phase2(M1{}, 0);
  phase2(M1{}, 1);
  ST(2);
#pragma clang loop unroll(disable)
  for (int q = 2; q < 6; ++q) phase2(M0{}, q);
  PEEK(hflag, fo);
  phase2(M0{}, 6);
  asm volatile("" : "+v"(seen));
  ord0 |= (unsigned long long)ord1 << 20;
  wait_flags(seen, 0x0full);
#pragma clang loop unroll(disable)
  for (int q = 7; q < 13; ++q) phase2(M0{}, q);
  if (w == 4 && lane == 0) __hip_atomic_fetch_add(gcnt, 1u, __ATOMIC_RELAXED, __HIP_MEMORY_SCOPE_AGENT);
#pragma clang loop unroll(disable)
  for (int q = 13; q < 16; ++q) phase2(M0{}, q);
  phase2(M2{}, 16);
  PEEK(gcnt, 0u);
  phase2(M2{}, 17);
  asm volatile("s_waitcnt vmcnt(0)" : "+v"(seen) :: "memory");
  wait_cnt(seen, gcnt, 8u);
  ST(3);
  const int tile = mb * 8 + nt;
  const auto rs = __builtin_amdgcn_make_buffer_rsrc(ws + WS_H + (size_t)mb * 4 * 36 * 16384, 0, 4 * 36 * 16384, 0x00020000);
  auto xo = [&](int src, int i) { const int u = 15 + nt * 3 + i; return (unsigned)((src * 36 + (u >= 36 ? u - 36 : u)) * 16384 + w * 2048) + lane * 16; };
#pragma unroll
  for (int i = 1; i < 4; ++i) {
    const unsigned o = xo(kq, i);
    __builtin_amdgcn_raw_buffer_store_b128(__builtin_bit_cast(u32x4, pack8(ac2[i][0], ac2[i][1])), rs, o, 0, 16);
    __builtin_amdgcn_raw_buffer_store_b128(__builtin_bit_cast(u32x4, pack8(ac2[i][2], ac2[i][3])), rs, o + 1024, 0, 16);
  }
  GAS unsigned* cnt = (GAS unsigned*)(ws + WS_CNT) + (tile * 32 + w) * 16;
  GAS unsigned* mine = cnt + kq * 128 + (lane & 3);
  seen = __hip_atomic_load(mine, __ATOMIC_RELAXED, __HIP_MEMORY_SCOPE_AGENT);
  SCHED();
#pragma unroll
  for (int n = 0; n < 4; ++n)
#pragma unroll
    for (int u = 0; u < 4; ++u) ac2[0][n] = mfma(kb[u >> 1][u & 1][n], ka[u >> 1][u & 1], ac2[0][n]);
  SCHED();
  WAIT_V(0);
  ST(4);
  asm volatile("" : "+v"(xr[0]), "+v"(xr[1]));
  if (lane >= 1 && lane < 4) __hip_atomic_store(cnt + (kq ^ lane) * 128 + lane, 1u, __ATOMIC_RELAXED, __HIP_MEMORY_SCOPE_AGENT);
  u32x4 p[3][2];
#pragma unroll
  for (int i = 0; i < 3; ++i) p[i][0] = p[i][1] = (u32x4){0u, 0u, 0u, 0u};
  {
    unsigned done = 0;
#pragma clang loop unroll(disable)
    for (unsigned spins = 0; spins < SPIN_CAP; ++spins) {
      const unsigned rdy = (unsigned)__builtin_amdgcn_ballot_w64(seen != 0u) & 0xeu & ~done;
      asm volatile("" ::: "memory");
#pragma unroll
      for (int i = 1; i < 4; ++i)
        if (rdy & (1u << i)) {
          const unsigned o = xo(kq ^ i, i);
          p[i - 1][0] = __builtin_amdgcn_raw_buffer_load_b128(rs, o, 0, 16);
          p[i - 1][1] = __builtin_amdgcn_raw_buffer_load_b128(rs, o + 1024, 0, 16);
        }
      done |= rdy;
      if (done == 0xeu) break;
      if (!rdy) __builtin_amdgcn_s_sleep(1);
      seen = __hip_atomic_load(mine, __ATOMIC_RELAXED, __HIP_MEMORY_SCOPE_AGENT);
    }
  }
  ST(5);
  const auto ors = __builtin_amdgcn_make_buffer_rsrc(out, 0, 4 << 20, 0x00020000);
#pragma unroll
  for (int h = 0; h < 2; ++h) {
    const auto x0 = __builtin_amdgcn_permlane16_swap(xr[h][0], xr[h][2], false, false), x1 = __builtin_amdgcn_permlane16_swap(xr[h][1], xr[h][3], false, false);
    const u32x4 xv = {x0[0], x1[0], x0[1], x1[1]};
    unsigned r[4];
#pragma unroll
    for (int d = 0; d < 4; ++d) {
      float lo = ac2[0][2 * h + (d >> 1)][2 * (d & 1)] + __uint_as_float(xv[d] << 16), hi = ac2[0][2 * h + (d >> 1)][2 * (d & 1) + 1] + __uint_as_float(xv[d] & 0xffff0000u);
#pragma unroll
      for (int i = 0; i < 3; ++i) { lo += __uint_as_float(p[i][h][d] << 16); hi += __uint_as_float(p[i][h][d] & 0xffff0000u); }
      r[d] = pk2(lo, hi);
    }
    const auto y0 = __builtin_amdgcn_permlane16_swap(r[0], r[2], false, false), y1 = __builtin_amdgcn_permlane16_swap(r[1], r[3], false, false);
    __builtin_amdgcn_raw_buffer_store_b128((u32x4){y0[0], y1[0], y0[1], y1[1]}, ors, orow + 64 * h, 0, OUT_AUX);
  }
  ST(6);
  ST(7);
  ST_OUT(1, 8);
  ST_REST(); ST_OUT(0, 6);
}

}

extern "C" __global__ __launch_bounds__(512, 2) void psum_g1_kernel(const u16* __restrict__ x, u16* __restrict__ out, const u16* __restrict__ wqkv,
                                                                    const u16* __restrict__ wg, const u16* __restrict__ wu,
                                                                    const u16* __restrict__ wo, const u16* __restrict__ wd, char* __restrict__ ws) {
  if (blockIdx.x < 128) body<true>(x, out, wqkv, wg, wu, wo, wd, ws);
  else body<false>(x, out, wqkv, wg, wu, wo, wd, ws);
}

extern "C" void kernel_launch(void* const* d_in, const int* in_sizes, int n_in, void* d_out, int out_size, void* d_ws,
                              size_t ws_size, hipStream_t stream) {
  const u16* x = (const u16*)d_in[0];
  const u16* wqkv = (const u16*)d_in[1];
  const u16* wo = (const u16*)d_in[2];
  const u16* wg = (const u16*)d_in[4];
  const u16* wu = (const u16*)d_in[5];
  const u16* wd = (const u16*)d_in[6];
  char* ws = (char*)d_ws;
  (void)hipFuncSetAttribute((const void*)psum_g1_kernel, hipFuncAttributeMaxDynamicSharedMemorySize, G1_LDS);
  psum_g1_kernel<<<256, 512, G1_LDS, stream>>>(x, (u16*)d_out, wqkv, wg, wu, wo, wd, ws);
}
```

```cpp
#include <hip/hip_runtime.h>
#include <hip/hip_bf16.h>
#include <type_traits>
#define EARLY 1
#define HALO_SKIP 1
#define PF_W2 1
#define PF_LOOP 0
#define PF_R0 192
#define PF_PER 2
#define PF_LN_ROWS 96
#define PRIO_L 0
#define PRIO_M() __builtin_amdgcn_s_setprio(1)
#define PRIO_T() __builtin_amdgcn_s_setprio(3)
#define PRIO_E() __builtin_amdgcn_s_setprio(0)
#define PF_CODE 0
#define PRECLEAN 0
#define OUT_AUX 16
#define G2ORD 0
#define EARLY2 1
#define CHAIN 1
#define PF_X 0
#define XH 1
#define ACQ 0
#define EPI_PRIO 1
#define LORD 1
#define RPOS 1
#define FA 4
#define FB 0
#define PFD 0
#define SGLU 0
#define TLBW 0
#define WDELAY 0
#define HDEFER 1
#define P2L 1
#define P2_M() __builtin_amdgcn_s_setprio(0)
#define P2_T() __builtin_amdgcn_s_setprio(1)
#define P2_E()
#define SPIN_CAP (1u << 17)
#define H_AUX 16

namespace {

typedef unsigned short u16;
typedef short bf16x8 __attribute__((ext_vector_type(8)));
typedef short bf16x4 __attribute__((ext_vector_type(4)));
typedef float f32x4 __attribute__((ext_vector_type(4)));
typedef float f32x2 __attribute__((ext_vector_type(2)));
typedef unsigned u32x4 __attribute__((ext_vector_type(4)));
typedef unsigned u32x2 __attribute__((ext_vector_type(2)));
typedef __bf16 bf2 __attribute__((ext_vector_type(2)));
#define LAS __attribute__((address_space(3)))
#define GAS __attribute__((address_space(1)))

#define WAIT_V(n) asm volatile("s_waitcnt vmcnt(" #n ")" ::: "memory")
#define WAIT_V_IF0(n, c) asm volatile("s_cmp_lg_u32 %0, 0\n\ts_cbranch_scc1 1f\n\ts_waitcnt vmcnt(" #n ")\n1:" :: "s"(c) : "memory", "scc")
#define BAR_IF0(c) asm volatile("s_cmp_lg_u32 %0, 0\n\ts_cbranch_scc1 1f\n\ts_barrier\n\ts_setprio 0\n1:" :: "s"(c) : "memory", "scc")
#define WAIT_L0() do { asm volatile("" ::: "memory"); __builtin_amdgcn_s_waitcnt(0xC07F); asm volatile("" ::: "memory"); } while (0)
#define BAR() do { asm volatile("" ::: "memory"); __builtin_amdgcn_s_barrier(); asm volatile("" ::: "memory"); } while (0)
#define SCHED() __builtin_amdgcn_sched_barrier(0)

__device__ __forceinline__ float b2f(u16 v) { return __uint_as_float(((unsigned)v) << 16); }
__device__ __forceinline__ unsigned pk2(float lo, float hi) {
  bf2 r = {(__bf16)lo, (__bf16)hi};
  return __builtin_bit_cast(unsigned, r);
}
__device__ __forceinline__ float rbf(float f) { return (float)(__bf16)f; }
__device__ __forceinline__ bf16x8 pack8(f32x4 a, f32x4 b) {
  u32x4 r = {pk2(a[0], a[1]), pk2(a[2], a[3]), pk2(b[0], b[1]), pk2(b[2], b[3])};
  return __builtin_bit_cast(bf16x8, r);
}
__device__ __forceinline__ bf16x4 trd(LAS const char* p) {
  return __builtin_amdgcn_ds_read_tr16_b64_v4i16((LAS bf16x4*)p);
}
__device__ __forceinline__ bf16x8 cat(bf16x4 a, bf16x4 b) { return __builtin_shufflevector(a, b, 0, 1, 2, 3, 4, 5, 6, 7); }
__device__ __forceinline__ f32x4 mfma(bf16x8 a, bf16x8 b, f32x4 c) {
  return __builtin_amdgcn_mfma_f32_16x16x32_bf16(a, b, c, 0, 0, 0);
}
__device__ __forceinline__ void dma_first(unsigned lds_dst, unsigned voff, const char* b0) {
  asm volatile("s_mov_b32 m0, %0\n\ts_nop 0\n\tglobal_load_lds_dwordx4 %1, %2" :: "s"(lds_dst), "v"(voff), "s"(b0) : "memory");
}
template <int OFF>
__device__ __forceinline__ void dma_next(unsigned voff, const char* b) {
  asm volatile("global_load_lds_dwordx4 %0, %1 offset:%2" :: "v"(voff), "s"(b), "i"(OFF) : "memory");
}
__device__ __forceinline__ void dmc_first(unsigned lds_dst, unsigned voff, const char* b0) {
  asm volatile("s_mov_b32 m0, %0\n\ts_nop 0\n\tglobal_load_lds_dwordx4 %1, %2 sc1" :: "s"(lds_dst), "v"(voff), "s"(b0) : "memory");
}
template <int OFF>
__device__ __forceinline__ void dmc_next(unsigned voff, const char* b) {
  asm volatile("global_load_lds_dwordx4 %0, %1 offset:%2 sc1" :: "v"(voff), "s"(b), "i"(OFF) : "memory");
}
__device__ __forceinline__ void dma1m(unsigned lds_dst, const char* p, unsigned long long mask) {
  asm volatile("s_mov_b32 m0, %0\n\ts_mov_b64 exec, %2\n\t"
               "global_load_lds_dwordx4 %1, off sc1\n\t"
               "s_mov_b64 exec, -1"
               :: "s"(lds_dst), "v"(p), "s"(mask) : "memory");
}
__device__ __forceinline__ void touch(unsigned lds_dst, const void* p) {
  asm volatile("s_mov_b32 m0, %0\n\ts_nop 0\n\tglobal_load_lds_dword %1, off" :: "s"(lds_dst), "v"(p) : "memory");
}
#define CODE_ADDR(dst, sym) do { unsigned lo_, hi_; asm volatile("s_getpc_b64 s[90:91]\n\ts_add_u32 %0, s90, " #sym "@rel32@lo+4\n\ts_addc_u32 %1, s91, " #sym "@rel32@hi+12" \
    : "=s"(lo_), "=s"(hi_) :: "s90", "s91", "scc"); dst = (const char*)(((unsigned long long)hi_ << 32) | lo_); } while (0)
constexpr int LN_LINES = 16, G1_LINES = 80, G2_LINES = 30;
__device__ __forceinline__ LAS char* lp(unsigned a) { return (LAS char*)(size_t)a; }
__device__ __forceinline__ float fmx(float a, float b) { return __builtin_elementwise_maximum(a, b); }
__device__ __forceinline__ float xmax(float v) {
  auto a = __builtin_amdgcn_permlane16_swap(__float_as_uint(v), __float_as_uint(v), false, false);
  v = fmaxf(__uint_as_float(a[0]), __uint_as_float(a[1]));
  auto b = __builtin_amdgcn_permlane32_swap(__float_as_uint(v), __float_as_uint(v), false, false);
  return fmaxf(__uint_as_float(b[0]), __uint_as_float(b[1]));
}
__device__ __forceinline__ float xsum(float v) {
  auto a = __builtin_amdgcn_permlane16_swap(__float_as_uint(v), __float_as_uint(v), false, false);
  v = __uint_as_float(a[0]) + __uint_as_float(a[1]);
  auto b = __builtin_amdgcn_permlane32_swap(__float_as_uint(v), __float_as_uint(v), false, false);
  return __uint_as_float(b[0]) + __uint_as_float(b[1]);
}

template <int CTRL>
__device__ __forceinline__ float dpp_add(float v) {
  return v + __uint_as_float(__builtin_amdgcn_update_dpp(0u, __float_as_uint(v), CTRL, 0xf, 0xf, true));
}
__device__ __forceinline__ float hsum(float v) {
  v = dpp_add<0xB1>(v);
  v = dpp_add<0x4E>(v);
  v = dpp_add<0x128>(v);
  return xsum(v);
}

#define DG(kid, slot)
#define ST_DECL
#define LT_DECL
#define LT(k)
#define ET(k)
#define ST(k)
#define ST_OUT(kid, n)
#define ST_SAVE()
#define ST_REST()
constexpr int XN_STEP = 17 * 1024;
constexpr size_t WS_XN = 0;
constexpr size_t WS_H = WS_XN + (size_t)8 * 32 * XN_STEP;
constexpr size_t H_BYTES = (size_t)8 * 4 * 36 * 16 * 1024;
constexpr size_t WS_CNT = WS_H + H_BYTES;
constexpr size_t WS_GCNT = WS_CNT + 131072;
constexpr size_t WS_HCNT = WS_GCNT + 8192;
constexpr size_t WS_LNF = WS_HCNT + 32768;
constexpr size_t WS_ST = WS_LNF + 32768;

constexpr int G1_SA = 17 * 1024, G1_SB = 20 * 1024, G1_SLOT = G1_SA + G1_SB;
constexpr int T_SCR = 159488;
constexpr int T_BIAS = T_SCR + 256;
constexpr int G1_LDS = T_BIAS + 4096;
constexpr int G2_SA = 32768, G2_SB = 16384, G2_SLOT = G2_SA + G2_SB;
constexpr int T_SYNC = T_SCR - 64;
constexpr int T_KV = 2 * G2_SLOT, KV_BYTES = 7 * 1024 + 272 * 80;

template <bool MAX>
__device__ __forceinline__ float tred(float a, float b, float c, float d) {
  const auto r1 = __builtin_amdgcn_permlane16_swap(__float_as_uint(a), __float_as_uint(b), false, false);
  const auto r2 = __builtin_amdgcn_permlane16_swap(__float_as_uint(c), __float_as_uint(d), false, false);
  const float x0 = __uint_as_float(r1[0]), x1 = __uint_as_float(r1[1]), y0 = __uint_as_float(r2[0]), y1 = __uint_as_float(r2[1]);
  const float t = MAX ? fmx(x0, x1) : x0 + x1, u = MAX ? fmx(y0, y1) : y0 + y1;
  const auto r3 = __builtin_amdgcn_permlane32_swap(__float_as_uint(t), __float_as_uint(u), false, false);
  const float z0 = __uint_as_float(r3[0]), z1 = __uint_as_float(r3[1]);
  return MAX ? fmx(z0, z1) : z0 + z1;
}
__device__ __forceinline__ u32x2 glu4(f32x4 g, f32x4 u) {
  f32x4 e = g * -1.4426950408889634f;
#pragma unroll
  for (int i = 0; i < 4; ++i) e[i] = __builtin_amdgcn_exp2f(e[i]);
  e = e + 1.0f;
#pragma unroll
  for (int i = 0; i < 4; ++i) e[i] = __builtin_amdgcn_rcpf(e[i]);
  e = e * u;
  return (u32x2){pk2(e[0], e[1]), pk2(e[2], e[3])};
}
__device__ __forceinline__ u32x2 glu4s(f32x4 g, f32x4 u) {
  float e[4];
#pragma unroll
  for (int i = 0; i < 4; ++i) { e[i] = g[i] * -1.4426950408889634f; asm volatile("" : "+v"(e[i])); e[i] = __builtin_amdgcn_exp2f(e[i]); }
#pragma unroll
  for (int i = 0; i < 4; ++i) { e[i] = e[i] + 1.0f; asm volatile("" : "+v"(e[i])); e[i] = __builtin_amdgcn_rcpf(e[i]); }
#pragma unroll
  for (int i = 0; i < 4; ++i) { e[i] = e[i] * u[i]; asm volatile("" : "+v"(e[i])); }
  return (u32x2){pk2(e[0], e[1]), pk2(e[2], e[3])};
}
__device__ __forceinline__ u32x4 cat4(u32x2 a, u32x2 b) { return (u32x4){a[0], a[1], b[0], b[1]}; }

constexpr unsigned long long step_order(bool ta, int j, int part) {
  const int own0 = ta ? j : (16 + 5 * j + (j & 1)) / 2, own1 = ta ? 4 + j : own0 + 1;
  unsigned long long r[2] = {0, 0};
  int p = 0;
  for (int t = 0; t < 18; ++t) {
    const int s = (t + 8) % 18;
    if (s == own0 || s == own1) continue;
    r[p / 12] |= (unsigned long long)s << (5 * (p % 12));
    ++p;
  }
  return r[part];
}

template <bool typeA>
__device__ __forceinline__ void body(const u16* __restrict__ x, u16* __restrict__ out, const u16* __restrict__ wqkv, const u16* __restrict__ wg,
                                     const u16* __restrict__ wu, const u16* __restrict__ wo, const u16* __restrict__ wd, char* __restrict__ ws) {
  extern __shared__ __attribute__((aligned(16))) char smem[];
  LAS char* lds = (LAS char*)smem;
  ST_DECL LT_DECL ST(0);
  const int tid = threadIdx.x;
  int lane = tid & 63;
  const int w = __builtin_amdgcn_readfirstlane(tid >> 6), g = w >> 2, wi = w & 3;
  const int b = blockIdx.x, xcd = b & 7, idx = b >> 3;
  const int mb = (xcd & 1) * 4 + (idx & 3), kq = xcd >> 1, ntl = idx >> 2;
  const int j = ntl & 3;
  int fr = lane & 15, fq = lane >> 4;
  const int fA = 896 * kq + 64 * j;
  const int f0 = 896 * kq + 256 + 160 * j, odd = j & 1;
  const int fa = f0 + 32 * odd, fb = fa + 64, fc = odd ? f0 : f0 + 128;

  const int mi = kq * 8 + ntl;
  const bool lnh = !typeA && w == 4 && j < 2, lnw = g == 0 || lnh;
  const char* const xnb = ws + WS_XN + (size_t)mb * 32 * XN_STEP;
  if (lnw) {
    __builtin_amdgcn_s_setprio(2);
    const int lr = (lnh ? (kq * 2 + j) * 2 : (mi & 1) * 8 + wi * 2) + ((lane >> 2) & 1), lf = lnh ? 16 : mi >> 1;
    const int lrow = lnh ? (mb > 0 ? mb * 256 - 16 : 0) + 2 * (lr & 7) + (lr >> 3) : mb * 256 + 32 * (lf >> 1) + 2 * lr + (lf & 1);
    const char* xr_ = (const char*)x + (lrow * 2048 + (lane >> 3) * 64 + (lane & 3) * 16);
    u32x4 raw[4];
#pragma unroll
    for (int i = 0; i < 4; ++i) raw[i] = *(const u32x4*)(xr_ + 512 * i);
    const auto xrs = __builtin_amdgcn_make_buffer_rsrc((void*)xnb, 0, 32 * XN_STEP, 0x00020000);
    const unsigned o = (lane >> 3) * XN_STEP + lf * 1024 + lr * 64 + (((lane & 3) ^ (2 * (lr >> 3))) * 16);
    ET(1);
    f32x2 v[16];
#pragma unroll
    for (int i = 0; i < 16; ++i) v[i] = (f32x2){__uint_as_float(raw[i >> 2][i & 3] << 16), __uint_as_float(raw[i >> 2][i & 3] & 0xffff0000u)};
    f32x2 s2 = (v[0] + v[1]) + (v[2] + v[3]);
#pragma unroll
    for (int i = 4; i < 16; i += 4) s2 += (v[i] + v[i + 1]) + (v[i + 2] + v[i + 3]);
    ET(2);
    const float mean = hsum(s2[0] + s2[1]) * (1.0f / 1024.0f);
    f32x2 qa = {0.f, 0.f}, qb = {0.f, 0.f};
#pragma unroll
    for (int i = 0; i < 16; i += 2) { const f32x2 d = v[i] - mean, e = v[i + 1] - mean; qa += d * d; qb += e * e; }
    qa += qb;
    const float var = hsum(qa[0] + qa[1]) * (1.0f / 1024.0f);
    const float inv = __builtin_amdgcn_rcpf(rbf(__builtin_amdgcn_sqrtf(rbf(rbf(var) + 1e-5f)))), sh = -rbf(mean);
#pragma unroll
    for (int i = 0; i < 4; ++i) {
      u32x4 t;
#pragma unroll
      for (int e = 0; e < 4; ++e) { const f32x2 y = (v[4 * i + e] + sh) * inv; t[e] = pk2(y[0], y[1]); }
      __builtin_amdgcn_raw_buffer_store_b128(t, xrs, o + i * 8 * XN_STEP, 0, 16);
    }
    __builtin_amdgcn_s_setprio(0);
  }
  GAS unsigned* lnf = (GAS unsigned*)(ws + WS_LNF) + mb * 1024 + (lane < 32 ? mi * 32 + lane : (lane - 32) * 32 + mi);
  unsigned entry = 0;
  asm volatile("s_cmp_lg_u32 %2, 7\n\ts_cbranch_scc1 1f\n\tglobal_load_dword %0, %1, off sc1\n1:" : "+v"(entry) : "v"(lnf), "s"(w) : "memory", "scc");
  if (w == 3) {
    char* z = lane < 8 ? ws + WS_CNT + (((mb * 8 + ntl) * 4 + kq) * 8 + lane) * 64
            : (lane < 10 ? ws + WS_HCNT + ((mb * 4 + kq) * 8 + ntl) * 128 + (lane - 8) * 16 : ws + WS_GCNT + (mb * 4 + kq) * 64);
    if (lane < (ntl == 0 ? 11 : 10)) asm volatile("global_store_dwordx4 %0, %1, off sc1\n\ts_nop 1" :: "v"(z), "v"((u32x4){0u, 0u, 0u, 0u}) : "memory");
  }

  const bool isq = typeA && wi < 3;
  const int ld = isq ? 3072 : 3584;
  const u16* q1 = isq ? wqkv + (wi * 1024 + 256 * kq + 64 * j) : ((!typeA && (wi & 2)) ? wu : wg) + (typeA ? fA : ((wi & 1) ? fb : fa));
  const char* c1 = g ? (const char*)q1 : xnb + wi * 4096;
  const int s1 = g ? 8 * ld - 1024 : 0, s2 = g ? 32 * ld - 2048 : 0, s3 = g ? 40 * ld - 3072 : 0;
  const int p1 = g ? 64 * ld : XN_STEP, p2 = g ? XN_STEP : 64 * 3584;
  const int d1 = (g ? G1_SA : 0) + wi * 4096, d2 = g ? 16384 + wi * 256 : G1_SA + 16384 + wi * 1024;
  const unsigned long long m2 = g ? 0xffffull : ~0ull;
  const int rr = lane >> 3, pc = lane & 7, fi = (pc >> 1) ^ ((rr >> 1) & 3), kr = (rr & 3) + 8 * (rr >> 2);
  const unsigned v1 = g ? kr * ld * 2 + fi * 32 + (pc & 1) * 16 : lane * 16;
  const unsigned lb = (unsigned)(size_t)lds;
  const char* c2;
  {
    const unsigned ow = (kr + 16 * (wi >> 1) + 4 * (wi & 1)) * 7168 + (typeA ? fi * 32 : (fi >> 1) * 32) + (pc & 1) * 16;
    const char* be = g ? xnb : (const char*)(typeA ? wu + fA : wg + fc);
    const char* bo = g ? be : (const char*)(wu + (typeA ? fA : fc));
    c2 = ((fi & 1) ? bo : be) + (g ? 16384 + wi * 256 + (lane & 15) * 16 : ow);
  }
  auto chain1 = [&](unsigned dst, const char* p) { dmc_first(dst + d1, v1, p); dmc_next<1024>(v1, p + s1); dmc_next<2048>(v1, p + s2); dmc_next<3072>(v1, p + s3); };
  auto first = [&](bool one) {
    if (one) { chain1(lb, c1); chain1(lb + G1_SLOT, c1 + p1); }
    else { dma1m(lb + d2, c2, m2); dma1m(lb + G1_SLOT + d2, c2 + p2, m2); }
  };
  if (!lnw) first(true);
  else WAIT_V(0);
  asm volatile("s_waitcnt vmcnt(8)" : "+v"(entry) :: "memory");
  ET(3);
  BAR();
  if (w == 7 && lane >= 32) __hip_atomic_store(lnf, entry + 1u, __ATOMIC_RELAXED, __HIP_MEMORY_SCOPE_AGENT);
  if (lnw) first(g == 1);
  ST(1);

  const int nt = ntl;
  const int kba = 8 + ((fa - 896 * kq) >> 5);
  const int own0 = typeA ? j : (kba >> 1), own1 = typeA ? 4 + j : (kba >> 1) + 1;
  f32x4 acc[4][10];
  f32x4 hacc = {0.f, 0.f, 0.f, 0.f};
#pragma unroll
  for (int i = 0; i < 4; ++i)
#pragma unroll
    for (int n = 0; n < 10; ++n) { acc[i][n] = (f32x4){0.f, 0.f, 0.f, 0.f}; asm volatile("" : "+v"(acc[i][n])); }
  const int rl = 4 * (fq & 1) + (fr >> 2);
  unsigned hoffA = lb + 16384 + fr * 64 + ((fq ^ (2 * (fr >> 3))) * 16);
  unsigned aoff = hoffA - 16384 + wi * 4096;
  unsigned boff0 = lb + G1_SA + (fq >> 1) * 2048 + rl * 128 + 32 * ((2 * g) ^ ((rl >> 1) & 3)) + 8 * (lane & 3);
  unsigned boff1 = boff0 ^ 32;
  unsigned hoffB = (boff0 ^ (32 * (wi & 1))) + (1 + (wi >> 1)) * 4096;
  asm volatile("" : "+v"(aoff), "+v"(hoffA), "+v"(boff0), "+v"(boff1), "+v"(hoffB), "+v"(hacc));
  {
    const int lo = fr + (wi == 0 ? -7 : (wi == 2 ? 1 : 9));
    const int hi = wi == 0 ? fr + 1 : ((wi >= 2 && mb == 0) ? 0 : (wi == 2 ? 8 : 16));
    f32x4 bv;
#pragma unroll
    for (int e = 0; e < 4; ++e) bv[e] = (4 * fq + e >= lo && 4 * fq + e < hi) ? 0.f : -3.0e38f;
    *(LAS f32x4*)(lds + T_BIAS + wi * 1024 + lane * 16) = bv;
    *(LAS unsigned*)(lds + T_SYNC + (lane & 1) * 4) = 0u;
  }
  ET(4);
  if (w == 7) {
    for (unsigned spins = 0; spins < SPIN_CAP; ++spins) {
      unsigned now = entry + 1u;
      if (lane < 32) now = __hip_atomic_load(lnf, __ATOMIC_RELAXED, __HIP_MEMORY_SCOPE_AGENT);
      if (__builtin_amdgcn_ballot_w64(now == entry) == 0ull) break;
      __builtin_amdgcn_s_sleep(1);
    }
    if (lane >= 32) __hip_atomic_store(lnf, entry + 2u, __ATOMIC_RELAXED, __HIP_MEMORY_SCOPE_AGENT);
  }
  WAIT_V_IF0(0, g);
  BAR();
  ET(5);
  first(g == 0);
  c1 += 2 * p1; c2 += 2 * p2;
  unsigned ns = lb + 2 * G1_SLOT;
  auto adv = [&]() { c1 += p1; c2 += p2; ns = (ns == lb + 3 * G1_SLOT) ? lb : ns + G1_SLOT; };
  WAIT_V(0);
  ST(2);
  if (g == 1) BAR();

  auto step = [&](auto halo_c, auto last_c, int q) {
    constexpr bool HALO = decltype(halo_c)::value, LAST = decltype(last_c)::value;
    bf16x8 a[2][4], wn[4][2];
    asm volatile("" ::: "memory");
    auto rdq = [&](int e, int h) {
      if (e >= (HALO ? 12 : 10)) return;
      const int kk = h >> 1, t = h & 1;
      bf16x8& d = wn[e & 3][kk];
      if (e == 11) { if (t == 0) d = *(const LAS bf16x8*)(lp(hoffA) + kk * G1_SLOT); return; }
      const int n = e < 2 ? 8 + e : e - 2;
      const bf16x4 v = trd(lp(e == 10 ? hoffB : ((n & 1) ? boff1 : boff0)) + (e == 10 ? 0 : (n >> 1) * 4096) + t * 1024 + kk * G1_SLOT);
      if (t == 0) { d[0] = v[0]; d[1] = v[1]; d[2] = v[2]; d[3] = v[3]; } else { d[4] = v[0]; d[5] = v[1]; d[6] = v[2]; d[7] = v[3]; }
    };
#define RA1(kk, i) a[kk][i] = *(const LAS bf16x8*)(lp(aoff) + (kk) * G1_SLOT + (i) * 1024);
    const int dlt = (q & 1) ? -2 * G1_SLOT : 2 * G1_SLOT;
    if (!LAST) {
#pragma unroll
      for (int kk = 0; kk < 2; ++kk) {
        dmc_first(ns + d1, v1, c1); RA1(kk, 0) rdq(kk, 0);
        dmc_next<1024>(v1, c1 + s1); RA1(kk, 1) rdq(kk, 1);
        dmc_next<2048>(v1, c1 + s2); RA1(kk, 2) rdq(kk, 2);
        dmc_next<3072>(v1, c1 + s3); RA1(kk, 3) rdq(kk, 3);
        dma1m(ns + d2, c2, m2);
        adv();
      }
    } else
    {
#pragma unroll
      for (int kk = 0; kk < 2; ++kk)
#pragma unroll
        for (int i = 0; i < 4; ++i) RA1(kk, i)
#pragma unroll
      for (int h = 0; h < 8; ++h) rdq(h >> 2, h & 3);
    }
    aoff += dlt;
    SCHED();
    if (!LAST) {
    } else {
#if PF_W2 && !(PFD & 1)
      int t = w * 64 + __builtin_amdgcn_mbcnt_hi(~0u, __builtin_amdgcn_mbcnt_lo(~0u, 0u));
      t = t < 288 ? t : t - 288;
      const int r2 = (t >> 4) * 64 + (xcd & 1) * 32 + idx;
      touch(lb + T_SCR, (r2 < 256 ? wo + (256 * kq + r2) * 1024 : wd + (896 * kq + r2 - 256) * 1024) + (t & 15) * 64);
#endif
    }
    WAIT_L0();
    SCHED();
    BAR();
    SCHED();
    PRIO_M();
    auto fin = [&]() { SCHED(); PRIO_T(); if (!LAST) { if (PFD) WAIT_V(1); else WAIT_V(0); } BAR(); SCHED(); };
#pragma unroll
    for (int e = 0; e < 10; ++e) {
      const int n = e < 2 ? 8 + e : e - 2;
#pragma unroll
      for (int i = 0; i < 4; ++i) {
        acc[i][n] = mfma(wn[e & 3][0], a[0][i], acc[i][n]);
        SCHED();
        if (!HALO && e == 9 && i == 3) fin();
        SCHED();
        acc[i][n] = mfma(wn[e & 3][1], a[1][i], acc[i][n]);
        SCHED();
        if (e == 0) { rdq(2 + (i >> 1), 2 * (i & 1)); rdq(2 + (i >> 1), 2 * (i & 1) + 1); } else rdq(e + 3, i);
        if (i == 3 && e < 9) { asm volatile("" :: "v"(wn[(e + 1) & 3][0]), "v"(wn[(e + 1) & 3][1])); }
        SCHED();
      }
    }
    if (HALO) {
      hacc = mfma(wn[2][0], wn[3][0], hacc);
      WAIT_L0();
      fin();
      hacc = mfma(wn[2][1], wn[3][1], hacc);
    }
    hoffA += dlt; boff0 += dlt; boff1 += dlt; hoffB += dlt;
    PRIO_E();
    SCHED();
  };
  const char* const hbase = ws + WS_H + (size_t)(mb * 4 + kq) * 36 * 16384;
  auto brow = [&](int stp_, int w_) {
    return (const char*)(stp_ < 4 ? wo + (size_t)(256 * kq) * 1024 : wd + (size_t)(896 * kq) * 1024 - 256 * 1024) + ((stp_ * 64 + 8 * w_) * 1024 + nt * 128) * 2;
  };
  unsigned va, vb;
  auto stage1 = [&](auto halo_c) {
    constexpr bool TA = decltype(halo_c)::value;
    typedef std::integral_constant<bool, TA || !HALO_SKIP> HC;
#pragma clang loop unroll(disable)
    for (int q = 0; q < 15; ++q) step(HC{}, std::false_type{}, q);
    step(HC{}, std::true_type{}, 15);
    ST(3);
    asm volatile("" ::: "memory");
    if (g == 0) __builtin_amdgcn_s_setprio(2); else __builtin_amdgcn_s_setprio(EPI_PRIO);
    lane = __builtin_amdgcn_mbcnt_hi(~0u, __builtin_amdgcn_mbcnt_lo(~0u, 0u));
    asm volatile("" : "+v"(lane));
    fr = lane & 15; fq = lane >> 4;
    va = lane * 16;
    vb = (lane >> 3) * 2048 + ((((lane & 7) >> 1) ^ ((lane >> 4) & 3)) * 32) + (lane & 1) * 16;
    if (g == 1) {
#pragma unroll
      for (int u = 0; u < 4; ++u) {
        const int w_ = wi * 2 + (u & 1);
        const char* p0 = brow((u >> 1) ? own1 : own0, w_);
        dma_first(lb + (u >> 1) * G2_SLOT + G2_SA + w_ * 2048, vb, p0); dma_next<1024>(vb, p0 - 896);
      }
    }
    const auto hrs = __builtin_amdgcn_make_buffer_rsrc((void*)hbase, 0, 36 * 16384, 0x00020000);
    const unsigned hv = wi * 4096 + lane * 16;
    auto put = [&](u32x4 v, int s, int kb, int i) {
      __builtin_amdgcn_raw_buffer_store_b128(v, hrs, hv + kb * 16384 + i * 1024, 0, H_AUX);
      *(LAS u32x4*)(lds + s * G2_SLOT + g * 16384 + ((wi * 4096 + kq * 1024 + lane * 16) ^ (i * 1024))) = v;
    };
#define GLU(i, a, b) glu4(acc[i][a], acc[i][b])
    if (!TA) {
      const int kbc = 8 + ((fc - 896 * kq) >> 5);
      auto part = [&](int i) {
        put(cat4(GLU(i, 0, 4), GLU(i, 1, 5)), 0, kba + g, i);
        put(cat4(GLU(i, 2, 6), GLU(i, 3, 7)), 1, kba + 2 + g, i);
      };
      if (g == 0) { part(0); SCHED(); BAR(); __builtin_amdgcn_s_setprio(0); }
#pragma unroll
      for (int i = 0; i < 4; ++i) *(LAS u32x2*)(lds + T_KV + hv + i * 1024 + g * 8) = GLU(i, 8, 9);
      WAIT_L0();
      BAR();
#pragma unroll
      for (int i = 0; i < 2; ++i)
        __builtin_amdgcn_raw_buffer_store_b128(*(const LAS u32x4*)(lds + T_KV + hv + g * 2048 + i * 1024), hrs, hv + kbc * 16384 + g * 2048 + i * 1024, 0, H_AUX);
#pragma unroll
      for (int i = 1; i < 4; ++i) part(i);
      if (g == 1) part(0);
    } else {
      auto glu_part = [&](int i0, int i1) {
#pragma unroll
        for (int i = i0; i < i1; ++i) put(cat4(GLU(i, 6, 8), GLU(i, 7, 9)), 1, 8 + 2 * j + g, i);
      };
      if (g == 0) { glu_part(0, FA); SCHED(); BAR(); __builtin_amdgcn_s_setprio(0); asm volatile("" ::: "memory"); }
    LAS char* kim = lds + T_KV + g * KV_BYTES;
    LAS char* vim = kim + 7 * 1024;
    bf16x8 kf[4], qf[4];
#pragma unroll
    for (int i = 0; i < 4; ++i) {
      kf[i] = pack8(acc[i][2], acc[i][3]);
      *(LAS bf16x8*)(vim + ((wi * 4 + i) * 16 + fr) * 80 + fq * 16) = pack8(acc[i][4], acc[i][5]);
    }
    if (wi < 3) {
      *(LAS bf16x8*)(kim + wi * 2048 + lane * 16) = kf[2];
      *(LAS bf16x8*)(kim + wi * 2048 + 1024 + lane * 16) = kf[3];
    }
    {
      LAS char* hp = (wi < 2) ? (kim + 6 * 1024 + lane * 16 + wi * 8) : (vim + (256 + fr) * 80 + fq * 16 + (wi - 2) * 8);
      *(LAS u32x2*)hp = (u32x2){pk2(hacc[0], hacc[1]), pk2(hacc[2], hacc[3])};
    }
    WAIT_L0();
    BAR();
    asm volatile("" ::: "memory");
    const bool halo = wi == 0;
    const f32x4 bc = *(const LAS f32x4*)(lds + T_BIAS + lane * 16), bp = *(const LAS f32x4*)(lds + T_BIAS + 1024 + lane * 16);
    const f32x4 bp0 = *(const LAS f32x4*)(lds + T_BIAS + (halo ? 2048 : 1024) + lane * 16);
    const f32x4 bp1 = *(const LAS f32x4*)(lds + T_BIAS + (halo ? 3072 : 1024) + lane * 16);
    const int pf0 = halo ? 16 : wi * 4 - 2, pf1 = halo ? 16 : wi * 4 - 1;
    const bf16x8 kp0 = *(const LAS bf16x8*)(kim + (halo ? 6 : 2 * wi - 2) * 1024 + lane * 16);
    const bf16x8 kp1 = *(const LAS bf16x8*)(kim + (halo ? 6 : 2 * wi - 1) * 1024 + lane * 16);
    const LAS char* vr = vim + (4 * fq + (fr >> 2)) * 80 + (lane & 3) * 16;
    bf16x4 vt[6][2];
#pragma unroll
    for (int d = 0; d < 2; ++d) {
      vt[0][d] = trd(vr + pf0 * 1280 + d * 8); vt[1][d] = trd(vr + pf1 * 1280 + d * 8);
#pragma unroll
      for (int i = 0; i < 4; ++i) vt[2 + i][d] = trd(vr + (wi * 4 + i) * 1280 + d * 8);
    }
    f32x4 sp[4], sc[4];
#pragma unroll
    for (int i = 0; i < 4; ++i) {
      qf[i] = pack8(acc[i][0], acc[i][1]);
      sc[i] = mfma(kf[i], qf[i], bc);
    }
    sp[2] = mfma(kf[0], qf[2], bp); sp[3] = mfma(kf[1], qf[3], bp);
    sp[0] = mfma(kp0, qf[0], bp0); sp[1] = mfma(kp1, qf[1], bp1);
    const float sl = 0.17677669529663687f * 1.4426950408889634f;
    const f32x4 z4 = {0.f, 0.f, 0.f, 0.f};
    float m[4];
#pragma unroll
    for (int i = 0; i < 4; ++i)
      m[i] = fmx(fmx(fmx(fmx(sp[i][0], sp[i][1]), sp[i][2]), fmx(fmx(sp[i][3], sc[i][0]), sc[i][1])), fmx(sc[i][2], sc[i][3]));
    {
      const unsigned t = __float_as_uint(tred<true>(m[0], m[1], m[2], m[3]));
      const auto u = __builtin_amdgcn_permlane32_swap(t, t, false, false);
      const auto u0 = __builtin_amdgcn_permlane16_swap(u[0], u[0], false, false), u1 = __builtin_amdgcn_permlane16_swap(u[1], u[1], false, false);
      m[0] = __uint_as_float(u0[0]); m[1] = __uint_as_float(u0[1]); m[2] = __uint_as_float(u1[0]); m[3] = __uint_as_float(u1[1]);
    }
    const u32x4 one4 = {0x3f803f80u, 0x3f803f80u, 0x3f803f80u, 0x3f803f80u};
#pragma unroll
    for (int i = 0; i < 4; ++i) {
      const float mm = -sl * m[i];
#pragma unroll
      for (int e = 0; e < 4; ++e) {
        sp[i][e] = __builtin_amdgcn_exp2f(__builtin_fmaf(sp[i][e], sl, mm));
        sc[i][e] = __builtin_amdgcn_exp2f(__builtin_fmaf(sc[i][e], sl, mm));
      }
      const bf16x8 pfr = pack8(sp[i], sc[i]);
      const f32x4 sm = mfma(__builtin_bit_cast(bf16x8, one4), pfr, z4);
      const f32x4 o0 = mfma(cat(vt[i][0], vt[2 + i][0]), pfr, z4);
      const f32x4 o1 = mfma(cat(vt[i][1], vt[2 + i][1]), pfr, z4);
      const float inv = __builtin_amdgcn_rcpf(sm[0]);
      put(__builtin_bit_cast(u32x4, pack8(o0 * inv, o1 * inv)), 0, 2 * j + g, i);
    }
      glu_part(FA, 4);
      if (g == 1) glu_part(0, FA);
    }
  };
  stage1(std::integral_constant<bool, typeA>{});
  ST(4);

  const int lag = g ^ 1;
  GAS unsigned* hflag = (GAS unsigned*)(ws + WS_HCNT) + (mb * 4 + kq) * 256;
  const unsigned fo = (nt * 32 + (lane & 7)) * 4;
  GAS unsigned* gcnt = (GAS unsigned*)(ws + WS_GCNT) + (mb * 4 + kq) * 16;
  const int e0 = kq * 1024, e1 = ((1 ^ kq) - 1) * 1024, e2 = ((2 ^ kq) - 2) * 1024, e3 = ((3 ^ kq) - 3) * 1024;
  auto wait_cnt = [&](unsigned seen, GAS unsigned* c, unsigned need) {
    if ((unsigned)__builtin_amdgcn_readfirstlane(seen) < need) {
      asm volatile("" ::: "memory");
      unsigned spins = 0;
      while ((unsigned)__builtin_amdgcn_readfirstlane(__hip_atomic_load(c, __ATOMIC_RELAXED, __HIP_MEMORY_SCOPE_AGENT)) < need)
        if (++spins > SPIN_CAP) break;
    }
    asm volatile("" ::: "memory");
  };
  auto wait_flags = [&](unsigned seen, unsigned long long need) {
    if ((__builtin_amdgcn_ballot_w64(seen != 0u) & need) != need) {
      asm volatile("" ::: "memory");
      unsigned spins = 0;
      while ((__builtin_amdgcn_ballot_w64(__hip_atomic_load(hflag + (fo >> 2), __ATOMIC_RELAXED, __HIP_MEMORY_SCOPE_AGENT) != 0u) & need) != need) {
        __builtin_amdgcn_s_sleep(1);
        if (++spins > SPIN_CAP) break;
      }
    }
    asm volatile("" ::: "memory");
  };
  unsigned long long ord0 = j & 2 ? (j & 1 ? step_order(typeA, 3, 0) : step_order(typeA, 2, 0)) : (j & 1 ? step_order(typeA, 1, 0) : step_order(typeA, 0, 0));
  const unsigned ord1 = (unsigned)(j & 2 ? (j & 1 ? step_order(typeA, 3, 1) : step_order(typeA, 2, 1)) : (j & 1 ? step_order(typeA, 1, 1) : step_order(typeA, 0, 1)));
  const char* const caw = hbase + w * 4096;
  const char* const cbo = brow(0, w); const char* const cbd = brow(4, w) - 4 * 131072;
  auto pop = [&](const char*& ca_, const char*& cb_) {
    const unsigned s = (unsigned)ord0 & 31u;
    ord0 >>= 5;
    ca_ = caw + (s << 15); cb_ = (s < 4u ? cbo : cbd) + (s << 17);
  };
  const int mf = wi * 4 + kq;
  const unsigned orow = ((mb * 256 + 32 * (mf >> 1) + 2 * fr + (mf & 1)) * 1024 + nt * 128 + g * 64 + 16 * (fq & 1) + 8 * (fq >> 1)) * 2;
  u32x4 xr[2] = {{0u, 0u, 0u, 0u}, {0u, 0u, 0u, 0u}};
  const int rl2 = 4 * (fq & 1) + (fr >> 2);
  unsigned ao = lb + wi * 4096 + lane * 16;
  unsigned bo = lb + G2_SA + (fq >> 1) * 2048 + g * 1024 + rl2 * 128 + 32 * ((rl2 >> 1) & 3) + 8 * (lane & 3);
  f32x4 ac2[4][4];
#pragma unroll
  for (int i = 0; i < 4; ++i)
#pragma unroll
    for (int n = 0; n < 4; ++n) ac2[i][n] = (f32x4){0.f, 0.f, 0.f, 0.f};
  ns = lb + 2 * G2_SLOT;
  const char *ca, *cb;
  const unsigned ordf = (unsigned)ord0;
  pop(ca, cb);
  unsigned sl2 = lb;
  WAIT_V_IF0(0, lag);
  WAIT_L0();
  BAR();
  ST(5);
  ST_SAVE(); ST(0); ST(1);
  unsigned seen = 0;
#define PEEK(c, o) asm volatile("global_load_dword %0, %1, %2 sc1" : "+v"(seen) : "v"(o), "s"(c) : "memory")
  auto publish = [&]() {
    WAIT_V(0);
    unsigned old = 0;
    if (lane == 0) old = __hip_atomic_fetch_add((LAS unsigned*)(lds + T_SYNC), 1u, __ATOMIC_RELAXED, __HIP_MEMORY_SCOPE_WORKGROUP);
    if (__builtin_amdgcn_readfirstlane(old) == 3u && lane < 8) __hip_atomic_store(hflag + lane * 32 + nt, 1u, __ATOMIC_RELAXED, __HIP_MEMORY_SCOPE_AGENT);
  };
  if (lag) { if (!typeA) publish(); BAR(); }

  bf16x8 ka[2][2], kb[2][2][4];
  auto phase2 = [&](auto mode, int q) {
    constexpr int MODE = decltype(mode)::value;
    bf16x8 a[2][4], bfr[2][4];
#define RA(kk, i) a[kk][i] = *(const LAS bf16x8*)(lp(ao) + (kk) * 16384 + (i) * 1024);
#define RB(kk, n) bfr[kk][n] = cat(trd(lp(bo ^ ((n) * 32)) + (kk) * 8192), trd(lp(bo ^ ((n) * 32)) + (kk) * 8192 + 4096));
    int dlt;
    asm volatile("" ::: "memory");
    if (MODE == 1 && q == 1 && w == 4) PEEK(hflag, fo);
    if (MODE == 2 && q == 16)
      asm volatile("global_load_dwordx4 %0, %2, %3\n\tglobal_load_dwordx4 %1, %2, %3 offset:64" : "+v"(xr[0]), "+v"(xr[1]) : "v"(orow), "s"(x) : "memory");
    RA(0, 0) RA(0, 1)
    if (MODE == 0) dmc_first(ns + w * 4096, va, ca + e0);
    RA(0, 2) RA(0, 3) RB(0, 0)
    if (MODE == 0) dmc_next<1024>(va, ca + e1);
    RB(0, 1) RB(0, 2) RB(0, 3)
    if (MODE == 0) dmc_next<2048>(va, ca + e2);
    RA(1, 0) RA(1, 1) RA(1, 2)
    if (MODE == 0) dmc_next<3072>(va, ca + e3);
    RA(1, 3) RB(1, 0)
    if (MODE < 2) dma_first(ns + G2_SA + w * 2048, vb, cb);
    RB(1, 1)
    if (MODE < 2) dma_next<1024>(vb, cb - 896);
    dlt = (sl2 == lb + 2 * G2_SLOT) ? -2 * G2_SLOT : G2_SLOT; ns = sl2; sl2 += dlt;
    if (MODE < 2) pop(ca, cb);
    asm volatile("" : "+s"(ca), "+s"(cb), "+s"(ns), "+s"(sl2), "+s"(dlt), "+s"(ord0) :: "memory");
    RB(1, 2) RB(1, 3)
#undef RA
#undef RB
    ao += dlt; bo += dlt;
    SCHED();
    asm volatile("" : "+v"(bo), "+v"(ao));
    if (MODE == 0) WAIT_V_IF0(6, lag ^ 1); else if (MODE == 2 && q == 16) WAIT_V_IF0(2, lag ^ 1);
    if (MODE == 1 && q == 1) {
      asm volatile("s_waitcnt vmcnt(2)" : "+v"(seen) :: "memory");
      if (!lag) {
        volatile LAS unsigned* go = (volatile LAS unsigned*)(lds + T_SYNC + 4);
        if (w == 4) { wait_flags(seen, 0xf0ull); *go = 1u; }
        else { unsigned spins = 0; while (__builtin_amdgcn_readfirstlane(*go) == 0u) { __builtin_amdgcn_s_sleep(1); if (++spins > (SPIN_CAP << 3)) break; } }
        asm volatile("" ::: "memory");
      }
      const int s = (int)(ordf >> (5 * lag)) & 31;
      const char* p = hbase + s * 32768 + wi * 8192;
      const unsigned d = lb + (lag ? 0 : 2 * G2_SLOT) + wi * 8192;
      dmc_first(d, va, p + e0); dmc_next<1024>(va, p + e1); dmc_next<2048>(va, p + e2); dmc_next<3072>(va, p + e3);
      dmc_first(d + 4096, va, p + 4096 + e0); dmc_next<1024>(va, p + 4096 + e1); dmc_next<2048>(va, p + 4096 + e2); dmc_next<3072>(va, p + 4096 + e3);
    }
    WAIT_L0();
    SCHED();
    BAR();
    SCHED();
    P2_M();
#pragma unroll
    for (int n = 0; n < 4; ++n)
#pragma unroll
      for (int i = (MODE == 2 ? 1 : 0); i < 4; ++i) {
        ac2[i][n] = mfma(bfr[0][n], a[0][i], ac2[i][n]);
        SCHED();
        if (n == 3 && i == 3) {
          P2_T();
          if (MODE == 0) WAIT_V_IF0(6, lag); else if (MODE == 2 && q == 16) WAIT_V_IF0(2, lag);
          if (MODE == 1 && q == 1) WAIT_V_IF0(0, lag);
          if (q < 17 || !lag) BAR();
          SCHED();
        }
        ac2[i][n] = mfma(bfr[1][n], a[1][i], ac2[i][n]);
        SCHED();
        if (typeA && MODE == 1 && q == 0 && n == 1 && i == 3 && lag) { publish(); SCHED(); }
      }
    if (MODE == 2) {
#pragma unroll
      for (int kk = 0; kk < 2; ++kk) {
        ka[q & 1][kk] = a[kk][0];
#pragma unroll
        for (int n = 0; n < 4; ++n) kb[q & 1][kk][n] = bfr[kk][n];
      }
    }
    P2_E();
    SCHED();
  };
  typedef std::integral_constant<int, 0> M0; typedef std::integral_constant<int, 1> M1; typedef std::integral_constant<int, 2> M2;
  phase2(M1{}, 0);
  phase2(M1{}, 1);
  ST(2);
#pragma clang loop unroll(disable)
  for (int q = 2; q < 6; ++q) phase2(M0{}, q);
  PEEK(hflag, fo);
  phase2(M0{}, 6);
  asm volatile("" : "+v"(seen));
  ord0 |= (unsigned long long)ord1 << 20;
  wait_flags(seen, 0x0full);
#pragma clang loop unroll(disable)
  for (int q = 7; q < 13; ++q) phase2(M0{}, q);
  if (w == 4 && lane == 0) __hip_atomic_fetch_add(gcnt, 1u, __ATOMIC_RELAXED, __HIP_MEMORY_SCOPE_AGENT);
#pragma clang loop unroll(disable)
  for (int q = 13; q < 16; ++q) phase2(M0{}, q);
  phase2(M2{}, 16);
  PEEK(gcnt, 0u);
  phase2(M2{}, 17);
  asm volatile("s_waitcnt vmcnt(0)" : "+v"(seen) :: "memory");
  wait_cnt(seen, gcnt, 8u);
  ST(3);
  const int tile = mb * 8 + nt;
  const auto rs = __builtin_amdgcn_make_buffer_rsrc(ws + WS_H + (size_t)mb * 4 * 36 * 16384, 0, 4 * 36 * 16384, 0x00020000);
  auto xo = [&](int src, int i) { const int u = 15 + nt * 3 + i; return (unsigned)((src * 36 + (u >= 36 ? u - 36 : u)) * 16384 + w * 2048) + lane * 16; };
#pragma unroll
  for (int i = 1; i < 4; ++i) {
    const unsigned o = xo(kq, i);
    __builtin_amdgcn_raw_buffer_store_b128(__builtin_bit_cast(u32x4, pack8(ac2[i][0], ac2[i][1])), rs, o, 0, 16);
    __builtin_amdgcn_raw_buffer_store_b128(__builtin_bit_cast(u32x4, pack8(ac2[i][2], ac2[i][3])), rs, o + 1024, 0, 16);
  }
  GAS unsigned* cnt = (GAS unsigned*)(ws + WS_CNT) + (tile * 32 + w) * 16;
  GAS unsigned* mine = cnt + kq * 128 + (lane & 3);
  seen = __hip_atomic_load(mine, __ATOMIC_RELAXED, __HIP_MEMORY_SCOPE_AGENT);
  SCHED();
#pragma unroll
  for (int n = 0; n < 4; ++n)
#pragma unroll
    for (int u = 0; u < 4; ++u) ac2[0][n] = mfma(kb[u >> 1][u & 1][n], ka[u >> 1][u & 1], ac2[0][n]);
  SCHED();
  WAIT_V(0);
  ST(4);
  asm volatile("" : "+v"(xr[0]), "+v"(xr[1]));
  if (lane >= 1 && lane < 4) __hip_atomic_store(cnt + (kq ^ lane) * 128 + lane, 1u, __ATOMIC_RELAXED, __HIP_MEMORY_SCOPE_AGENT);
  u32x4 p[3][2];
#pragma unroll
  for (int i = 0; i < 3; ++i) p[i][0] = p[i][1] = (u32x4){0u, 0u, 0u, 0u};
  {
    unsigned done = 0;
#pragma clang loop unroll(disable)
    for (unsigned spins = 0; spins < SPIN_CAP; ++spins) {
      const unsigned rdy = (unsigned)__builtin_amdgcn_ballot_w64(seen != 0u) & 0xeu & ~done;
      asm volatile("" ::: "memory");
#pragma unroll
      for (int i = 1; i < 4; ++i)
        if (rdy & (1u << i)) {
          const unsigned o = xo(kq ^ i, i);
          p[i - 1][0] = __builtin_amdgcn_raw_buffer_load_b128(rs, o, 0, 16);
          p[i - 1][1] = __builtin_amdgcn_raw_buffer_load_b128(rs, o + 1024, 0, 16);
        }
      done |= rdy;
      if (done == 0xeu) break;
      if (!rdy) __builtin_amdgcn_s_sleep(1);
      seen = __hip_atomic_load(mine, __ATOMIC_RELAXED, __HIP_MEMORY_SCOPE_AGENT);
    }
  }
  ST(5);
  const auto ors = __builtin_amdgcn_make_buffer_rsrc(out, 0, 4 << 20, 0x00020000);
#pragma unroll
  for (int h = 0; h < 2; ++h) {
    const auto x0 = __builtin_amdgcn_permlane16_swap(xr[h][0], xr[h][2], false, false), x1 = __builtin_amdgcn_permlane16_swap(xr[h][1], xr[h][3], false, false);
    const u32x4 xv = {x0[0], x1[0], x0[1], x1[1]};
    unsigned r[4];
#pragma unroll
    for (int d = 0; d < 4; ++d) {
      float lo = ac2[0][2 * h + (d >> 1)][2 * (d & 1)] + __uint_as_float(xv[d] << 16), hi = ac2[0][2 * h + (d >> 1)][2 * (d & 1) + 1] + __uint_as_float(xv[d] & 0xffff0000u);
#pragma unroll
      for (int i = 0; i < 3; ++i) { lo += __uint_as_float(p[i][h][d] << 16); hi += __uint_as_float(p[i][h][d] & 0xffff0000u); }
      r[d] = pk2(lo, hi);
    }
    const auto y0 = __builtin_amdgcn_permlane16_swap(r[0], r[2], false, false), y1 = __builtin_amdgcn_permlane16_swap(r[1], r[3], false, false);
    __builtin_amdgcn_raw_buffer_store_b128((u32x4){y0[0], y1[0], y0[1], y1[1]}, ors, orow + 64 * h, 0, OUT_AUX);
  }
  ST(6);
  ST(7);
  ST_OUT(1, 8);
  ST_REST(); ST_OUT(0, 6);
}

}

extern "C" __global__ __launch_bounds__(512, 2) void psum_g1_kernel(const u16* __restrict__ x, u16* __restrict__ out, const u16* __restrict__ wqkv,
                                                                    const u16* __restrict__ wg, const u16* __restrict__ wu,
                                                                    const u16* __restrict__ wo, const u16* __restrict__ wd, char* __restrict__ ws) {
  if (blockIdx.x < 128) body<true>(x, out, wqkv, wg, wu, wo, wd, ws);
  else body<false>(x, out, wqkv, wg, wu, wo, wd, ws);
}

extern "C" void kernel_launch(void* const* d_in, const int* in_sizes, int n_in, void* d_out, int out_size, void* d_ws,
                              size_t ws_size, hipStream_t stream) {
  const u16* x = (const u16*)d_in[0];
  const u16* wqkv = (const u16*)d_in[1];
  const u16* wo = (const u16*)d_in[2];
  const u16* wg = (const u16*)d_in[4];
  const u16* wu = (const u16*)d_in[5];
  const u16* wd = (const u16*)d_in[6];
  char* ws = (char*)d_ws;
  (void)hipFuncSetAttribute((const void*)psum_g1_kernel, hipFuncAttributeMaxDynamicSharedMemorySize, G1_LDS);
  psum_g1_kernel<<<256, 512, G1_LDS, stream>>>(x, (u16*)d_out, wqkv, wg, wu, wo, wd, ws);
}
```

```cpp
#include <hip/hip_runtime.h>
#include <hip/hip_bf16.h>
#include <type_traits>

#define SPIN_CAP (1u << 17)
#define SC1 16
#define PRIO(n) __builtin_amdgcn_s_setprio(n)

namespace {

typedef unsigned short u16;
typedef short bf16x8 __attribute__((ext_vector_type(8)));
typedef short bf16x4 __attribute__((ext_vector_type(4)));
typedef float f32x4 __attribute__((ext_vector_type(4)));
typedef float f32x2 __attribute__((ext_vector_type(2)));
typedef unsigned u32x4 __attribute__((ext_vector_type(4)));
typedef unsigned u32x2 __attribute__((ext_vector_type(2)));
typedef __bf16 bf2 __attribute__((ext_vector_type(2)));
#define LAS __attribute__((address_space(3)))
#define GAS __attribute__((address_space(1)))

#define WAIT_V(n) asm volatile("s_waitcnt vmcnt(" #n ")" ::: "memory")
#define WAIT_V_IF0(n, c) asm volatile("s_cmp_lg_u32 %0, 0\n\ts_cbranch_scc1 1f\n\ts_waitcnt vmcnt(" #n ")\n1:" :: "s"(c) : "memory", "scc")
#define WAIT_L0() do { asm volatile("" ::: "memory"); __builtin_amdgcn_s_waitcnt(0xC07F); asm volatile("" ::: "memory"); } while (0)
#define BAR() do { asm volatile("" ::: "memory"); __builtin_amdgcn_s_barrier(); asm volatile("" ::: "memory"); } while (0)
#define SCHED() __builtin_amdgcn_sched_barrier(0)

__device__ __forceinline__ unsigned pk2(float lo, float hi) {
  bf2 r = {(__bf16)lo, (__bf16)hi};
  return __builtin_bit_cast(unsigned, r);
}
__device__ __forceinline__ float rbf(float f) { return (float)(__bf16)f; }
__device__ __forceinline__ bf16x8 pack8(f32x4 a, f32x4 b) {
  u32x4 r = {pk2(a[0], a[1]), pk2(a[2], a[3]), pk2(b[0], b[1]), pk2(b[2], b[3])};
  return __builtin_bit_cast(bf16x8, r);
}
__device__ __forceinline__ bf16x4 trd(LAS const char* p) {
  return __builtin_amdgcn_ds_read_tr16_b64_v4i16((LAS bf16x4*)p);
}
__device__ __forceinline__ bf16x8 cat(bf16x4 a, bf16x4 b) { return __builtin_shufflevector(a, b, 0, 1, 2, 3, 4, 5, 6, 7); }
__device__ __forceinline__ f32x4 mfma(bf16x8 a, bf16x8 b, f32x4 c) {
  return __builtin_amdgcn_mfma_f32_16x16x32_bf16(a, b, c, 0, 0, 0);
}
__device__ __forceinline__ void dma_first(unsigned lds_dst, unsigned voff, const char* b0) {
  asm volatile("s_mov_b32 m0, %0\n\ts_nop 0\n\tglobal_load_lds_dwordx4 %1, %2" :: "s"(lds_dst), "v"(voff), "s"(b0) : "memory");
}
template <int OFF>
__device__ __forceinline__ void dma_next(unsigned voff, const char* b) {
  asm volatile("global_load_lds_dwordx4 %0, %1 offset:%2" :: "v"(voff), "s"(b), "i"(OFF) : "memory");
}
__device__ __forceinline__ void dmc_first(unsigned lds_dst, unsigned voff, const char* b0) {
  asm volatile("s_mov_b32 m0, %0\n\ts_nop 0\n\tglobal_load_lds_dwordx4 %1, %2 sc1" :: "s"(lds_dst), "v"(voff), "s"(b0) : "memory");
}
template <int OFF>
__device__ __forceinline__ void dmc_next(unsigned voff, const char* b) {
  asm volatile("global_load_lds_dwordx4 %0, %1 offset:%2 sc1" :: "v"(voff), "s"(b), "i"(OFF) : "memory");
}
__device__ __forceinline__ void dma1m(unsigned lds_dst, const char* p, unsigned long long mask) {
  asm volatile("s_mov_b32 m0, %0\n\ts_mov_b64 exec, %2\n\t"
               "global_load_lds_dwordx4 %1, off sc1\n\t"
               "s_mov_b64 exec, -1"
               :: "s"(lds_dst), "v"(p), "s"(mask) : "memory");
}
__device__ __forceinline__ void touch(unsigned lds_dst, const void* p) {
  asm volatile("s_mov_b32 m0, %0\n\ts_nop 0\n\tglobal_load_lds_dword %1, off" :: "s"(lds_dst), "v"(p) : "memory");
}
__device__ __forceinline__ LAS char* lp(unsigned a) { return (LAS char*)(size_t)a; }
__device__ __forceinline__ float fmx(float a, float b) { return __builtin_elementwise_maximum(a, b); }
__device__ __forceinline__ float xsum(float v) {
  auto a = __builtin_amdgcn_permlane16_swap(__float_as_uint(v), __float_as_uint(v), false, false);
  v = __uint_as_float(a[0]) + __uint_as_float(a[1]);
  auto b = __builtin_amdgcn_permlane32_swap(__float_as_uint(v), __float_as_uint(v), false, false);
  return __uint_as_float(b[0]) + __uint_as_float(b[1]);
}

template <int CTRL>
__device__ __forceinline__ float dpp_add(float v) {
  return v + __uint_as_float(__builtin_amdgcn_update_dpp(0u, __float_as_uint(v), CTRL, 0xf, 0xf, true));
}
__device__ __forceinline__ float hsum(float v) {
  v = dpp_add<0xB1>(v);
  v = dpp_add<0x4E>(v);
  v = dpp_add<0x128>(v);
  return xsum(v);
}

constexpr int XN_STEP = 17 * 1024;
constexpr size_t WS_XN = 0;
constexpr size_t WS_H = WS_XN + (size_t)8 * 32 * XN_STEP;
constexpr size_t H_BYTES = (size_t)8 * 4 * 36 * 16 * 1024;
constexpr size_t WS_CNT = WS_H + H_BYTES;
constexpr size_t WS_GCNT = WS_CNT + 131072;
constexpr size_t WS_HCNT = WS_GCNT + 8192;
constexpr size_t WS_LNF = WS_HCNT + 32768;


constexpr int G1_SA = 17 * 1024, G1_SB = 20 * 1024, G1_SLOT = G1_SA + G1_SB;
constexpr int T_SCR = 159488;
constexpr int T_BIAS = T_SCR + 256;
constexpr int G1_LDS = T_BIAS + 4096;
constexpr int G2_SA = 32768, G2_SB = 16384, G2_SLOT = G2_SA + G2_SB;
constexpr int T_SYNC = T_SCR - 64;
constexpr int T_KV = 2 * G2_SLOT, KV_BYTES = 7 * 1024 + 272 * 80;

template <bool MAX>
__device__ __forceinline__ float tred(float a, float b, float c, float d) {
  const auto r1 = __builtin_amdgcn_permlane16_swap(__float_as_uint(a), __float_as_uint(b), false, false);
  const auto r2 = __builtin_amdgcn_permlane16_swap(__float_as_uint(c), __float_as_uint(d), false, false);
  const float x0 = __uint_as_float(r1[0]), x1 = __uint_as_float(r1[1]), y0 = __uint_as_float(r2[0]), y1 = __uint_as_float(r2[1]);
  const float t = MAX ? fmx(x0, x1) : x0 + x1, u = MAX ? fmx(y0, y1) : y0 + y1;
  const auto r3 = __builtin_amdgcn_permlane32_swap(__float_as_uint(t), __float_as_uint(u), false, false);
  const float z0 = __uint_as_float(r3[0]), z1 = __uint_as_float(r3[1]);
  return MAX ? fmx(z0, z1) : z0 + z1;
}
__device__ __forceinline__ u32x2 glu4(f32x4 g, f32x4 u) {
  f32x4 e = g * -1.4426950408889634f;
#pragma unroll
  for (int i = 0; i < 4; ++i) e[i] = __builtin_amdgcn_exp2f(e[i]);
  e = e + 1.0f;
#pragma unroll
  for (int i = 0; i < 4; ++i) e[i] = __builtin_amdgcn_rcpf(e[i]);
  e = e * u;
  return (u32x2){pk2(e[0], e[1]), pk2(e[2], e[3])};
}
__device__ __forceinline__ u32x4 cat4(u32x2 a, u32x2 b) { return (u32x4){a[0], a[1], b[0], b[1]}; }

constexpr unsigned long long step_order(bool ta, int j, int part) {
  const int own0 = ta ? j : (16 + 5 * j + (j & 1)) / 2, own1 = ta ? 4 + j : own0 + 1;
  unsigned long long r[2] = {0, 0};
  int p = 0;
  for (int t = 0; t < 18; ++t) {
    const int s = (t + 8) % 18;
    if (s == own0 || s == own1) continue;
    r[p / 12] |= (unsigned long long)s << (5 * (p % 12));
    ++p;
  }
  return r[part];
}

template <bool typeA>
__device__ __forceinline__ void body(const u16* __restrict__ x, u16* __restrict__ out, const u16* __restrict__ wqkv, const u16* __restrict__ wg,
                                     const u16* __restrict__ wu, const u16* __restrict__ wo, const u16* __restrict__ wd, char* __restrict__ ws) {
  extern __shared__ __attribute__((aligned(16))) char smem[];
  LAS char* lds = (LAS char*)smem;
  const int tid = threadIdx.x;
  int lane = tid & 63;
  const int w = __builtin_amdgcn_readfirstlane(tid >> 6), g = w >> 2, wi = w & 3;
  const int b = blockIdx.x, xcd = b & 7, idx = b >> 3;
  const int mb = (xcd & 1) * 4 + (idx & 3), kq = xcd >> 1, ntl = idx >> 2;
  const int j = ntl & 3;
  int fr = lane & 15, fq = lane >> 4;
  const int fA = 896 * kq + 64 * j;
  const int f0 = 896 * kq + 256 + 160 * j, odd = j & 1;
  const int fa = f0 + 32 * odd, fb = fa + 64, fc = odd ? f0 : f0 + 128;

  const int mi = kq * 8 + ntl;
  const bool lnh = !typeA && w == 4 && j < 2, lnw = g == 0 || lnh;
  const char* const xnb = ws + WS_XN + (size_t)mb * 32 * XN_STEP;
  if (lnw) {
    PRIO(2);
    const int lr = (lnh ? (kq * 2 + j) * 2 : (mi & 1) * 8 + wi * 2) + ((lane >> 2) & 1), lf = lnh ? 16 : mi >> 1;
    const int lrow = lnh ? (mb > 0 ? mb * 256 - 16 : 0) + 2 * (lr & 7) + (lr >> 3) : mb * 256 + 32 * (lf >> 1) + 2 * lr + (lf & 1);
    const char* xr_ = (const char*)x + (lrow * 2048 + (lane >> 3) * 64 + (lane & 3) * 16);
    u32x4 raw[4];
#pragma unroll
    for (int i = 0; i < 4; ++i) raw[i] = *(const u32x4*)(xr_ + 512 * i);
    const auto xrs = __builtin_amdgcn_make_buffer_rsrc((void*)xnb, 0, 32 * XN_STEP, 0x00020000);
    const unsigned o = (lane >> 3) * XN_STEP + lf * 1024 + lr * 64 + (((lane & 3) ^ (2 * (lr >> 3))) * 16);
    f32x2 v[16];
#pragma unroll
    for (int i = 0; i < 16; ++i) v[i] = (f32x2){__uint_as_float(raw[i >> 2][i & 3] << 16), __uint_as_float(raw[i >> 2][i & 3] & 0xffff0000u)};
    f32x2 s2 = (v[0] + v[1]) + (v[2] + v[3]);
#pragma unroll
    for (int i = 4; i < 16; i += 4) s2 += (v[i] + v[i + 1]) + (v[i + 2] + v[i + 3]);
    const float mean = hsum(s2[0] + s2[1]) * (1.0f / 1024.0f);
    f32x2 qa = {0.f, 0.f}, qb = {0.f, 0.f};
#pragma unroll
    for (int i = 0; i < 16; i += 2) { const f32x2 d = v[i] - mean, e = v[i + 1] - mean; qa += d * d; qb += e * e; }
    qa += qb;
    const float var = hsum(qa[0] + qa[1]) * (1.0f / 1024.0f);
    const float inv = __builtin_amdgcn_rcpf(rbf(__builtin_amdgcn_sqrtf(rbf(rbf(var) + 1e-5f)))), sh = -rbf(mean);
#pragma unroll
    for (int i = 0; i < 4; ++i) {
      u32x4 t;
#pragma unroll
      for (int e = 0; e < 4; ++e) { const f32x2 y = (v[4 * i + e] + sh) * inv; t[e] = pk2(y[0], y[1]); }
      __builtin_amdgcn_raw_buffer_store_b128(t, xrs, o + i * 8 * XN_STEP, 0, SC1);
    }
    PRIO(0);
  }
  GAS unsigned* lnf = (GAS unsigned*)(ws + WS_LNF) + mb * 1024 + (lane < 32 ? mi * 32 + lane : (lane - 32) * 32 + mi);
  unsigned entry = 0;
  asm volatile("s_cmp_lg_u32 %2, 7\n\ts_cbranch_scc1 1f\n\tglobal_load_dword %0, %1, off sc1\n1:" : "+v"(entry) : "v"(lnf), "s"(w) : "memory", "scc");
  if (w == 3) {
    char* z = lane < 8 ? ws + WS_CNT + (((mb * 8 + ntl) * 4 + kq) * 8 + lane) * 64
            : (lane < 10 ? ws + WS_HCNT + ((mb * 4 + kq) * 8 + ntl) * 128 + (lane - 8) * 16 : ws + WS_GCNT + (mb * 4 + kq) * 64);
    if (lane < (ntl == 0 ? 11 : 10)) asm volatile("global_store_dwordx4 %0, %1, off sc1\n\ts_nop 1" :: "v"(z), "v"((u32x4){0u, 0u, 0u, 0u}) : "memory");
  }

  const bool isq = typeA && wi < 3;
  const int ld = isq ? 3072 : 3584;
  const u16* q1 = isq ? wqkv + (wi * 1024 + 256 * kq + 64 * j) : ((!typeA && (wi & 2)) ? wu : wg) + (typeA ? fA : ((wi & 1) ? fb : fa));
  const char* c1 = g ? (const char*)q1 : xnb + wi * 4096;
  const int s1 = g ? 8 * ld - 1024 : 0, s2 = g ? 32 * ld - 2048 : 0, s3 = g ? 40 * ld - 3072 : 0;
  const int p1 = g ? 64 * ld : XN_STEP, p2 = g ? XN_STEP : 64 * 3584;
  const int d1 = (g ? G1_SA : 0) + wi * 4096, d2 = g ? 16384 + wi * 256 : G1_SA + 16384 + wi * 1024;
  const unsigned long long m2 = g ? 0xffffull : ~0ull;
  const int rr = lane >> 3, pc = lane & 7, fi = (pc >> 1) ^ ((rr >> 1) & 3), kr = (rr & 3) + 8 * (rr >> 2);
  const unsigned v1 = g ? kr * ld * 2 + fi * 32 + (pc & 1) * 16 : lane * 16;
  const unsigned lb = (unsigned)(size_t)lds;
  const char* c2;
  {
    const unsigned ow = (kr + 16 * (wi >> 1) + 4 * (wi & 1)) * 7168 + (typeA ? fi * 32 : (fi >> 1) * 32) + (pc & 1) * 16;
    const char* be = g ? xnb : (const char*)(typeA ? wu + fA : wg + fc);
    const char* bo = g ? be : (const char*)(wu + (typeA ? fA : fc));
    c2 = ((fi & 1) ? bo : be) + (g ? 16384 + wi * 256 + (lane & 15) * 16 : ow);
  }
  auto chain1 = [&](unsigned dst, const char* p) { dmc_first(dst + d1, v1, p); dmc_next<1024>(v1, p + s1); dmc_next<2048>(v1, p + s2); dmc_next<3072>(v1, p + s3); };
  auto first = [&](bool one) {
    if (one) { chain1(lb, c1); chain1(lb + G1_SLOT, c1 + p1); }
    else { dma1m(lb + d2, c2, m2); dma1m(lb + G1_SLOT + d2, c2 + p2, m2); }
  };
  if (!lnw) first(true);
  else WAIT_V(0);
  asm volatile("s_waitcnt vmcnt(8)" : "+v"(entry) :: "memory");
  BAR();
  if (w == 7 && lane >= 32) __hip_atomic_store(lnf, entry + 1u, __ATOMIC_RELAXED, __HIP_MEMORY_SCOPE_AGENT);
  if (lnw) first(g == 1);
  const int nt = ntl;
  const int kba = 8 + ((fa - 896 * kq) >> 5);
  const int own0 = typeA ? j : (kba >> 1), own1 = typeA ? 4 + j : (kba >> 1) + 1;
  f32x4 acc[4][10];
  f32x4 hacc = {0.f, 0.f, 0.f, 0.f};
#pragma unroll
  for (int i = 0; i < 4; ++i)
#pragma unroll
    for (int n = 0; n < 10; ++n) { acc[i][n] = (f32x4){0.f, 0.f, 0.f, 0.f}; asm volatile("" : "+v"(acc[i][n])); }
  const int rl = 4 * (fq & 1) + (fr >> 2);
  unsigned hoffA = lb + 16384 + fr * 64 + ((fq ^ (2 * (fr >> 3))) * 16);
  unsigned aoff = hoffA - 16384 + wi * 4096;
  unsigned boff0 = lb + G1_SA + (fq >> 1) * 2048 + rl * 128 + 32 * ((2 * g) ^ ((rl >> 1) & 3)) + 8 * (lane & 3);
  unsigned boff1 = boff0 ^ 32;
  unsigned hoffB = (boff0 ^ (32 * (wi & 1))) + (1 + (wi >> 1)) * 4096;
  asm volatile("" : "+v"(aoff), "+v"(hoffA), "+v"(boff0), "+v"(boff1), "+v"(hoffB), "+v"(hacc));
  {
    const int lo = fr + (wi == 0 ? -7 : (wi == 2 ? 1 : 9));
    const int hi = wi == 0 ? fr + 1 : ((wi >= 2 && mb == 0) ? 0 : (wi == 2 ? 8 : 16));
    f32x4 bv;
#pragma unroll
    for (int e = 0; e < 4; ++e) bv[e] = (4 * fq + e >= lo && 4 * fq + e < hi) ? 0.f : -3.0e38f;
    *(LAS f32x4*)(lds + T_BIAS + wi * 1024 + lane * 16) = bv;
    *(LAS unsigned*)(lds + T_SYNC + (lane & 1) * 4) = 0u;
  }
  if (w == 7) {
    for (unsigned spins = 0; spins < SPIN_CAP; ++spins) {
      unsigned now = entry + 1u;
      if (lane < 32) now = __hip_atomic_load(lnf, __ATOMIC_RELAXED, __HIP_MEMORY_SCOPE_AGENT);
      if (__builtin_amdgcn_ballot_w64(now == entry) == 0ull) break;
      __builtin_amdgcn_s_sleep(1);
    }
    if (lane >= 32) __hip_atomic_store(lnf, entry + 2u, __ATOMIC_RELAXED, __HIP_MEMORY_SCOPE_AGENT);
  }
  WAIT_V_IF0(0, g);
  BAR();
  first(g == 0);
  c1 += 2 * p1; c2 += 2 * p2;
  unsigned ns = lb + 2 * G1_SLOT;
  auto adv = [&]() { c1 += p1; c2 += p2; ns = (ns == lb + 3 * G1_SLOT) ? lb : ns + G1_SLOT; };
  WAIT_V(0);
  if (g == 1) BAR();

  auto step = [&](auto halo_c, auto last_c, int q) {
    constexpr bool HALO = decltype(halo_c)::value, LAST = decltype(last_c)::value;
    bf16x8 a[2][4], wn[4][2];
    asm volatile("" ::: "memory");
    auto rdq = [&](int e, int h) {
      if (e >= (HALO ? 12 : 10)) return;
      const int kk = h >> 1, t = h & 1;
      bf16x8& d = wn[e & 3][kk];
      if (e == 11) { if (t == 0) d = *(const LAS bf16x8*)(lp(hoffA) + kk * G1_SLOT); return; }
      const int n = e < 2 ? 8 + e : e - 2;
      const bf16x4 v = trd(lp(e == 10 ? hoffB : ((n & 1) ? boff1 : boff0)) + (e == 10 ? 0 : (n >> 1) * 4096) + t * 1024 + kk * G1_SLOT);
      if (t == 0) { d[0] = v[0]; d[1] = v[1]; d[2] = v[2]; d[3] = v[3]; } else { d[4] = v[0]; d[5] = v[1]; d[6] = v[2]; d[7] = v[3]; }
    };
#define RA1(kk, i) a[kk][i] = *(const LAS bf16x8*)(lp(aoff) + (kk) * G1_SLOT + (i) * 1024);
    const int dlt = (q & 1) ? -2 * G1_SLOT : 2 * G1_SLOT;
    if (!LAST) {
#pragma unroll
      for (int kk = 0; kk < 2; ++kk) {
        dmc_first(ns + d1, v1, c1); RA1(kk, 0) rdq(kk, 0);
        dmc_next<1024>(v1, c1 + s1); RA1(kk, 1) rdq(kk, 1);
        dmc_next<2048>(v1, c1 + s2); RA1(kk, 2) rdq(kk, 2);
        dmc_next<3072>(v1, c1 + s3); RA1(kk, 3) rdq(kk, 3);
        dma1m(ns + d2, c2, m2);
        adv();
      }
    } else
    {
#pragma unroll
      for (int kk = 0; kk < 2; ++kk)
#pragma unroll
        for (int i = 0; i < 4; ++i) RA1(kk, i)
#pragma unroll
      for (int h = 0; h < 8; ++h) rdq(h >> 2, h & 3);
    }
    aoff += dlt;
    SCHED();
    if (LAST) {
      int t = w * 64 + __builtin_amdgcn_mbcnt_hi(~0u, __builtin_amdgcn_mbcnt_lo(~0u, 0u));
      t = t < 288 ? t : t - 288;
      const int r2 = (t >> 4) * 64 + (xcd & 1) * 32 + idx;
      touch(lb + T_SCR, (r2 < 256 ? wo + (256 * kq + r2) * 1024 : wd + (896 * kq + r2 - 256) * 1024) + (t & 15) * 64);
    }
    WAIT_L0();
    SCHED();
    BAR();
    SCHED();
    PRIO(1);
    auto fin = [&]() { SCHED(); PRIO(3); if (!LAST) WAIT_V(0); BAR(); SCHED(); };
#pragma unroll
    for (int e = 0; e < 10; ++e) {
      const int n = e < 2 ? 8 + e : e - 2;
#pragma unroll
      for (int i = 0; i < 4; ++i) {
        acc[i][n] = mfma(wn[e & 3][0], a[0][i], acc[i][n]);
        SCHED();
        if (!HALO && e == 9 && i == 3) fin();
        SCHED();
        acc[i][n] = mfma(wn[e & 3][1], a[1][i], acc[i][n]);
        SCHED();
        if (e == 0) { rdq(2 + (i >> 1), 2 * (i & 1)); rdq(2 + (i >> 1), 2 * (i & 1) + 1); } else rdq(e + 3, i);
        if (i == 3 && e < 9) { asm volatile("" :: "v"(wn[(e + 1) & 3][0]), "v"(wn[(e + 1) & 3][1])); }
        SCHED();
      }
    }
    if (HALO) {
      hacc = mfma(wn[2][0], wn[3][0], hacc);
      WAIT_L0();
      fin();
      hacc = mfma(wn[2][1], wn[3][1], hacc);
    }
    hoffA += dlt; boff0 += dlt; boff1 += dlt; hoffB += dlt;
    PRIO(0);
    SCHED();
  };
  const char* const hbase = ws + WS_H + (size_t)(mb * 4 + kq) * 36 * 16384;
  auto brow = [&](int stp_, int w_) {
    return (const char*)(stp_ < 4 ? wo + (size_t)(256 * kq) * 1024 : wd + (size_t)(896 * kq) * 1024 - 256 * 1024) + ((stp_ * 64 + 8 * w_) * 1024 + nt * 128) * 2;
  };
  unsigned va, vb;
  auto stage1 = [&](auto halo_c) {
    constexpr bool TA = decltype(halo_c)::value;
    typedef std::integral_constant<bool, TA> HC;
#pragma clang loop unroll(disable)
    for (int q = 0; q < 15; ++q) step(HC{}, std::false_type{}, q);
    step(HC{}, std::true_type{}, 15);
    asm volatile("" ::: "memory");
    if (g == 0) PRIO(2); else PRIO(1);
    lane = __builtin_amdgcn_mbcnt_hi(~0u, __builtin_amdgcn_mbcnt_lo(~0u, 0u));
    asm volatile("" : "+v"(lane));
    fr = lane & 15; fq = lane >> 4;
    va = lane * 16;
    vb = (lane >> 3) * 2048 + ((((lane & 7) >> 1) ^ ((lane >> 4) & 3)) * 32) + (lane & 1) * 16;
    if (g == 1) {
#pragma unroll
      for (int u = 0; u < 4; ++u) {
        const int w_ = wi * 2 + (u & 1);
        const char* p0 = brow((u >> 1) ? own1 : own0, w_);
        dma_first(lb + (u >> 1) * G2_SLOT + G2_SA + w_ * 2048, vb, p0); dma_next<1024>(vb, p0 - 896);
      }
    }
    const auto hrs = __builtin_amdgcn_make_buffer_rsrc((void*)hbase, 0, 36 * 16384, 0x00020000);
    const unsigned hv = wi * 4096 + lane * 16;
    auto put = [&](u32x4 v, int s, int kb, int i) {
      __builtin_amdgcn_raw_buffer_store_b128(v, hrs, hv + kb * 16384 + i * 1024, 0, SC1);
      *(LAS u32x4*)(lds + s * G2_SLOT + g * 16384 + ((wi * 4096 + kq * 1024 + lane * 16) ^ (i * 1024))) = v;
    };
#define GLU(i, a, b) glu4(acc[i][a], acc[i][b])
    if (!TA) {
      const int kbc = 8 + ((fc - 896 * kq) >> 5);
      auto part = [&](int i) {
        put(cat4(GLU(i, 0, 4), GLU(i, 1, 5)), 0, kba + g, i);
        put(cat4(GLU(i, 2, 6), GLU(i, 3, 7)), 1, kba + 2 + g, i);
      };
      if (g == 0) { part(0); SCHED(); BAR(); PRIO(0); }
#pragma unroll
      for (int i = 0; i < 4; ++i) *(LAS u32x2*)(lds + T_KV + hv + i * 1024 + g * 8) = GLU(i, 8, 9);
      WAIT_L0();
      BAR();
#pragma unroll
      for (int i = 0; i < 2; ++i)
        __builtin_amdgcn_raw_buffer_store_b128(*(const LAS u32x4*)(lds + T_KV + hv + g * 2048 + i * 1024), hrs, hv + kbc * 16384 + g * 2048 + i * 1024, 0, SC1);
#pragma unroll
      for (int i = 1; i < 4; ++i) part(i);
      if (g == 1) part(0);
    } else {
      auto glu_part = [&]() {
#pragma unroll
        for (int i = 0; i < 4; ++i) put(cat4(GLU(i, 6, 8), GLU(i, 7, 9)), 1, 8 + 2 * j + g, i);
      };
      if (g == 0) { glu_part(); SCHED(); BAR(); PRIO(0); asm volatile("" ::: "memory"); }
    LAS char* kim = lds + T_KV + g * KV_BYTES;
    LAS char* vim = kim + 7 * 1024;
    bf16x8 kf[4], qf[4];
#pragma unroll
    for (int i = 0; i < 4; ++i) {
      kf[i] = pack8(acc[i][2], acc[i][3]);
      *(LAS bf16x8*)(vim + ((wi * 4 + i) * 16 + fr) * 80 + fq * 16) = pack8(acc[i][4], acc[i][5]);
    }
    if (wi < 3) {
      *(LAS bf16x8*)(kim + wi * 2048 + lane * 16) = kf[2];
      *(LAS bf16x8*)(kim + wi * 2048 + 1024 + lane * 16) = kf[3];
    }
    {
      LAS char* hp = (wi < 2) ? (kim + 6 * 1024 + lane * 16 + wi * 8) : (vim + (256 + fr) * 80 + fq * 16 + (wi - 2) * 8);
      *(LAS u32x2*)hp = (u32x2){pk2(hacc[0], hacc[1]), pk2(hacc[2], hacc[3])};
    }
    WAIT_L0();
    BAR();
    asm volatile("" ::: "memory");
    const bool halo = wi == 0;
    const f32x4 bc = *(const LAS f32x4*)(lds + T_BIAS + lane * 16), bp = *(const LAS f32x4*)(lds + T_BIAS + 1024 + lane * 16);
    const f32x4 bp0 = *(const LAS f32x4*)(lds + T_BIAS + (halo ? 2048 : 1024) + lane * 16);
    const f32x4 bp1 = *(const LAS f32x4*)(lds + T_BIAS + (halo ? 3072 : 1024) + lane * 16);
    const int pf0 = halo ? 16 : wi * 4 - 2, pf1 = halo ? 16 : wi * 4 - 1;
    const bf16x8 kp0 = *(const LAS bf16x8*)(kim + (halo ? 6 : 2 * wi - 2) * 1024 + lane * 16);
    const bf16x8 kp1 = *(const LAS bf16x8*)(kim + (halo ? 6 : 2 * wi - 1) * 1024 + lane * 16);
    const LAS char* vr = vim + (4 * fq + (fr >> 2)) * 80 + (lane & 3) * 16;
    bf16x4 vt[6][2];
#pragma unroll
    for (int d = 0; d < 2; ++d) {
      vt[0][d] = trd(vr + pf0 * 1280 + d * 8); vt[1][d] = trd(vr + pf1 * 1280 + d * 8);
#pragma unroll
      for (int i = 0; i < 4; ++i) vt[2 + i][d] = trd(vr + (wi * 4 + i) * 1280 + d * 8);
    }
    f32x4 sp[4], sc[4];
#pragma unroll
    for (int i = 0; i < 4; ++i) {
      qf[i] = pack8(acc[i][0], acc[i][1]);
      sc[i] = mfma(kf[i], qf[i], bc);
    }
    sp[2] = mfma(kf[0], qf[2], bp); sp[3] = mfma(kf[1], qf[3], bp);
    sp[0] = mfma(kp0, qf[0], bp0); sp[1] = mfma(kp1, qf[1], bp1);
    const float sl = 0.17677669529663687f * 1.4426950408889634f;
    const f32x4 z4 = {0.f, 0.f, 0.f, 0.f};
    float m[4];
#pragma unroll
    for (int i = 0; i < 4; ++i)
      m[i] = fmx(fmx(fmx(fmx(sp[i][0], sp[i][1]), sp[i][2]), fmx(fmx(sp[i][3], sc[i][0]), sc[i][1])), fmx(sc[i][2], sc[i][3]));
    {
      const unsigned t = __float_as_uint(tred<true>(m[0], m[1], m[2], m[3]));
      const auto u = __builtin_amdgcn_permlane32_swap(t, t, false, false);
      const auto u0 = __builtin_amdgcn_permlane16_swap(u[0], u[0], false, false), u1 = __builtin_amdgcn_permlane16_swap(u[1], u[1], false, false);
      m[0] = __uint_as_float(u0[0]); m[1] = __uint_as_float(u0[1]); m[2] = __uint_as_float(u1[0]); m[3] = __uint_as_float(u1[1]);
    }
    const u32x4 one4 = {0x3f803f80u, 0x3f803f80u, 0x3f803f80u, 0x3f803f80u};
#pragma unroll
    for (int i = 0; i < 4; ++i) {
      const float mm = -sl * m[i];
#pragma unroll
      for (int e = 0; e < 4; ++e) {
        sp[i][e] = __builtin_amdgcn_exp2f(__builtin_fmaf(sp[i][e], sl, mm));
        sc[i][e] = __builtin_amdgcn_exp2f(__builtin_fmaf(sc[i][e], sl, mm));
      }
      const bf16x8 pfr = pack8(sp[i], sc[i]);
      const f32x4 sm = mfma(__builtin_bit_cast(bf16x8, one4), pfr, z4);
      const f32x4 o0 = mfma(cat(vt[i][0], vt[2 + i][0]), pfr, z4);
      const f32x4 o1 = mfma(cat(vt[i][1], vt[2 + i][1]), pfr, z4);
      const float inv = __builtin_amdgcn_rcpf(sm[0]);
      put(__builtin_bit_cast(u32x4, pack8(o0 * inv, o1 * inv)), 0, 2 * j + g, i);
    }
      if (g == 1) glu_part();
    }
  };
  stage1(std::integral_constant<bool, typeA>{});
  const int lag = g ^ 1;
  GAS unsigned* hflag = (GAS unsigned*)(ws + WS_HCNT) + (mb * 4 + kq) * 256;
  const unsigned fo = (nt * 32 + (lane & 7)) * 4;
  GAS unsigned* gcnt = (GAS unsigned*)(ws + WS_GCNT) + (mb * 4 + kq) * 16;
  const int e0 = kq * 1024, e1 = ((1 ^ kq) - 1) * 1024, e2 = ((2 ^ kq) - 2) * 1024, e3 = ((3 ^ kq) - 3) * 1024;
  auto wait_cnt = [&](unsigned seen, GAS unsigned* c, unsigned need) {
    if ((unsigned)__builtin_amdgcn_readfirstlane(seen) < need) {
      asm volatile("" ::: "memory");
      unsigned spins = 0;
      while ((unsigned)__builtin_amdgcn_readfirstlane(__hip_atomic_load(c, __ATOMIC_RELAXED, __HIP_MEMORY_SCOPE_AGENT)) < need)
        if (++spins > SPIN_CAP) break;
    }
    asm volatile("" ::: "memory");
  };
  auto wait_flags = [&](unsigned seen, unsigned long long need) {
    if ((__builtin_amdgcn_ballot_w64(seen != 0u) & need) != need) {
      asm volatile("" ::: "memory");
      unsigned spins = 0;
      while ((__builtin_amdgcn_ballot_w64(__hip_atomic_load(hflag + (fo >> 2), __ATOMIC_RELAXED, __HIP_MEMORY_SCOPE_AGENT) != 0u) & need) != need) {
        __builtin_amdgcn_s_sleep(1);
        if (++spins > SPIN_CAP) break;
      }
    }
    asm volatile("" ::: "memory");
  };
  unsigned long long ord0 = j & 2 ? (j & 1 ? step_order(typeA, 3, 0) : step_order(typeA, 2, 0)) : (j & 1 ? step_order(typeA, 1, 0) : step_order(typeA, 0, 0));
  const unsigned ord1 = (unsigned)(j & 2 ? (j & 1 ? step_order(typeA, 3, 1) : step_order(typeA, 2, 1)) : (j & 1 ? step_order(typeA, 1, 1) : step_order(typeA, 0, 1)));
  const char* const caw = hbase + w * 4096;
  const char* const cbo = brow(0, w); const char* const cbd = brow(4, w) - 4 * 131072;
  auto pop = [&](const char*& ca_, const char*& cb_) {
    const unsigned s = (unsigned)ord0 & 31u;
    ord0 >>= 5;
    ca_ = caw + (s << 15); cb_ = (s < 4u ? cbo : cbd) + (s << 17);
  };
  const int mf = wi * 4 + kq;
  const unsigned orow = ((mb * 256 + 32 * (mf >> 1) + 2 * fr + (mf & 1)) * 1024 + nt * 128 + g * 64 + 16 * (fq & 1) + 8 * (fq >> 1)) * 2;
  u32x4 xr[2] = {{0u, 0u, 0u, 0u}, {0u, 0u, 0u, 0u}};
  const int rl2 = 4 * (fq & 1) + (fr >> 2);
  unsigned ao = lb + wi * 4096 + lane * 16;
  unsigned bo = lb + G2_SA + (fq >> 1) * 2048 + g * 1024 + rl2 * 128 + 32 * ((rl2 >> 1) & 3) + 8 * (lane & 3);
  f32x4 ac2[4][4];
#pragma unroll
  for (int i = 0; i < 4; ++i)
#pragma unroll
    for (int n = 0; n < 4; ++n) ac2[i][n] = (f32x4){0.f, 0.f, 0.f, 0.f};
  ns = lb + 2 * G2_SLOT;
  const char *ca, *cb;
  const unsigned ordf = (unsigned)ord0;
  pop(ca, cb);
  unsigned sl2 = lb;
  WAIT_V_IF0(0, lag);
  WAIT_L0();
  BAR();
  unsigned seen = 0;
#define PEEK(c, o) asm volatile("global_load_dword %0, %1, %2 sc1" : "+v"(seen) : "v"(o), "s"(c) : "memory")
  auto publish = [&]() {
    WAIT_V(0);
    unsigned old = 0;
    if (lane == 0) old = __hip_atomic_fetch_add((LAS unsigned*)(lds + T_SYNC), 1u, __ATOMIC_RELAXED, __HIP_MEMORY_SCOPE_WORKGROUP);
    if (__builtin_amdgcn_readfirstlane(old) == 3u && lane < 8) __hip_atomic_store(hflag + lane * 32 + nt, 1u, __ATOMIC_RELAXED, __HIP_MEMORY_SCOPE_AGENT);
  };
  if (lag) { if (!typeA) publish(); BAR(); }

  bf16x8 ka[2][2], kb[2][2][4];
  auto phase2 = [&](auto mode, int q) {
    constexpr int MODE = decltype(mode)::value;
    bf16x8 a[2][4], bfr[2][4];
#define RA(kk, i) a[kk][i] = *(const LAS bf16x8*)(lp(ao) + (kk) * 16384 + (i) * 1024);
#define RB(kk, n) bfr[kk][n] = cat(trd(lp(bo ^ ((n) * 32)) + (kk) * 8192), trd(lp(bo ^ ((n) * 32)) + (kk) * 8192 + 4096));
    int dlt;
    asm volatile("" ::: "memory");
    if (MODE == 1 && q == 1 && w == 4) PEEK(hflag, fo);
    if (MODE == 2 && q == 16)
      asm volatile("global_load_dwordx4 %0, %2, %3\n\tglobal_load_dwordx4 %1, %2, %3 offset:64" : "+v"(xr[0]), "+v"(xr[1]) : "v"(orow), "s"(x) : "memory");
    RA(0, 0) RA(0, 1)
    if (MODE == 0) dmc_first(ns + w * 4096, va, ca + e0);
    RA(0, 2) RA(0, 3) RB(0, 0)
    if (MODE == 0) dmc_next<1024>(va, ca + e1);
    RB(0, 1) RB(0, 2) RB(0, 3)
    if (MODE == 0) dmc_next<2048>(va, ca + e2);
    RA(1, 0) RA(1, 1) RA(1, 2)
    if (MODE == 0) dmc_next<3072>(va, ca + e3);
    RA(1, 3) RB(1, 0)
    if (MODE < 2) dma_first(ns + G2_SA + w * 2048, vb, cb);
    RB(1, 1)
    if (MODE < 2) dma_next<1024>(vb, cb - 896);
    dlt = (sl2 == lb + 2 * G2_SLOT) ? -2 * G2_SLOT : G2_SLOT; ns = sl2; sl2 += dlt;
    if (MODE < 2) pop(ca, cb);
    asm volatile("" : "+s"(ca), "+s"(cb), "+s"(ns), "+s"(sl2), "+s"(dlt), "+s"(ord0) :: "memory");
    RB(1, 2) RB(1, 3)
#undef RA
#undef RB
    ao += dlt; bo += dlt;
    SCHED();
    asm volatile("" : "+v"(bo), "+v"(ao));
    if (MODE == 0) WAIT_V_IF0(6, lag ^ 1); else if (MODE == 2 && q == 16) WAIT_V_IF0(2, lag ^ 1);
    if (MODE == 1 && q == 1) {
      asm volatile("s_waitcnt vmcnt(2)" : "+v"(seen) :: "memory");
      if (!lag) {
        volatile LAS unsigned* go = (volatile LAS unsigned*)(lds + T_SYNC + 4);
        if (w == 4) { wait_flags(seen, 0xf0ull); *go = 1u; }
        else { unsigned spins = 0; while (__builtin_amdgcn_readfirstlane(*go) == 0u) { __builtin_amdgcn_s_sleep(1); if (++spins > (SPIN_CAP << 3)) break; } }
        asm volatile("" ::: "memory");
      }
      const int s = (int)(ordf >> (5 * lag)) & 31;
      const char* p = hbase + s * 32768 + wi * 8192;
      const unsigned d = lb + (lag ? 0 : 2 * G2_SLOT) + wi * 8192;
      dmc_first(d, va, p + e0); dmc_next<1024>(va, p + e1); dmc_next<2048>(va, p + e2); dmc_next<3072>(va, p + e3);
      dmc_first(d + 4096, va, p + 4096 + e0); dmc_next<1024>(va, p + 4096 + e1); dmc_next<2048>(va, p + 4096 + e2); dmc_next<3072>(va, p + 4096 + e3);
    }
    WAIT_L0();
    SCHED();
    BAR();
    SCHED();
    PRIO(0);
#pragma unroll
    for (int n = 0; n < 4; ++n)
#pragma unroll
      for (int i = (MODE == 2 ? 1 : 0); i < 4; ++i) {
        ac2[i][n] = mfma(bfr[0][n], a[0][i], ac2[i][n]);
        SCHED();
        if (n == 3 && i == 3) {
          PRIO(1);
          if (MODE == 0) WAIT_V_IF0(6, lag); else if (MODE == 2 && q == 16) WAIT_V_IF0(2, lag);
          if (MODE == 1 && q == 1) WAIT_V_IF0(0, lag);
          if (q < 17 || !lag) BAR();
          SCHED();
        }
        ac2[i][n] = mfma(bfr[1][n], a[1][i], ac2[i][n]);
        SCHED();
        if (typeA && MODE == 1 && q == 0 && n == 1 && i == 3 && lag) { publish(); SCHED(); }
      }
    if (MODE == 2) {
#pragma unroll
      for (int kk = 0; kk < 2; ++kk) {
        ka[q & 1][kk] = a[kk][0];
#pragma unroll
        for (int n = 0; n < 4; ++n) kb[q & 1][kk][n] = bfr[kk][n];
      }
    }
    SCHED();
  };
  typedef std::integral_constant<int, 0> M0; typedef std::integral_constant<int, 1> M1; typedef std::integral_constant<int, 2> M2;
  phase2(M1{}, 0);
  phase2(M1{}, 1);
#pragma clang loop unroll(disable)
  for (int q = 2; q < 6; ++q) phase2(M0{}, q);
  PEEK(hflag, fo);
  phase2(M0{}, 6);
  asm volatile("" : "+v"(seen));
  ord0 |= (unsigned long long)ord1 << 20;
  wait_flags(seen, 0x0full);
#pragma clang loop unroll(disable)
  for (int q = 7; q < 13; ++q) phase2(M0{}, q);
  if (w == 4 && lane == 0) __hip_atomic_fetch_add(gcnt, 1u, __ATOMIC_RELAXED, __HIP_MEMORY_SCOPE_AGENT);
#pragma clang loop unroll(disable)
  for (int q = 13; q < 16; ++q) phase2(M0{}, q);
  phase2(M2{}, 16);
  PEEK(gcnt, 0u);
  phase2(M2{}, 17);
  asm volatile("s_waitcnt vmcnt(0)" : "+v"(seen) :: "memory");
  wait_cnt(seen, gcnt, 8u);
  const int tile = mb * 8 + nt;
  const auto rs = __builtin_amdgcn_make_buffer_rsrc(ws + WS_H + (size_t)mb * 4 * 36 * 16384, 0, 4 * 36 * 16384, 0x00020000);
  auto xo = [&](int src, int i) { const int u = 15 + nt * 3 + i; return (unsigned)((src * 36 + (u >= 36 ? u - 36 : u)) * 16384 + w * 2048) + lane * 16; };
#pragma unroll
  for (int i = 1; i < 4; ++i) {
    const unsigned o = xo(kq, i);
    __builtin_amdgcn_raw_buffer_store_b128(__builtin_bit_cast(u32x4, pack8(ac2[i][0], ac2[i][1])), rs, o, 0, SC1);
    __builtin_amdgcn_raw_buffer_store_b128(__builtin_bit_cast(u32x4, pack8(ac2[i][2], ac2[i][3])), rs, o + 1024, 0, SC1);
  }
  GAS unsigned* cnt = (GAS unsigned*)(ws + WS_CNT) + (tile * 32 + w) * 16;
  GAS unsigned* mine = cnt + kq * 128 + (lane & 3);
  seen = __hip_atomic_load(mine, __ATOMIC_RELAXED, __HIP_MEMORY_SCOPE_AGENT);
  SCHED();
#pragma unroll
  for (int n = 0; n < 4; ++n)
#pragma unroll
    for (int u = 0; u < 4; ++u) ac2[0][n] = mfma(kb[u >> 1][u & 1][n], ka[u >> 1][u & 1], ac2[0][n]);
  SCHED();
  WAIT_V(0);
  asm volatile("" : "+v"(xr[0]), "+v"(xr[1]));
  if (lane >= 1 && lane < 4) __hip_atomic_store(cnt + (kq ^ lane) * 128 + lane, 1u, __ATOMIC_RELAXED, __HIP_MEMORY_SCOPE_AGENT);
  u32x4 p[3][2];
#pragma unroll
  for (int i = 0; i < 3; ++i) p[i][0] = p[i][1] = (u32x4){0u, 0u, 0u, 0u};
  {
    unsigned done = 0;
#pragma clang loop unroll(disable)
    for (unsigned spins = 0; spins < SPIN_CAP; ++spins) {
      const unsigned rdy = (unsigned)__builtin_amdgcn_ballot_w64(seen != 0u) & 0xeu & ~done;
      asm volatile("" ::: "memory");
#pragma unroll
      for (int i = 1; i < 4; ++i)
        if (rdy & (1u << i)) {
          const unsigned o = xo(kq ^ i, i);
          p[i - 1][0] = __builtin_amdgcn_raw_buffer_load_b128(rs, o, 0, SC1);
          p[i - 1][1] = __builtin_amdgcn_raw_buffer_load_b128(rs, o + 1024, 0, SC1);
        }
      done |= rdy;
      if (done == 0xeu) break;
      if (!rdy) __builtin_amdgcn_s_sleep(1);
      seen = __hip_atomic_load(mine, __ATOMIC_RELAXED, __HIP_MEMORY_SCOPE_AGENT);
    }
  }
  const auto ors = __builtin_amdgcn_make_buffer_rsrc(out, 0, 4 << 20, 0x00020000);
#pragma unroll
  for (int h = 0; h < 2; ++h) {
    const auto x0 = __builtin_amdgcn_permlane16_swap(xr[h][0], xr[h][2], false, false), x1 = __builtin_amdgcn_permlane16_swap(xr[h][1], xr[h][3], false, false);
    const u32x4 xv = {x0[0], x1[0], x0[1], x1[1]};
    unsigned r[4];
#pragma unroll
    for (int d = 0; d < 4; ++d) {
      float lo = ac2[0][2 * h + (d >> 1)][2 * (d & 1)] + __uint_as_float(xv[d] << 16), hi = ac2[0][2 * h + (d >> 1)][2 * (d & 1) + 1] + __uint_as_float(xv[d] & 0xffff0000u);
#pragma unroll
      for (int i = 0; i < 3; ++i) { lo += __uint_as_float(p[i][h][d] << 16); hi += __uint_as_float(p[i][h][d] & 0xffff0000u); }
      r[d] = pk2(lo, hi);
    }
    const auto y0 = __builtin_amdgcn_permlane16_swap(r[0], r[2], false, false), y1 = __builtin_amdgcn_permlane16_swap(r[1], r[3], false, false);
    __builtin_amdgcn_raw_buffer_store_b128((u32x4){y0[0], y1[0], y0[1], y1[1]}, ors, orow + 64 * h, 0, SC1);
  }
}

}

extern "C" __global__ __launch_bounds__(512, 2) void psum_kernel(const u16* __restrict__ x, u16* __restrict__ out, const u16* __restrict__ wqkv,
                                                                    const u16* __restrict__ wg, const u16* __restrict__ wu,
                                                                    const u16* __restrict__ wo, const u16* __restrict__ wd, char* __restrict__ ws) {
  if (blockIdx.x < 128) body<true>(x, out, wqkv, wg, wu, wo, wd, ws);
  else body<false>(x, out, wqkv, wg, wu, wo, wd, ws);
}

extern "C" void kernel_launch(void* const* d_in, const int* in_sizes, int n_in, void* d_out, int out_size, void* d_ws,
                              size_t ws_size, hipStream_t stream) {
  const u16* x = (const u16*)d_in[0];
  const u16* wqkv = (const u16*)d_in[1];
  const u16* wo = (const u16*)d_in[2];
  const u16* wg = (const u16*)d_in[4];
  const u16* wu = (const u16*)d_in[5];
  const u16* wd = (const u16*)d_in[6];
  char* ws = (char*)d_ws;
  (void)hipFuncSetAttribute((const void*)psum_kernel, hipFuncAttributeMaxDynamicSharedMemorySize, G1_LDS);
  psum_kernel<<<256, 512, G1_LDS, stream>>>(x, (u16*)d_out, wqkv, wg, wu, wo, wd, ws);
}
```
